```python
import torch

def _softmax(x, axis=-1):
    return torch.softmax(x, dim=axis)
def _gelu(x):
    return torch.nn.functional.gelu(x, approximate="tanh")
def _silu(x):
    return torch.nn.functional.silu(x)
def _sigmoid(x):
    return torch.sigmoid(x)
def _layernorm(x, axis=-1, eps=1e-5):
    m = x.mean(dim=axis, keepdim=True)
    v = x.var(dim=axis, keepdim=True, unbiased=False)
    return (x - m) / torch.sqrt(v + eps)
def _scatter_add(tgt, idx, src, axis=0):
    return tgt.clone().index_add_(axis, idx, src)
def _segment_sum(src, seg, n):
    out = torch.zeros((n,) + src.shape[1:], dtype=src.dtype)
    return out.index_add_(0, seg, src)
def _pad(x, pw):
    flat = tuple(v for pair in reversed(pw) for v in pair)
    return torch.nn.functional.pad(x, flat)

kernel_name = 'interleave_layers__mha_block_cached__glu_gated_bilinear__decode'

def setup_inputs():

    _w = (lambda a: (lambda t: t.to(torch.bfloat16) if t.is_floating_point() else t)(torch.as_tensor(a)))
    def _A_setup():
        B, Sq, D = 2, 1, 448
        H, Sk, Dh = 16, 8192, 256
        return {
            "x": _w((lambda *s: torch.randn(*s, dtype=torch.bfloat16) * 0.05)(B, Sq, D)),
            "k_cache": _w((lambda *s: torch.randn(*s, dtype=torch.bfloat16) * 0.8)(B, H, Sk, Dh)),
            "v_cache": _w((lambda *s: torch.randn(*s, dtype=torch.bfloat16) * 1.6)(B, H, Sk, Dh)),
            "wq": _w((lambda *s: torch.randn(*s, dtype=torch.bfloat16) * 0.8)(D, H * Dh) * (D ** -0.5)),
            "wo": _w((lambda *s: torch.randn(*s, dtype=torch.bfloat16) * 1.6)(H * Dh, D) * ((H * Dh) ** -0.5)),
        }
    _A_in = _A_setup()

    def _B_setup():
        B, S, D, F = 2, 1, 448, 1792
        return {
            "x": _w((lambda *s: torch.randn(*s, dtype=torch.bfloat16) * 0.05)(B, S, D)),
            "w_gate": _w((lambda *s: torch.randn(*s, dtype=torch.bfloat16) * 0.4)(D, F) * (D ** -0.5)),
            "w_up": _w((lambda *s: torch.randn(*s, dtype=torch.bfloat16) * 0.4)(D, F) * (D ** -0.5)),
            "w_down": _w((lambda *s: torch.randn(*s, dtype=torch.bfloat16) * 0.4)(F, D) * (F ** -0.5)),
        }
    _B_in = _B_setup()

    return {
        "A_x": _A_in["x"],
        "A_k_cache": _A_in["k_cache"],
        "A_v_cache": _A_in["v_cache"],
        "A_wq": _A_in["wq"],
        "A_wo": _A_in["wo"],
        "B_x": _B_in["x"],
        "B_w_gate": _B_in["w_gate"],
        "B_w_up": _B_in["w_up"],
        "B_w_down": _B_in["w_down"],
    }

def reference(A_x, A_k_cache, A_v_cache, A_wq, A_wo, B_x, B_w_gate, B_w_up, B_w_down):
    def _A_ref(x, k_cache, v_cache, wq, wo):
        B, Sq, D = x.shape
        H, Dh = 16, 256
        q = (lambda x, *d: x.permute(*d))((x @ wq).reshape(B, Sq, H, Dh), 0, 2, 1, 3)
        scores = torch.einsum("bhqd,bhkd->bhqk", q, k_cache) * (Dh ** -0.5)
        p = _softmax(scores, axis=-1)
        o = torch.einsum("bhqk,bhkd->bhqd", p, v_cache)
        return (lambda x, *d: x.permute(*d))(o, 0, 2, 1, 3).reshape(B, Sq, H * Dh) @ wo

    def _B_ref(x, w_gate, w_up, w_down):
        g = torch.einsum("bsd,df->bsf", x, w_gate)
        u = torch.einsum("bsd,df->bsf", x, w_up)
        h = g * u
        return torch.einsum("bsf,fd->bsd", h, w_down)

    x = A_x
    x = x + _A_ref(_layernorm(x, axis=-1), A_k_cache, A_v_cache, A_wq, A_wo)
    x = x + _B_ref(_layernorm(x, axis=-1), B_w_gate, B_w_up, B_w_down)
    x = x + _A_ref(_layernorm(x, axis=-1), A_k_cache, A_v_cache, A_wq, A_wo)
    x = x + _B_ref(_layernorm(x, axis=-1), B_w_gate, B_w_up, B_w_down)
    return x
```

```cpp
#include <hip/hip_runtime.h>
#include <hip/hip_bf16.h>
#include <stdint.h>

typedef __attribute__((ext_vector_type(8))) __bf16 bf16x8;
typedef __attribute__((ext_vector_type(4))) float f32x4;
typedef __attribute__((ext_vector_type(4))) unsigned u32x4;
typedef unsigned short u16;

constexpr int B = 2, D = 448, H = 16, SK = 8192, DH = 256, F = 1792, HD = H * DH;
constexpr int NSPLIT = 32;
constexpr int KEYS_PER_SPLIT = SK / NSPLIT;
constexpr int KEYS_PER_WAVE = KEYS_PER_SPLIT / 4;

__device__ __forceinline__ float bf2f(u16 v) { return __uint_as_float(((unsigned)v) << 16); }
__device__ __forceinline__ u16 f2bf(float f) { __bf16 b = (__bf16)f; return __builtin_bit_cast(u16, b); }
__device__ __forceinline__ float rbf(float f) { return bf2f(f2bf(f)); }
__device__ __forceinline__ float lo16(unsigned w) { return __uint_as_float(w << 16); }
__device__ __forceinline__ float hi16(unsigned w) { return __uint_as_float(w & 0xffff0000u); }

__device__ void block_ln(const float* xs, float* xn, float* red, int tid) {
  float s = 0.f;
  for (int i = tid; i < D; i += 256) s += xs[i];
  for (int o = 32; o; o >>= 1) s += __shfl_xor(s, o);
  if ((tid & 63) == 0) red[tid >> 6] = s;
  __syncthreads();
  float mean = (red[0] + red[1] + red[2] + red[3]) * (1.f / D);
  __syncthreads();
  float v = 0.f;
  for (int i = tid; i < D; i += 256) { float d = xs[i] - mean; v += d * d; }
  for (int o = 32; o; o >>= 1) v += __shfl_xor(v, o);
  if ((tid & 63) == 0) red[tid >> 6] = v;
  __syncthreads();
  float var = (red[0] + red[1] + red[2] + red[3]) * (1.f / D);
  __syncthreads();
  float mb = rbf(mean), vb = rbf(var);
  float den = rbf(sqrtf(rbf(vb + 1e-5f)));
  for (int i = tid; i < D; i += 256) xn[i] = rbf(rbf(xs[i] - mb) / den);
  __syncthreads();
}

__global__ void __launch_bounds__(256) k_q(const u16* __restrict__ xin_bf, const float* __restrict__ xin_f, const u16* __restrict__ wq, u16* __restrict__ q) {
  __shared__ float xs[B][D], xn[B][D], red[4], part[32][B][64];
  int tid = threadIdx.x;
  for (int i = tid; i < B * D; i += 256) xs[i / D][i % D] = xin_bf ? bf2f(xin_bf[i]) : xin_f[i];
  __syncthreads();
  block_ln(xs[0], xn[0], red, tid);
  block_ln(xs[1], xn[1], red, tid);
  int c = tid & 7, ks = tid >> 3;
  int col0 = blockIdx.x * 64 + c * 8;
  float acc[B][8] = {};
  for (int r = 0; r < 14; ++r) {
    int k = ks * 14 + r;
    u32x4 w = *(const u32x4*)(wq + (size_t)k * HD + col0);
    for (int b = 0; b < B; ++b) { float a = xn[b][k];
      for (int j = 0; j < 4; ++j) { acc[b][2 * j] += a * lo16(w[j]); acc[b][2 * j + 1] += a * hi16(w[j]); } }
  }
  for (int b = 0; b < B; ++b) for (int j = 0; j < 8; ++j) part[ks][b][c * 8 + j] = acc[b][j];
  __syncthreads();
  if (tid < 128) { int b = tid >> 6, n = tid & 63; float s = 0.f; for (int i = 0; i < 32; ++i) s += part[i][b][n]; q[b * HD + blockIdx.x * 64 + n] = f2bf(s); }
}

__global__ void __launch_bounds__(256) k_attn(const u16* __restrict__ q, const u16* __restrict__ kc, const u16* __restrict__ vc, float* __restrict__ pml, float* __restrict__ po) {
  __shared__ float mrg[4][DH + 4];
  const int s = blockIdx.x, h = blockIdx.y, b = blockIdx.z;
  const int tid = threadIdx.x, l = tid & 63, w = tid >> 6, g = l >> 4, hi = (l >> 3) & 1;
  const size_t bh = (size_t)(b * H + h);
  const u16* kb = kc + (bh * SK + (size_t)s * KEYS_PER_SPLIT + w * KEYS_PER_WAVE) * DH;
  const u16* vb = vc + (bh * SK + (size_t)s * KEYS_PER_SPLIT + w * KEYS_PER_WAVE) * DH;
  bf16x8 qf[8];
  for (int i = 0; i < 8; ++i) qf[i] = *(const bf16x8*)(q + bh * DH + (4 * i + g) * 8);
  f32x4 acc[4] = {};
  float m = -INFINITY, lsum = 0.f;
  const float C = 0.0625f * 1.4426950408889634f;
  for (int blk = 0; blk < KEYS_PER_WAVE / 16; ++blk) {
    const u16* kp = kb + (size_t)(blk * 16 + (l & 15)) * DH + g * 8;
    bf16x8 kf[8];
    for (int i = 0; i < 8; ++i) kf[i] = __builtin_nontemporal_load((const bf16x8*)(kp + i * 32));
    bf16x8 vf[2][4];
    for (int t = 0; t < 2; ++t) { const u16* vp = vb + (size_t)(blk * 16 + 4 * g + 2 * t + hi) * DH + (l & 7) * 8;
      for (int i = 0; i < 4; ++i) vf[t][i] = __builtin_nontemporal_load((const bf16x8*)(vp + i * 64)); }
    f32x4 d = {0.f, 0.f, 0.f, 0.f};
    for (int i = 0; i < 8; ++i) d = __builtin_amdgcn_mfma_f32_16x16x32_bf16(kf[i], qf[i], d, 0, 0, 0);
    float sc[4];
    for (int r = 0; r < 4; ++r) sc[r] = rbf(d[r]) * C;
    float bm = fmaxf(fmaxf(sc[0], sc[1]), fmaxf(sc[2], sc[3]));
    bm = fmaxf(bm, __shfl_xor(bm, 16)); bm = fmaxf(bm, __shfl_xor(bm, 32));
    float mn = fmaxf(m, bm);
    float alpha = exp2f(m - mn);
    m = mn; lsum *= alpha;
    for (int i = 0; i < 4; ++i) acc[i] *= alpha;
    for (int t = 0; t < 2; ++t) {
      float st = hi ? sc[2 * t + 1] : sc[2 * t];
      float p = exp2f(st - m);
      lsum += p;
      unsigned pk = ((unsigned)f2bf(p)) << (16 * (l & 1));
      u32x4 bw;
      for (int r = 0; r < 4; ++r) bw[r] = (((l & 7) >> 1) == r) ? pk : 0u;
      bf16x8 bfr = __builtin_bit_cast(bf16x8, bw);
      for (int i = 0; i < 4; ++i) acc[i] = __builtin_amdgcn_mfma_f32_16x16x32_bf16(vf[t][i], bfr, acc[i], 0, 0, 0);
    }
  }
  float lt = lsum; lt += __shfl_xor(lt, 8); lt += __shfl_xor(lt, 16); lt += __shfl_xor(lt, 32);
  for (int i = 0; i < 4; ++i) for (int r = 0; r < 4; ++r) {
    float o = acc[i][r] + __shfl_xor(acc[i][r], 40);
    if (l < 32 && hi == 0) mrg[w][4 + 64 * i + 32 * (g & 1) + 8 * r + (l & 7)] = o;
  }
  if (l == 0) { mrg[w][0] = m; mrg[w][1] = lt; }
  __syncthreads();
  float M = fmaxf(fmaxf(mrg[0][0], mrg[1][0]), fmaxf(mrg[2][0], mrg[3][0]));
  float L = 0.f, o = 0.f;
  for (int ww = 0; ww < 4; ++ww) { float wt = exp2f(mrg[ww][0] - M); L += wt * mrg[ww][1]; o += wt * mrg[ww][4 + tid]; }
  size_t slot = bh * NSPLIT + s;
  po[slot * DH + tid] = o;
  if (tid == 0) { pml[slot * 2] = M; pml[slot * 2 + 1] = L; }
}

__global__ void __launch_bounds__(256) k_wo(const float* __restrict__ pml, const float* __restrict__ po, const u16* __restrict__ wo, float* __restrict__ ypart) {
  __shared__ float o[B][DH], part[32][B][64];
  int seg = blockIdx.x, h = blockIdx.y, tid = threadIdx.x;
  for (int b = 0; b < B; ++b) {
    size_t bh = b * H + h;
    float M = -INFINITY;
    for (int s = 0; s < NSPLIT; ++s) M = fmaxf(M, pml[(bh * NSPLIT + s) * 2]);
    float L = 0.f, a = 0.f;
    for (int s = 0; s < NSPLIT; ++s) { float wt = exp2f(pml[(bh * NSPLIT + s) * 2] - M); L += wt * pml[(bh * NSPLIT + s) * 2 + 1]; a += wt * po[(bh * NSPLIT + s) * DH + tid]; }
    o[b][tid] = rbf(a / L);
  }
  __syncthreads();
  int c = tid & 7, ks = tid >> 3;
  float acc[B][8] = {};
  for (int r = 0; r < 8; ++r) {
    int k = ks * 8 + r;
    u32x4 w = *(const u32x4*)(wo + (size_t)(h * DH + k) * D + seg * 64 + c * 8);
    for (int b = 0; b < B; ++b) { float a = o[b][k];
      for (int j = 0; j < 4; ++j) { acc[b][2 * j] += a * lo16(w[j]); acc[b][2 * j + 1] += a * hi16(w[j]); } }
  }
  for (int b = 0; b < B; ++b) for (int j = 0; j < 8; ++j) part[ks][b][c * 8 + j] = acc[b][j];
  __syncthreads();
  if (tid < 128) { int b = tid >> 6, n = tid & 63; float s = 0.f; for (int i = 0; i < 32; ++i) s += part[i][b][n]; ypart[(h * B + b) * D + seg * 64 + n] = s; }
}

__global__ void __launch_bounds__(256) k_gu(const u16* __restrict__ xin_bf, const float* __restrict__ xin_f, const float* __restrict__ ypart, const u16* __restrict__ wg, const u16* __restrict__ wu, float* __restrict__ x1out, float* __restrict__ hout) {
  __shared__ float xs[B][D], xn[B][D], red[4], part[32][2][B][64];
  int tid = threadIdx.x;
  for (int i = tid; i < B * D; i += 256) {
    int b = i / D, n = i % D;
    float s = 0.f; for (int h = 0; h < H; ++h) s += ypart[(h * B + b) * D + n];
    float x = xin_bf ? bf2f(xin_bf[i]) : xin_f[i];
    float x1 = rbf(x + rbf(s));
    xs[b][n] = x1;
    if (blockIdx.x == 0) x1out[i] = x1;
  }
  __syncthreads();
  block_ln(xs[0], xn[0], red, tid);
  block_ln(xs[1], xn[1], red, tid);
  int c = tid & 7, ks = tid >> 3;
  int col0 = blockIdx.x * 64 + c * 8;
  float ag[B][8] = {}, au[B][8] = {};
  for (int r = 0; r < 14; ++r) {
    int k = ks * 14 + r;
    u32x4 w1 = *(const u32x4*)(wg + (size_t)k * F + col0);
    u32x4 w2 = *(const u32x4*)(wu + (size_t)k * F + col0);
    for (int b = 0; b < B; ++b) { float a = xn[b][k];
      for (int j = 0; j < 4; ++j) { ag[b][2 * j] += a * lo16(w1[j]); ag[b][2 * j + 1] += a * hi16(w1[j]); au[b][2 * j] += a * lo16(w2[j]); au[b][2 * j + 1] += a * hi16(w2[j]); } }
  }
  for (int b = 0; b < B; ++b) for (int j = 0; j < 8; ++j) { part[ks][0][b][c * 8 + j] = ag[b][j]; part[ks][1][b][c * 8 + j] = au[b][j]; }
  __syncthreads();
  if (tid < 128) { int b = tid >> 6, n = tid & 63; float g = 0.f, u = 0.f; for (int i = 0; i < 32; ++i) { g += part[i][0][b][n]; u += part[i][1][b][n]; }
    hout[b * F + blockIdx.x * 64 + n] = rbf(rbf(g) * rbf(u)); }
}

__global__ void __launch_bounds__(256) k_down(const float* __restrict__ hin, const float* __restrict__ x1, const u16* __restrict__ wd, float* __restrict__ x2out, u16* __restrict__ out_bf) {
  __shared__ float hs[B][F], part[32][B][64];
  int tid = threadIdx.x, seg = blockIdx.x;
  for (int i = tid; i < B * F; i += 256) hs[i / F][i % F] = hin[i];
  __syncthreads();
  int c = tid & 7, ks = tid >> 3;
  float acc[B][8] = {};
  for (int r = 0; r < 56; ++r) {
    int k = ks * 56 + r;
    u32x4 w = *(const u32x4*)(wd + (size_t)k * D + seg * 64 + c * 8);
    for (int b = 0; b < B; ++b) { float a = hs[b][k];
      for (int j = 0; j < 4; ++j) { acc[b][2 * j] += a * lo16(w[j]); acc[b][2 * j + 1] += a * hi16(w[j]); } }
  }
  for (int b = 0; b < B; ++b) for (int j = 0; j < 8; ++j) part[ks][b][c * 8 + j] = acc[b][j];
  __syncthreads();
  if (tid < 128) { int b = tid >> 6, n = tid & 63; float s = 0.f; for (int i = 0; i < 32; ++i) s += part[i][b][n];
    float x2 = rbf(x1[b * D + seg * 64 + n] + rbf(s));
    if (x2out) x2out[b * D + seg * 64 + n] = x2;
    if (out_bf) out_bf[b * D + seg * 64 + n] = f2bf(x2); }
}

extern "C" void kernel_launch(void* const* d_in, const int* in_sizes, int n_in, void* d_out, int out_size, void* d_ws, size_t ws_size, hipStream_t stream) {
  const u16* x = (const u16*)d_in[0];
  const u16* kc = (const u16*)d_in[1];
  const u16* vc = (const u16*)d_in[2];
  const u16* wq = (const u16*)d_in[3];
  const u16* wo = (const u16*)d_in[4];
  const u16* wg = (const u16*)d_in[6];
  const u16* wu = (const u16*)d_in[7];
  const u16* wd = (const u16*)d_in[8];
  char* ws = (char*)d_ws;
  u16* q = (u16*)(ws);
  float* pml = (float*)(ws + 65536);
  float* po = (float*)(ws + 131072);
  float* ypart = (float*)(ws + 2 * 1048576);
  float* x1 = (float*)(ws + 3 * 1048576);
  float* x2 = (float*)(ws + 3 * 1048576 + 8192);
  float* hh = (float*)(ws + 3 * 1048576 + 16384);
  for (int layer = 0; layer < 2; ++layer) {
    const u16* xb = layer == 0 ? x : nullptr; const float* xf = layer == 0 ? nullptr : x2;
    k_q<<<64, 256, 0, stream>>>(xb, xf, wq, q);
    k_attn<<<dim3(NSPLIT, H, B), 256, 0, stream>>>(q, kc, vc, pml, po);
    k_wo<<<dim3(7, 16), 256, 0, stream>>>(pml, po, wo, ypart);
    k_gu<<<28, 256, 0, stream>>>(xb, xf, ypart, wg, wu, x1, hh);
    k_down<<<7, 256, 0, stream>>>(hh, x1, wd, layer == 0 ? x2 : nullptr, layer == 1 ? (u16*)d_out : nullptr);
  }
}
```

```cpp
#include <hip/hip_runtime.h>
#include <hip/hip_bf16.h>
#include <stdint.h>

typedef __attribute__((ext_vector_type(8))) __bf16 bf16x8;
typedef __attribute__((ext_vector_type(4))) float f32x4;
typedef __attribute__((ext_vector_type(4))) unsigned u32x4;
typedef unsigned short u16;

constexpr int B = 2, D = 448, H = 16, SK = 8192, DH = 256, F = 1792, HD = H * DH;
constexpr int NSPLIT = 32;
constexpr int KEYS_PER_SPLIT = SK / NSPLIT;
constexpr int KEYS_PER_WAVE = KEYS_PER_SPLIT / 4;

__device__ __forceinline__ float bf2f(u16 v) { return __uint_as_float(((unsigned)v) << 16); }
__device__ __forceinline__ u16 f2bf(float f) { __bf16 b = (__bf16)f; return __builtin_bit_cast(u16, b); }
__device__ __forceinline__ float rbf(float f) { return bf2f(f2bf(f)); }
__device__ __forceinline__ float lo16(unsigned w) { return __uint_as_float(w << 16); }
__device__ __forceinline__ float hi16(unsigned w) { return __uint_as_float(w & 0xffff0000u); }

__device__ void block_ln(const float* xs, float* xn, float* red, int tid) {
  float s = 0.f;
  for (int i = tid; i < D; i += 256) s += xs[i];
  for (int o = 32; o; o >>= 1) s += __shfl_xor(s, o);
  if ((tid & 63) == 0) red[tid >> 6] = s;
  __syncthreads();
  float mean = (red[0] + red[1] + red[2] + red[3]) * (1.f / D);
  __syncthreads();
  float v = 0.f;
  for (int i = tid; i < D; i += 256) { float d = xs[i] - mean; v += d * d; }
  for (int o = 32; o; o >>= 1) v += __shfl_xor(v, o);
  if ((tid & 63) == 0) red[tid >> 6] = v;
  __syncthreads();
  float var = (red[0] + red[1] + red[2] + red[3]) * (1.f / D);
  __syncthreads();
  float mb = rbf(mean), vb = rbf(var);
  float den = rbf(sqrtf(rbf(vb + 1e-5f)));
  for (int i = tid; i < D; i += 256) xn[i] = rbf(rbf(xs[i] - mb) / den);
  __syncthreads();
}

__global__ void __launch_bounds__(256) k_q(const u16* __restrict__ xin_bf, const float* __restrict__ xin_f, const u16* __restrict__ wq, u16* __restrict__ q) {
  __shared__ float xs[B][D], xn[B][D], red[4], part[32][B][64];
  int tid = threadIdx.x;
  for (int i = tid; i < B * D; i += 256) xs[i / D][i % D] = xin_bf ? bf2f(xin_bf[i]) : xin_f[i];
  __syncthreads();
  block_ln(xs[0], xn[0], red, tid);
  block_ln(xs[1], xn[1], red, tid);
  int c = tid & 7, ks = tid >> 3;
  int col0 = blockIdx.x * 64 + c * 8;
  float acc[B][8] = {};
  for (int r = 0; r < 14; ++r) {
    int k = ks * 14 + r;
    u32x4 w = *(const u32x4*)(wq + (size_t)k * HD + col0);
    for (int b = 0; b < B; ++b) { float a = xn[b][k];
      for (int j = 0; j < 4; ++j) { acc[b][2 * j] += a * lo16(w[j]); acc[b][2 * j + 1] += a * hi16(w[j]); } }
  }
  for (int b = 0; b < B; ++b) for (int j = 0; j < 8; ++j) part[ks][b][c * 8 + j] = acc[b][j];
  __syncthreads();
  if (tid < 128) { int b = tid >> 6, n = tid & 63; float s = 0.f; for (int i = 0; i < 32; ++i) s += part[i][b][n]; q[b * HD + blockIdx.x * 64 + n] = f2bf(s); }
}

__global__ void __launch_bounds__(256) k_attn(const u16* __restrict__ q, const u16* __restrict__ kc, const u16* __restrict__ vc, float* __restrict__ pml, float* __restrict__ po) {
  __shared__ float mrg[4][DH + 4];
  const int s = blockIdx.x, h = blockIdx.y, b = blockIdx.z;
  const int tid = threadIdx.x, l = tid & 63, w = tid >> 6, g = l >> 4, hi = (l >> 3) & 1;
  const size_t bh = (size_t)(b * H + h);
  const u16* kb = kc + (bh * SK + (size_t)s * KEYS_PER_SPLIT + w * KEYS_PER_WAVE) * DH;
  const u16* vb = vc + (bh * SK + (size_t)s * KEYS_PER_SPLIT + w * KEYS_PER_WAVE) * DH;
  bf16x8 qf[8];
  for (int i = 0; i < 8; ++i) qf[i] = *(const bf16x8*)(q + bh * DH + (4 * i + g) * 8);
  f32x4 acc[4] = {};
  float m = -INFINITY, lsum = 0.f;
  const float C = 0.0625f * 1.4426950408889634f;
  for (int blk = 0; blk < KEYS_PER_WAVE / 16; ++blk) {
    const u16* kp = kb + (size_t)(blk * 16 + (l & 15)) * DH + g * 8;
    bf16x8 kf[8];
    for (int i = 0; i < 8; ++i) kf[i] = __builtin_nontemporal_load((const bf16x8*)(kp + i * 32));
    bf16x8 vf[2][4];
    for (int t = 0; t < 2; ++t) { const u16* vp = vb + (size_t)(blk * 16 + 4 * g + 2 * t + hi) * DH + (l & 7) * 8;
      for (int i = 0; i < 4; ++i) vf[t][i] = __builtin_nontemporal_load((const bf16x8*)(vp + i * 64)); }
    f32x4 d = {0.f, 0.f, 0.f, 0.f};
    for (int i = 0; i < 8; ++i) d = __builtin_amdgcn_mfma_f32_16x16x32_bf16(kf[i], qf[i], d, 0, 0, 0);
    float sc[4];
    for (int r = 0; r < 4; ++r) sc[r] = rbf(d[r]) * C;
    float bm = fmaxf(fmaxf(sc[0], sc[1]), fmaxf(sc[2], sc[3]));
    bm = fmaxf(bm, __shfl_xor(bm, 16)); bm = fmaxf(bm, __shfl_xor(bm, 32));
    float mn = fmaxf(m, bm);
    float alpha = exp2f(m - mn);
    m = mn; lsum *= alpha;
    for (int i = 0; i < 4; ++i) acc[i] *= alpha;
    for (int t = 0; t < 2; ++t) {
      float st = hi ? sc[2 * t + 1] : sc[2 * t];
      float p = exp2f(st - m);
      lsum += p;
      unsigned pk = ((unsigned)f2bf(p)) << (16 * (l & 1));
      u32x4 bw;
      for (int r = 0; r < 4; ++r) bw[r] = (((l & 7) >> 1) == r) ? pk : 0u;
      bf16x8 bfr = __builtin_bit_cast(bf16x8, bw);
      for (int i = 0; i < 4; ++i) acc[i] = __builtin_amdgcn_mfma_f32_16x16x32_bf16(vf[t][i], bfr, acc[i], 0, 0, 0);
    }
  }
  float lt = lsum; lt += __shfl_xor(lt, 8); lt += __shfl_xor(lt, 16); lt += __shfl_xor(lt, 32);
  for (int i = 0; i < 4; ++i) for (int r = 0; r < 4; ++r) {
    float o = acc[i][r] + __shfl_xor(acc[i][r], 40);
    if (l < 32 && hi == 0) mrg[w][4 + 64 * i + 32 * (g & 1) + 8 * r + (l & 7)] = o;
  }
  if (l == 0) { mrg[w][0] = m; mrg[w][1] = lt; }
  __syncthreads();
  float M = fmaxf(fmaxf(mrg[0][0], mrg[1][0]), fmaxf(mrg[2][0], mrg[3][0]));
  float L = 0.f, o = 0.f;
  for (int ww = 0; ww < 4; ++ww) { float wt = exp2f(mrg[ww][0] - M); L += wt * mrg[ww][1]; o += wt * mrg[ww][4 + tid]; }
  size_t slot = bh * NSPLIT + s;
  po[slot * DH + tid] = o;
  if (tid == 0) { pml[slot * 2] = M; pml[slot * 2 + 1] = L; }
}

__global__ void __launch_bounds__(256) k_wo(const float* __restrict__ pml, const float* __restrict__ po, const u16* __restrict__ wo, float* __restrict__ ypart) {
  __shared__ float o[B][DH], part[32][B][64];
  int seg = blockIdx.x, h = blockIdx.y, tid = threadIdx.x;
  for (int b = 0; b < B; ++b) {
    size_t bh = b * H + h;
    float M = -INFINITY;
    for (int s = 0; s < NSPLIT; ++s) M = fmaxf(M, pml[(bh * NSPLIT + s) * 2]);
    float L = 0.f, a = 0.f;
    for (int s = 0; s < NSPLIT; ++s) { float wt = exp2f(pml[(bh * NSPLIT + s) * 2] - M); L += wt * pml[(bh * NSPLIT + s) * 2 + 1]; a += wt * po[(bh * NSPLIT + s) * DH + tid]; }
    o[b][tid] = rbf(a / L);
  }
  __syncthreads();
  int c = tid & 7, ks = tid >> 3;
  float acc[B][8] = {};
  for (int r = 0; r < 8; ++r) {
    int k = ks * 8 + r;
    u32x4 w = *(const u32x4*)(wo + (size_t)(h * DH + k) * D + seg * 64 + c * 8);
    for (int b = 0; b < B; ++b) { float a = o[b][k];
      for (int j = 0; j < 4; ++j) { acc[b][2 * j] += a * lo16(w[j]); acc[b][2 * j + 1] += a * hi16(w[j]); } }
  }
  for (int b = 0; b < B; ++b) for (int j = 0; j < 8; ++j) part[ks][b][c * 8 + j] = acc[b][j];
  __syncthreads();
  if (tid < 128) { int b = tid >> 6, n = tid & 63; float s = 0.f; for (int i = 0; i < 32; ++i) s += part[i][b][n]; ypart[(h * B + b) * D + seg * 64 + n] = s; }
}

__global__ void __launch_bounds__(256) k_gu(const u16* __restrict__ xin_bf, const float* __restrict__ xin_f, const float* __restrict__ ypart, const u16* __restrict__ wg, const u16* __restrict__ wu, float* __restrict__ x1out, float* __restrict__ hout) {
  __shared__ float xs[B][D], xn[B][D], red[4], part[32][2][B][64];
  int tid = threadIdx.x;
  for (int i = tid; i < B * D; i += 256) {
    int b = i / D, n = i % D;
    float s = 0.f; for (int h = 0; h < H; ++h) s += ypart[(h * B + b) * D + n];
    float x = xin_bf ? bf2f(xin_bf[i]) : xin_f[i];
    float x1 = rbf(x + rbf(s));
    xs[b][n] = x1;
    if (blockIdx.x == 0) x1out[i] = x1;
  }
  __syncthreads();
  block_ln(xs[0], xn[0], red, tid);
  block_ln(xs[1], xn[1], red, tid);
  int c = tid & 7, ks = tid >> 3;
  int col0 = blockIdx.x * 64 + c * 8;
  float ag[B][8] = {}, au[B][8] = {};
  for (int r = 0; r < 14; ++r) {
    int k = ks * 14 + r;
    u32x4 w1 = *(const u32x4*)(wg + (size_t)k * F + col0);
    u32x4 w2 = *(const u32x4*)(wu + (size_t)k * F + col0);
    for (int b = 0; b < B; ++b) { float a = xn[b][k];
      for (int j = 0; j < 4; ++j) { ag[b][2 * j] += a * lo16(w1[j]); ag[b][2 * j + 1] += a * hi16(w1[j]); au[b][2 * j] += a * lo16(w2[j]); au[b][2 * j + 1] += a * hi16(w2[j]); } }
  }
  for (int b = 0; b < B; ++b) for (int j = 0; j < 8; ++j) { part[ks][0][b][c * 8 + j] = ag[b][j]; part[ks][1][b][c * 8 + j] = au[b][j]; }
  __syncthreads();
  if (tid < 128) { int b = tid >> 6, n = tid & 63; float g = 0.f, u = 0.f; for (int i = 0; i < 32; ++i) { g += part[i][0][b][n]; u += part[i][1][b][n]; }
    hout[b * F + blockIdx.x * 64 + n] = rbf(rbf(g) * rbf(u)); }
}

__global__ void __launch_bounds__(256) k_down(const float* __restrict__ hin, const float* __restrict__ x1, const u16* __restrict__ wd, float* __restrict__ x2out, u16* __restrict__ out_bf) {
  __shared__ float hs[B][F], part[32][B][64];
  int tid = threadIdx.x, seg = blockIdx.x;
  for (int i = tid; i < B * F; i += 256) hs[i / F][i % F] = hin[i];
  __syncthreads();
  int c = tid & 7, ks = tid >> 3;
  float acc[B][8] = {};
  for (int r = 0; r < 56; ++r) {
    int k = ks * 56 + r;
    u32x4 w = *(const u32x4*)(wd + (size_t)k * D + seg * 64 + c * 8);
    for (int b = 0; b < B; ++b) { float a = hs[b][k];
      for (int j = 0; j < 4; ++j) { acc[b][2 * j] += a * lo16(w[j]); acc[b][2 * j + 1] += a * hi16(w[j]); } }
  }
  for (int b = 0; b < B; ++b) for (int j = 0; j < 8; ++j) part[ks][b][c * 8 + j] = acc[b][j];
  __syncthreads();
  if (tid < 128) { int b = tid >> 6, n = tid & 63; float s = 0.f; for (int i = 0; i < 32; ++i) s += part[i][b][n];
    float x2 = rbf(x1[b * D + seg * 64 + n] + rbf(s));
    if (x2out) x2out[b * D + seg * 64 + n] = x2;
    if (out_bf) out_bf[b * D + seg * 64 + n] = f2bf(x2); }
}

template <int NT, int DEPTH>
__global__ void __launch_bounds__(256) stream_ring(const u32x4* __restrict__ p, int nq_per_wave, unsigned* sink) {
  extern __shared__ char smem[];
  const int lane = threadIdx.x & 63, w = threadIdx.x >> 6;
  const size_t gw = (size_t)blockIdx.x * 4 + w;
  const u32x4* base = p + gw * (size_t)nq_per_wave * 64 + lane;
  u32x4 acc = {0, 0, 0, 0};
  u32x4 v[DEPTH];
#pragma unroll
  for (int j = 0; j < DEPTH; ++j) { const u32x4* a = base + (size_t)j * 64; v[j] = NT ? __builtin_nontemporal_load(a) : *a; }
  for (int i = DEPTH; i < nq_per_wave; i += DEPTH) {
#pragma unroll
    for (int j = 0; j < DEPTH; ++j) {
      acc ^= v[j];
      const u32x4* a = base + (size_t)(i + j) * 64;
      v[j] = NT ? __builtin_nontemporal_load(a) : *a;
    }
  }
#pragma unroll
  for (int j = 0; j < DEPTH; ++j) acc ^= v[j];
  unsigned r = acc[0] ^ acc[1] ^ acc[2] ^ acc[3];
  if (r == 0x12345678u) sink[0] = r;
}
extern "C" void kernel_launch(void* const* d_in, const int* in_sizes, int n_in, void* d_out, int out_size, void* d_ws, size_t ws_size, hipStream_t stream) {
  const u16* x = (const u16*)d_in[0];
  const u16* kc = (const u16*)d_in[1];
  const u16* vc = (const u16*)d_in[2];
  const u16* wq = (const u16*)d_in[3];
  const u16* wo = (const u16*)d_in[4];
  const u16* wg = (const u16*)d_in[6];
  const u16* wu = (const u16*)d_in[7];
  const u16* wd = (const u16*)d_in[8];
  char* ws = (char*)d_ws;
  u16* q = (u16*)(ws);
  float* pml = (float*)(ws + 65536);
  float* po = (float*)(ws + 131072);
  float* ypart = (float*)(ws + 2 * 1048576);
  float* x1 = (float*)(ws + 3 * 1048576);
  float* x2 = (float*)(ws + 3 * 1048576 + 8192);
  float* hh = (float*)(ws + 3 * 1048576 + 16384);
  for (int layer = 0; layer < 2; ++layer) {
    const u16* xb = layer == 0 ? x : nullptr; const float* xf = layer == 0 ? nullptr : x2;
    k_q<<<64, 256, 0, stream>>>(xb, xf, wq, q);
    k_attn<<<dim3(NSPLIT, H, B), 256, 0, stream>>>(q, kc, vc, pml, po);
    k_wo<<<dim3(7, 16), 256, 0, stream>>>(pml, po, wo, ypart);
    k_gu<<<28, 256, 0, stream>>>(xb, xf, ypart, wg, wu, x1, hh);
    k_down<<<7, 256, 0, stream>>>(hh, x1, wd, layer == 0 ? x2 : nullptr, layer == 1 ? (u16*)d_out : nullptr);
  }
}
```

```cpp
#include <hip/hip_runtime.h>
#include <stdint.h>

typedef __attribute__((ext_vector_type(8))) __bf16 bf16x8;
typedef __attribute__((ext_vector_type(4))) float f32x4;
typedef __attribute__((ext_vector_type(4))) unsigned u32x4;
typedef __attribute__((ext_vector_type(2))) unsigned u32x2;
typedef unsigned short u16;

constexpr unsigned WS_Q = 0;
constexpr unsigned WS_ML = 32768;
constexpr unsigned WS_Y = 40960;
constexpr unsigned WS_Z = 155648;
constexpr unsigned WS_P = 385024;
constexpr unsigned WS_END = 1302528;
constexpr unsigned SENT = 0xFFFFFFFFu;
constexpr unsigned SPIN_CAP = 1u << 18;

__device__ __forceinline__ float bf2f(u16 v) { return __uint_as_float(((unsigned)v) << 16); }
__device__ __forceinline__ unsigned pk2(float a, float b) {
  typedef __attribute__((ext_vector_type(2))) __bf16 bf16x2;
  bf16x2 t = {(__bf16)a, (__bf16)b};
  return __builtin_bit_cast(unsigned, t);
}
__device__ __forceinline__ float rbf(float f) { return __uint_as_float(pk2(f, f) & 0xffff0000u); }
template <int CTRL> __device__ __forceinline__ float dpp(float x) {
  return __int_as_float(__builtin_amdgcn_update_dpp(0, __float_as_int(x), CTRL, 0xf, 0xf, true));
}
__device__ __forceinline__ float row_sum(float x) {
  x += dpp<0x128>(x); x += dpp<0x124>(x); x += dpp<0x122>(x); x += dpp<0x121>(x); return x;
}
__device__ __forceinline__ float row_max(float x) {
  x = fmaxf(x, dpp<0x128>(x)); x = fmaxf(x, dpp<0x124>(x)); x = fmaxf(x, dpp<0x122>(x)); x = fmaxf(x, dpp<0x121>(x)); return x;
}
__device__ __forceinline__ float rows_sum(float x) {
  auto r = __builtin_amdgcn_permlane16_swap(__float_as_uint(x), __float_as_uint(x), false, false);
  x = __uint_as_float(r[0]) + __uint_as_float(r[1]);
  auto s = __builtin_amdgcn_permlane32_swap(__float_as_uint(x), __float_as_uint(x), false, false);
  return __uint_as_float(s[0]) + __uint_as_float(s[1]);
}
__device__ __forceinline__ float wave_sum(float x) { return rows_sum(row_sum(x)); }
__device__ __forceinline__ f32x4 fold2(f32x4 a, f32x4 b) {
  f32x4 o;
#pragma unroll
  for (int r = 0; r < 4; ++r) {
    auto s = __builtin_amdgcn_permlane32_swap(__float_as_uint(a[r]), __float_as_uint(b[r]), false, false);
    o[r] = __uint_as_float(s[0]) + dpp<0x128>(__uint_as_float(s[1]));
  }
  return o;
}
__device__ __forceinline__ f32x4 mfma(u32x4 a, u32x4 b, f32x4 c) {
  return __builtin_amdgcn_mfma_f32_16x16x32_bf16(__builtin_bit_cast(bf16x8, a), __builtin_bit_cast(bf16x8, b), c, 0, 0, 0);
}
__device__ __forceinline__ u32x4 selb(unsigned dup, u32x4 mask) { return u32x4{dup & mask[0], dup & mask[1], dup & mask[2], dup & mask[3]}; }
__device__ __forceinline__ unsigned san(float f) { unsigned u = __float_as_uint(f); return u < 0xFFFFFFFEu ? u : 0xFFFFFFFEu; }
__device__ __forceinline__ bool vld(u32x4 v) { return v[0] != SENT && v[1] != SENT && v[2] != SENT && v[3] != SENT; }
#define LD16(off) __builtin_amdgcn_raw_buffer_load_b128(rs, (off), 0, 16)
#define LD8(off) __builtin_amdgcn_raw_buffer_load_b64(rs, (off), 0, 16)
#define ST16(v, off) __builtin_amdgcn_raw_buffer_store_b128((v), rs, (off), 0, 16)
#define ST4(v, off) __builtin_amdgcn_raw_buffer_store_b32((v), rs, (off), 0, 16)
#define F(u) __uint_as_float(u)

struct ChainLds {
  u32x4 wC[4][14][64];
  float red[4][512];
  float red2[4][256];
  float xa[2][448], xb[2][448];
  unsigned xnT[4][128];
  unsigned hd[64];
};

__device__ __forceinline__ void ln_frag(const float (&v)[8], const int (&col)[8], bool v2, int w, int sub, unsigned* xnT, u32x4 (&xw)[4]) {
  float s = (v[0] + v[1]) + (v[2] + v[3]);
  if (v2) s += (v[4] + v[5]) + (v[6] + v[7]);
  const float mean = wave_sum(s) * (1.f / 448.f);
  float q = 0.f;
#pragma unroll
  for (int k = 0; k < 4; ++k) { float d = v[k] - mean; q += d * d; }
  if (v2) {
#pragma unroll
    for (int k = 4; k < 8; ++k) { float d = v[k] - mean; q += d * d; }
  }
  const float var = wave_sum(q) * (1.f / 448.f);
  const float mb = rbf(mean);
  const float den = rbf(__builtin_sqrtf(rbf(rbf(var) + 1e-5f)));
  const float inv = 1.f / den;
#pragma unroll
  for (int k = 0; k < 8; ++k) {
    const float t = rbf(v[k] - mb) * inv;
    const int r = col[k] - 112 * w;
    if ((k < 4 || v2) && r >= 0 && r < 112) xnT[(r & 7) * 16 + (r >> 3)] = pk2(t, t);
  }
  __builtin_amdgcn_s_waitcnt(0xC07F);
  __builtin_amdgcn_wave_barrier();
#pragma unroll
  for (int k = 0; k < 4; ++k) xw[k] = *(const u32x4*)(xnT + sub * 16 + 4 * k);
}

template <int ORDER>
__device__ __forceinline__ void chain_role(int c, ChainLds& S, const u16* __restrict__ x, const u16* __restrict__ wq, const u16* __restrict__ wo,
                                           const u16* __restrict__ wg, const u16* __restrict__ wu, const u16* __restrict__ wd, char* ws, u16* __restrict__ out) {
  const int tid = threadIdx.x, l = tid & 63, w = __builtin_amdgcn_readfirstlane(tid >> 6);
  const int g = l >> 4, hi = (l >> 3) & 1, c8 = l & 7, sub = 2 * g + hi;
  const int c7 = c8 < 7 ? c8 : 6;
  const __amdgpu_buffer_rsrc_t rs = __builtin_amdgcn_make_buffer_rsrc(ws, 0, WS_END, 0x00020000);
  u32x4 msk;
#pragma unroll
  for (int r = 0; r < 4; ++r) msk[r] = ((c8 >> 1) == r) ? ((c8 & 1) ? 0xffff0000u : 0x0000ffffu) : 0u;
  const int hA = c >> 1, nh = c & 1;
  u32x4 wD[14][2], wA[8][4], wB[14][2];
#pragma unroll
  for (int rg = 0; rg < 14; ++rg)
#pragma unroll
    for (int cq = 0; cq < 2; ++cq) wD[rg][cq] = *(const u32x4*)(wq + (size_t)(112 * w + 8 * rg + sub) * 4096 + 128 * c + 64 * cq + 8 * c8);
  for (int i = tid; i < 896; i += 256) S.xa[i / 448][i % 448] = bf2f(x[i]);
#pragma unroll
  for (int j = 0; j < 8; ++j)
#pragma unroll
    for (int cq = 0; cq < 4; ++cq) wA[j][cq] = *(const u32x4*)(wo + (size_t)(256 * hA + 64 * w + 8 * j + sub) * 448 + 224 * nh + 56 * cq + 8 * c7);
#pragma unroll
  for (int rg = 0; rg < 14; ++rg) {
    wB[rg][0] = *(const u32x4*)(wg + (size_t)(112 * w + 8 * rg + sub) * 1792 + 56 * c + 8 * c7);
    wB[rg][1] = *(const u32x4*)(wu + (size_t)(112 * w + 8 * rg + sub) * 1792 + 56 * c + 8 * c7);
  }
#pragma unroll
  for (int rg = 0; rg < 7; ++rg)
#pragma unroll
    for (int k = 0; k < 2; ++k) {
      const int cq = (w + 4 * k < 7) ? w + 4 * k : 6;
      S.wC[w][rg * 2 + k][l] = *(const u32x4*)(wd + (size_t)(56 * c + 8 * rg + sub) * 448 + 64 * cq + 8 * c8);
    }
  __syncthreads();
  const bool v2 = l < 48;
  int coln[8], colp[8];
#pragma unroll
  for (int k = 0; k < 8; ++k) {
    const int ch = (k < 4) ? l : 64 + (v2 ? l : 47);
    coln[k] = 4 * ch + (k & 3);
    colp[k] = 64 * (ch >> 4) + 32 * ((ch >> 3) & 1) + 8 * (k & 3) + (ch & 7);
  }
  auto qproj = [&](const u32x4 (&xw)[4], int ctx) {
    f32x4 a0 = {0, 0, 0, 0}, a1 = {0, 0, 0, 0};
#pragma unroll
    for (int rg = 0; rg < 14; ++rg) {
      const u32x4 b = selb(xw[rg >> 2][rg & 3], msk);
      a0 = mfma(wD[rg][0], b, a0); a1 = mfma(wD[rg][1], b, a1);
    }
    const f32x4 o = fold2(a0, a1);
    if (!hi) {
#pragma unroll
      for (int r = 0; r < 4; ++r) S.red2[w][64 * (l >> 5) + 32 * (g & 1) + 8 * r + c8] = o[r];
    }
    __syncthreads();
    if (tid < 64) {
      float s0 = 0.f, s1 = 0.f;
#pragma unroll
      for (int k = 0; k < 4; ++k) { s0 += S.red2[k][2 * tid]; s1 += S.red2[k][2 * tid + 1]; }
      unsigned u = pk2(s0, s1); u = u < 0xFFFFFFFEu ? u : 0xFFFFFFFEu;
      ST4(u, WS_Q + ctx * 8192 + c * 256 + tid * 4);
    }
  };
#pragma unroll 1
  for (int b = 0; b < 2; ++b) {
    float v[8];
#pragma unroll
    for (int k = 0; k < 8; ++k) v[k] = S.xa[b][coln[k]];
    u32x4 xw[4];
    ln_frag(v, coln, v2, w, sub, S.xnT[w], xw);
    qproj(xw, 2 * b);
    __syncthreads();
  }
#pragma unroll 1
  for (int inst = 0; inst < 4; ++inst) {
    const int ctx = ORDER == 0 ? inst : ((inst & 1) * 2 + (inst >> 1));
    const int b = ctx >> 1, layer = ctx & 1;
    {
      u32x4 pv[4]; u32x2 ml;
      const unsigned pb = WS_P + (ctx * 16 + hA) * 14 * 1024 + l * 16;
      const unsigned mo = WS_ML + (ctx * 16 + hA) * 128 + ((l & 15) < 14 ? (l & 15) : 13) * 8;
      for (unsigned spin = 0; spin < SPIN_CAP; ++spin) {
        asm volatile("" ::: "memory");
        bool ok = true;
#pragma unroll
        for (int j = 0; j < 4; ++j) { const int s = (w + 4 * j < 14) ? w + 4 * j : w; pv[j] = LD16(pb + s * 1024); ok &= vld(pv[j]); }
        ml = LD8(mo); ok &= (ml[0] != SENT) && (ml[1] != SENT);
        if (__all(ok)) break;
        __builtin_amdgcn_s_sleep(1);
      }
      const float C = 0.0625f * 1.4426950408889634f;
      const float m = F(ml[0]);
      const float M = row_max(m);
      const float wt = ((l & 15) < 14) ? __builtin_amdgcn_exp2f((m - M) * C) : 0.f;
      const float L = row_sum(wt * F(ml[1]));
      const float sc = wt * (1.f / L);
      f32x4 o = {0, 0, 0, 0};
#pragma unroll
      for (int j = 0; j < 4; ++j) {
        if (w + 4 * j < 14) {
          const float s = __int_as_float(__builtin_amdgcn_readlane(__float_as_int(sc), w + 4 * j));
#pragma unroll
          for (int r = 0; r < 4; ++r) o[r] += s * F(pv[j][r]);
        }
      }
      *(f32x4*)&S.red[w][l * 4] = o;
      __syncthreads();
      f32x4 acc[4] = {};
#pragma unroll
      for (int g1 = 0; g1 < 2; ++g1) {
        f32x4 t = *(const f32x4*)&S.red[0][(w * 16 + g1 * 8 + sub) * 4];
#pragma unroll
        for (int k = 1; k < 4; ++k) t += *(const f32x4*)&S.red[k][(w * 16 + g1 * 8 + sub) * 4];
#pragma unroll
        for (int r = 0; r < 4; ++r) {
          const u32x4 bb = selb(pk2(t[r], t[r]), msk);
#pragma unroll
          for (int cq = 0; cq < 4; ++cq) acc[cq] = mfma(wA[4 * g1 + r][cq], bb, acc[cq]);
        }
      }
      const f32x4 r0 = fold2(acc[0], acc[2]), r1 = fold2(acc[1], acc[3]);
      if (!hi) {
#pragma unroll
        for (int r = 0; r < 4; ++r) {
          const int ch = 4 * (g & 1) + r;
          if (ch < 7) { S.red2[w][56 * (2 * (l >> 5)) + 8 * ch + c8] = r0[r]; S.red2[w][56 * (2 * (l >> 5) + 1) + 8 * ch + c8] = r1[r]; }
        }
      }
      __syncthreads();
      if (tid < 56) {
        f32x4 t = *(const f32x4*)&S.red2[0][4 * tid];
#pragma unroll
        for (int k = 1; k < 4; ++k) t += *(const f32x4*)&S.red2[k][4 * tid];
        ST16((u32x4{san(t[0]), san(t[1]), san(t[2]), san(t[3])}), WS_Y + (ctx * 16 + hA) * 1792 + nh * 896 + tid * 16);
      }
    }
    {
      u32x4 yv[8];
      const unsigned y0 = WS_Y + (ctx * 16 + 4 * w) * 1792 + l * 16, y1 = WS_Y + (ctx * 16 + 4 * w) * 1792 + (64 + (v2 ? l : 47)) * 16;
      for (unsigned spin = 0; spin < SPIN_CAP; ++spin) {
        asm volatile("" ::: "memory");
        bool ok = true;
#pragma unroll
        for (int j = 0; j < 4; ++j) { yv[j] = LD16(y0 + j * 1792); yv[4 + j] = LD16(y1 + j * 1792); ok &= vld(yv[j]) && vld(yv[4 + j]); }
        if (__all(ok)) break;
      }
      f32x4 p0, p1;
#pragma unroll
      for (int r = 0; r < 4; ++r) { p0[r] = (F(yv[0][r]) + F(yv[1][r])) + (F(yv[2][r]) + F(yv[3][r])); p1[r] = (F(yv[4][r]) + F(yv[5][r])) + (F(yv[6][r]) + F(yv[7][r])); }
      *(f32x4*)&S.red[w][l * 4] = p0;
      *(f32x4*)&S.red[w][256 + l * 4] = p1;
      const float* xin = layer == 0 ? S.xa[b] : S.xa[b];
      float v[8];
#pragma unroll
      for (int k = 0; k < 8; ++k) v[k] = xin[coln[k]];
      __syncthreads();
      {
        f32x4 t0 = *(const f32x4*)&S.red[0][l * 4], t1 = *(const f32x4*)&S.red[0][256 + l * 4];
#pragma unroll
        for (int k = 1; k < 4; ++k) { t0 += *(const f32x4*)&S.red[k][l * 4]; t1 += *(const f32x4*)&S.red[k][256 + l * 4]; }
#pragma unroll
        for (int r = 0; r < 4; ++r) { v[r] = rbf(v[r] + rbf(t0[r])); v[4 + r] = rbf(v[4 + r] + rbf(t1[r])); }
      }
      if (w == 0) {
        *(f32x4*)&S.xb[b][4 * l] = f32x4{v[0], v[1], v[2], v[3]};
        if (v2) *(f32x4*)&S.xb[b][256 + 4 * l] = f32x4{v[4], v[5], v[6], v[7]};
      }
      u32x4 xw[4];
      ln_frag(v, coln, v2, w, sub, S.xnT[w], xw);
      f32x4 ag = {0, 0, 0, 0}, au = {0, 0, 0, 0};
#pragma unroll
      for (int rg = 0; rg < 14; ++rg) {
        const u32x4 bb = selb(xw[rg >> 2][rg & 3], msk);
        ag = mfma(wB[rg][0], bb, ag); au = mfma(wB[rg][1], bb, au);
      }
      u32x4 wc[14];
#pragma unroll
      for (int k = 0; k < 14; ++k) wc[k] = S.wC[w][k][l];
      const f32x4 o = fold2(ag, au);
      if (!hi) {
#pragma unroll
        for (int r = 0; r < 4; ++r) S.red2[w][64 * (l >> 5) + 32 * (g & 1) + 8 * r + c8] = o[r];
      }
      __syncthreads();
      if (tid < 56) {
        const float gg = (S.red2[0][tid] + S.red2[1][tid]) + (S.red2[2][tid] + S.red2[3][tid]);
        const float uu = (S.red2[0][64 + tid] + S.red2[1][64 + tid]) + (S.red2[2][64 + tid] + S.red2[3][64 + tid]);
        const float h = rbf(gg) * rbf(uu);
        S.hd[tid] = pk2(h, h);
      }
      __syncthreads();
      f32x4 d0 = {0, 0, 0, 0}, d1 = {0, 0, 0, 0};
#pragma unroll
      for (int rg = 0; rg < 7; ++rg) {
        const u32x4 bb = selb(S.hd[8 * rg + sub], msk);
        d0 = mfma(wc[2 * rg], bb, d0); d1 = mfma(wc[2 * rg + 1], bb, d1);
      }
      const f32x4 z = fold2(d0, d1);
      const int cq = w + 4 * (l >> 5);
      if (!hi && cq < 7)
        ST16((u32x4{san(z[0]), san(z[1]), san(z[2]), san(z[3])}), WS_Z + (ctx * 32 + c) * 1792 + (cq * 16 + (g & 1) * 8 + c8) * 16);
    }
    if (layer == 0) {
      u32x4 zv[16];
      const unsigned z0 = WS_Z + (ctx * 32 + 8 * w) * 1792 + l * 16, z1 = WS_Z + (ctx * 32 + 8 * w) * 1792 + (64 + (v2 ? l : 47)) * 16;
      for (unsigned spin = 0; spin < SPIN_CAP; ++spin) {
        asm volatile("" ::: "memory");
        bool ok = true;
#pragma unroll
        for (int j = 0; j < 8; ++j) { zv[j] = LD16(z0 + j * 1792); zv[8 + j] = LD16(z1 + j * 1792); ok &= vld(zv[j]) && vld(zv[8 + j]); }
        if (__all(ok)) break;
      }
      f32x4 p0 = {0, 0, 0, 0}, p1 = {0, 0, 0, 0};
#pragma unroll
      for (int j = 0; j < 8; ++j)
#pragma unroll
        for (int r = 0; r < 4; ++r) { p0[r] += F(zv[j][r]); p1[r] += F(zv[8 + j][r]); }
      *(f32x4*)&S.red[w][l * 4] = p0;
      *(f32x4*)&S.red[w][256 + l * 4] = p1;
      float v[8];
#pragma unroll
      for (int k = 0; k < 8; ++k) v[k] = S.xb[b][colp[k]];
      __syncthreads();
      {
        f32x4 t0 = *(const f32x4*)&S.red[0][l * 4], t1 = *(const f32x4*)&S.red[0][256 + l * 4];
#pragma unroll
        for (int k = 1; k < 4; ++k) { t0 += *(const f32x4*)&S.red[k][l * 4]; t1 += *(const f32x4*)&S.red[k][256 + l * 4]; }
#pragma unroll
        for (int r = 0; r < 4; ++r) { v[r] = rbf(v[r] + rbf(t0[r])); v[4 + r] = rbf(v[4 + r] + rbf(t1[r])); }
      }
      if (w == 0) {
#pragma unroll
        for (int k = 0; k < 8; ++k) if (k < 4 || v2) S.xa[b][colp[k]] = v[k];
      }
      u32x4 xw[4];
      ln_frag(v, colp, v2, w, sub, S.xnT[w], xw);
      qproj(xw, ctx + 1);
    } else {
      if (w == 0 && c < 28) {
        const int p16 = l >> 2, ch = 4 * c + (l & 3);
        u32x4 za, zb;
        const unsigned zo = WS_Z + (ctx * 32 + p16) * 1792 + ch * 16;
        for (unsigned spin = 0; spin < SPIN_CAP; ++spin) {
        asm volatile("" ::: "memory");
          za = LD16(zo); zb = LD16(zo + 16 * 1792);
          if (__all(vld(za) && vld(zb))) break;
        }
#pragma unroll
        for (int r = 0; r < 4; ++r) {
          float t = F(za[r]) + F(zb[r]);
          t += dpp<0x124>(t); t += dpp<0x128>(t); t = rows_sum(t);
          const int col = 64 * (ch >> 4) + 32 * ((ch >> 3) & 1) + 8 * r + (ch & 7);
          if (l < 4) out[b * 448 + col] = (u16)(pk2(S.xb[b][col] + rbf(t), 0.f) & 0xffffu);
        }
      }
      __syncthreads();
    }
  }
}

struct AttnLds {
  float rec[4][4][256];
  float hm[4][4], hl[4][4];
  unsigned cnt[4];
  unsigned q[4][128];
};

#define NTP 0
__device__ __forceinline__ void attn_role(int h, int s, AttnLds& S, const u16* __restrict__ kc, const u16* __restrict__ vc, char* ws) {
  const int tid = threadIdx.x, l = tid & 63, w = __builtin_amdgcn_readfirstlane(tid >> 6);
  const int g = l >> 4, hi = (l >> 3) & 1, c8 = l & 7;
  const __amdgpu_buffer_rsrc_t rs = __builtin_amdgcn_make_buffer_rsrc(ws, 0, WS_END, 0x00020000);
  if (tid < 4) S.cnt[tid] = 0;
  __syncthreads();
  const int j = 4 * s + w;
  const bool hasE = s < 8 && w == (s & 3);
  const int nblk = hasE ? 10 : 9;
  const float C = 0.0625f * 1.4426950408889634f;
#pragma unroll 1
  for (int inst = 0; inst < 4; ++inst) {
    const int ctx = (inst & 1) * 2 + (inst >> 1), b = ctx >> 1;
    const u16* kb = kc + ((size_t)(b * 16 + h) * 8192) * 256 + (l & 15) * 256 + g * 8;
    const u16* vb = vc + ((size_t)(b * 16 + h) * 8192) * 256 + (4 * g + hi) * 256 + c8 * 8;
    bf16x8 kf[2][8], vf[2][8];
#define LDX(p) (NTP ? __builtin_nontemporal_load((const bf16x8*)(p)) : *(const bf16x8*)(p))
#define ISSUE(buf, n_) do { const int bk = (n_) < 9 ? (n_) * 56 + j : 504 + s; const u16* kp = kb + (size_t)bk * 4096; const u16* vp = vb + (size_t)bk * 4096; \
    _Pragma("unroll") for (int i = 0; i < 8; ++i) kf[buf][i] = LDX(kp + i * 32); \
    _Pragma("unroll") for (int t = 0; t < 2; ++t) _Pragma("unroll") for (int i = 0; i < 4; ++i) vf[buf][t * 4 + i] = LDX(vp + t * 512 + i * 64); } while (0)
    ISSUE(0, 0);
    ISSUE(1, 1);
    u32x2 qv;
    for (unsigned spin = 0; spin < SPIN_CAP; ++spin) {
        asm volatile("" ::: "memory");
      qv = LD8(WS_Q + ctx * 8192 + h * 512 + l * 8);
      if (__all(qv[0] != SENT && qv[1] != SENT)) break;
    }
    *(u32x2*)&S.q[w][2 * l] = qv;
    __builtin_amdgcn_wave_barrier();
    bf16x8 qf[8];
#pragma unroll
    for (int i = 0; i < 8; ++i) qf[i] = *(const bf16x8*)&S.q[w][(4 * i + g) * 4];
    f32x4 acc[4] = {};
    float m = -INFINITY, lsum = 0.f;
#define COMPUTE(buf) do { \
    f32x4 d = {0.f, 0.f, 0.f, 0.f}; \
    _Pragma("unroll") for (int i = 0; i < 8; ++i) d = __builtin_amdgcn_mfma_f32_16x16x32_bf16(kf[buf][i], qf[i], d, 0, 0, 0); \
    float bm = fmaxf(fmaxf(d[0], d[1]), fmaxf(d[2], d[3])); \
    bm = fmaxf(bm, __shfl_xor(bm, 16)); bm = fmaxf(bm, __shfl_xor(bm, 32)); \
    if (bm > m) { float alpha = exp2f((m - bm) * C); m = bm; lsum *= alpha; _Pragma("unroll") for (int i = 0; i < 4; ++i) acc[i] *= alpha; } \
    _Pragma("unroll") for (int t = 0; t < 2; ++t) { \
      float st = hi ? d[2 * t + 1] : d[2 * t]; \
      float p = exp2f((st - m) * C); lsum += p; \
      unsigned pk = (pk2(p, 0.f) & 0xffffu) << (16 * (l & 1)); \
      u32x4 bw; _Pragma("unroll") for (int r = 0; r < 4; ++r) bw[r] = (((l & 7) >> 1) == r) ? pk : 0u; \
      bf16x8 bfr = __builtin_bit_cast(bf16x8, bw); \
      _Pragma("unroll") for (int i = 0; i < 4; ++i) acc[i] = __builtin_amdgcn_mfma_f32_16x16x32_bf16(vf[buf][t * 4 + i], bfr, acc[i], 0, 0, 0); \
    } } while (0)
#pragma unroll 1
    for (int n = 0; n < 8; n += 2) {
      COMPUTE(0);
      ISSUE(0, n + 2);
      COMPUTE(1);
      if (n + 3 < nblk) ISSUE(1, n + 3);
    }
    COMPUTE(0);
    if (hasE) COMPUTE(1);
    float lt = lsum; lt += dpp<0x128>(lt); lt = rows_sum(lt);
    const f32x4 r0 = fold2(acc[0], acc[2]), r1 = fold2(acc[1], acc[3]);
    if (!hi) {
      const int ch = (l >> 5) * 32 + (g & 1) * 8 + c8;
      *(f32x4*)&S.rec[ctx][w][ch * 4] = r0;
      *(f32x4*)&S.rec[ctx][w][(ch + 16) * 4] = r1;
    }
    if (l == 0) { S.hm[ctx][w] = m; S.hl[ctx][w] = lt; }
    unsigned old = 0;
    if (l == 0) old = atomicAdd(&S.cnt[ctx], 1u);
    old = __builtin_amdgcn_readfirstlane(old);
    if (old == 3) {
      const f32x4 mm = *(const f32x4*)S.hm[ctx], ll = *(const f32x4*)S.hl[ctx];
      const float M = fmaxf(fmaxf(mm[0], mm[1]), fmaxf(mm[2], mm[3]));
      float L = 0.f; f32x4 o = {0, 0, 0, 0};
#pragma unroll
      for (int k = 0; k < 4; ++k) { const float wt = exp2f((mm[k] - M) * C); L += wt * ll[k]; o += wt * *(const f32x4*)&S.rec[ctx][k][l * 4]; }
      ST16((u32x4{san(o[0]), san(o[1]), san(o[2]), san(o[3])}), WS_P + ((ctx * 16 + h) * 14 + s) * 1024 + l * 16);
      if (l == 0) __builtin_amdgcn_raw_buffer_store_b64(u32x2{san(M), san(L)}, rs, WS_ML + (ctx * 16 + h) * 128 + s * 8, 0, 16);
    }
  }
}

__global__ void __launch_bounds__(256, 1) fused_kernel(const u16* __restrict__ x, const u16* __restrict__ kc, const u16* __restrict__ vc, const u16* __restrict__ wq,
                                                       const u16* __restrict__ wo, const u16* __restrict__ wg, const u16* __restrict__ wu, const u16* __restrict__ wd,
                                                       char* __restrict__ ws, u16* __restrict__ out) {
  __shared__ __attribute__((aligned(16))) char smem[160 * 1024];
  const int xcd = blockIdx.x & 7, i = blockIdx.x >> 3;
  if (i >= 28) chain_role<1>(4 * xcd + (i - 28), *(ChainLds*)smem, x, wq, wo, wg, wu, wd, ws, out);
  else attn_role(2 * xcd + i / 14, i % 14, *(AttnLds*)smem, kc, vc, ws);
}

extern "C" void kernel_launch(void* const* d_in, const int* in_sizes, int n_in, void* d_out, int out_size, void* d_ws, size_t ws_size, hipStream_t stream) {
  (void)hipMemsetAsync(d_ws, 0xFF, WS_END, stream);
  fused_kernel<<<256, 256, 0, stream>>>((const u16*)d_in[0], (const u16*)d_in[1], (const u16*)d_in[2], (const u16*)d_in[3], (const u16*)d_in[4],
                                        (const u16*)d_in[6], (const u16*)d_in[7], (const u16*)d_in[8], (char*)d_ws, (u16*)d_out);
}
```

```cpp
#include <hip/hip_runtime.h>
#include <stdint.h>

typedef unsigned short u16;

constexpr unsigned WS_EPOCH = 0;
constexpr unsigned WS_FLAG = 256;
constexpr unsigned WS_Q = 2048;
constexpr unsigned WS_ML = 526336;
constexpr unsigned WS_Y = 534528;
constexpr unsigned WS_Z = 591872;
constexpr unsigned WS_P = 722944;
constexpr unsigned WS_END = 1771520;

__device__ __forceinline__ void chain_role(int cu, const u16* __restrict__ x, const u16* __restrict__ kc, const u16* __restrict__ vc, const u16* __restrict__ wq,
                                           const u16* __restrict__ wo, const u16* __restrict__ wg, const u16* __restrict__ wu, const u16* __restrict__ wd,
                                           char* ws, u16* __restrict__ out) {
  const int w = __builtin_amdgcn_readfirstlane(threadIdx.x >> 6);
  asm volatile(
      "s_mov_b64 s[36:37], %[ws]\n"
      "s_mov_b32 s38, %[wid]\n"
      "s_mov_b32 s39, %[cu]\n"
      "s_lshr_b32 s40, s39, 1\n"
      "s_and_b32 s41, s39, 1\n"
      "s_mov_b32 s100, s40\n"
      "s_mov_b32 s101, s41\n"
      "v_mbcnt_lo_u32_b32 v56, -1, 0\n"
      "v_mbcnt_hi_u32_b32 v56, -1, v56\n"
      "v_mov_b32_e32 v57, 0\n"
      "s_mov_b64 exec, 1\n"
      "global_load_dword v59, v57, s[36:37] sc1\n"
      "s_mov_b64 exec, -1\n"
      "v_mov_b32_e32 v60, -1\n"
      "v_mov_b32_e32 v61, -1\n"
      "v_mov_b32_e32 v62, -1\n"
      "v_mov_b32_e32 v63, -1\n"
      "v_lshlrev_b32_e32 v57, 4, v56\n"
      "s_mov_b32 s88, 0x82800\n"
      "s_mul_i32 s89, s38, 14336\n"
      "s_add_u32 s88, s88, s89\n"
      "s_mul_i32 s89, s40, 896\n"
      "s_add_u32 s88, s88, s89\n"
      "s_mul_i32 s89, s41, 448\n"
      "s_add_u32 s88, s88, s89\n"
      "s_add_u32 s46, s36, s88\n"
      "s_addc_u32 s47, s37, 0\n"
      "s_mov_b64 exec, 0xfffffff\n"
      "global_store_dwordx4 v57, v[60:63], s[46:47] sc1\n"
      "s_mov_b32 s88, 0x90800\n"
      "s_mul_i32 s89, s38, 28672\n"
      "s_add_u32 s88, s88, s89\n"
      "s_mul_i32 s89, s39, 896\n"
      "s_add_u32 s88, s88, s89\n"
      "s_add_u32 s46, s36, s88\n"
      "s_addc_u32 s47, s37, 0\n"
      "s_mov_b32 exec_lo, -1\n"
      "s_mov_b32 exec_hi, 0x00ffffff\n"
      "global_store_dwordx4 v57, v[60:63], s[46:47] sc1\n"
      "s_mov_b64 exec, -1\n"
      "s_mov_b32 s88, 0xb4000\n"
      "s_mul_i32 s89, s38, 262144\n"
      "s_add_u32 s88, s88, s89\n"
      "s_mul_i32 s89, s100, 16384\n"
      "s_add_u32 s88, s88, s89\n"
      "s_mul_i32 s89, s101, 1024\n"
      "s_add_u32 s88, s88, s89\n"
      "s_add_u32 s46, s36, s88\n"
      "s_addc_u32 s47, s37, 0\n"
      "global_store_dwordx4 v57, v[60:63], s[46:47] sc1\n"
      "s_mov_b32 s88, 0x80858\n"
      "s_mul_i32 s89, s38, 2048\n"
      "s_add_u32 s88, s88, s89\n"
      "s_mul_i32 s89, s100, 128\n"
      "s_add_u32 s88, s88, s89\n"
      "s_mul_i32 s89, s101, 32\n"
      "s_add_u32 s88, s88, s89\n"
      "s_add_u32 s46, s36, s88\n"
      "s_addc_u32 s47, s37, 0\n"
      "v_mov_b32_e32 v58, 0\n"
      "s_mov_b64 exec, 1\n"
      "global_store_dwordx2 v58, v[60:61], s[46:47] sc1\n"
      "s_mov_b64 exec, -1\n"
      "v_lshrrev_b32_e32 v64, 3, v56\n"
      "v_lshlrev_b32_e32 v64, 5, v64\n"
      "v_and_b32_e32 v57, 7, v56\n"
      "v_lshl_add_u32 v64, v57, 1, v64\n"
      "s_mul_i32 s88, s38, 224\n"
      "v_add_u32_e32 v64, s88, v64\n"
      "v_mov_b32_e32 v68, 0\n"
      "v_mov_b32_e32 v69, 0\n"
      "v_mov_b32_e32 v70, 0\n"
      "v_mov_b32_e32 v71, 0\n"
      "s_mov_b32 exec_lo, -1\n"
      "s_mov_b32 exec_hi, 0x00ffffff\n"
      "global_load_ushort v68, v64, %[x] offset:0\n"
      "global_load_ushort v69, v64, %[x] offset:16\n"
      "global_load_ushort v70, v64, %[x] offset:896\n"
      "global_load_ushort v71, v64, %[x] offset:912\n"
      "s_mov_b64 exec, -1\n"
      "v_mbcnt_lo_u32_b32 v76, -1, 0\n"
      "v_mbcnt_hi_u32_b32 v76, -1, v76\n"
      "v_bfe_u32 v77, v76, 2, 2\n"
      "v_lshlrev_b32_e32 v60, 9, v77\n"
      "v_lshrrev_b32_e32 v77, 4, v76\n"
      "v_lshl_add_u32 v60, v77, 6, v60\n"
      "v_lshlrev_b32_e32 v61, 9, v77\n"
      "v_and_b32_e32 v78, 3, v76\n"
      "v_lshl_add_u32 v60, v78, 4, v60\n"
      "v_bfe_u32 v78, v76, 3, 1\n"
      "v_lshl_add_u32 v61, v78, 12, v61\n"
      "v_and_b32_e32 v78, 7, v76\n"
      "v_lshl_add_u32 v61, v78, 4, v61\n"
      "s_lshl_b32 s88, s100, 22\n"
      "s_lshl_b32 s89, s101, 2\n"
      "s_add_u32 s89, s89, s38\n"
      "s_add_u32 s89, s89, 504\n"
      "s_lshl_b32 s89, s89, 13\n"
      "s_add_u32 s88, s88, s89\n"
      "s_add_u32 s88, s88, 0x0\n"
      "s_lshl_b32 s90, s38, 14\n"
      "s_add_u32 s90, s90, 0xe000\n"
      "s_mov_b64 s[92:93], %[kc]\n"
      "s_add_u32 s92, s92, s88\n"
      "s_addc_u32 s93, s93, 0\n"
      "s_add_u32 s46, s92, 0x0\n"
      "s_addc_u32 s47, s93, 0\n"
      "s_add_u32 m0, s90, 0x0\n"
      "s_nop 0\n"
      "global_load_lds_dwordx4 v60, s[46:47]\n"
      "s_add_u32 s46, s92, 0x100\n"
      "s_addc_u32 s47, s93, 0\n"
      "s_add_u32 m0, s90, 0x400\n"
      "s_nop 0\n"
      "global_load_lds_dwordx4 v60, s[46:47]\n"
      "s_add_u32 s46, s92, 0x800\n"
      "s_addc_u32 s47, s93, 0\n"
      "s_add_u32 m0, s90, 0x800\n"
      "s_nop 0\n"
      "global_load_lds_dwordx4 v60, s[46:47]\n"
      "s_add_u32 s46, s92, 0x900\n"
      "s_addc_u32 s47, s93, 0\n"
      "s_add_u32 m0, s90, 0xc00\n"
      "s_nop 0\n"
      "global_load_lds_dwordx4 v60, s[46:47]\n"
      "s_add_u32 s46, s92, 0x1000\n"
      "s_addc_u32 s47, s93, 0\n"
      "s_add_u32 m0, s90, 0x1000\n"
      "s_nop 0\n"
      "global_load_lds_dwordx4 v60, s[46:47]\n"
      "s_add_u32 s46, s92, 0x1100\n"
      "s_addc_u32 s47, s93, 0\n"
      "s_add_u32 m0, s90, 0x1400\n"
      "s_nop 0\n"
      "global_load_lds_dwordx4 v60, s[46:47]\n"
      "s_add_u32 s46, s92, 0x1800\n"
      "s_addc_u32 s47, s93, 0\n"
      "s_add_u32 m0, s90, 0x1800\n"
      "s_nop 0\n"
      "global_load_lds_dwordx4 v60, s[46:47]\n"
      "s_add_u32 s46, s92, 0x1900\n"
      "s_addc_u32 s47, s93, 0\n"
      "s_add_u32 m0, s90, 0x1c00\n"
      "s_nop 0\n"
      "global_load_lds_dwordx4 v60, s[46:47]\n"
      "s_mov_b64 s[92:93], %[vc]\n"
      "s_add_u32 s92, s92, s88\n"
      "s_addc_u32 s93, s93, 0\n"
      "s_add_u32 s46, s92, 0x0\n"
      "s_addc_u32 s47, s93, 0\n"
      "s_add_u32 m0, s90, 0x2000\n"
      "s_nop 0\n"
      "global_load_lds_dwordx4 v61, s[46:47]\n"
      "s_add_u32 s46, s92, 0x80\n"
      "s_addc_u32 s47, s93, 0\n"
      "s_add_u32 m0, s90, 0x2400\n"
      "s_nop 0\n"
      "global_load_lds_dwordx4 v61, s[46:47]\n"
      "s_add_u32 s46, s92, 0x100\n"
      "s_addc_u32 s47, s93, 0\n"
      "s_add_u32 m0, s90, 0x2800\n"
      "s_nop 0\n"
      "global_load_lds_dwordx4 v61, s[46:47]\n"
      "s_add_u32 s46, s92, 0x180\n"
      "s_addc_u32 s47, s93, 0\n"
      "s_add_u32 m0, s90, 0x2c00\n"
      "s_nop 0\n"
      "global_load_lds_dwordx4 v61, s[46:47]\n"
      "s_add_u32 s46, s92, 0x800\n"
      "s_addc_u32 s47, s93, 0\n"
      "s_add_u32 m0, s90, 0x3000\n"
      "s_nop 0\n"
      "global_load_lds_dwordx4 v61, s[46:47]\n"
      "s_add_u32 s46, s92, 0x880\n"
      "s_addc_u32 s47, s93, 0\n"
      "s_add_u32 m0, s90, 0x3400\n"
      "s_nop 0\n"
      "global_load_lds_dwordx4 v61, s[46:47]\n"
      "s_add_u32 s46, s92, 0x900\n"
      "s_addc_u32 s47, s93, 0\n"
      "s_add_u32 m0, s90, 0x3800\n"
      "s_nop 0\n"
      "global_load_lds_dwordx4 v61, s[46:47]\n"
      "s_add_u32 s46, s92, 0x980\n"
      "s_addc_u32 s47, s93, 0\n"
      "s_add_u32 m0, s90, 0x3c00\n"
      "s_nop 0\n"
      "global_load_lds_dwordx4 v61, s[46:47]\n"
      "v_mbcnt_lo_u32_b32 v56, -1, 0\n"
      "v_mbcnt_hi_u32_b32 v56, -1, v56\n"
      "v_cmp_le_u32_e32 vcc, 28, v56\n"
      "v_subrev_u32_e32 v57, 28, v56\n"
      "s_nop 0\n"
      "v_cndmask_b32_e32 v57, v56, v57, vcc\n"
      "v_lshlrev_b32_e32 v132, 4, v57\n"
      "v_add_u32_e32 v57, 896, v132\n"
      "v_cndmask_b32_e32 v132, v132, v57, vcc\n"
      "s_min_u32 s94, s39, 27\n"
      "s_mov_b64 s[48:49], %[wo]\n"
      "s_mul_i32 s88, s40, 0x38000\n"
      "s_mul_i32 s89, s38, 0x7000\n"
      "s_add_u32 s88, s88, s89\n"
      "s_mul_i32 s89, s41, 0x1c0\n"
      "s_add_u32 s88, s88, s89\n"
      "s_add_u32 s48, s48, s88\n"
      "s_addc_u32 s49, s49, 0\n"
      "v_lshrrev_b32_e32 v133, 3, v56\n"
      "v_mul_u32_u24_e32 v133, 3584, v133\n"
      "v_and_b32_e32 v57, 7, v56\n"
      "v_lshl_add_u32 v133, v57, 4, v133\n"
      "s_mov_b64 s[50:51], %[wg]\n"
      "s_mul_i32 s88, s94, 0x80\n"
      "s_mul_i32 s89, s38, 0x62000\n"
      "s_add_u32 s88, s88, s89\n"
      "s_add_u32 s50, s50, s88\n"
      "s_addc_u32 s51, s51, 0\n"
      "s_mov_b64 s[52:53], %[wu]\n"
      "s_mul_i32 s88, s94, 0x80\n"
      "s_mul_i32 s89, s38, 0x62000\n"
      "s_add_u32 s88, s88, s89\n"
      "s_add_u32 s52, s52, s88\n"
      "s_addc_u32 s53, s53, 0\n"
      "v_lshrrev_b32_e32 v134, 4, v56\n"
      "v_lshlrev_b32_e32 v134, 13, v134\n"
      "v_and_b32_e32 v57, 15, v56\n"
      "v_lshl_add_u32 v134, v57, 4, v134\n"
      "s_mov_b64 s[54:55], %[wq]\n"
      "s_mul_i32 s88, s39, 0x100\n"
      "s_mul_i32 s89, s38, 0x70000\n"
      "s_add_u32 s88, s88, s89\n"
      "s_add_u32 s54, s54, s88\n"
      "s_addc_u32 s55, s55, 0\n"
      "v_lshlrev_b32_e32 v135, 4, v56\n"
      "s_mov_b64 s[56:57], %[wd]\n"
      "s_mul_i32 s88, s94, 0xe000\n"
      "s_mul_i32 s89, s38, 0x3800\n"
      "s_add_u32 s88, s88, s89\n"
      "s_add_u32 s56, s56, s88\n"
      "s_addc_u32 s57, s57, 0\n"
      "s_mov_b32 exec_lo, -1\n"
      "s_mov_b32 exec_hi, 0x00ffffff\n"
      "global_load_dwordx4 v[160:163], v132, s[48:49] \n"
      "s_add_u32 s48, s48, 0x700\n"
      "s_addc_u32 s49, s49, 0\n"
      "global_load_dwordx4 v[164:167], v132, s[48:49] \n"
      "s_add_u32 s48, s48, 0x700\n"
      "s_addc_u32 s49, s49, 0\n"
      "global_load_dwordx4 v[168:171], v132, s[48:49] \n"
      "s_add_u32 s48, s48, 0x700\n"
      "s_addc_u32 s49, s49, 0\n"
      "global_load_dwordx4 v[172:175], v132, s[48:49] \n"
      "s_add_u32 s48, s48, 0x700\n"
      "s_addc_u32 s49, s49, 0\n"
      "global_load_dwordx4 v[176:179], v132, s[48:49] \n"
      "s_add_u32 s48, s48, 0x700\n"
      "s_addc_u32 s49, s49, 0\n"
      "global_load_dwordx4 v[180:183], v132, s[48:49] \n"
      "s_add_u32 s48, s48, 0x700\n"
      "s_addc_u32 s49, s49, 0\n"
      "global_load_dwordx4 v[184:187], v132, s[48:49] \n"
      "s_add_u32 s48, s48, 0x700\n"
      "s_addc_u32 s49, s49, 0\n"
      "global_load_dwordx4 v[188:191], v132, s[48:49] \n"
      "s_add_u32 s48, s48, 0x700\n"
      "s_addc_u32 s49, s49, 0\n"
      "global_load_dwordx4 v[192:195], v132, s[48:49] \n"
      "s_add_u32 s48, s48, 0x700\n"
      "s_addc_u32 s49, s49, 0\n"
      "global_load_dwordx4 v[196:199], v132, s[48:49] \n"
      "s_add_u32 s48, s48, 0x700\n"
      "s_addc_u32 s49, s49, 0\n"
      "global_load_dwordx4 v[200:203], v132, s[48:49] \n"
      "s_add_u32 s48, s48, 0x700\n"
      "s_addc_u32 s49, s49, 0\n"
      "s_mov_b64 exec, -1\n"
      "v_mbcnt_lo_u32_b32 v48, -1, 0\n"
      "v_mbcnt_hi_u32_b32 v48, -1, v48\n"
      "v_and_b32_e32 v49, 1, v48\n"
      "v_cmp_eq_u32_e32 vcc, 1, v49\n"
      "v_mov_b32_e32 v63, 0xffff\n"
      "v_mov_b32_e32 v49, 0xffff0000\n"
      "v_cndmask_b32_e32 v63, v63, v49, vcc\n"
      "s_mov_b32 s64, 0x3030303\n"
      "s_mov_b32 s65, 0x3030303\n"
      "s_mov_b32 s66, 0xc0c0c0c\n"
      "s_mov_b32 s67, 0xc0c0c0c\n"
      "s_mov_b32 s68, 0x30303030\n"
      "s_mov_b32 s69, 0x30303030\n"
      "s_mov_b32 s70, 0xc0c0c0c0\n"
      "s_mov_b32 s71, 0xc0c0c0c0\n"
      "s_mov_b32 s62, 0xff00ff00\n"
      "s_mov_b32 s63, 0xff00ff00\n"
      "s_mov_b32 s72, 0x00ff00ff\n"
      "s_mov_b32 s73, 0x00ff00ff\n"
      "s_mov_b32 s34, 0x3db8aa3b\n"
      "s_getpc_b64 s[78:79]\n"
      "PC_1_%=:\n"
      "s_add_u32 s78, s78, C_RESC_%=-PC_1_%=\n"
      "s_addc_u32 s79, s79, 0\n"
      "s_getpc_b64 s[80:81]\n"
      "PC_2_%=:\n"
      "s_add_u32 s80, s80, C_FIN_%=-PC_2_%=\n"
      "s_addc_u32 s81, s81, 0\n"
      "s_mov_b32 s96, 0xaaaaaaaa\n"
      "s_mov_b32 s97, 0xaaaaaaaa\n"
      "v_mbcnt_lo_u32_b32 v56, -1, 0\n"
      "v_mbcnt_hi_u32_b32 v56, -1, v56\n"
      "v_and_b32_e32 v159, 0x3f803f80, v63\n"
      "v_lshrrev_b32_e32 v57, 4, v56\n"
      "v_mul_u32_u24_e32 v154, 896, v57\n"
      "v_and_b32_e32 v58, 15, v56\n"
      "v_lshl_add_u32 v154, v58, 4, v154\n"
      "s_mul_i32 s88, s38, 224\n"
      "v_add_u32_e32 v154, s88, v154\n"
      "s_lshl_b32 s88, s38, 8\n"
      "s_add_u32 s88, s88, 0x1e000\n"
      "v_lshlrev_b32_e32 v155, 2, v56\n"
      "v_add_u32_e32 v155, s88, v155\n"
      "v_lshlrev_b32_e32 v156, 4, v57\n"
      "v_add_u32_e32 v156, 0x1e000, v156\n"
      "v_and_b32_e32 v58, 1, v57\n"
      "v_lshlrev_b32_e32 v157, 4, v58\n"
      "v_lshrrev_b32_e32 v58, 1, v57\n"
      "v_lshl_add_u32 v157, v58, 8, v157\n"
      "v_add_u32_e32 v157, 0x1e000, v157\n"
      "v_mov_b32_e32 v58, 0\n"
      "ds_write_b32 v155, v58\n"
      "ds_write_b32 v155, v58 offset:1024\n"
      "v_lshlrev_b32_e32 v57, 2, v56\n"
      "v_add_u32_e32 v57, 0x20400, v57\n"
      "s_mov_b64 exec, 0xf\n"
      "ds_write_b32 v57, v58\n"
      "s_mov_b64 exec, -1\n"
      "ds_write_b32 v57, v58 offset:64\n"
      "ds_write_b32 v57, v58 offset:320\n"
      "s_waitcnt vmcnt(31)\n"
      "v_readfirstlane_b32 s35, v59\n"
      "s_add_u32 s35, s35, 1\n"
      "s_waitcnt lgkmcnt(0)\n"
      "s_barrier\n"
      "s_cmp_lg_u32 s38, 0\n"
      "s_nop 0\n"
      "s_cbranch_scc1 NF_3_%=\n"
      "s_lshl_b32 s88, s39, 2\n"
      "s_add_u32 s88, s88, 0x100\n"
      "v_mov_b32_e32 v57, s88\n"
      "v_mov_b32_e32 v58, s35\n"
      "s_mov_b64 exec, 1\n"
      "global_store_dword v57, v58, s[36:37] sc1\n"
      "s_mov_b64 exec, -1\n"
      "s_nop 1\n"
      "NF_3_%=:\n"
      "s_waitcnt vmcnt(27)\n"
      "v_lshl_or_b32 v72, v69, 16, v68\n"
      "v_lshlrev_b32_e32 v140, 16, v72\n"
      "v_and_b32_e32 v141, 0xffff0000, v72\n"
      "v_lshl_or_b32 v73, v71, 16, v70\n"
      "v_lshlrev_b32_e32 v142, 16, v73\n"
      "v_and_b32_e32 v143, 0xffff0000, v73\n"
      "s_mov_b32 exec_lo, -1\n"
      "s_mov_b32 exec_hi, 0x00ffffff\n"
      "global_load_dwordx4 v[204:207], v132, s[48:49] \n"
      "s_add_u32 s48, s48, 0x700\n"
      "s_addc_u32 s49, s49, 0\n"
      "global_load_dwordx4 v[208:211], v132, s[48:49] \n"
      "s_add_u32 s48, s48, 0x700\n"
      "s_addc_u32 s49, s49, 0\n"
      "global_load_dwordx4 v[212:215], v132, s[48:49] \n"
      "s_add_u32 s48, s48, 0x700\n"
      "s_addc_u32 s49, s49, 0\n"
      "global_load_dwordx4 v[216:219], v132, s[48:49] \n"
      "s_add_u32 s48, s48, 0x700\n"
      "s_addc_u32 s49, s49, 0\n"
      "global_load_dwordx4 v[220:223], v132, s[48:49] \n"
      "s_add_u32 s48, s48, 0x15700\n"
      "s_addc_u32 s49, s49, 0\n"
      "global_load_dwordx4 v[224:227], v132, s[48:49] \n"
      "s_add_u32 s48, s48, 0x700\n"
      "s_addc_u32 s49, s49, 0\n"
      "global_load_dwordx4 v[228:231], v132, s[48:49] \n"
      "s_add_u32 s48, s48, 0x700\n"
      "s_addc_u32 s49, s49, 0\n"
      "global_load_dwordx4 v[232:235], v132, s[48:49] \n"
      "s_add_u32 s48, s48, 0x700\n"
      "s_addc_u32 s49, s49, 0\n"
      "s_mov_b64 exec, -1\n"
      "s_waitcnt vmcnt(3)\n"
      "s_mul_i32 s90, s38, 14336\n"
      "v_mbcnt_lo_u32_b32 v61, -1, 0\n"
      "v_mbcnt_hi_u32_b32 v61, -1, v61\n"
      "v_lshlrev_b32_e32 v61, 4, v61\n"
      "v_add_u32_e32 v61, s90, v61\n"
      "s_mov_b32 exec_lo, -1\n"
      "s_mov_b32 exec_hi, 0x00ffffff\n"
      "ds_write_b128 v61, v[160:163] offset:0\n"
      "ds_write_b128 v61, v[164:167] offset:896\n"
      "ds_write_b128 v61, v[168:171] offset:1792\n"
      "ds_write_b128 v61, v[172:175] offset:2688\n"
      "ds_write_b128 v61, v[176:179] offset:3584\n"
      "ds_write_b128 v61, v[180:183] offset:4480\n"
      "ds_write_b128 v61, v[184:187] offset:5376\n"
      "ds_write_b128 v61, v[188:191] offset:6272\n"
      "ds_write_b128 v61, v[192:195] offset:7168\n"
      "ds_write_b128 v61, v[196:199] offset:8064\n"
      "ds_write_b128 v61, v[200:203] offset:8960\n"
      "ds_write_b128 v61, v[204:207] offset:9856\n"
      "ds_write_b128 v61, v[208:211] offset:10752\n"
      "ds_write_b128 v61, v[212:215] offset:11648\n"
      "ds_write_b128 v61, v[216:219] offset:12544\n"
      "ds_write_b128 v61, v[220:223] offset:13440\n"
      "s_mov_b64 exec, -1\n"
      "s_waitcnt lgkmcnt(0)\n"
      "s_barrier\n"
      "s_mov_b32 exec_lo, -1\n"
      "s_mov_b32 exec_hi, 0x00ffffff\n"
      "global_load_dwordx4 v[236:239], v132, s[48:49] \n"
      "s_add_u32 s48, s48, 0x700\n"
      "s_addc_u32 s49, s49, 0\n"
      "global_load_dwordx4 v[240:243], v132, s[48:49] \n"
      "s_add_u32 s48, s48, 0x700\n"
      "s_addc_u32 s49, s49, 0\n"
      "global_load_dwordx4 v[244:247], v132, s[48:49] \n"
      "s_add_u32 s48, s48, 0x700\n"
      "s_addc_u32 s49, s49, 0\n"
      "global_load_dwordx4 v[248:251], v132, s[48:49] \n"
      "s_add_u32 s48, s48, 0x700\n"
      "s_addc_u32 s49, s49, 0\n"
      "global_load_dwordx4 v[252:255], v132, s[48:49] \n"
      "s_add_u32 s48, s48, 0x700\n"
      "s_addc_u32 s49, s49, 0\n"
      "global_load_dwordx4 a[0:3], v132, s[48:49] \n"
      "s_add_u32 s48, s48, 0x700\n"
      "s_addc_u32 s49, s49, 0\n"
      "global_load_dwordx4 a[4:7], v132, s[48:49] \n"
      "s_add_u32 s48, s48, 0x700\n"
      "s_addc_u32 s49, s49, 0\n"
      "global_load_dwordx4 a[8:11], v132, s[48:49] \n"
      "s_add_u32 s48, s48, 0x700\n"
      "s_addc_u32 s49, s49, 0\n"
      "global_load_dwordx4 a[12:15], v132, s[48:49] \n"
      "s_add_u32 s48, s48, 0x700\n"
      "s_addc_u32 s49, s49, 0\n"
      "global_load_dwordx4 a[16:19], v132, s[48:49] \n"
      "s_add_u32 s48, s48, 0x700\n"
      "s_addc_u32 s49, s49, 0\n"
      "global_load_dwordx4 a[20:23], v132, s[48:49] \n"
      "s_add_u32 s48, s48, 0x700\n"
      "s_addc_u32 s49, s49, 0\n"
      "global_load_dwordx4 a[24:27], v132, s[48:49] \n"
      "s_add_u32 s48, s48, 0x700\n"
      "s_addc_u32 s49, s49, 0\n"
      "global_load_dwordx4 a[28:31], v132, s[48:49] \n"
      "s_add_u32 s48, s48, 0x15700\n"
      "s_addc_u32 s49, s49, 0\n"
      "s_mov_b64 exec, -1\n"
      "global_load_dwordx4 a[144:147], v133, s[50:51] \n"
      "s_add_u32 s50, s50, 0x7000\n"
      "s_addc_u32 s51, s51, 0\n"
      "global_load_dwordx4 a[148:151], v133, s[50:51] \n"
      "s_add_u32 s50, s50, 0x7000\n"
      "s_addc_u32 s51, s51, 0\n"
      "global_load_dwordx4 a[152:155], v133, s[50:51] \n"
      "s_add_u32 s50, s50, 0x7000\n"
      "s_addc_u32 s51, s51, 0\n"
      "global_load_dwordx4 a[156:159], v133, s[50:51] \n"
      "s_add_u32 s50, s50, 0x7000\n"
      "s_addc_u32 s51, s51, 0\n"
      "global_load_dwordx4 a[160:163], v133, s[50:51] \n"
      "s_add_u32 s50, s50, 0x7000\n"
      "s_addc_u32 s51, s51, 0\n"
      "global_load_dwordx4 a[164:167], v133, s[50:51] \n"
      "s_add_u32 s50, s50, 0x7000\n"
      "s_addc_u32 s51, s51, 0\n"
      "global_load_dwordx4 a[168:171], v133, s[50:51] \n"
      "s_add_u32 s50, s50, 0x7000\n"
      "s_addc_u32 s51, s51, 0\n"
      "global_load_dwordx4 a[172:175], v133, s[50:51] \n"
      "s_add_u32 s50, s50, 0x7000\n"
      "s_addc_u32 s51, s51, 0\n"
      "global_load_dwordx4 a[176:179], v133, s[50:51] \n"
      "s_add_u32 s50, s50, 0x7000\n"
      "s_addc_u32 s51, s51, 0\n"
      "global_load_dwordx4 a[180:183], v133, s[50:51] \n"
      "s_add_u32 s50, s50, 0x7000\n"
      "s_addc_u32 s51, s51, 0\n"
      "global_load_dwordx4 a[184:187], v133, s[50:51] \n"
      "s_add_u32 s50, s50, 0x7000\n"
      "s_addc_u32 s51, s51, 0\n"
      "global_load_dwordx4 a[188:191], v133, s[50:51] \n"
      "s_add_u32 s50, s50, 0x7000\n"
      "s_addc_u32 s51, s51, 0\n"
      "global_load_dwordx4 a[192:195], v133, s[50:51] \n"
      "s_add_u32 s50, s50, 0x7000\n"
      "s_addc_u32 s51, s51, 0\n"
      "global_load_dwordx4 a[196:199], v133, s[50:51] \n"
      "s_add_u32 s50, s50, 0x7000\n"
      "s_addc_u32 s51, s51, 0\n"
      "global_load_dwordx4 a[200:203], v133, s[52:53] \n"
      "s_add_u32 s52, s52, 0x7000\n"
      "s_addc_u32 s53, s53, 0\n"
      "global_load_dwordx4 a[204:207], v133, s[52:53] \n"
      "s_add_u32 s52, s52, 0x7000\n"
      "s_addc_u32 s53, s53, 0\n"
      "global_load_dwordx4 a[208:211], v133, s[52:53] \n"
      "s_add_u32 s52, s52, 0x7000\n"
      "s_addc_u32 s53, s53, 0\n"
      "global_load_dwordx4 a[212:215], v133, s[52:53] \n"
      "s_add_u32 s52, s52, 0x7000\n"
      "s_addc_u32 s53, s53, 0\n"
      "global_load_dwordx4 a[216:219], v133, s[52:53] \n"
      "s_add_u32 s52, s52, 0x7000\n"
      "s_addc_u32 s53, s53, 0\n"
      "global_load_dwordx4 a[220:223], v133, s[52:53] \n"
      "s_add_u32 s52, s52, 0x7000\n"
      "s_addc_u32 s53, s53, 0\n"
      "v_mbcnt_lo_u32_b32 v56, -1, 0\n"
      "v_mbcnt_hi_u32_b32 v56, -1, v56\n"
      "v_lshrrev_b32_e32 v57, 4, v56\n"
      "v_lshlrev_b32_e32 v59, 3, v57\n"
      "v_bfe_u32 v57, v56, 2, 2\n"
      "v_add_u32_e32 v59, v59, v57\n"
      "v_mul_u32_u24_e32 v59, 448, v59\n"
      "v_and_b32_e32 v57, 3, v56\n"
      "v_lshl_add_u32 v59, v57, 3, v59\n"
      "s_lshl_b32 s88, s38, 7\n"
      "s_cmp_eq_u32 s38, 3\n"
      "s_cselect_b32 s89, 32, 0\n"
      "s_sub_u32 s88, s88, s89\n"
      "v_add_u32_e32 v59, s88, v59\n"
      "v_add_u32_e32 v61, 0x0, v59\n"
      "ds_read_b64_tr_b16 v[160:161], v61 offset:0\n"
      "ds_read_b64_tr_b16 v[162:163], v61 offset:1792\n"
      "ds_read_b64_tr_b16 v[164:165], v61 offset:32\n"
      "ds_read_b64_tr_b16 v[166:167], v61 offset:1824\n"
      "ds_read_b64_tr_b16 v[168:169], v61 offset:64\n"
      "ds_read_b64_tr_b16 v[170:171], v61 offset:1856\n"
      "ds_read_b64_tr_b16 v[172:173], v61 offset:96\n"
      "ds_read_b64_tr_b16 v[174:175], v61 offset:1888\n"
      "s_waitcnt lgkmcnt(0)\n"
      "v_add_u32_e32 v61, 0x3800, v59\n"
      "ds_read_b64_tr_b16 v[176:177], v61 offset:0\n"
      "ds_read_b64_tr_b16 v[178:179], v61 offset:1792\n"
      "ds_read_b64_tr_b16 v[180:181], v61 offset:32\n"
      "ds_read_b64_tr_b16 v[182:183], v61 offset:1824\n"
      "ds_read_b64_tr_b16 v[184:185], v61 offset:64\n"
      "ds_read_b64_tr_b16 v[186:187], v61 offset:1856\n"
      "ds_read_b64_tr_b16 v[188:189], v61 offset:96\n"
      "ds_read_b64_tr_b16 v[190:191], v61 offset:1888\n"
      "s_waitcnt lgkmcnt(0)\n"
      "v_add_u32_e32 v61, 0x7000, v59\n"
      "ds_read_b64_tr_b16 v[192:193], v61 offset:0\n"
      "ds_read_b64_tr_b16 v[194:195], v61 offset:1792\n"
      "ds_read_b64_tr_b16 v[196:197], v61 offset:32\n"
      "ds_read_b64_tr_b16 v[198:199], v61 offset:1824\n"
      "ds_read_b64_tr_b16 v[200:201], v61 offset:64\n"
      "ds_read_b64_tr_b16 v[202:203], v61 offset:1856\n"
      "ds_read_b64_tr_b16 v[204:205], v61 offset:96\n"
      "ds_read_b64_tr_b16 v[206:207], v61 offset:1888\n"
      "s_waitcnt lgkmcnt(0)\n"
      "v_add_u32_e32 v61, 0xa800, v59\n"
      "ds_read_b64_tr_b16 v[208:209], v61 offset:0\n"
      "ds_read_b64_tr_b16 v[210:211], v61 offset:1792\n"
      "ds_read_b64_tr_b16 v[212:213], v61 offset:32\n"
      "ds_read_b64_tr_b16 v[214:215], v61 offset:1824\n"
      "ds_read_b64_tr_b16 v[216:217], v61 offset:64\n"
      "ds_read_b64_tr_b16 v[218:219], v61 offset:1856\n"
      "ds_read_b64_tr_b16 v[220:221], v61 offset:96\n"
      "ds_read_b64_tr_b16 v[222:223], v61 offset:1888\n"
      "s_waitcnt lgkmcnt(0)\n"
      "s_waitcnt lgkmcnt(0)\n"
      "s_barrier\n"
      "v_mbcnt_lo_u32_b32 v56, -1, 0\n"
      "v_mbcnt_hi_u32_b32 v56, -1, v56\n"
      "v_lshrrev_b32_e32 v104, 4, v56\n"
      "v_lshlrev_b32_e32 v104, 9, v104\n"
      "v_bfe_u32 v57, v56, 3, 1\n"
      "v_lshl_add_u32 v104, v57, 12, v104\n"
      "v_and_b32_e32 v57, 7, v56\n"
      "v_lshl_add_u32 v104, v57, 4, v104\n"
      "s_mov_b32 s88, 0x800\n"
      "s_mul_i32 s89, s100, 8192\n"
      "s_add_u32 s88, s88, s89\n"
      "s_add_u32 s44, s36, s88\n"
      "s_addc_u32 s45, s37, 0\n"
      "global_load_dwordx4 v[100:103], v104, s[44:45] sc1\n"
      "v_mbcnt_lo_u32_b32 v56, -1, 0\n"
      "v_mbcnt_hi_u32_b32 v56, -1, v56\n"
      "v_lshlrev_b32_e32 v57, 2, v56\n"
      "s_lshl_b32 s88, s38, 8\n"
      "s_add_u32 s88, s88, 0x100\n"
      "v_add_u32_e32 v137, s88, v57\n"
      "global_load_dword v136, v137, s[36:37] sc1\n"
      "s_waitcnt vmcnt(22)\n"
      "s_mul_i32 s90, s38, 14336\n"
      "v_mbcnt_lo_u32_b32 v61, -1, 0\n"
      "v_mbcnt_hi_u32_b32 v61, -1, v61\n"
      "v_lshlrev_b32_e32 v61, 4, v61\n"
      "v_add_u32_e32 v61, s90, v61\n"
      "s_mov_b32 exec_lo, -1\n"
      "s_mov_b32 exec_hi, 0x00ffffff\n"
      "ds_write_b128 v61, v[224:227] offset:0\n"
      "ds_write_b128 v61, v[228:231] offset:896\n"
      "ds_write_b128 v61, v[232:235] offset:1792\n"
      "ds_write_b128 v61, v[236:239] offset:2688\n"
      "ds_write_b128 v61, v[240:243] offset:3584\n"
      "ds_write_b128 v61, v[244:247] offset:4480\n"
      "ds_write_b128 v61, v[248:251] offset:5376\n"
      "ds_write_b128 v61, v[252:255] offset:6272\n"
      "ds_write_b128 v61, a[0:3] offset:7168\n"
      "ds_write_b128 v61, a[4:7] offset:8064\n"
      "ds_write_b128 v61, a[8:11] offset:8960\n"
      "ds_write_b128 v61, a[12:15] offset:9856\n"
      "ds_write_b128 v61, a[16:19] offset:10752\n"
      "ds_write_b128 v61, a[20:23] offset:11648\n"
      "ds_write_b128 v61, a[24:27] offset:12544\n"
      "ds_write_b128 v61, a[28:31] offset:13440\n"
      "s_mov_b64 exec, -1\n"
      "s_waitcnt lgkmcnt(0)\n"
      "s_barrier\n"
      "global_load_dwordx4 a[224:227], v133, s[52:53] \n"
      "s_add_u32 s52, s52, 0x7000\n"
      "s_addc_u32 s53, s53, 0\n"
      "global_load_dwordx4 a[228:231], v133, s[52:53] \n"
      "s_add_u32 s52, s52, 0x7000\n"
      "s_addc_u32 s53, s53, 0\n"
      "global_load_dwordx4 a[232:235], v133, s[52:53] \n"
      "s_add_u32 s52, s52, 0x7000\n"
      "s_addc_u32 s53, s53, 0\n"
      "global_load_dwordx4 a[236:239], v133, s[52:53] \n"
      "s_add_u32 s52, s52, 0x7000\n"
      "s_addc_u32 s53, s53, 0\n"
      "global_load_dwordx4 a[240:243], v133, s[52:53] \n"
      "s_add_u32 s52, s52, 0x7000\n"
      "s_addc_u32 s53, s53, 0\n"
      "global_load_dwordx4 a[244:247], v133, s[52:53] \n"
      "s_add_u32 s52, s52, 0x7000\n"
      "s_addc_u32 s53, s53, 0\n"
      "global_load_dwordx4 a[248:251], v133, s[52:53] \n"
      "s_add_u32 s52, s52, 0x7000\n"
      "s_addc_u32 s53, s53, 0\n"
      "global_load_dwordx4 a[252:255], v133, s[52:53] \n"
      "s_add_u32 s52, s52, 0x7000\n"
      "s_addc_u32 s53, s53, 0\n"
      "global_load_dwordx4 a[32:35], v134, s[54:55] \n"
      "s_add_u32 s54, s54, 0x8000\n"
      "s_addc_u32 s55, s55, 0\n"
      "global_load_dwordx4 a[36:39], v134, s[54:55] \n"
      "s_add_u32 s54, s54, 0x8000\n"
      "s_addc_u32 s55, s55, 0\n"
      "global_load_dwordx4 a[40:43], v134, s[54:55] \n"
      "s_add_u32 s54, s54, 0x8000\n"
      "s_addc_u32 s55, s55, 0\n"
      "global_load_dwordx4 a[44:47], v134, s[54:55] \n"
      "s_add_u32 s54, s54, 0x8000\n"
      "s_addc_u32 s55, s55, 0\n"
      "global_load_dwordx4 a[48:51], v134, s[54:55] \n"
      "s_add_u32 s54, s54, 0x8000\n"
      "s_addc_u32 s55, s55, 0\n"
      "global_load_dwordx4 a[52:55], v134, s[54:55] \n"
      "s_add_u32 s54, s54, 0x8000\n"
      "s_addc_u32 s55, s55, 0\n"
      "v_mbcnt_lo_u32_b32 v56, -1, 0\n"
      "v_mbcnt_hi_u32_b32 v56, -1, v56\n"
      "v_lshrrev_b32_e32 v57, 4, v56\n"
      "v_lshlrev_b32_e32 v59, 3, v57\n"
      "v_bfe_u32 v57, v56, 2, 2\n"
      "v_add_u32_e32 v59, v59, v57\n"
      "v_mul_u32_u24_e32 v59, 448, v59\n"
      "v_and_b32_e32 v57, 3, v56\n"
      "v_lshl_add_u32 v59, v57, 3, v59\n"
      "s_lshl_b32 s88, s38, 7\n"
      "s_cmp_eq_u32 s38, 3\n"
      "s_cselect_b32 s89, 32, 0\n"
      "s_sub_u32 s88, s88, s89\n"
      "v_add_u32_e32 v59, s88, v59\n"
      "v_add_u32_e32 v61, 0x0, v59\n"
      "ds_read_b64_tr_b16 v[224:225], v61 offset:0\n"
      "ds_read_b64_tr_b16 v[226:227], v61 offset:1792\n"
      "ds_read_b64_tr_b16 v[228:229], v61 offset:32\n"
      "ds_read_b64_tr_b16 v[230:231], v61 offset:1824\n"
      "ds_read_b64_tr_b16 v[232:233], v61 offset:64\n"
      "ds_read_b64_tr_b16 v[234:235], v61 offset:1856\n"
      "ds_read_b64_tr_b16 v[236:237], v61 offset:96\n"
      "ds_read_b64_tr_b16 v[238:239], v61 offset:1888\n"
      "s_waitcnt lgkmcnt(0)\n"
      "v_add_u32_e32 v61, 0x3800, v59\n"
      "ds_read_b64_tr_b16 v[240:241], v61 offset:0\n"
      "ds_read_b64_tr_b16 v[242:243], v61 offset:1792\n"
      "ds_read_b64_tr_b16 v[244:245], v61 offset:32\n"
      "ds_read_b64_tr_b16 v[246:247], v61 offset:1824\n"
      "ds_read_b64_tr_b16 v[248:249], v61 offset:64\n"
      "ds_read_b64_tr_b16 v[250:251], v61 offset:1856\n"
      "ds_read_b64_tr_b16 v[252:253], v61 offset:96\n"
      "ds_read_b64_tr_b16 v[254:255], v61 offset:1888\n"
      "s_waitcnt lgkmcnt(0)\n"
      "v_add_u32_e32 v61, 0x7000, v59\n"
      "ds_read_b64_tr_b16 a[0:1], v61 offset:0\n"
      "ds_read_b64_tr_b16 a[2:3], v61 offset:1792\n"
      "ds_read_b64_tr_b16 a[4:5], v61 offset:32\n"
      "ds_read_b64_tr_b16 a[6:7], v61 offset:1824\n"
      "ds_read_b64_tr_b16 a[8:9], v61 offset:64\n"
      "ds_read_b64_tr_b16 a[10:11], v61 offset:1856\n"
      "ds_read_b64_tr_b16 a[12:13], v61 offset:96\n"
      "ds_read_b64_tr_b16 a[14:15], v61 offset:1888\n"
      "s_waitcnt lgkmcnt(0)\n"
      "v_add_u32_e32 v61, 0xa800, v59\n"
      "ds_read_b64_tr_b16 a[16:17], v61 offset:0\n"
      "ds_read_b64_tr_b16 a[18:19], v61 offset:1792\n"
      "ds_read_b64_tr_b16 a[20:21], v61 offset:32\n"
      "ds_read_b64_tr_b16 a[22:23], v61 offset:1824\n"
      "ds_read_b64_tr_b16 a[24:25], v61 offset:64\n"
      "ds_read_b64_tr_b16 a[26:27], v61 offset:1856\n"
      "ds_read_b64_tr_b16 a[28:29], v61 offset:96\n"
      "ds_read_b64_tr_b16 a[30:31], v61 offset:1888\n"
      "s_waitcnt lgkmcnt(0)\n"
      "s_waitcnt lgkmcnt(0)\n"
      "s_barrier\n"
      "s_waitcnt vmcnt(14)\n"
      "s_mov_b32 s83, 0\n"
      "s_mov_b32 s43, 0\n"
      "s_nop 0\n"
      "XQ_4_%=:\n"
      "v_cmp_ne_u32_e64 s[88:89], s35, v101\n"
      "v_cmp_ne_u32_e64 s[90:91], s35, v103\n"
      "s_or_b64 s[88:89], s[88:89], s[90:91]\n"
      "s_cmp_eq_u64 s[88:89], 0\n"
      "s_nop 2\n"
      "s_cbranch_scc1 XD_5_%=\n"
      "global_load_dwordx4 v[100:103], v104, s[44:45] sc1\n"
      "s_waitcnt vmcnt(0)\n"
      "s_add_u32 s43, s43, 1\n"
      "s_cmp_lt_u32 s43, 0x80000\n"
      "s_nop 0\n"
      "s_cbranch_scc1 XQ_4_%=\n"
      "s_nop 0\n"
      "XD_5_%=:\n"
      "v_mbcnt_lo_u32_b32 v96, -1, 0\n"
      "v_mbcnt_hi_u32_b32 v96, -1, v96\n"
      "s_lshl_b32 s88, s38, 9\n"
      "s_add_u32 s88, s88, 0x1eb00\n"
      "v_mov_b32_e32 v101, v102\n"
      "v_lshlrev_b32_e32 v98, 3, v96\n"
      "v_add_u32_e32 v98, s88, v98\n"
      "ds_write_b64 v98, v[100:101]\n"
      "s_getpc_b64 s[88:89]\n"
      "XPC_6_%=:\n"
      "s_add_u32 s88, s88, C_EXTRA2_%=-XPC_6_%=\n"
      "s_addc_u32 s89, s89, 0\n"
      "s_nop 0\n"
      "s_swappc_b64 s[98:99], s[88:89]\n"
      "global_load_dwordx4 a[80:83], v134, s[54:55] \n"
      "s_add_u32 s54, s54, 0x8000\n"
      "s_addc_u32 s55, s55, 0\n"
      "global_load_dwordx4 a[88:91], v134, s[54:55] \n"
      "s_add_u32 s54, s54, 0x8000\n"
      "s_addc_u32 s55, s55, 0\n"
      "global_load_dwordx4 a[92:95], v134, s[54:55] \n"
      "s_add_u32 s54, s54, 0x8000\n"
      "s_addc_u32 s55, s55, 0\n"
      "global_load_dwordx4 a[96:99], v134, s[54:55] \n"
      "s_add_u32 s54, s54, 0x8000\n"
      "s_addc_u32 s55, s55, 0\n"
      "global_load_dwordx4 a[100:103], v134, s[54:55] \n"
      "s_add_u32 s54, s54, 0x8000\n"
      "s_addc_u32 s55, s55, 0\n"
      "global_load_dwordx4 a[104:107], v134, s[54:55] \n"
      "s_add_u32 s54, s54, 0x8000\n"
      "s_addc_u32 s55, s55, 0\n"
      "global_load_dwordx4 a[108:111], v134, s[54:55] \n"
      "s_add_u32 s54, s54, 0x8000\n"
      "s_addc_u32 s55, s55, 0\n"
      "global_load_dwordx4 a[136:139], v134, s[54:55] \n"
      "s_add_u32 s54, s54, 0x158000\n"
      "s_addc_u32 s55, s55, 0\n"
      "global_load_dwordx4 a[56:59], v134, s[54:55] \n"
      "s_add_u32 s54, s54, 0x8000\n"
      "s_addc_u32 s55, s55, 0\n"
      "global_load_dwordx4 a[60:63], v134, s[54:55] \n"
      "s_add_u32 s54, s54, 0x8000\n"
      "s_addc_u32 s55, s55, 0\n"
      "global_load_dwordx4 a[64:67], v134, s[54:55] \n"
      "s_add_u32 s54, s54, 0x8000\n"
      "s_addc_u32 s55, s55, 0\n"
      "global_load_dwordx4 a[68:71], v134, s[54:55] \n"
      "s_add_u32 s54, s54, 0x8000\n"
      "s_addc_u32 s55, s55, 0\n"
      "global_load_dwordx4 a[72:75], v134, s[54:55] \n"
      "s_add_u32 s54, s54, 0x8000\n"
      "s_addc_u32 s55, s55, 0\n"
      "global_load_dwordx4 a[76:79], v134, s[54:55] \n"
      "s_add_u32 s54, s54, 0x8000\n"
      "s_addc_u32 s55, s55, 0\n"
      "global_load_dwordx4 a[84:87], v134, s[54:55] \n"
      "s_add_u32 s54, s54, 0x8000\n"
      "s_addc_u32 s55, s55, 0\n"
      "global_load_dwordx4 a[112:115], v134, s[54:55] \n"
      "s_add_u32 s54, s54, 0x8000\n"
      "s_addc_u32 s55, s55, 0\n"
      "global_load_dwordx4 a[116:119], v134, s[54:55] \n"
      "s_add_u32 s54, s54, 0x8000\n"
      "s_addc_u32 s55, s55, 0\n"
      "global_load_dwordx4 a[120:123], v134, s[54:55] \n"
      "s_add_u32 s54, s54, 0x8000\n"
      "s_addc_u32 s55, s55, 0\n"
      "global_load_dwordx4 a[124:127], v134, s[54:55] \n"
      "s_add_u32 s54, s54, 0x8000\n"
      "s_addc_u32 s55, s55, 0\n"
      "global_load_dwordx4 a[128:131], v134, s[54:55] \n"
      "s_add_u32 s54, s54, 0x8000\n"
      "s_addc_u32 s55, s55, 0\n"
      "s_cmp_lg_u32 s39, 0\n"
      "s_nop 0\n"
      "s_cbranch_scc1 NC0_7_%=\n"
      "s_mov_b32 s43, 0\n"
      "s_nop 0\n"
      "FL_8_%=:\n"
      "v_cmp_ne_u32_e32 vcc, s35, v136\n"
      "s_nop 4\n"
      "s_cbranch_vccz NC0_7_%=\n"
      "global_load_dword v136, v137, s[36:37] sc1\n"
      "s_waitcnt vmcnt(0)\n"
      "s_add_u32 s43, s43, 1\n"
      "s_cmp_lt_u32 s43, 0x80000\n"
      "s_nop 0\n"
      "s_cbranch_scc1 FL_8_%=\n"
      "s_nop 0\n"
      "NC0_7_%=:\n"
      "s_mul_i32 s90, s38, 14336\n"
      "v_mbcnt_lo_u32_b32 v61, -1, 0\n"
      "v_mbcnt_hi_u32_b32 v61, -1, v61\n"
      "v_lshlrev_b32_e32 v61, 4, v61\n"
      "v_add_u32_e32 v61, s90, v61\n"
      "ds_write_b128 v61, a[144:147] offset:0\n"
      "ds_write_b128 v61, a[148:151] offset:1024\n"
      "ds_write_b128 v61, a[152:155] offset:2048\n"
      "ds_write_b128 v61, a[156:159] offset:3072\n"
      "ds_write_b128 v61, a[160:163] offset:4096\n"
      "ds_write_b128 v61, a[164:167] offset:5120\n"
      "ds_write_b128 v61, a[168:171] offset:6144\n"
      "ds_write_b128 v61, a[172:175] offset:7168\n"
      "ds_write_b128 v61, a[176:179] offset:8192\n"
      "ds_write_b128 v61, a[180:183] offset:9216\n"
      "ds_write_b128 v61, a[184:187] offset:10240\n"
      "ds_write_b128 v61, a[188:191] offset:11264\n"
      "ds_write_b128 v61, a[192:195] offset:12288\n"
      "ds_write_b128 v61, a[196:199] offset:13312\n"
      "s_waitcnt lgkmcnt(0)\n"
      "s_barrier\n"
      "s_or_b32 s88, s39, s38\n"
      "s_cmp_lg_u32 s88, 0\n"
      "s_nop 0\n"
      "s_cbranch_scc1 NEP_9_%=\n"
      "v_mov_b32_e32 v57, 0\n"
      "v_mov_b32_e32 v58, s35\n"
      "s_mov_b64 exec, 1\n"
      "global_store_dword v57, v58, s[36:37] sc1\n"
      "s_mov_b64 exec, -1\n"
      "s_nop 1\n"
      "NEP_9_%=:\n"
      "s_lshl_b32 s92, s38, 5\n"
      "v_mbcnt_lo_u32_b32 v56, -1, 0\n"
      "v_mbcnt_hi_u32_b32 v56, -1, v56\n"
      "v_bfe_u32 v57, v56, 4, 1\n"
      "v_lshlrev_b32_e32 v59, 2, v57\n"
      "v_bfe_u32 v57, v56, 2, 1\n"
      "v_lshl_add_u32 v59, v57, 3, v59\n"
      "v_bfe_u32 v57, v56, 3, 1\n"
      "v_add_u32_e32 v59, v59, v57\n"
      "v_lshrrev_b32_e32 v57, 5, v56\n"
      "v_mul_u32_u24_e32 v58, 112, v57\n"
      "v_add_u32_e32 v60, v59, v58\n"
      "v_lshl_add_u32 v59, v57, 4, v59\n"
      "v_mul_u32_u24_e32 v59, 128, v59\n"
      "v_mul_u32_u24_e32 v60, 128, v60\n"
      "v_and_b32_e32 v57, 3, v56\n"
      "v_lshl_add_u32 v59, v57, 3, v59\n"
      "v_add_u32_e32 v59, s92, v59\n"
      "v_lshl_add_u32 v60, v57, 3, v60\n"
      "v_add_u32_e32 v60, s92, v60\n"
      "v_add_u32_e32 v61, 0x0, v59\n"
      "ds_read_b64_tr_b16 a[144:145], v61 offset:0\n"
      "ds_read_b64_tr_b16 a[146:147], v61 offset:256\n"
      "v_add_u32_e32 v61, 0x1000, v59\n"
      "ds_read_b64_tr_b16 a[148:149], v61 offset:0\n"
      "ds_read_b64_tr_b16 a[150:151], v61 offset:256\n"
      "v_add_u32_e32 v61, 0x2000, v59\n"
      "ds_read_b64_tr_b16 a[152:153], v61 offset:0\n"
      "ds_read_b64_tr_b16 a[154:155], v61 offset:256\n"
      "s_waitcnt lgkmcnt(0)\n"
      "v_add_u32_e32 v61, 0x3800, v59\n"
      "ds_read_b64_tr_b16 a[156:157], v61 offset:0\n"
      "ds_read_b64_tr_b16 a[158:159], v61 offset:256\n"
      "v_add_u32_e32 v61, 0x4800, v59\n"
      "ds_read_b64_tr_b16 a[160:161], v61 offset:0\n"
      "ds_read_b64_tr_b16 a[162:163], v61 offset:256\n"
      "v_add_u32_e32 v61, 0x5800, v59\n"
      "ds_read_b64_tr_b16 a[164:165], v61 offset:0\n"
      "ds_read_b64_tr_b16 a[166:167], v61 offset:256\n"
      "s_waitcnt lgkmcnt(0)\n"
      "v_add_u32_e32 v61, 0x7000, v59\n"
      "ds_read_b64_tr_b16 a[168:169], v61 offset:0\n"
      "ds_read_b64_tr_b16 a[170:171], v61 offset:256\n"
      "v_add_u32_e32 v61, 0x8000, v59\n"
      "ds_read_b64_tr_b16 a[172:173], v61 offset:0\n"
      "ds_read_b64_tr_b16 a[174:175], v61 offset:256\n"
      "v_add_u32_e32 v61, 0x9000, v59\n"
      "ds_read_b64_tr_b16 a[176:177], v61 offset:0\n"
      "ds_read_b64_tr_b16 a[178:179], v61 offset:256\n"
      "s_waitcnt lgkmcnt(0)\n"
      "v_add_u32_e32 v61, 0xa800, v59\n"
      "ds_read_b64_tr_b16 a[180:181], v61 offset:0\n"
      "ds_read_b64_tr_b16 a[182:183], v61 offset:256\n"
      "v_add_u32_e32 v61, 0xb800, v59\n"
      "ds_read_b64_tr_b16 a[184:185], v61 offset:0\n"
      "ds_read_b64_tr_b16 a[186:187], v61 offset:256\n"
      "v_add_u32_e32 v61, 0xc800, v59\n"
      "ds_read_b64_tr_b16 a[188:189], v61 offset:0\n"
      "ds_read_b64_tr_b16 a[190:191], v61 offset:256\n"
      "s_waitcnt lgkmcnt(0)\n"
      "v_add_u32_e32 v61, 0x3000, v60\n"
      "ds_read_b64_tr_b16 a[192:193], v61 offset:0\n"
      "ds_read_b64_tr_b16 a[194:195], v61 offset:256\n"
      "v_add_u32_e32 v61, 0xa000, v60\n"
      "ds_read_b64_tr_b16 a[196:197], v61 offset:0\n"
      "ds_read_b64_tr_b16 a[198:199], v61 offset:256\n"
      "s_waitcnt lgkmcnt(0)\n"
      "s_waitcnt lgkmcnt(0)\n"
      "s_barrier\n"
      "v_mov_b32_e32 v64, 0x3f803f80\n"
      "v_mov_b32_e32 v65, 0x3f803f80\n"
      "v_mov_b32_e32 v66, 0x3f803f80\n"
      "v_mov_b32_e32 v67, 0x3f803f80\n"
      "s_nop 1\n"
      "v_mfma_f32_16x16x32_bf16 v[68:71], a[144:147], v[64:67], 0\n"
      "v_mfma_f32_16x16x32_bf16 v[68:71], a[148:151], v[64:67], v[68:71]\n"
      "v_mfma_f32_16x16x32_bf16 v[68:71], a[152:155], v[64:67], v[68:71]\n"
      "v_mfma_f32_16x16x32_bf16 v[68:71], a[156:159], v[64:67], v[68:71]\n"
      "v_mfma_f32_16x16x32_bf16 v[68:71], a[160:163], v[64:67], v[68:71]\n"
      "v_mfma_f32_16x16x32_bf16 v[68:71], a[164:167], v[64:67], v[68:71]\n"
      "v_mfma_f32_16x16x32_bf16 v[68:71], a[168:171], v[64:67], v[68:71]\n"
      "v_mfma_f32_16x16x32_bf16 v[68:71], a[172:175], v[64:67], v[68:71]\n"
      "v_mfma_f32_16x16x32_bf16 v[68:71], a[176:179], v[64:67], v[68:71]\n"
      "v_mfma_f32_16x16x32_bf16 v[68:71], a[180:183], v[64:67], v[68:71]\n"
      "v_mfma_f32_16x16x32_bf16 v[68:71], a[184:187], v[64:67], v[68:71]\n"
      "v_mfma_f32_16x16x32_bf16 v[68:71], a[188:191], v[64:67], v[68:71]\n"
      "v_mfma_f32_16x16x32_bf16 v[68:71], a[192:195], v[64:67], v[68:71]\n"
      "v_mfma_f32_16x16x32_bf16 v[68:71], a[196:199], v[64:67], v[68:71]\n"
      "s_nop 7\n"
      "v_cndmask_b32_e64 v72, v68, v69, s[96:97]\n"
      "v_cndmask_b32_e64 v73, v70, v71, s[96:97]\n"
      "s_mov_b32 s88, 0xcccccccc\n"
      "s_mov_b32 s89, 0xcccccccc\n"
      "v_cndmask_b32_e64 v152, v72, v73, s[88:89]\n"
      "s_waitcnt vmcnt(26)\n"
      "s_mul_i32 s90, s38, 14336\n"
      "v_mbcnt_lo_u32_b32 v61, -1, 0\n"
      "v_mbcnt_hi_u32_b32 v61, -1, v61\n"
      "v_lshlrev_b32_e32 v61, 4, v61\n"
      "v_add_u32_e32 v61, s90, v61\n"
      "ds_write_b128 v61, a[200:203] offset:0\n"
      "ds_write_b128 v61, a[204:207] offset:1024\n"
      "ds_write_b128 v61, a[208:211] offset:2048\n"
      "ds_write_b128 v61, a[212:215] offset:3072\n"
      "ds_write_b128 v61, a[216:219] offset:4096\n"
      "ds_write_b128 v61, a[220:223] offset:5120\n"
      "ds_write_b128 v61, a[224:227] offset:6144\n"
      "ds_write_b128 v61, a[228:231] offset:7168\n"
      "ds_write_b128 v61, a[232:235] offset:8192\n"
      "ds_write_b128 v61, a[236:239] offset:9216\n"
      "ds_write_b128 v61, a[240:243] offset:10240\n"
      "ds_write_b128 v61, a[244:247] offset:11264\n"
      "ds_write_b128 v61, a[248:251] offset:12288\n"
      "ds_write_b128 v61, a[252:255] offset:13312\n"
      "s_waitcnt lgkmcnt(0)\n"
      "s_barrier\n"
      "global_load_dwordx4 a[132:135], v134, s[54:55] \n"
      "s_add_u32 s54, s54, 0x8000\n"
      "s_addc_u32 s55, s55, 0\n"
      "global_load_dwordx4 a[140:143], v134, s[54:55] \n"
      "s_add_u32 s54, s54, 0x158000\n"
      "s_addc_u32 s55, s55, 0\n"
      "s_mov_b32 exec_lo, -1\n"
      "s_mov_b32 exec_hi, 0x00ffffff\n"
      "global_load_dwordx4 v[0:3], v135, s[56:57] \n"
      "s_add_u32 s56, s56, 0x380\n"
      "s_addc_u32 s57, s57, 0\n"
      "global_load_dwordx4 v[4:7], v135, s[56:57] \n"
      "s_add_u32 s56, s56, 0x380\n"
      "s_addc_u32 s57, s57, 0\n"
      "global_load_dwordx4 v[8:11], v135, s[56:57] \n"
      "s_add_u32 s56, s56, 0x380\n"
      "s_addc_u32 s57, s57, 0\n"
      "global_load_dwordx4 v[12:15], v135, s[56:57] \n"
      "s_add_u32 s56, s56, 0x380\n"
      "s_addc_u32 s57, s57, 0\n"
      "global_load_dwordx4 v[16:19], v135, s[56:57] \n"
      "s_add_u32 s56, s56, 0x380\n"
      "s_addc_u32 s57, s57, 0\n"
      "s_mov_b64 exec, -1\n"
      "s_lshl_b32 s92, s38, 5\n"
      "v_mbcnt_lo_u32_b32 v56, -1, 0\n"
      "v_mbcnt_hi_u32_b32 v56, -1, v56\n"
      "v_bfe_u32 v57, v56, 4, 1\n"
      "v_lshlrev_b32_e32 v59, 2, v57\n"
      "v_bfe_u32 v57, v56, 2, 1\n"
      "v_lshl_add_u32 v59, v57, 3, v59\n"
      "v_bfe_u32 v57, v56, 3, 1\n"
      "v_add_u32_e32 v59, v59, v57\n"
      "v_lshrrev_b32_e32 v57, 5, v56\n"
      "v_mul_u32_u24_e32 v58, 112, v57\n"
      "v_add_u32_e32 v60, v59, v58\n"
      "v_lshl_add_u32 v59, v57, 4, v59\n"
      "v_mul_u32_u24_e32 v59, 128, v59\n"
      "v_mul_u32_u24_e32 v60, 128, v60\n"
      "v_and_b32_e32 v57, 3, v56\n"
      "v_lshl_add_u32 v59, v57, 3, v59\n"
      "v_add_u32_e32 v59, s92, v59\n"
      "v_lshl_add_u32 v60, v57, 3, v60\n"
      "v_add_u32_e32 v60, s92, v60\n"
      "v_add_u32_e32 v61, 0x0, v59\n"
      "ds_read_b64_tr_b16 a[200:201], v61 offset:0\n"
      "ds_read_b64_tr_b16 a[202:203], v61 offset:256\n"
      "v_add_u32_e32 v61, 0x1000, v59\n"
      "ds_read_b64_tr_b16 a[204:205], v61 offset:0\n"
      "ds_read_b64_tr_b16 a[206:207], v61 offset:256\n"
      "v_add_u32_e32 v61, 0x2000, v59\n"
      "ds_read_b64_tr_b16 a[208:209], v61 offset:0\n"
      "ds_read_b64_tr_b16 a[210:211], v61 offset:256\n"
      "s_waitcnt lgkmcnt(0)\n"
      "v_add_u32_e32 v61, 0x3800, v59\n"
      "ds_read_b64_tr_b16 a[212:213], v61 offset:0\n"
      "ds_read_b64_tr_b16 a[214:215], v61 offset:256\n"
      "v_add_u32_e32 v61, 0x4800, v59\n"
      "ds_read_b64_tr_b16 a[216:217], v61 offset:0\n"
      "ds_read_b64_tr_b16 a[218:219], v61 offset:256\n"
      "v_add_u32_e32 v61, 0x5800, v59\n"
      "ds_read_b64_tr_b16 a[220:221], v61 offset:0\n"
      "ds_read_b64_tr_b16 a[222:223], v61 offset:256\n"
      "s_waitcnt lgkmcnt(0)\n"
      "v_add_u32_e32 v61, 0x7000, v59\n"
      "ds_read_b64_tr_b16 a[224:225], v61 offset:0\n"
      "ds_read_b64_tr_b16 a[226:227], v61 offset:256\n"
      "v_add_u32_e32 v61, 0x8000, v59\n"
      "ds_read_b64_tr_b16 a[228:229], v61 offset:0\n"
      "ds_read_b64_tr_b16 a[230:231], v61 offset:256\n"
      "v_add_u32_e32 v61, 0x9000, v59\n"
      "ds_read_b64_tr_b16 a[232:233], v61 offset:0\n"
      "ds_read_b64_tr_b16 a[234:235], v61 offset:256\n"
      "s_waitcnt lgkmcnt(0)\n"
      "v_add_u32_e32 v61, 0xa800, v59\n"
      "ds_read_b64_tr_b16 a[236:237], v61 offset:0\n"
      "ds_read_b64_tr_b16 a[238:239], v61 offset:256\n"
      "v_add_u32_e32 v61, 0xb800, v59\n"
      "ds_read_b64_tr_b16 a[240:241], v61 offset:0\n"
      "ds_read_b64_tr_b16 a[242:243], v61 offset:256\n"
      "v_add_u32_e32 v61, 0xc800, v59\n"
      "ds_read_b64_tr_b16 a[244:245], v61 offset:0\n"
      "ds_read_b64_tr_b16 a[246:247], v61 offset:256\n"
      "s_waitcnt lgkmcnt(0)\n"
      "v_add_u32_e32 v61, 0x3000, v60\n"
      "ds_read_b64_tr_b16 a[248:249], v61 offset:0\n"
      "ds_read_b64_tr_b16 a[250:251], v61 offset:256\n"
      "v_add_u32_e32 v61, 0xa000, v60\n"
      "ds_read_b64_tr_b16 a[252:253], v61 offset:0\n"
      "ds_read_b64_tr_b16 a[254:255], v61 offset:256\n"
      "s_waitcnt lgkmcnt(0)\n"
      "s_waitcnt lgkmcnt(0)\n"
      "s_barrier\n"
      "v_mov_b32_e32 v64, 0x3f803f80\n"
      "v_mov_b32_e32 v65, 0x3f803f80\n"
      "v_mov_b32_e32 v66, 0x3f803f80\n"
      "v_mov_b32_e32 v67, 0x3f803f80\n"
      "s_nop 1\n"
      "v_mfma_f32_16x16x32_bf16 v[68:71], a[200:203], v[64:67], 0\n"
      "v_mfma_f32_16x16x32_bf16 v[68:71], a[204:207], v[64:67], v[68:71]\n"
      "v_mfma_f32_16x16x32_bf16 v[68:71], a[208:211], v[64:67], v[68:71]\n"
      "v_mfma_f32_16x16x32_bf16 v[68:71], a[212:215], v[64:67], v[68:71]\n"
      "v_mfma_f32_16x16x32_bf16 v[68:71], a[216:219], v[64:67], v[68:71]\n"
      "v_mfma_f32_16x16x32_bf16 v[68:71], a[220:223], v[64:67], v[68:71]\n"
      "v_mfma_f32_16x16x32_bf16 v[68:71], a[224:227], v[64:67], v[68:71]\n"
      "v_mfma_f32_16x16x32_bf16 v[68:71], a[228:231], v[64:67], v[68:71]\n"
      "v_mfma_f32_16x16x32_bf16 v[68:71], a[232:235], v[64:67], v[68:71]\n"
      "v_mfma_f32_16x16x32_bf16 v[68:71], a[236:239], v[64:67], v[68:71]\n"
      "v_mfma_f32_16x16x32_bf16 v[68:71], a[240:243], v[64:67], v[68:71]\n"
      "v_mfma_f32_16x16x32_bf16 v[68:71], a[244:247], v[64:67], v[68:71]\n"
      "v_mfma_f32_16x16x32_bf16 v[68:71], a[248:251], v[64:67], v[68:71]\n"
      "v_mfma_f32_16x16x32_bf16 v[68:71], a[252:255], v[64:67], v[68:71]\n"
      "s_nop 7\n"
      "v_cndmask_b32_e64 v72, v68, v69, s[96:97]\n"
      "v_cndmask_b32_e64 v73, v70, v71, s[96:97]\n"
      "s_mov_b32 s88, 0xcccccccc\n"
      "s_mov_b32 s89, 0xcccccccc\n"
      "v_cndmask_b32_e64 v153, v72, v73, s[88:89]\n"
      "s_waitcnt vmcnt(19)\n"
      "s_mul_i32 s90, s38, 14336\n"
      "v_mbcnt_lo_u32_b32 v61, -1, 0\n"
      "v_mbcnt_hi_u32_b32 v61, -1, v61\n"
      "v_lshlrev_b32_e32 v61, 4, v61\n"
      "v_add_u32_e32 v61, s90, v61\n"
      "ds_write_b128 v61, a[32:35] offset:0\n"
      "ds_write_b128 v61, a[36:39] offset:1024\n"
      "ds_write_b128 v61, a[40:43] offset:2048\n"
      "ds_write_b128 v61, a[44:47] offset:3072\n"
      "ds_write_b128 v61, a[48:51] offset:4096\n"
      "ds_write_b128 v61, a[52:55] offset:5120\n"
      "ds_write_b128 v61, a[80:83] offset:6144\n"
      "ds_write_b128 v61, a[88:91] offset:7168\n"
      "ds_write_b128 v61, a[92:95] offset:8192\n"
      "ds_write_b128 v61, a[96:99] offset:9216\n"
      "ds_write_b128 v61, a[100:103] offset:10240\n"
      "ds_write_b128 v61, a[104:107] offset:11264\n"
      "ds_write_b128 v61, a[108:111] offset:12288\n"
      "ds_write_b128 v61, a[136:139] offset:13312\n"
      "s_waitcnt lgkmcnt(0)\n"
      "s_barrier\n"
      "s_mov_b32 exec_lo, -1\n"
      "s_mov_b32 exec_hi, 0x00ffffff\n"
      "global_load_dwordx4 v[20:23], v135, s[56:57] \n"
      "s_add_u32 s56, s56, 0x380\n"
      "s_addc_u32 s57, s57, 0\n"
      "global_load_dwordx4 v[24:27], v135, s[56:57] \n"
      "s_add_u32 s56, s56, 0x380\n"
      "s_addc_u32 s57, s57, 0\n"
      "global_load_dwordx4 v[28:31], v135, s[56:57] \n"
      "s_add_u32 s56, s56, 0x380\n"
      "s_addc_u32 s57, s57, 0\n"
      "global_load_dwordx4 v[32:35], v135, s[56:57] \n"
      "s_add_u32 s56, s56, 0x380\n"
      "s_addc_u32 s57, s57, 0\n"
      "global_load_dwordx4 v[36:39], v135, s[56:57] \n"
      "s_add_u32 s56, s56, 0x380\n"
      "s_addc_u32 s57, s57, 0\n"
      "global_load_dwordx4 v[40:43], v135, s[56:57] \n"
      "s_add_u32 s56, s56, 0x380\n"
      "s_addc_u32 s57, s57, 0\n"
      "global_load_dwordx4 v[44:47], v135, s[56:57] \n"
      "s_add_u32 s56, s56, 0x380\n"
      "s_addc_u32 s57, s57, 0\n"
      "global_load_dwordx4 v[48:51], v135, s[56:57] \n"
      "s_add_u32 s56, s56, 0x380\n"
      "s_addc_u32 s57, s57, 0\n"
      "global_load_dwordx4 v[52:55], v135, s[56:57] \n"
      "s_add_u32 s56, s56, 0x380\n"
      "s_addc_u32 s57, s57, 0\n"
      "global_load_dwordx4 v[124:127], v135, s[56:57] \n"
      "s_add_u32 s56, s56, 0x380\n"
      "s_addc_u32 s57, s57, 0\n"
      "global_load_dwordx4 v[128:131], v135, s[56:57] \n"
      "s_add_u32 s56, s56, 0x380\n"
      "s_addc_u32 s57, s57, 0\n"
      "s_mov_b64 exec, -1\n"
      "s_lshl_b32 s92, s38, 6\n"
      "v_mbcnt_lo_u32_b32 v56, -1, 0\n"
      "v_mbcnt_hi_u32_b32 v56, -1, v56\n"
      "v_bfe_u32 v57, v56, 4, 1\n"
      "v_lshlrev_b32_e32 v59, 2, v57\n"
      "v_bfe_u32 v57, v56, 2, 1\n"
      "v_lshl_add_u32 v59, v57, 3, v59\n"
      "v_bfe_u32 v57, v56, 3, 1\n"
      "v_add_u32_e32 v59, v59, v57\n"
      "v_lshrrev_b32_e32 v57, 5, v56\n"
      "v_mul_u32_u24_e32 v58, 112, v57\n"
      "v_add_u32_e32 v60, v59, v58\n"
      "v_lshl_add_u32 v59, v57, 4, v59\n"
      "v_mul_u32_u24_e32 v59, 256, v59\n"
      "v_mul_u32_u24_e32 v60, 256, v60\n"
      "v_and_b32_e32 v57, 3, v56\n"
      "v_lshl_add_u32 v59, v57, 3, v59\n"
      "v_add_u32_e32 v59, s92, v59\n"
      "v_lshl_add_u32 v60, v57, 3, v60\n"
      "v_add_u32_e32 v60, s92, v60\n"
      "v_add_u32_e32 v61, 0x0, v59\n"
      "ds_read_b64_tr_b16 a[32:33], v61 offset:0\n"
      "ds_read_b64_tr_b16 a[34:35], v61 offset:512\n"
      "v_add_u32_e32 v61, 0x20, v59\n"
      "ds_read_b64_tr_b16 a[88:89], v61 offset:0\n"
      "ds_read_b64_tr_b16 a[90:91], v61 offset:512\n"
      "v_add_u32_e32 v61, 0x2000, v59\n"
      "ds_read_b64_tr_b16 a[36:37], v61 offset:0\n"
      "ds_read_b64_tr_b16 a[38:39], v61 offset:512\n"
      "v_add_u32_e32 v61, 0x2020, v59\n"
      "ds_read_b64_tr_b16 a[92:93], v61 offset:0\n"
      "ds_read_b64_tr_b16 a[94:95], v61 offset:512\n"
      "v_add_u32_e32 v61, 0x4000, v59\n"
      "ds_read_b64_tr_b16 a[40:41], v61 offset:0\n"
      "ds_read_b64_tr_b16 a[42:43], v61 offset:512\n"
      "v_add_u32_e32 v61, 0x4020, v59\n"
      "ds_read_b64_tr_b16 a[96:97], v61 offset:0\n"
      "ds_read_b64_tr_b16 a[98:99], v61 offset:512\n"
      "s_waitcnt lgkmcnt(0)\n"
      "v_add_u32_e32 v61, 0x7000, v59\n"
      "ds_read_b64_tr_b16 a[44:45], v61 offset:0\n"
      "ds_read_b64_tr_b16 a[46:47], v61 offset:512\n"
      "v_add_u32_e32 v61, 0x7020, v59\n"
      "ds_read_b64_tr_b16 a[100:101], v61 offset:0\n"
      "ds_read_b64_tr_b16 a[102:103], v61 offset:512\n"
      "v_add_u32_e32 v61, 0x9000, v59\n"
      "ds_read_b64_tr_b16 a[48:49], v61 offset:0\n"
      "ds_read_b64_tr_b16 a[50:51], v61 offset:512\n"
      "v_add_u32_e32 v61, 0x9020, v59\n"
      "ds_read_b64_tr_b16 a[104:105], v61 offset:0\n"
      "ds_read_b64_tr_b16 a[106:107], v61 offset:512\n"
      "v_add_u32_e32 v61, 0xb000, v59\n"
      "ds_read_b64_tr_b16 a[52:53], v61 offset:0\n"
      "ds_read_b64_tr_b16 a[54:55], v61 offset:512\n"
      "v_add_u32_e32 v61, 0xb020, v59\n"
      "ds_read_b64_tr_b16 a[108:109], v61 offset:0\n"
      "ds_read_b64_tr_b16 a[110:111], v61 offset:512\n"
      "s_waitcnt lgkmcnt(0)\n"
      "v_add_u32_e32 v61, 0x6000, v60\n"
      "ds_read_b64_tr_b16 a[80:81], v61 offset:0\n"
      "ds_read_b64_tr_b16 a[82:83], v61 offset:512\n"
      "v_add_u32_e32 v61, 0x6020, v60\n"
      "ds_read_b64_tr_b16 a[136:137], v61 offset:0\n"
      "ds_read_b64_tr_b16 a[138:139], v61 offset:512\n"
      "s_waitcnt lgkmcnt(0)\n"
      "s_waitcnt lgkmcnt(0)\n"
      "s_barrier\n"
      "s_waitcnt vmcnt(16)\n"
      "s_mul_i32 s90, s38, 14336\n"
      "v_mbcnt_lo_u32_b32 v61, -1, 0\n"
      "v_mbcnt_hi_u32_b32 v61, -1, v61\n"
      "v_lshlrev_b32_e32 v61, 4, v61\n"
      "v_add_u32_e32 v61, s90, v61\n"
      "ds_write_b128 v61, a[56:59] offset:0\n"
      "ds_write_b128 v61, a[60:63] offset:1024\n"
      "ds_write_b128 v61, a[64:67] offset:2048\n"
      "ds_write_b128 v61, a[68:71] offset:3072\n"
      "ds_write_b128 v61, a[72:75] offset:4096\n"
      "ds_write_b128 v61, a[76:79] offset:5120\n"
      "ds_write_b128 v61, a[84:87] offset:6144\n"
      "ds_write_b128 v61, a[112:115] offset:7168\n"
      "ds_write_b128 v61, a[116:119] offset:8192\n"
      "ds_write_b128 v61, a[120:123] offset:9216\n"
      "ds_write_b128 v61, a[124:127] offset:10240\n"
      "ds_write_b128 v61, a[128:131] offset:11264\n"
      "ds_write_b128 v61, a[132:135] offset:12288\n"
      "ds_write_b128 v61, a[140:143] offset:13312\n"
      "s_waitcnt lgkmcnt(0)\n"
      "s_barrier\n"
      "v_mov_b32_e32 v137, 0\n"
      "global_load_dword v136, v137, s[36:37] sc1\n"
      "s_lshl_b32 s92, s38, 6\n"
      "v_mbcnt_lo_u32_b32 v56, -1, 0\n"
      "v_mbcnt_hi_u32_b32 v56, -1, v56\n"
      "v_bfe_u32 v57, v56, 4, 1\n"
      "v_lshlrev_b32_e32 v59, 2, v57\n"
      "v_bfe_u32 v57, v56, 2, 1\n"
      "v_lshl_add_u32 v59, v57, 3, v59\n"
      "v_bfe_u32 v57, v56, 3, 1\n"
      "v_add_u32_e32 v59, v59, v57\n"
      "v_lshrrev_b32_e32 v57, 5, v56\n"
      "v_mul_u32_u24_e32 v58, 112, v57\n"
      "v_add_u32_e32 v60, v59, v58\n"
      "v_lshl_add_u32 v59, v57, 4, v59\n"
      "v_mul_u32_u24_e32 v59, 256, v59\n"
      "v_mul_u32_u24_e32 v60, 256, v60\n"
      "v_and_b32_e32 v57, 3, v56\n"
      "v_lshl_add_u32 v59, v57, 3, v59\n"
      "v_add_u32_e32 v59, s92, v59\n"
      "v_lshl_add_u32 v60, v57, 3, v60\n"
      "v_add_u32_e32 v60, s92, v60\n"
      "v_add_u32_e32 v61, 0x0, v59\n"
      "ds_read_b64_tr_b16 a[56:57], v61 offset:0\n"
      "ds_read_b64_tr_b16 a[58:59], v61 offset:512\n"
      "v_add_u32_e32 v61, 0x20, v59\n"
      "ds_read_b64_tr_b16 a[112:113], v61 offset:0\n"
      "ds_read_b64_tr_b16 a[114:115], v61 offset:512\n"
      "v_add_u32_e32 v61, 0x2000, v59\n"
      "ds_read_b64_tr_b16 a[60:61], v61 offset:0\n"
      "ds_read_b64_tr_b16 a[62:63], v61 offset:512\n"
      "v_add_u32_e32 v61, 0x2020, v59\n"
      "ds_read_b64_tr_b16 a[116:117], v61 offset:0\n"
      "ds_read_b64_tr_b16 a[118:119], v61 offset:512\n"
      "v_add_u32_e32 v61, 0x4000, v59\n"
      "ds_read_b64_tr_b16 a[64:65], v61 offset:0\n"
      "ds_read_b64_tr_b16 a[66:67], v61 offset:512\n"
      "v_add_u32_e32 v61, 0x4020, v59\n"
      "ds_read_b64_tr_b16 a[120:121], v61 offset:0\n"
      "ds_read_b64_tr_b16 a[122:123], v61 offset:512\n"
      "s_waitcnt lgkmcnt(0)\n"
      "v_add_u32_e32 v61, 0x7000, v59\n"
      "ds_read_b64_tr_b16 a[68:69], v61 offset:0\n"
      "ds_read_b64_tr_b16 a[70:71], v61 offset:512\n"
      "v_add_u32_e32 v61, 0x7020, v59\n"
      "ds_read_b64_tr_b16 a[124:125], v61 offset:0\n"
      "ds_read_b64_tr_b16 a[126:127], v61 offset:512\n"
      "v_add_u32_e32 v61, 0x9000, v59\n"
      "ds_read_b64_tr_b16 a[72:73], v61 offset:0\n"
      "ds_read_b64_tr_b16 a[74:75], v61 offset:512\n"
      "v_add_u32_e32 v61, 0x9020, v59\n"
      "ds_read_b64_tr_b16 a[128:129], v61 offset:0\n"
      "ds_read_b64_tr_b16 a[130:131], v61 offset:512\n"
      "v_add_u32_e32 v61, 0xb000, v59\n"
      "ds_read_b64_tr_b16 a[76:77], v61 offset:0\n"
      "ds_read_b64_tr_b16 a[78:79], v61 offset:512\n"
      "v_add_u32_e32 v61, 0xb020, v59\n"
      "ds_read_b64_tr_b16 a[132:133], v61 offset:0\n"
      "ds_read_b64_tr_b16 a[134:135], v61 offset:512\n"
      "s_waitcnt lgkmcnt(0)\n"
      "v_add_u32_e32 v61, 0x6000, v60\n"
      "ds_read_b64_tr_b16 a[84:85], v61 offset:0\n"
      "ds_read_b64_tr_b16 a[86:87], v61 offset:512\n"
      "v_add_u32_e32 v61, 0x6020, v60\n"
      "ds_read_b64_tr_b16 a[140:141], v61 offset:0\n"
      "ds_read_b64_tr_b16 a[142:143], v61 offset:512\n"
      "s_waitcnt lgkmcnt(0)\n"
      "s_waitcnt lgkmcnt(0)\n"
      "s_barrier\n"
      "v_mov_b32_e32 v64, 0x3f803f80\n"
      "v_mov_b32_e32 v65, 0x3f803f80\n"
      "v_mov_b32_e32 v66, 0x3f803f80\n"
      "v_mov_b32_e32 v67, 0x3f803f80\n"
      "s_nop 1\n"
      "v_mfma_f32_16x16x32_bf16 v[144:147], a[32:35], v[64:67], 0\n"
      "v_mfma_f32_16x16x32_bf16 v[144:147], a[36:39], v[64:67], v[144:147]\n"
      "v_mfma_f32_16x16x32_bf16 v[144:147], a[40:43], v[64:67], v[144:147]\n"
      "v_mfma_f32_16x16x32_bf16 v[144:147], a[44:47], v[64:67], v[144:147]\n"
      "v_mfma_f32_16x16x32_bf16 v[144:147], a[48:51], v[64:67], v[144:147]\n"
      "v_mfma_f32_16x16x32_bf16 v[144:147], a[52:55], v[64:67], v[144:147]\n"
      "v_mfma_f32_16x16x32_bf16 v[144:147], a[56:59], v[64:67], v[144:147]\n"
      "v_mfma_f32_16x16x32_bf16 v[144:147], a[60:63], v[64:67], v[144:147]\n"
      "v_mfma_f32_16x16x32_bf16 v[144:147], a[64:67], v[64:67], v[144:147]\n"
      "v_mfma_f32_16x16x32_bf16 v[144:147], a[68:71], v[64:67], v[144:147]\n"
      "v_mfma_f32_16x16x32_bf16 v[144:147], a[72:75], v[64:67], v[144:147]\n"
      "v_mfma_f32_16x16x32_bf16 v[144:147], a[76:79], v[64:67], v[144:147]\n"
      "v_mfma_f32_16x16x32_bf16 v[144:147], a[80:83], v[64:67], v[144:147]\n"
      "v_mfma_f32_16x16x32_bf16 v[144:147], a[84:87], v[64:67], v[144:147]\n"
      "v_mov_b32_e32 v64, 0x3f803f80\n"
      "v_mov_b32_e32 v65, 0x3f803f80\n"
      "v_mov_b32_e32 v66, 0x3f803f80\n"
      "v_mov_b32_e32 v67, 0x3f803f80\n"
      "s_nop 1\n"
      "v_mfma_f32_16x16x32_bf16 v[148:151], a[88:91], v[64:67], 0\n"
      "v_mfma_f32_16x16x32_bf16 v[148:151], a[92:95], v[64:67], v[148:151]\n"
      "v_mfma_f32_16x16x32_bf16 v[148:151], a[96:99], v[64:67], v[148:151]\n"
      "v_mfma_f32_16x16x32_bf16 v[148:151], a[100:103], v[64:67], v[148:151]\n"
      "v_mfma_f32_16x16x32_bf16 v[148:151], a[104:107], v[64:67], v[148:151]\n"
      "v_mfma_f32_16x16x32_bf16 v[148:151], a[108:111], v[64:67], v[148:151]\n"
      "v_mfma_f32_16x16x32_bf16 v[148:151], a[112:115], v[64:67], v[148:151]\n"
      "v_mfma_f32_16x16x32_bf16 v[148:151], a[116:119], v[64:67], v[148:151]\n"
      "v_mfma_f32_16x16x32_bf16 v[148:151], a[120:123], v[64:67], v[148:151]\n"
      "v_mfma_f32_16x16x32_bf16 v[148:151], a[124:127], v[64:67], v[148:151]\n"
      "v_mfma_f32_16x16x32_bf16 v[148:151], a[128:131], v[64:67], v[148:151]\n"
      "v_mfma_f32_16x16x32_bf16 v[148:151], a[132:135], v[64:67], v[148:151]\n"
      "v_mfma_f32_16x16x32_bf16 v[148:151], a[136:139], v[64:67], v[148:151]\n"
      "v_mfma_f32_16x16x32_bf16 v[148:151], a[140:143], v[64:67], v[148:151]\n"
      "s_waitcnt vmcnt(1)\n"
      "s_mul_i32 s90, s38, 14336\n"
      "v_mbcnt_lo_u32_b32 v61, -1, 0\n"
      "v_mbcnt_hi_u32_b32 v61, -1, v61\n"
      "v_lshlrev_b32_e32 v61, 4, v61\n"
      "v_add_u32_e32 v61, s90, v61\n"
      "s_mov_b32 exec_lo, -1\n"
      "s_mov_b32 exec_hi, 0x00ffffff\n"
      "ds_write_b128 v61, v[0:3] offset:0\n"
      "ds_write_b128 v61, v[4:7] offset:896\n"
      "ds_write_b128 v61, v[8:11] offset:1792\n"
      "ds_write_b128 v61, v[12:15] offset:2688\n"
      "ds_write_b128 v61, v[16:19] offset:3584\n"
      "ds_write_b128 v61, v[20:23] offset:4480\n"
      "ds_write_b128 v61, v[24:27] offset:5376\n"
      "ds_write_b128 v61, v[28:31] offset:6272\n"
      "ds_write_b128 v61, v[32:35] offset:7168\n"
      "ds_write_b128 v61, v[36:39] offset:8064\n"
      "ds_write_b128 v61, v[40:43] offset:8960\n"
      "ds_write_b128 v61, v[44:47] offset:9856\n"
      "ds_write_b128 v61, v[48:51] offset:10752\n"
      "ds_write_b128 v61, v[52:55] offset:11648\n"
      "ds_write_b128 v61, v[124:127] offset:12544\n"
      "ds_write_b128 v61, v[128:131] offset:13440\n"
      "s_mov_b64 exec, -1\n"
      "s_waitcnt lgkmcnt(0)\n"
      "s_barrier\n"
      "v_mbcnt_lo_u32_b32 v56, -1, 0\n"
      "v_mbcnt_hi_u32_b32 v56, -1, v56\n"
      "v_lshrrev_b32_e32 v57, 4, v56\n"
      "v_lshlrev_b32_e32 v59, 3, v57\n"
      "v_bfe_u32 v57, v56, 2, 2\n"
      "v_add_u32_e32 v59, v59, v57\n"
      "v_mul_u32_u24_e32 v59, 896, v59\n"
      "v_and_b32_e32 v57, 3, v56\n"
      "v_lshl_add_u32 v59, v57, 3, v59\n"
      "s_mul_i32 s88, s38, 224\n"
      "v_add_u32_e32 v59, s88, v59\n"
      "ds_read_b64_tr_b16 v[0:1], v59 offset:0\n"
      "ds_read_b64_tr_b16 v[2:3], v59 offset:3584\n"
      "ds_read_b64_tr_b16 v[4:5], v59 offset:28672\n"
      "ds_read_b64_tr_b16 v[6:7], v59 offset:32256\n"
      "ds_read_b64_tr_b16 v[8:9], v59 offset:32\n"
      "ds_read_b64_tr_b16 v[10:11], v59 offset:3616\n"
      "ds_read_b64_tr_b16 v[12:13], v59 offset:28704\n"
      "ds_read_b64_tr_b16 v[14:15], v59 offset:32288\n"
      "s_waitcnt lgkmcnt(0)\n"
      "ds_read_b64_tr_b16 v[16:17], v59 offset:64\n"
      "ds_read_b64_tr_b16 v[18:19], v59 offset:3648\n"
      "ds_read_b64_tr_b16 v[20:21], v59 offset:28736\n"
      "ds_read_b64_tr_b16 v[22:23], v59 offset:32320\n"
      "ds_read_b64_tr_b16 v[24:25], v59 offset:96\n"
      "ds_read_b64_tr_b16 v[26:27], v59 offset:3680\n"
      "ds_read_b64_tr_b16 v[28:29], v59 offset:28768\n"
      "ds_read_b64_tr_b16 v[30:31], v59 offset:32352\n"
      "s_waitcnt lgkmcnt(0)\n"
      "ds_read_b64_tr_b16 v[32:33], v59 offset:128\n"
      "ds_read_b64_tr_b16 v[34:35], v59 offset:3712\n"
      "ds_read_b64_tr_b16 v[36:37], v59 offset:28800\n"
      "ds_read_b64_tr_b16 v[38:39], v59 offset:32384\n"
      "ds_read_b64_tr_b16 v[40:41], v59 offset:160\n"
      "ds_read_b64_tr_b16 v[42:43], v59 offset:3744\n"
      "ds_read_b64_tr_b16 v[44:45], v59 offset:28832\n"
      "ds_read_b64_tr_b16 v[46:47], v59 offset:32416\n"
      "s_waitcnt lgkmcnt(0)\n"
      "ds_read_b64_tr_b16 v[48:49], v59 offset:192\n"
      "ds_read_b64_tr_b16 v[50:51], v59 offset:3776\n"
      "ds_read_b64_tr_b16 v[52:53], v59 offset:28864\n"
      "ds_read_b64_tr_b16 v[54:55], v59 offset:32448\n"
      "s_waitcnt lgkmcnt(0)\n"
      "s_barrier\n"
      "v_lshlrev_b32_e32 v61, 4, v56\n"
      "s_mul_i32 s88, s38, 14336\n"
      "v_add_u32_e32 v61, s88, v61\n"
      "ds_write_b128 v61, v[0:3] offset:0\n"
      "ds_write_b128 v61, v[4:7] offset:1024\n"
      "ds_write_b128 v61, v[8:11] offset:2048\n"
      "ds_write_b128 v61, v[12:15] offset:3072\n"
      "ds_write_b128 v61, v[16:19] offset:4096\n"
      "ds_write_b128 v61, v[20:23] offset:5120\n"
      "ds_write_b128 v61, v[24:27] offset:6144\n"
      "ds_write_b128 v61, v[28:31] offset:7168\n"
      "ds_write_b128 v61, v[32:35] offset:8192\n"
      "ds_write_b128 v61, v[36:39] offset:9216\n"
      "ds_write_b128 v61, v[40:43] offset:10240\n"
      "ds_write_b128 v61, v[44:47] offset:11264\n"
      "ds_write_b128 v61, v[48:51] offset:12288\n"
      "ds_write_b128 v61, v[52:55] offset:13312\n"
      "s_waitcnt vmcnt(0)\n"
      "v_mov_b32_e32 v57, 0\n"
      "v_mov_b32_e32 v58, v136\n"
      "s_mov_b32 s43, 0\n"
      "s_nop 3\n"
      "EP_10_%=:\n"
      "v_cmp_ne_u32_e32 vcc, s35, v58\n"
      "s_nop 4\n"
      "s_cbranch_vccz ED_11_%=\n"
      "global_load_dword v58, v57, s[36:37] sc1\n"
      "s_waitcnt vmcnt(0)\n"
      "s_add_u32 s43, s43, 1\n"
      "s_cmp_lt_u32 s43, 0x80000\n"
      "s_nop 0\n"
      "s_cbranch_scc1 EP_10_%=\n"
      "s_nop 0\n"
      "ED_11_%=:\n"
      "s_mov_b32 s42, 0\n"
      "v_mov_b32_e32 v96, -1\n"
      "v_mov_b32_e32 v97, -1\n"
      "v_mov_b32_e32 v98, -1\n"
      "v_mov_b32_e32 v99, -1\n"
      "v_mov_b32_e32 v100, -1\n"
      "v_mov_b32_e32 v101, -1\n"
      "v_mov_b32_e32 v102, -1\n"
      "v_mov_b32_e32 v103, -1\n"
      "v_mov_b32_e32 v104, -1\n"
      "v_mov_b32_e32 v105, -1\n"
      "v_mov_b32_e32 v106, -1\n"
      "v_mov_b32_e32 v107, -1\n"
      "v_mov_b32_e32 v108, -1\n"
      "v_mov_b32_e32 v109, -1\n"
      "v_mov_b32_e32 v110, -1\n"
      "v_mov_b32_e32 v111, -1\n"
      "v_mov_b32_e32 v112, -1\n"
      "v_mov_b32_e32 v113, -1\n"
      "v_mov_b32_e32 v114, -1\n"
      "v_mov_b32_e32 v115, -1\n"
      "v_mov_b32_e32 v116, -1\n"
      "v_mov_b32_e32 v117, -1\n"
      "v_mov_b32_e32 v118, -1\n"
      "v_mov_b32_e32 v119, -1\n"
      "v_mov_b32_e32 v124, -1\n"
      "v_mov_b32_e32 v125, -1\n"
      "v_mov_b32_e32 v126, -1\n"
      "v_mov_b32_e32 v127, -1\n"
      "v_mov_b32_e32 v128, -1\n"
      "v_mov_b32_e32 v129, -1\n"
      "v_mov_b32_e32 v130, -1\n"
      "v_mov_b32_e32 v131, -1\n"
      "v_mov_b32_e32 v132, -1\n"
      "v_mov_b32_e32 v133, -1\n"
      "v_mov_b32_e32 v134, -1\n"
      "v_mov_b32_e32 v135, -1\n"
      "v_mov_b32_e32 v136, -1\n"
      "v_mov_b32_e32 v137, -1\n"
      "v_mov_b32_e32 v138, -1\n"
      "v_mov_b32_e32 v139, -1\n"
      "v_mbcnt_lo_u32_b32 v56, -1, 0\n"
      "v_mbcnt_hi_u32_b32 v56, -1, v56\n"
      "v_lshrrev_b32_e32 v57, 4, v56\n"
      "v_lshlrev_b32_e32 v93, 5, v57\n"
      "v_lshlrev_b32_e32 v92, 10, v57\n"
      "v_and_b32_e32 v57, 15, v56\n"
      "v_lshl_add_u32 v92, v57, 4, v92\n"
      "s_lshl_b32 s88, s38, 8\n"
      "v_add_u32_e32 v92, s88, v92\n"
      "v_bfe_u32 v57, v56, 3, 1\n"
      "v_lshlrev_b32_e32 v94, 6, v57\n"
      "v_lshrrev_b32_e32 v57, 4, v56\n"
      "v_lshl_add_u32 v94, v57, 4, v94\n"
      "v_and_b32_e32 v57, 7, v56\n"
      "v_lshl_add_u32 v94, v57, 1, v94\n"
      "s_lshl_b32 s88, s38, 7\n"
      "s_add_u32 s88, s88, 0x1e800\n"
      "v_add_u32_e32 v94, s88, v94\n"
      "s_mov_b32 s88, 0xb0800\n"
      "s_mul_i32 s89, s42, 262144\n"
      "s_add_u32 s88, s88, s89\n"
      "s_mul_i32 s89, s40, 16384\n"
      "s_add_u32 s88, s88, s89\n"
      "s_add_u32 s44, s36, s88\n"
      "s_addc_u32 s45, s37, 0\n"
      "s_mov_b32 s88, 0x80800\n"
      "s_mul_i32 s89, s42, 2048\n"
      "s_add_u32 s88, s88, s89\n"
      "s_mul_i32 s89, s40, 128\n"
      "s_add_u32 s88, s88, s89\n"
      "s_add_u32 s86, s36, s88\n"
      "s_addc_u32 s87, s37, 0\n"
      "s_mov_b64 s[92:93], -1\n"
      "s_mov_b64 s[48:49], -1\n"
      "s_mov_b64 s[50:51], -1\n"
      "s_mov_b64 s[52:53], -1\n"
      "s_mov_b64 s[54:55], -1\n"
      "s_mov_b64 s[56:57], -1\n"
      "s_mov_b32 s43, 0\n"
      "s_nop 0\n"
      "A3_13_%=:\n"
      "s_mov_b64 exec, s[48:49]\n"
      "s_nop 0\n"
      "s_cbranch_execz AS_15_%=\n"
      "s_add_u32 s46, s44, 0x0\n"
      "s_addc_u32 s47, s45, 0\n"
      "global_load_dwordx4 v[96:99], v92, s[46:47] sc1\n"
      "AS_15_%=:\n"
      "s_mov_b64 exec, s[50:51]\n"
      "s_nop 0\n"
      "s_cbranch_execz AS_16_%=\n"
      "s_add_u32 s46, s44, 0x1000\n"
      "s_addc_u32 s47, s45, 0\n"
      "global_load_dwordx4 v[100:103], v92, s[46:47] sc1\n"
      "AS_16_%=:\n"
      "s_mov_b64 exec, s[52:53]\n"
      "s_nop 0\n"
      "s_cbranch_execz AS_17_%=\n"
      "s_add_u32 s46, s44, 0x2000\n"
      "s_addc_u32 s47, s45, 0\n"
      "global_load_dwordx4 v[104:107], v92, s[46:47] sc1\n"
      "AS_17_%=:\n"
      "s_mov_b64 exec, s[54:55]\n"
      "s_nop 0\n"
      "s_cbranch_execz AS_18_%=\n"
      "s_add_u32 s46, s44, 0x3000\n"
      "s_addc_u32 s47, s45, 0\n"
      "global_load_dwordx4 v[108:111], v92, s[46:47] sc1\n"
      "AS_18_%=:\n"
      "s_mov_b64 exec, -1\n"
      "s_cmp_eq_u64 s[56:57], 0\n"
      "s_nop 0\n"
      "s_cbranch_scc1 AS_19_%=\n"
      "global_load_dwordx4 v[112:115], v93, s[86:87] sc1\n"
      "global_load_dwordx4 v[116:119], v93, s[86:87] offset:16 sc1\n"
      "AS_19_%=:\n"
      "s_sleep 1\n"
      "v_max3_u32 v57, v96, v97, v98\n"
      "v_max_u32_e32 v57, v57, v99\n"
      "v_cmp_eq_u32_e64 s[48:49], -1, v57\n"
      "v_max3_u32 v58, v100, v101, v102\n"
      "v_max_u32_e32 v58, v58, v103\n"
      "v_cmp_eq_u32_e64 s[50:51], -1, v58\n"
      "v_max3_u32 v57, v104, v105, v106\n"
      "v_max_u32_e32 v57, v57, v107\n"
      "v_cmp_eq_u32_e64 s[52:53], -1, v57\n"
      "v_max3_u32 v58, v108, v109, v110\n"
      "v_max_u32_e32 v58, v58, v111\n"
      "v_cmp_eq_u32_e64 s[54:55], -1, v58\n"
      "v_max3_u32 v57, v112, v113, v114\n"
      "v_max3_u32 v58, v115, v116, v117\n"
      "v_max3_u32 v57, v57, v118, v119\n"
      "v_max_u32_e32 v57, v57, v58\n"
      "v_cmp_eq_u32_e64 s[56:57], -1, v57\n"
      "s_mov_b64 s[88:89], s[48:49]\n"
      "s_or_b64 s[88:89], s[88:89], s[50:51]\n"
      "s_or_b64 s[88:89], s[88:89], s[52:53]\n"
      "s_or_b64 s[88:89], s[88:89], s[54:55]\n"
      "s_or_b64 s[88:89], s[88:89], s[56:57]\n"
      "s_cmp_eq_u64 s[88:89], 0\n"
      "s_nop 0\n"
      "s_cbranch_scc1 A4_14_%=\n"
      "s_mov_b32 s90, 0x00010001\n"
      "s_mov_b32 s91, 0x00010001\n"
      "s_mov_b32 s88, 0\n"
      "v_cmp_ne_u32_e64 s[58:59], -1, v112\n"
      "v_cmp_ne_u32_e64 s[60:61], -1, v114\n"
      "v_cmp_ne_u32_e64 s[98:99], -1, v116\n"
      "v_cmp_ne_u32_e64 s[76:77], -1, v118\n"
      "s_and_b64 s[84:85], s[58:59], s[90:91]\n"
      "s_bcnt1_i32_b64 s89, s[84:85]\n"
      "s_add_u32 s88, s88, s89\n"
      "s_and_b64 s[84:85], s[60:61], s[90:91]\n"
      "s_bcnt1_i32_b64 s89, s[84:85]\n"
      "s_add_u32 s88, s88, s89\n"
      "s_and_b64 s[84:85], s[98:99], s[90:91]\n"
      "s_bcnt1_i32_b64 s89, s[84:85]\n"
      "s_add_u32 s88, s88, s89\n"
      "s_and_b64 s[84:85], s[76:77], s[90:91]\n"
      "s_bcnt1_i32_b64 s89, s[84:85]\n"
      "s_add_u32 s88, s88, s89\n"
      "s_cmp_ge_u32 s88, 0\n"
      "s_cselect_b64 s[92:93], -1, 0\n"
      "s_or_b64 s[58:59], s[58:59], s[92:93]\n"
      "s_and_b64 s[48:49], s[48:49], s[58:59]\n"
      "s_or_b64 s[60:61], s[60:61], s[92:93]\n"
      "s_and_b64 s[50:51], s[50:51], s[60:61]\n"
      "s_or_b64 s[98:99], s[98:99], s[92:93]\n"
      "s_and_b64 s[52:53], s[52:53], s[98:99]\n"
      "s_or_b64 s[76:77], s[76:77], s[92:93]\n"
      "s_and_b64 s[54:55], s[54:55], s[76:77]\n"
      "s_cmp_ge_u32 s88, 0\n"
      "s_nop 0\n"
      "s_cbranch_scc1 AF_20_%=\n"
      "s_sleep 4\n"
      "AF_20_%=:\n"
      "s_add_u32 s43, s43, 1\n"
      "s_cmp_lt_u32 s43, 0x80000\n"
      "s_waitcnt vmcnt(6)\n"
      "s_nop 0\n"
      "s_cbranch_scc1 A3_13_%=\n"
      "s_nop 0\n"
      "A4_14_%=:\n"
      "v_max3_f32 v56, v112, v114, v116\n"
      "v_max_f32_e32 v56, v56, v118\n"
      "v_mov_b32_e32 v57, v56\n"
      "s_nop 1\n"
      "v_permlane16_swap_b32_e32 v57, v56\n"
      "v_max_f32_e32 v56, v56, v57\n"
      "v_mov_b32_e32 v57, v56\n"
      "s_nop 1\n"
      "v_permlane32_swap_b32_e32 v57, v56\n"
      "v_max_f32_e32 v56, v56, v57\n"
      "v_sub_f32_e32 v60, v112, v56\n"
      "v_sub_f32_e32 v61, v114, v56\n"
      "v_sub_f32_e32 v62, v116, v56\n"
      "v_sub_f32_e32 v63, v118, v56\n"
      "v_mul_f32_e32 v60, s34, v60\n"
      "v_mul_f32_e32 v61, s34, v61\n"
      "v_mul_f32_e32 v62, s34, v62\n"
      "v_mul_f32_e32 v63, s34, v63\n"
      "v_exp_f32_e32 v60, v60\n"
      "v_exp_f32_e32 v61, v61\n"
      "v_exp_f32_e32 v62, v62\n"
      "v_exp_f32_e32 v63, v63\n"
      "v_mul_f32_e32 v59, v60, v113\n"
      "v_mul_f32_e32 v64, v60, v96\n"
      "v_mul_f32_e32 v65, v60, v97\n"
      "v_mul_f32_e32 v66, v60, v98\n"
      "v_mul_f32_e32 v67, v60, v99\n"
      "v_fmac_f32_e32 v59, v61, v115\n"
      "v_fmac_f32_e32 v64, v61, v100\n"
      "v_fmac_f32_e32 v65, v61, v101\n"
      "v_fmac_f32_e32 v66, v61, v102\n"
      "v_fmac_f32_e32 v67, v61, v103\n"
      "v_fmac_f32_e32 v59, v62, v117\n"
      "v_fmac_f32_e32 v64, v62, v104\n"
      "v_fmac_f32_e32 v65, v62, v105\n"
      "v_fmac_f32_e32 v66, v62, v106\n"
      "v_fmac_f32_e32 v67, v62, v107\n"
      "v_fmac_f32_e32 v59, v63, v119\n"
      "v_fmac_f32_e32 v64, v63, v108\n"
      "v_fmac_f32_e32 v65, v63, v109\n"
      "v_fmac_f32_e32 v66, v63, v110\n"
      "v_fmac_f32_e32 v67, v63, v111\n"
      "v_mov_b32_e32 v57, v59\n"
      "v_permlane32_swap_b32_e32 v64, v66\n"
      "v_permlane32_swap_b32_e32 v65, v67\n"
      "v_permlane32_swap_b32_e32 v57, v59\n"
      "v_add_f32_e32 v64, v64, v66\n"
      "v_add_f32_e32 v65, v65, v67\n"
      "v_add_f32_e32 v59, v59, v57\n"
      "s_nop 0\n"
      "v_permlane16_swap_b32_e32 v64, v65\n"
      "v_mov_b32_e32 v57, v59\n"
      "v_add_f32_e32 v64, v64, v65\n"
      "s_nop 0\n"
      "v_permlane16_swap_b32_e32 v57, v59\n"
      "v_add_f32_e32 v59, v59, v57\n"
      "v_rcp_f32_e32 v59, v59\n"
      "s_nop 0\n"
      "v_mul_f32_e32 v64, v64, v59\n"
      "v_cvt_pk_bf16_f32 v64, v64, v64\n"
      "ds_write_b16 v94, v64\n"
      "s_mov_b32 s88, 0x82800\n"
      "s_mul_i32 s89, s42, 14336\n"
      "s_add_u32 s88, s88, s89\n"
      "s_mul_i32 s89, s40, 896\n"
      "s_add_u32 s88, s88, s89\n"
      "s_mul_i32 s89, s41, 448\n"
      "s_add_u32 s88, s88, s89\n"
      "s_mul_i32 s89, s38, 128\n"
      "s_add_u32 s88, s88, s89\n"
      "s_add_u32 s44, s36, s88\n"
      "s_addc_u32 s45, s37, 0\n"
      "s_cmp_eq_u32 s38, 3\n"
      "s_cselect_b32 s88, 32, 0\n"
      "s_sub_u32 s44, s44, s88\n"
      "s_subb_u32 s45, s45, 0\n"
      "v_mbcnt_lo_u32_b32 v93, -1, 0\n"
      "v_mbcnt_hi_u32_b32 v93, -1, v93\n"
      "v_and_b32_e32 v92, 15, v93\n"
      "v_lshlrev_b32_e32 v92, 5, v92\n"
      "v_lshrrev_b32_e32 v93, 4, v93\n"
      "v_lshl_add_u32 v92, v93, 3, v92\n"
      "s_waitcnt lgkmcnt(0)\n"
      "s_barrier\n"
      "ds_read_b128 v[0:3], v156 offset:2048\n"
      "ds_read_b128 v[4:7], v156 offset:2112\n"
      "ds_read_b128 v[8:11], v156 offset:2176\n"
      "ds_read_b128 v[12:15], v156 offset:2240\n"
      "ds_read_b128 v[16:19], v156 offset:2304\n"
      "ds_read_b128 v[20:23], v156 offset:2368\n"
      "ds_read_b128 v[24:27], v156 offset:2432\n"
      "ds_read_b128 v[28:31], v156 offset:2496\n"
      "s_waitcnt lgkmcnt(7)\n"
      "v_mfma_f32_16x16x32_bf16 v[64:67], v[160:163], v[0:3], 0\n"
      "v_mfma_f32_16x16x32_bf16 v[68:71], v[164:167], v[0:3], 0\n"
      "v_mfma_f32_16x16x32_bf16 v[72:75], v[168:171], v[0:3], 0\n"
      "v_mfma_f32_16x16x32_bf16 v[76:79], v[172:175], v[0:3], 0\n"
      "s_waitcnt lgkmcnt(6)\n"
      "v_mfma_f32_16x16x32_bf16 v[64:67], v[176:179], v[4:7], v[64:67]\n"
      "v_mfma_f32_16x16x32_bf16 v[68:71], v[180:183], v[4:7], v[68:71]\n"
      "v_mfma_f32_16x16x32_bf16 v[72:75], v[184:187], v[4:7], v[72:75]\n"
      "v_mfma_f32_16x16x32_bf16 v[76:79], v[188:191], v[4:7], v[76:79]\n"
      "s_waitcnt lgkmcnt(5)\n"
      "v_mfma_f32_16x16x32_bf16 v[64:67], v[192:195], v[8:11], v[64:67]\n"
      "v_mfma_f32_16x16x32_bf16 v[68:71], v[196:199], v[8:11], v[68:71]\n"
      "v_mfma_f32_16x16x32_bf16 v[72:75], v[200:203], v[8:11], v[72:75]\n"
      "v_mfma_f32_16x16x32_bf16 v[76:79], v[204:207], v[8:11], v[76:79]\n"
      "s_waitcnt lgkmcnt(4)\n"
      "v_mfma_f32_16x16x32_bf16 v[64:67], v[208:211], v[12:15], v[64:67]\n"
      "v_mfma_f32_16x16x32_bf16 v[68:71], v[212:215], v[12:15], v[68:71]\n"
      "v_mfma_f32_16x16x32_bf16 v[72:75], v[216:219], v[12:15], v[72:75]\n"
      "v_mfma_f32_16x16x32_bf16 v[76:79], v[220:223], v[12:15], v[76:79]\n"
      "s_waitcnt lgkmcnt(3)\n"
      "v_mfma_f32_16x16x32_bf16 v[64:67], v[224:227], v[16:19], v[64:67]\n"
      "v_mfma_f32_16x16x32_bf16 v[68:71], v[228:231], v[16:19], v[68:71]\n"
      "v_mfma_f32_16x16x32_bf16 v[72:75], v[232:235], v[16:19], v[72:75]\n"
      "v_mfma_f32_16x16x32_bf16 v[76:79], v[236:239], v[16:19], v[76:79]\n"
      "s_waitcnt lgkmcnt(2)\n"
      "v_mfma_f32_16x16x32_bf16 v[64:67], v[240:243], v[20:23], v[64:67]\n"
      "v_mfma_f32_16x16x32_bf16 v[68:71], v[244:247], v[20:23], v[68:71]\n"
      "v_mfma_f32_16x16x32_bf16 v[72:75], v[248:251], v[20:23], v[72:75]\n"
      "v_mfma_f32_16x16x32_bf16 v[76:79], v[252:255], v[20:23], v[76:79]\n"
      "s_waitcnt lgkmcnt(1)\n"
      "v_mfma_f32_16x16x32_bf16 v[64:67], a[0:3], v[24:27], v[64:67]\n"
      "v_mfma_f32_16x16x32_bf16 v[68:71], a[4:7], v[24:27], v[68:71]\n"
      "v_mfma_f32_16x16x32_bf16 v[72:75], a[8:11], v[24:27], v[72:75]\n"
      "v_mfma_f32_16x16x32_bf16 v[76:79], a[12:15], v[24:27], v[76:79]\n"
      "s_waitcnt lgkmcnt(0)\n"
      "v_mfma_f32_16x16x32_bf16 v[64:67], a[16:19], v[28:31], v[64:67]\n"
      "v_mfma_f32_16x16x32_bf16 v[68:71], a[20:23], v[28:31], v[68:71]\n"
      "v_mfma_f32_16x16x32_bf16 v[72:75], a[24:27], v[28:31], v[72:75]\n"
      "v_mfma_f32_16x16x32_bf16 v[76:79], a[28:31], v[28:31], v[76:79]\n"
      "s_nop 4\n"
      "v_cvt_pk_bf16_f32 v64, v64, v65\n"
      "v_cvt_pk_bf16_f32 v65, v66, v67\n"
      "v_min_u32_e32 v64, -2, v64\n"
      "v_min_u32_e32 v65, -2, v65\n"
      "v_cvt_pk_bf16_f32 v82, v68, v69\n"
      "v_cvt_pk_bf16_f32 v83, v70, v71\n"
      "v_min_u32_e32 v82, -2, v82\n"
      "v_min_u32_e32 v83, -2, v83\n"
      "s_mov_b32 s46, 0x20002\n"
      "s_mov_b32 s47, 0x20002\n"
      "v_cndmask_b32_e64 v64, v64, v82, s[46:47]\n"
      "v_cndmask_b32_e64 v65, v65, v83, s[46:47]\n"
      "v_cvt_pk_bf16_f32 v84, v72, v73\n"
      "v_cvt_pk_bf16_f32 v85, v74, v75\n"
      "v_min_u32_e32 v84, -2, v84\n"
      "v_min_u32_e32 v85, -2, v85\n"
      "s_mov_b32 s46, 0x40004\n"
      "s_mov_b32 s47, 0x40004\n"
      "v_cndmask_b32_e64 v64, v64, v84, s[46:47]\n"
      "v_cndmask_b32_e64 v65, v65, v85, s[46:47]\n"
      "v_cvt_pk_bf16_f32 v86, v76, v77\n"
      "v_cvt_pk_bf16_f32 v87, v78, v79\n"
      "v_min_u32_e32 v86, -2, v86\n"
      "v_min_u32_e32 v87, -2, v87\n"
      "s_mov_b32 s46, 0x80008\n"
      "s_mov_b32 s47, 0x80008\n"
      "v_cndmask_b32_e64 v64, v64, v86, s[46:47]\n"
      "v_cndmask_b32_e64 v65, v65, v87, s[46:47]\n"
      "s_mov_b32 s46, 0x000f000f\n"
      "s_cmp_ge_u32 s38, 2\n"
      "s_cselect_b32 exec_lo, 0x00070007, s46\n"
      "s_mov_b32 exec_hi, exec_lo\n"
      "global_store_dwordx2 v92, v[64:65], s[44:45] sc1\n"
      "s_mov_b64 exec, -1\n"
      "s_mov_b32 s84, 0xcccccccc\n"
      "s_mov_b32 s85, 0xcccccccc\n"
      "v_cndmask_b32_e64 v84, 0, v159, s[64:65]\n"
      "v_cndmask_b32_e64 v85, 0, v159, s[66:67]\n"
      "v_cndmask_b32_e64 v86, 0, v159, s[68:69]\n"
      "v_cndmask_b32_e64 v87, 0, v159, s[70:71]\n"
      "s_mov_b32 s88, 0x82800\n"
      "s_mul_i32 s89, s42, 14336\n"
      "s_add_u32 s88, s88, s89\n"
      "s_add_u32 s44, s36, s88\n"
      "s_addc_u32 s45, s37, 0\n"
      "s_mov_b32 s94, 0x3fff3fff\n"
      "s_mov_b32 s95, 0x3fff3fff\n"
      "s_mov_b64 s[48:49], s[94:95]\n"
      "s_mov_b64 s[50:51], s[94:95]\n"
      "s_mov_b64 s[52:53], s[94:95]\n"
      "s_mov_b64 s[54:55], s[94:95]\n"
      "s_mov_b32 s43, 0\n"
      "s_sleep 8\n"
      "s_nop 0\n"
      "GT_21_%=:\n"
      "s_mov_b64 exec, s[48:49]\n"
      "s_nop 0\n"
      "s_cbranch_execz GS_24_%=\n"
      "s_add_u32 s46, s44, 0x0\n"
      "s_addc_u32 s47, s45, 0\n"
      "global_load_dwordx4 v[124:127], v154, s[46:47] sc1\n"
      "GS_24_%=:\n"
      "s_mov_b64 exec, s[50:51]\n"
      "s_nop 0\n"
      "s_cbranch_execz GS_25_%=\n"
      "s_add_u32 s46, s44, 0xe00\n"
      "s_addc_u32 s47, s45, 0\n"
      "global_load_dwordx4 v[128:131], v154, s[46:47] sc1\n"
      "GS_25_%=:\n"
      "s_mov_b64 exec, s[52:53]\n"
      "s_nop 0\n"
      "s_cbranch_execz GS_26_%=\n"
      "s_add_u32 s46, s44, 0x1c00\n"
      "s_addc_u32 s47, s45, 0\n"
      "global_load_dwordx4 v[132:135], v154, s[46:47] sc1\n"
      "GS_26_%=:\n"
      "s_mov_b64 exec, s[54:55]\n"
      "s_nop 0\n"
      "s_cbranch_execz GS_27_%=\n"
      "s_add_u32 s46, s44, 0x2a00\n"
      "s_addc_u32 s47, s45, 0\n"
      "global_load_dwordx4 v[136:139], v154, s[46:47] sc1\n"
      "GS_27_%=:\n"
      "s_mov_b64 exec, -1\n"
      "s_sleep 1\n"
      "v_max3_u32 v56, v124, v125, v126\n"
      "v_max_u32_e32 v56, v56, v127\n"
      "v_cmp_eq_u32_e64 s[48:49], -1, v56\n"
      "v_max3_u32 v57, v128, v129, v130\n"
      "v_max_u32_e32 v57, v57, v131\n"
      "v_cmp_eq_u32_e64 s[50:51], -1, v57\n"
      "v_max3_u32 v56, v132, v133, v134\n"
      "v_max_u32_e32 v56, v56, v135\n"
      "v_cmp_eq_u32_e64 s[52:53], -1, v56\n"
      "v_max3_u32 v57, v136, v137, v138\n"
      "v_max_u32_e32 v57, v57, v139\n"
      "v_cmp_eq_u32_e64 s[54:55], -1, v57\n"
      "s_and_b64 s[48:49], s[48:49], s[94:95]\n"
      "s_and_b64 s[50:51], s[50:51], s[94:95]\n"
      "s_and_b64 s[52:53], s[52:53], s[94:95]\n"
      "s_and_b64 s[54:55], s[54:55], s[94:95]\n"
      "s_mov_b64 s[88:89], s[48:49]\n"
      "s_or_b64 s[88:89], s[88:89], s[50:51]\n"
      "s_or_b64 s[88:89], s[88:89], s[52:53]\n"
      "s_or_b64 s[88:89], s[88:89], s[54:55]\n"
      "s_cmp_eq_u64 s[88:89], 0\n"
      "s_nop 0\n"
      "s_cbranch_scc1 GD_22_%=\n"
      "s_add_u32 s43, s43, 1\n"
      "s_cmp_lt_u32 s43, 0x80000\n"
      "s_waitcnt vmcnt(4)\n"
      "s_nop 0\n"
      "s_cbranch_scc1 GT_21_%=\n"
      "s_nop 0\n"
      "GD_22_%=:\n"
      "v_mfma_f32_16x16x32_bf16 v[88:91], v[124:127], v[84:87], 0\n"
      "v_mfma_f32_16x16x32_bf16 v[88:91], v[128:131], v[84:87], v[88:91]\n"
      "v_mfma_f32_16x16x32_bf16 v[88:91], v[132:135], v[84:87], v[88:91]\n"
      "v_mfma_f32_16x16x32_bf16 v[88:91], v[136:139], v[84:87], v[88:91]\n"
      "s_nop 7\n"
      "v_cndmask_b32_e64 v56, v88, v90, s[62:63]\n"
      "v_cndmask_b32_e64 v57, v89, v91, s[62:63]\n"
      "v_cvt_pk_bf16_f32 v58, v56, v57\n"
      "v_lshlrev_b32_e32 v56, 16, v58\n"
      "v_and_b32_e32 v57, 0xffff0000, v58\n"
      "v_add_f32_e32 v56, v140, v56\n"
      "v_add_f32_e32 v57, v141, v57\n"
      "v_cvt_pk_bf16_f32 v58, v56, v57\n"
      "s_cmp_ge_u32 s39, 28\n"
      "v_lshlrev_b32_e32 v140, 16, v58\n"
      "v_and_b32_e32 v141, 0xffff0000, v58\n"
      "s_nop 4\n"
      "s_cbranch_scc1 NOBC_28_%=\n"
      "s_mov_b32 exec_lo, -1\n"
      "s_mov_b32 exec_hi, 0x00ffffff\n"
      "ds_write_b32 v155, v58 offset:0\n"
      "s_mov_b64 exec, -1\n"
      "s_waitcnt lgkmcnt(0)\n"
      "s_barrier\n"
      "v_mbcnt_lo_u32_b32 v81, -1, 0\n"
      "v_mbcnt_hi_u32_b32 v81, -1, v81\n"
      "v_lshlrev_b32_e32 v81, 4, v81\n"
      "v_add_u32_e32 v76, 0x1e000, v81\n"
      "ds_read_b128 v[60:63], v76\n"
      "ds_read_b128 v[0:3], v156 offset:0\n"
      "ds_read_b128 v[4:7], v156 offset:64\n"
      "ds_read_b128 v[8:11], v156 offset:128\n"
      "ds_read_b128 v[12:15], v156 offset:256\n"
      "ds_read_b128 v[16:19], v156 offset:320\n"
      "ds_read_b128 v[20:23], v156 offset:384\n"
      "ds_read_b128 v[24:27], v156 offset:512\n"
      "ds_read_b128 v[28:31], v156 offset:576\n"
      "ds_read_b128 v[32:35], v156 offset:640\n"
      "ds_read_b128 v[36:39], v156 offset:768\n"
      "ds_read_b128 v[40:43], v156 offset:832\n"
      "ds_read_b128 v[44:47], v156 offset:896\n"
      "ds_read_b128 v[48:51], v157 offset:192\n"
      "ds_read_b128 v[52:55], v157 offset:704\n"
      "s_mul_i32 s88, s38, 14336\n"
      "v_add_u32_e32 v82, s88, v81\n"
      "s_mov_b32 s89, 0x3f803f80\n"
      "s_waitcnt lgkmcnt(13)\n"
      "v_mfma_f32_16x16x32_bf16 v[64:67], a[144:147], v[0:3], 0\n"
      "v_dot2_f32_bf16 v72, v60, s89, 0\n"
      "v_dot2_f32_bf16 v74, v60, v60, 0\n"
      "v_mfma_f32_16x16x32_bf16 v[68:71], a[200:203], v[0:3], 0\n"
      "v_dot2_f32_bf16 v73, v62, s89, 0\n"
      "v_dot2_f32_bf16 v75, v62, v62, 0\n"
      "ds_read_b128 v[0:3], v82 offset:0\n"
      "s_waitcnt lgkmcnt(13)\n"
      "v_mfma_f32_16x16x32_bf16 v[64:67], a[148:151], v[4:7], v[64:67]\n"
      "v_dot2_f32_bf16 v72, v61, s89, v72\n"
      "v_dot2_f32_bf16 v74, v61, v61, v74\n"
      "v_mfma_f32_16x16x32_bf16 v[68:71], a[204:207], v[4:7], v[68:71]\n"
      "v_dot2_f32_bf16 v73, v63, s89, v73\n"
      "v_dot2_f32_bf16 v75, v63, v63, v75\n"
      "ds_read_b128 v[4:7], v82 offset:1024\n"
      "s_waitcnt lgkmcnt(13)\n"
      "v_mfma_f32_16x16x32_bf16 v[64:67], a[152:155], v[8:11], v[64:67]\n"
      "s_nop 0\n"
      "s_nop 0\n"
      "v_mfma_f32_16x16x32_bf16 v[68:71], a[208:211], v[8:11], v[68:71]\n"
      "v_add_f32_e32 v72, v72, v73\n"
      "v_add_f32_e32 v74, v74, v75\n"
      "ds_read_b128 v[8:11], v82 offset:2048\n"
      "s_waitcnt lgkmcnt(13)\n"
      "v_mfma_f32_16x16x32_bf16 v[64:67], a[156:159], v[12:15], v[64:67]\n"
      "v_add_f32_dpp v72, v72, v72 row_ror:8 row_mask:0xf bank_mask:0xf\n"
      "v_add_f32_dpp v74, v74, v74 row_ror:8 row_mask:0xf bank_mask:0xf\n"
      "v_mfma_f32_16x16x32_bf16 v[68:71], a[212:215], v[12:15], v[68:71]\n"
      "v_add_f32_dpp v72, v72, v72 row_ror:4 row_mask:0xf bank_mask:0xf\n"
      "v_add_f32_dpp v74, v74, v74 row_ror:4 row_mask:0xf bank_mask:0xf\n"
      "ds_read_b128 v[12:15], v82 offset:3072\n"
      "s_waitcnt lgkmcnt(13)\n"
      "v_mfma_f32_16x16x32_bf16 v[64:67], a[160:163], v[16:19], v[64:67]\n"
      "v_add_f32_dpp v72, v72, v72 row_ror:2 row_mask:0xf bank_mask:0xf\n"
      "v_add_f32_dpp v74, v74, v74 row_ror:2 row_mask:0xf bank_mask:0xf\n"
      "v_mfma_f32_16x16x32_bf16 v[68:71], a[216:219], v[16:19], v[68:71]\n"
      "v_add_f32_dpp v72, v72, v72 row_ror:1 row_mask:0xf bank_mask:0xf\n"
      "v_add_f32_dpp v74, v74, v74 row_ror:1 row_mask:0xf bank_mask:0xf\n"
      "ds_read_b128 v[16:19], v82 offset:4096\n"
      "s_waitcnt lgkmcnt(13)\n"
      "v_mfma_f32_16x16x32_bf16 v[64:67], a[164:167], v[20:23], v[64:67]\n"
      "v_mov_b32_e32 v76, v72\n"
      "v_mov_b32_e32 v77, v74\n"
      "v_mfma_f32_16x16x32_bf16 v[68:71], a[220:223], v[20:23], v[68:71]\n"
      "v_permlane16_swap_b32_e32 v76, v72\n"
      "v_permlane16_swap_b32_e32 v77, v74\n"
      "ds_read_b128 v[20:23], v82 offset:5120\n"
      "s_waitcnt lgkmcnt(13)\n"
      "v_mfma_f32_16x16x32_bf16 v[64:67], a[168:171], v[24:27], v[64:67]\n"
      "v_add_f32_e32 v72, v72, v76\n"
      "v_add_f32_e32 v74, v74, v77\n"
      "v_mfma_f32_16x16x32_bf16 v[68:71], a[224:227], v[24:27], v[68:71]\n"
      "v_mov_b32_e32 v76, v72\n"
      "v_mov_b32_e32 v77, v74\n"
      "ds_read_b128 v[24:27], v82 offset:6144\n"
      "s_waitcnt lgkmcnt(13)\n"
      "v_mfma_f32_16x16x32_bf16 v[64:67], a[172:175], v[28:31], v[64:67]\n"
      "v_permlane32_swap_b32_e32 v76, v72\n"
      "v_permlane32_swap_b32_e32 v77, v74\n"
      "v_mfma_f32_16x16x32_bf16 v[68:71], a[228:231], v[28:31], v[68:71]\n"
      "v_add_f32_e32 v72, v72, v76\n"
      "v_add_f32_e32 v74, v74, v77\n"
      "ds_read_b128 v[28:31], v82 offset:7168\n"
      "s_waitcnt lgkmcnt(13)\n"
      "v_mfma_f32_16x16x32_bf16 v[64:67], a[176:179], v[32:35], v[64:67]\n"
      "v_mul_f32_e32 v78, 0x3b124925, v72\n"
      "v_mul_f32_e32 v74, 0x3b124925, v74\n"
      "v_mfma_f32_16x16x32_bf16 v[68:71], a[232:235], v[32:35], v[68:71]\n"
      "v_fma_f32 v74, -v78, v78, v74\n"
      "v_cvt_pk_bf16_f32 v79, v78, v78\n"
      "ds_read_b128 v[32:35], v82 offset:8192\n"
      "s_waitcnt lgkmcnt(13)\n"
      "v_mfma_f32_16x16x32_bf16 v[64:67], a[180:183], v[36:39], v[64:67]\n"
      "v_max_f32_e32 v74, 0, v74\n"
      "v_and_b32_e32 v79, 0xffff0000, v79\n"
      "v_mfma_f32_16x16x32_bf16 v[68:71], a[236:239], v[36:39], v[68:71]\n"
      "v_cvt_pk_bf16_f32 v74, v74, v74\n"
      "v_xor_b32_e32 v79, 0x80000000, v79\n"
      "ds_read_b128 v[36:39], v82 offset:9216\n"
      "s_waitcnt lgkmcnt(13)\n"
      "v_mfma_f32_16x16x32_bf16 v[64:67], a[184:187], v[40:43], v[64:67]\n"
      "v_and_b32_e32 v74, 0xffff0000, v74\n"
      "v_add_f32_e32 v74, 0x3727c5ac, v74\n"
      "v_mfma_f32_16x16x32_bf16 v[68:71], a[240:243], v[40:43], v[68:71]\n"
      "v_cvt_pk_bf16_f32 v74, v74, v74\n"
      "v_and_b32_e32 v74, 0xffff0000, v74\n"
      "ds_read_b128 v[40:43], v82 offset:10240\n"
      "s_waitcnt lgkmcnt(13)\n"
      "v_mfma_f32_16x16x32_bf16 v[64:67], a[188:191], v[44:47], v[64:67]\n"
      "v_sqrt_f32_e32 v74, v74\n"
      "s_nop 0\n"
      "v_mfma_f32_16x16x32_bf16 v[68:71], a[244:247], v[44:47], v[68:71]\n"
      "v_cvt_pk_bf16_f32 v74, v74, v74\n"
      "v_and_b32_e32 v74, 0xffff0000, v74\n"
      "ds_read_b128 v[44:47], v82 offset:11264\n"
      "s_waitcnt lgkmcnt(13)\n"
      "v_mfma_f32_16x16x32_bf16 v[64:67], a[192:195], v[48:51], v[64:67]\n"
      "v_rcp_f32_e32 v80, v74\n"
      "v_mfma_f32_16x16x32_bf16 v[68:71], a[248:251], v[48:51], v[68:71]\n"
      "ds_read_b128 v[48:51], v82 offset:12288\n"
      "s_waitcnt lgkmcnt(13)\n"
      "v_mfma_f32_16x16x32_bf16 v[64:67], a[196:199], v[52:55], v[64:67]\n"
      "v_mfma_f32_16x16x32_bf16 v[68:71], a[252:255], v[52:55], v[68:71]\n"
      "ds_read_b128 v[52:55], v82 offset:13312\n"
      "v_mbcnt_lo_u32_b32 v57, -1, 0\n"
      "v_mbcnt_hi_u32_b32 v57, -1, v57\n"
      "v_lshrrev_b32_e32 v83, 4, v57\n"
      "v_lshlrev_b32_e32 v83, 3, v83\n"
      "v_and_b32_e32 v57, 3, v57\n"
      "v_lshl_add_u32 v83, v57, 1, v83\n"
      "s_lshl_b32 s88, s38, 5\n"
      "s_add_u32 s88, s88, 0x1ea00\n"
      "v_add_u32_e32 v83, s88, v83\n"
      "v_cndmask_b32_e64 v56, v64, v65, s[96:97]\n"
      "v_cndmask_b32_e64 v57, v66, v67, s[96:97]\n"
      "v_cndmask_b32_e64 v58, v68, v69, s[96:97]\n"
      "v_cndmask_b32_e64 v59, v70, v71, s[96:97]\n"
      "v_cndmask_b32_e64 v56, v56, v57, s[84:85]\n"
      "v_cndmask_b32_e64 v58, v58, v59, s[84:85]\n"
      "v_fma_f32 v56, v79, v152, v56\n"
      "v_fma_f32 v58, v79, v153, v58\n"
      "v_mul_f32_e32 v56, v80, v56\n"
      "v_mul_f32_e32 v58, v80, v58\n"
      "v_cvt_pk_bf16_f32 v57, v56, v58\n"
      "v_lshlrev_b32_e32 v56, 16, v57\n"
      "v_and_b32_e32 v58, 0xffff0000, v57\n"
      "v_mul_f32_e32 v56, v56, v58\n"
      "v_cvt_pk_bf16_f32 v56, v56, v56\n"
      "s_mov_b32 exec_lo, 0xf000f\n"
      "s_mov_b32 exec_hi, 0xf000f\n"
      "ds_write_b16 v83, v56\n"
      "s_mov_b64 exec, -1\n"
      "s_mov_b32 s88, 0x90800\n"
      "s_mul_i32 s89, s42, 28672\n"
      "s_add_u32 s88, s88, s89\n"
      "s_mul_i32 s89, s39, 896\n"
      "s_add_u32 s88, s88, s89\n"
      "s_mul_i32 s89, s38, 224\n"
      "s_add_u32 s88, s88, s89\n"
      "s_add_u32 s44, s36, s88\n"
      "s_addc_u32 s45, s37, 0\n"
      "v_mbcnt_lo_u32_b32 v93, -1, 0\n"
      "v_mbcnt_hi_u32_b32 v93, -1, v93\n"
      "v_and_b32_e32 v92, 15, v93\n"
      "v_lshlrev_b32_e32 v92, 5, v92\n"
      "v_lshrrev_b32_e32 v93, 4, v93\n"
      "v_lshl_add_u32 v92, v93, 3, v92\n"
      "s_waitcnt lgkmcnt(0)\n"
      "s_barrier\n"
      "ds_read_b128 v[56:59], v156 offset:2560\n"
      "ds_read_b128 v[60:63], v156 offset:2624\n"
      "s_waitcnt lgkmcnt(1)\n"
      "v_mfma_f32_16x16x32_bf16 v[64:67], v[0:3], v[56:59], 0\n"
      "v_mfma_f32_16x16x32_bf16 v[68:71], v[8:11], v[56:59], 0\n"
      "s_waitcnt lgkmcnt(0)\n"
      "v_mfma_f32_16x16x32_bf16 v[64:67], v[4:7], v[60:63], v[64:67]\n"
      "v_mfma_f32_16x16x32_bf16 v[72:75], v[16:19], v[56:59], 0\n"
      "v_mfma_f32_16x16x32_bf16 v[68:71], v[12:15], v[60:63], v[68:71]\n"
      "v_mfma_f32_16x16x32_bf16 v[76:79], v[24:27], v[56:59], 0\n"
      "s_nop 4\n"
      "v_cvt_pk_bf16_f32 v64, v64, v65\n"
      "v_cvt_pk_bf16_f32 v65, v66, v67\n"
      "v_min_u32_e32 v64, -2, v64\n"
      "v_min_u32_e32 v65, -2, v65\n"
      "v_mfma_f32_16x16x32_bf16 v[72:75], v[20:23], v[60:63], v[72:75]\n"
      "v_mfma_f32_16x16x32_bf16 v[80:83], v[32:35], v[56:59], 0\n"
      "v_cvt_pk_bf16_f32 v94, v68, v69\n"
      "v_cvt_pk_bf16_f32 v95, v70, v71\n"
      "v_min_u32_e32 v94, -2, v94\n"
      "v_min_u32_e32 v95, -2, v95\n"
      "s_mov_b32 s46, 0x20002\n"
      "s_mov_b32 s47, 0x20002\n"
      "v_cndmask_b32_e64 v64, v64, v94, s[46:47]\n"
      "v_cndmask_b32_e64 v65, v65, v95, s[46:47]\n"
      "v_mfma_f32_16x16x32_bf16 v[76:79], v[28:31], v[60:63], v[76:79]\n"
      "v_mfma_f32_16x16x32_bf16 v[84:87], v[40:43], v[56:59], 0\n"
      "v_cvt_pk_bf16_f32 v94, v72, v73\n"
      "v_cvt_pk_bf16_f32 v95, v74, v75\n"
      "v_min_u32_e32 v94, -2, v94\n"
      "v_min_u32_e32 v95, -2, v95\n"
      "s_mov_b32 s46, 0x40004\n"
      "s_mov_b32 s47, 0x40004\n"
      "v_cndmask_b32_e64 v64, v64, v94, s[46:47]\n"
      "v_cndmask_b32_e64 v65, v65, v95, s[46:47]\n"
      "v_mfma_f32_16x16x32_bf16 v[80:83], v[36:39], v[60:63], v[80:83]\n"
      "v_mfma_f32_16x16x32_bf16 v[88:91], v[48:51], v[56:59], 0\n"
      "v_cvt_pk_bf16_f32 v94, v76, v77\n"
      "v_cvt_pk_bf16_f32 v95, v78, v79\n"
      "v_min_u32_e32 v94, -2, v94\n"
      "v_min_u32_e32 v95, -2, v95\n"
      "s_mov_b32 s46, 0x80008\n"
      "s_mov_b32 s47, 0x80008\n"
      "v_cndmask_b32_e64 v64, v64, v94, s[46:47]\n"
      "v_cndmask_b32_e64 v65, v65, v95, s[46:47]\n"
      "v_mfma_f32_16x16x32_bf16 v[84:87], v[44:47], v[60:63], v[84:87]\n"
      "v_mfma_f32_16x16x32_bf16 v[88:91], v[52:55], v[60:63], v[88:91]\n"
      "v_cvt_pk_bf16_f32 v94, v80, v81\n"
      "v_cvt_pk_bf16_f32 v95, v82, v83\n"
      "v_min_u32_e32 v94, -2, v94\n"
      "v_min_u32_e32 v95, -2, v95\n"
      "s_mov_b32 s46, 0x100010\n"
      "s_mov_b32 s47, 0x100010\n"
      "v_cndmask_b32_e64 v64, v64, v94, s[46:47]\n"
      "v_cndmask_b32_e64 v65, v65, v95, s[46:47]\n"
      "v_cvt_pk_bf16_f32 v94, v84, v85\n"
      "v_cvt_pk_bf16_f32 v95, v86, v87\n"
      "v_min_u32_e32 v94, -2, v94\n"
      "v_min_u32_e32 v95, -2, v95\n"
      "s_mov_b32 s46, 0x200020\n"
      "s_mov_b32 s47, 0x200020\n"
      "v_cndmask_b32_e64 v64, v64, v94, s[46:47]\n"
      "v_cndmask_b32_e64 v65, v65, v95, s[46:47]\n"
      "v_cvt_pk_bf16_f32 v94, v88, v89\n"
      "v_cvt_pk_bf16_f32 v95, v90, v91\n"
      "v_min_u32_e32 v94, -2, v94\n"
      "v_min_u32_e32 v95, -2, v95\n"
      "s_mov_b32 s46, 0x400040\n"
      "s_mov_b32 s47, 0x400040\n"
      "v_cndmask_b32_e64 v64, v64, v94, s[46:47]\n"
      "v_cndmask_b32_e64 v65, v65, v95, s[46:47]\n"
      "s_mov_b32 exec_lo, 0x7f007f\n"
      "s_mov_b32 exec_hi, 0x7f007f\n"
      "global_store_dwordx2 v92, v[64:65], s[44:45] sc1\n"
      "s_mov_b64 exec, -1\n"
      "s_nop 0\n"
      "NOBC_28_%=:\n"
      "v_mov_b32_e32 v96, -1\n"
      "v_mov_b32_e32 v97, -1\n"
      "v_mov_b32_e32 v98, -1\n"
      "v_mov_b32_e32 v99, -1\n"
      "v_mov_b32_e32 v100, -1\n"
      "v_mov_b32_e32 v101, -1\n"
      "v_mov_b32_e32 v102, -1\n"
      "v_mov_b32_e32 v103, -1\n"
      "v_mov_b32_e32 v104, -1\n"
      "v_mov_b32_e32 v105, -1\n"
      "v_mov_b32_e32 v106, -1\n"
      "v_mov_b32_e32 v107, -1\n"
      "v_mov_b32_e32 v108, -1\n"
      "v_mov_b32_e32 v109, -1\n"
      "v_mov_b32_e32 v110, -1\n"
      "v_mov_b32_e32 v111, -1\n"
      "v_mov_b32_e32 v112, -1\n"
      "v_mov_b32_e32 v113, -1\n"
      "v_mov_b32_e32 v114, -1\n"
      "v_mov_b32_e32 v115, -1\n"
      "v_mov_b32_e32 v116, -1\n"
      "v_mov_b32_e32 v117, -1\n"
      "v_mov_b32_e32 v118, -1\n"
      "v_mov_b32_e32 v119, -1\n"
      "v_mov_b32_e32 v120, -1\n"
      "v_mov_b32_e32 v121, -1\n"
      "v_mov_b32_e32 v122, -1\n"
      "v_mov_b32_e32 v123, -1\n"
      "v_cndmask_b32_e64 v84, 0, v159, s[64:65]\n"
      "v_cndmask_b32_e64 v85, 0, v159, s[66:67]\n"
      "v_cndmask_b32_e64 v86, 0, v159, s[68:69]\n"
      "v_cndmask_b32_e64 v87, 0, v159, s[70:71]\n"
      "s_mov_b32 s88, 0x90800\n"
      "s_mul_i32 s89, s42, 28672\n"
      "s_add_u32 s88, s88, s89\n"
      "s_add_u32 s44, s36, s88\n"
      "s_addc_u32 s45, s37, 0\n"
      "s_mov_b32 s94, 0x3fff3fff\n"
      "s_mov_b32 s95, 0x3fff3fff\n"
      "s_mov_b64 s[48:49], s[94:95]\n"
      "s_mov_b64 s[50:51], s[94:95]\n"
      "s_mov_b64 s[52:53], s[94:95]\n"
      "s_mov_b64 s[54:55], s[94:95]\n"
      "s_mov_b64 s[56:57], s[94:95]\n"
      "s_mov_b64 s[58:59], s[94:95]\n"
      "s_mov_b64 s[60:61], s[94:95]\n"
      "s_mov_b32 s43, 0\n"
      "s_sleep 8\n"
      "s_nop 0\n"
      "GT_29_%=:\n"
      "s_mov_b64 exec, s[48:49]\n"
      "s_nop 0\n"
      "s_cbranch_execz GS_32_%=\n"
      "s_add_u32 s46, s44, 0x0\n"
      "s_addc_u32 s47, s45, 0\n"
      "global_load_dwordx4 v[96:99], v154, s[46:47] sc1\n"
      "GS_32_%=:\n"
      "s_mov_b64 exec, s[50:51]\n"
      "s_nop 0\n"
      "s_cbranch_execz GS_33_%=\n"
      "s_add_u32 s46, s44, 0xe00\n"
      "s_addc_u32 s47, s45, 0\n"
      "global_load_dwordx4 v[100:103], v154, s[46:47] sc1\n"
      "GS_33_%=:\n"
      "s_mov_b64 exec, s[52:53]\n"
      "s_nop 0\n"
      "s_cbranch_execz GS_34_%=\n"
      "s_add_u32 s46, s44, 0x1c00\n"
      "s_addc_u32 s47, s45, 0\n"
      "global_load_dwordx4 v[104:107], v154, s[46:47] sc1\n"
      "GS_34_%=:\n"
      "s_mov_b64 exec, s[54:55]\n"
      "s_nop 0\n"
      "s_cbranch_execz GS_35_%=\n"
      "s_add_u32 s46, s44, 0x2a00\n"
      "s_addc_u32 s47, s45, 0\n"
      "global_load_dwordx4 v[108:111], v154, s[46:47] sc1\n"
      "GS_35_%=:\n"
      "s_mov_b64 exec, s[56:57]\n"
      "s_nop 0\n"
      "s_cbranch_execz GS_36_%=\n"
      "s_add_u32 s46, s44, 0x3800\n"
      "s_addc_u32 s47, s45, 0\n"
      "global_load_dwordx4 v[112:115], v154, s[46:47] sc1\n"
      "GS_36_%=:\n"
      "s_mov_b64 exec, s[58:59]\n"
      "s_nop 0\n"
      "s_cbranch_execz GS_37_%=\n"
      "s_add_u32 s46, s44, 0x4600\n"
      "s_addc_u32 s47, s45, 0\n"
      "global_load_dwordx4 v[116:119], v154, s[46:47] sc1\n"
      "GS_37_%=:\n"
      "s_mov_b64 exec, s[60:61]\n"
      "s_nop 0\n"
      "s_cbranch_execz GS_38_%=\n"
      "s_add_u32 s46, s44, 0x5400\n"
      "s_addc_u32 s47, s45, 0\n"
      "global_load_dwordx4 v[120:123], v154, s[46:47] sc1\n"
      "GS_38_%=:\n"
      "s_mov_b64 exec, -1\n"
      "s_sleep 1\n"
      "v_max3_u32 v56, v96, v97, v98\n"
      "v_max_u32_e32 v56, v56, v99\n"
      "v_cmp_eq_u32_e64 s[48:49], -1, v56\n"
      "v_max3_u32 v57, v100, v101, v102\n"
      "v_max_u32_e32 v57, v57, v103\n"
      "v_cmp_eq_u32_e64 s[50:51], -1, v57\n"
      "v_max3_u32 v56, v104, v105, v106\n"
      "v_max_u32_e32 v56, v56, v107\n"
      "v_cmp_eq_u32_e64 s[52:53], -1, v56\n"
      "v_max3_u32 v57, v108, v109, v110\n"
      "v_max_u32_e32 v57, v57, v111\n"
      "v_cmp_eq_u32_e64 s[54:55], -1, v57\n"
      "v_max3_u32 v56, v112, v113, v114\n"
      "v_max_u32_e32 v56, v56, v115\n"
      "v_cmp_eq_u32_e64 s[56:57], -1, v56\n"
      "v_max3_u32 v57, v116, v117, v118\n"
      "v_max_u32_e32 v57, v57, v119\n"
      "v_cmp_eq_u32_e64 s[58:59], -1, v57\n"
      "v_max3_u32 v56, v120, v121, v122\n"
      "v_max_u32_e32 v56, v56, v123\n"
      "v_cmp_eq_u32_e64 s[60:61], -1, v56\n"
      "s_and_b64 s[48:49], s[48:49], s[94:95]\n"
      "s_and_b64 s[50:51], s[50:51], s[94:95]\n"
      "s_and_b64 s[52:53], s[52:53], s[94:95]\n"
      "s_and_b64 s[54:55], s[54:55], s[94:95]\n"
      "s_and_b64 s[56:57], s[56:57], s[94:95]\n"
      "s_and_b64 s[58:59], s[58:59], s[94:95]\n"
      "s_and_b64 s[60:61], s[60:61], s[94:95]\n"
      "s_mov_b64 s[88:89], s[48:49]\n"
      "s_or_b64 s[88:89], s[88:89], s[50:51]\n"
      "s_or_b64 s[88:89], s[88:89], s[52:53]\n"
      "s_or_b64 s[88:89], s[88:89], s[54:55]\n"
      "s_or_b64 s[88:89], s[88:89], s[56:57]\n"
      "s_or_b64 s[88:89], s[88:89], s[58:59]\n"
      "s_or_b64 s[88:89], s[88:89], s[60:61]\n"
      "s_cmp_eq_u64 s[88:89], 0\n"
      "s_nop 0\n"
      "s_cbranch_scc1 GD_30_%=\n"
      "s_add_u32 s43, s43, 1\n"
      "s_cmp_lt_u32 s43, 0x80000\n"
      "s_waitcnt vmcnt(7)\n"
      "s_nop 0\n"
      "s_cbranch_scc1 GT_29_%=\n"
      "s_nop 0\n"
      "GD_30_%=:\n"
      "v_mfma_f32_16x16x32_bf16 v[88:91], v[96:99], v[84:87], 0\n"
      "v_mfma_f32_16x16x32_bf16 v[88:91], v[100:103], v[84:87], v[88:91]\n"
      "v_mfma_f32_16x16x32_bf16 v[88:91], v[104:107], v[84:87], v[88:91]\n"
      "v_mfma_f32_16x16x32_bf16 v[88:91], v[108:111], v[84:87], v[88:91]\n"
      "v_mfma_f32_16x16x32_bf16 v[88:91], v[112:115], v[84:87], v[88:91]\n"
      "v_mfma_f32_16x16x32_bf16 v[88:91], v[116:119], v[84:87], v[88:91]\n"
      "v_mfma_f32_16x16x32_bf16 v[88:91], v[120:123], v[84:87], v[88:91]\n"
      "s_nop 7\n"
      "v_cndmask_b32_e64 v56, v88, v90, s[62:63]\n"
      "v_cndmask_b32_e64 v57, v89, v91, s[62:63]\n"
      "v_cvt_pk_bf16_f32 v58, v56, v57\n"
      "v_lshlrev_b32_e32 v56, 16, v58\n"
      "v_and_b32_e32 v57, 0xffff0000, v58\n"
      "v_add_f32_e32 v56, v140, v56\n"
      "v_add_f32_e32 v57, v141, v57\n"
      "v_cvt_pk_bf16_f32 v58, v56, v57\n"
      "s_mov_b32 exec_lo, -1\n"
      "s_mov_b32 exec_hi, 0x00ffffff\n"
      "ds_write_b32 v155, v58 offset:0\n"
      "s_mov_b64 exec, -1\n"
      "v_lshlrev_b32_e32 v140, 16, v58\n"
      "v_and_b32_e32 v141, 0xffff0000, v58\n"
      "v_mbcnt_lo_u32_b32 v93, -1, 0\n"
      "v_mbcnt_hi_u32_b32 v93, -1, v93\n"
      "v_and_b32_e32 v92, 15, v93\n"
      "v_lshlrev_b32_e32 v92, 6, v92\n"
      "v_and_b32_e32 v93, 48, v93\n"
      "v_add_u32_e32 v92, v92, v93\n"
      "s_add_u32 s88, s42, 1\n"
      "s_lshl_b32 s88, s88, 17\n"
      "s_lshl_b32 s89, s40, 13\n"
      "s_add_u32 s88, s88, s89\n"
      "s_lshl_b32 s89, s41, 10\n"
      "s_add_u32 s88, s88, s89\n"
      "s_lshr_b32 s89, s38, 1\n"
      "s_lshl_b32 s89, s89, 9\n"
      "s_add_u32 s88, s88, s89\n"
      "s_and_b32 s89, s38, 1\n"
      "s_lshl_b32 s89, s89, 12\n"
      "s_add_u32 s88, s88, s89\n"
      "s_add_u32 s88, s88, 0x800\n"
      "s_add_u32 s46, s36, s88\n"
      "s_addc_u32 s47, s37, 0\n"
      "s_mov_b64 s[44:45], s[46:47]\n"
      "s_waitcnt lgkmcnt(0)\n"
      "s_barrier\n"
      "v_mbcnt_lo_u32_b32 v81, -1, 0\n"
      "v_mbcnt_hi_u32_b32 v81, -1, v81\n"
      "v_lshlrev_b32_e32 v81, 4, v81\n"
      "v_add_u32_e32 v76, 0x1e000, v81\n"
      "ds_read_b128 v[60:63], v76\n"
      "ds_read_b128 v[0:3], v156 offset:0\n"
      "ds_read_b128 v[4:7], v156 offset:64\n"
      "ds_read_b128 v[8:11], v156 offset:128\n"
      "ds_read_b128 v[12:15], v156 offset:256\n"
      "ds_read_b128 v[16:19], v156 offset:320\n"
      "ds_read_b128 v[20:23], v156 offset:384\n"
      "ds_read_b128 v[24:27], v156 offset:512\n"
      "ds_read_b128 v[28:31], v156 offset:576\n"
      "ds_read_b128 v[32:35], v156 offset:640\n"
      "ds_read_b128 v[36:39], v156 offset:768\n"
      "ds_read_b128 v[40:43], v156 offset:832\n"
      "ds_read_b128 v[44:47], v156 offset:896\n"
      "ds_read_b128 v[48:51], v157 offset:192\n"
      "ds_read_b128 v[52:55], v157 offset:704\n"
      "s_mov_b32 s89, 0x3f803f80\n"
      "s_waitcnt lgkmcnt(13)\n"
      "v_mfma_f32_16x16x32_bf16 v[64:67], a[32:35], v[0:3], 0\n"
      "v_dot2_f32_bf16 v72, v60, s89, 0\n"
      "v_dot2_f32_bf16 v74, v60, v60, 0\n"
      "v_mfma_f32_16x16x32_bf16 v[68:71], a[88:91], v[0:3], 0\n"
      "v_dot2_f32_bf16 v73, v62, s89, 0\n"
      "v_dot2_f32_bf16 v75, v62, v62, 0\n"
      "s_waitcnt lgkmcnt(12)\n"
      "v_mfma_f32_16x16x32_bf16 v[64:67], a[36:39], v[4:7], v[64:67]\n"
      "v_dot2_f32_bf16 v72, v61, s89, v72\n"
      "v_dot2_f32_bf16 v74, v61, v61, v74\n"
      "v_mfma_f32_16x16x32_bf16 v[68:71], a[92:95], v[4:7], v[68:71]\n"
      "v_dot2_f32_bf16 v73, v63, s89, v73\n"
      "v_dot2_f32_bf16 v75, v63, v63, v75\n"
      "s_waitcnt lgkmcnt(11)\n"
      "v_mfma_f32_16x16x32_bf16 v[64:67], a[40:43], v[8:11], v[64:67]\n"
      "s_nop 0\n"
      "s_nop 0\n"
      "v_mfma_f32_16x16x32_bf16 v[68:71], a[96:99], v[8:11], v[68:71]\n"
      "v_add_f32_e32 v72, v72, v73\n"
      "v_add_f32_e32 v74, v74, v75\n"
      "s_waitcnt lgkmcnt(10)\n"
      "v_mfma_f32_16x16x32_bf16 v[64:67], a[44:47], v[12:15], v[64:67]\n"
      "v_add_f32_dpp v72, v72, v72 row_ror:8 row_mask:0xf bank_mask:0xf\n"
      "v_add_f32_dpp v74, v74, v74 row_ror:8 row_mask:0xf bank_mask:0xf\n"
      "v_mfma_f32_16x16x32_bf16 v[68:71], a[100:103], v[12:15], v[68:71]\n"
      "v_add_f32_dpp v72, v72, v72 row_ror:4 row_mask:0xf bank_mask:0xf\n"
      "v_add_f32_dpp v74, v74, v74 row_ror:4 row_mask:0xf bank_mask:0xf\n"
      "s_waitcnt lgkmcnt(9)\n"
      "v_mfma_f32_16x16x32_bf16 v[64:67], a[48:51], v[16:19], v[64:67]\n"
      "v_add_f32_dpp v72, v72, v72 row_ror:2 row_mask:0xf bank_mask:0xf\n"
      "v_add_f32_dpp v74, v74, v74 row_ror:2 row_mask:0xf bank_mask:0xf\n"
      "v_mfma_f32_16x16x32_bf16 v[68:71], a[104:107], v[16:19], v[68:71]\n"
      "v_add_f32_dpp v72, v72, v72 row_ror:1 row_mask:0xf bank_mask:0xf\n"
      "v_add_f32_dpp v74, v74, v74 row_ror:1 row_mask:0xf bank_mask:0xf\n"
      "s_waitcnt lgkmcnt(8)\n"
      "v_mfma_f32_16x16x32_bf16 v[64:67], a[52:55], v[20:23], v[64:67]\n"
      "v_mov_b32_e32 v76, v72\n"
      "v_mov_b32_e32 v77, v74\n"
      "v_mfma_f32_16x16x32_bf16 v[68:71], a[108:111], v[20:23], v[68:71]\n"
      "v_permlane16_swap_b32_e32 v76, v72\n"
      "v_permlane16_swap_b32_e32 v77, v74\n"
      "s_waitcnt lgkmcnt(7)\n"
      "v_mfma_f32_16x16x32_bf16 v[64:67], a[56:59], v[24:27], v[64:67]\n"
      "v_add_f32_e32 v72, v72, v76\n"
      "v_add_f32_e32 v74, v74, v77\n"
      "v_mfma_f32_16x16x32_bf16 v[68:71], a[112:115], v[24:27], v[68:71]\n"
      "v_mov_b32_e32 v76, v72\n"
      "v_mov_b32_e32 v77, v74\n"
      "s_waitcnt lgkmcnt(6)\n"
      "v_mfma_f32_16x16x32_bf16 v[64:67], a[60:63], v[28:31], v[64:67]\n"
      "v_permlane32_swap_b32_e32 v76, v72\n"
      "v_permlane32_swap_b32_e32 v77, v74\n"
      "v_mfma_f32_16x16x32_bf16 v[68:71], a[116:119], v[28:31], v[68:71]\n"
      "v_add_f32_e32 v72, v72, v76\n"
      "v_add_f32_e32 v74, v74, v77\n"
      "s_waitcnt lgkmcnt(5)\n"
      "v_mfma_f32_16x16x32_bf16 v[64:67], a[64:67], v[32:35], v[64:67]\n"
      "v_mul_f32_e32 v78, 0x3b124925, v72\n"
      "v_mul_f32_e32 v74, 0x3b124925, v74\n"
      "v_mfma_f32_16x16x32_bf16 v[68:71], a[120:123], v[32:35], v[68:71]\n"
      "v_fma_f32 v74, -v78, v78, v74\n"
      "v_cvt_pk_bf16_f32 v79, v78, v78\n"
      "s_waitcnt lgkmcnt(4)\n"
      "v_mfma_f32_16x16x32_bf16 v[64:67], a[68:71], v[36:39], v[64:67]\n"
      "v_max_f32_e32 v74, 0, v74\n"
      "v_and_b32_e32 v79, 0xffff0000, v79\n"
      "v_mfma_f32_16x16x32_bf16 v[68:71], a[124:127], v[36:39], v[68:71]\n"
      "v_cvt_pk_bf16_f32 v74, v74, v74\n"
      "v_xor_b32_e32 v79, 0x80000000, v79\n"
      "s_waitcnt lgkmcnt(3)\n"
      "v_mfma_f32_16x16x32_bf16 v[64:67], a[72:75], v[40:43], v[64:67]\n"
      "v_and_b32_e32 v74, 0xffff0000, v74\n"
      "v_add_f32_e32 v74, 0x3727c5ac, v74\n"
      "v_mfma_f32_16x16x32_bf16 v[68:71], a[128:131], v[40:43], v[68:71]\n"
      "v_cvt_pk_bf16_f32 v74, v74, v74\n"
      "v_and_b32_e32 v74, 0xffff0000, v74\n"
      "s_waitcnt lgkmcnt(2)\n"
      "v_mfma_f32_16x16x32_bf16 v[64:67], a[76:79], v[44:47], v[64:67]\n"
      "v_sqrt_f32_e32 v74, v74\n"
      "s_nop 0\n"
      "v_mfma_f32_16x16x32_bf16 v[68:71], a[132:135], v[44:47], v[68:71]\n"
      "v_cvt_pk_bf16_f32 v74, v74, v74\n"
      "v_and_b32_e32 v74, 0xffff0000, v74\n"
      "s_waitcnt lgkmcnt(1)\n"
      "v_mfma_f32_16x16x32_bf16 v[64:67], a[80:83], v[48:51], v[64:67]\n"
      "v_rcp_f32_e32 v80, v74\n"
      "v_mfma_f32_16x16x32_bf16 v[68:71], a[136:139], v[48:51], v[68:71]\n"
      "s_waitcnt lgkmcnt(0)\n"
      "v_mfma_f32_16x16x32_bf16 v[64:67], a[84:87], v[52:55], v[64:67]\n"
      "v_mfma_f32_16x16x32_bf16 v[68:71], a[140:143], v[52:55], v[68:71]\n"
      "s_nop 6\n"
      "v_fma_f32 v64, v79, v144, v64\n"
      "v_fma_f32 v65, v79, v145, v65\n"
      "v_fma_f32 v66, v79, v146, v66\n"
      "v_fma_f32 v67, v79, v147, v67\n"
      "v_mul_f32_e32 v64, v80, v64\n"
      "v_mul_f32_e32 v65, v80, v65\n"
      "v_mul_f32_e32 v66, v80, v66\n"
      "v_mul_f32_e32 v67, v80, v67\n"
      "v_cvt_pk_bf16_f32 v84, v64, v65\n"
      "v_cvt_pk_bf16_f32 v86, v66, v67\n"
      "v_mov_b32_e32 v85, s35\n"
      "v_mov_b32_e32 v87, s35\n"
      "v_fma_f32 v68, v79, v148, v68\n"
      "v_fma_f32 v69, v79, v149, v69\n"
      "v_fma_f32 v70, v79, v150, v70\n"
      "v_fma_f32 v71, v79, v151, v71\n"
      "v_mul_f32_e32 v68, v80, v68\n"
      "v_mul_f32_e32 v69, v80, v69\n"
      "v_mul_f32_e32 v70, v80, v70\n"
      "v_mul_f32_e32 v71, v80, v71\n"
      "v_cvt_pk_bf16_f32 v88, v68, v69\n"
      "v_cvt_pk_bf16_f32 v90, v70, v71\n"
      "v_mov_b32_e32 v89, s35\n"
      "v_mov_b32_e32 v91, s35\n"
      "s_mov_b32 s46, 0x00020002\n"
      "s_mov_b32 s47, 0x00020002\n"
      "v_cndmask_b32_e64 v84, v84, v88, s[46:47]\n"
      "v_cndmask_b32_e64 v86, v86, v90, s[46:47]\n"
      "s_mov_b32 exec_lo, 0x30003\n"
      "s_mov_b32 exec_hi, 0x30003\n"
      "global_store_dwordx4 v92, v[84:87], s[44:45] sc1\n"
      "s_mov_b64 exec, -1\n"
      "s_mov_b32 s83, 1\n"
      "s_getpc_b64 s[88:89]\n"
      "XPC_39_%=:\n"
      "s_add_u32 s88, s88, C_EXTRA_%=-XPC_39_%=\n"
      "s_addc_u32 s89, s89, 0\n"
      "s_nop 0\n"
      "s_swappc_b64 s[98:99], s[88:89]\n"
      "v_mbcnt_lo_u32_b32 v76, -1, 0\n"
      "v_mbcnt_hi_u32_b32 v76, -1, v76\n"
      "v_bfe_u32 v77, v76, 2, 2\n"
      "v_lshlrev_b32_e32 v60, 9, v77\n"
      "v_lshrrev_b32_e32 v77, 4, v76\n"
      "v_lshl_add_u32 v60, v77, 6, v60\n"
      "v_lshlrev_b32_e32 v61, 9, v77\n"
      "v_and_b32_e32 v78, 3, v76\n"
      "v_lshl_add_u32 v60, v78, 4, v60\n"
      "v_bfe_u32 v78, v76, 3, 1\n"
      "v_lshl_add_u32 v61, v78, 12, v61\n"
      "v_and_b32_e32 v78, 7, v76\n"
      "v_lshl_add_u32 v61, v78, 4, v61\n"
      "s_lshl_b32 s88, s100, 22\n"
      "s_lshl_b32 s89, s101, 2\n"
      "s_add_u32 s89, s89, s38\n"
      "s_add_u32 s89, s89, 504\n"
      "s_lshl_b32 s89, s89, 13\n"
      "s_add_u32 s88, s88, s89\n"
      "s_add_u32 s88, s88, 0x4000000\n"
      "s_lshl_b32 s90, s38, 14\n"
      "s_add_u32 s90, s90, 0xe000\n"
      "s_mov_b64 s[92:93], %[kc]\n"
      "s_add_u32 s92, s92, s88\n"
      "s_addc_u32 s93, s93, 0\n"
      "s_add_u32 s46, s92, 0x0\n"
      "s_addc_u32 s47, s93, 0\n"
      "s_add_u32 m0, s90, 0x0\n"
      "s_nop 0\n"
      "global_load_lds_dwordx4 v60, s[46:47]\n"
      "s_add_u32 s46, s92, 0x100\n"
      "s_addc_u32 s47, s93, 0\n"
      "s_add_u32 m0, s90, 0x400\n"
      "s_nop 0\n"
      "global_load_lds_dwordx4 v60, s[46:47]\n"
      "s_add_u32 s46, s92, 0x800\n"
      "s_addc_u32 s47, s93, 0\n"
      "s_add_u32 m0, s90, 0x800\n"
      "s_nop 0\n"
      "global_load_lds_dwordx4 v60, s[46:47]\n"
      "s_add_u32 s46, s92, 0x900\n"
      "s_addc_u32 s47, s93, 0\n"
      "s_add_u32 m0, s90, 0xc00\n"
      "s_nop 0\n"
      "global_load_lds_dwordx4 v60, s[46:47]\n"
      "s_add_u32 s46, s92, 0x1000\n"
      "s_addc_u32 s47, s93, 0\n"
      "s_add_u32 m0, s90, 0x1000\n"
      "s_nop 0\n"
      "global_load_lds_dwordx4 v60, s[46:47]\n"
      "s_add_u32 s46, s92, 0x1100\n"
      "s_addc_u32 s47, s93, 0\n"
      "s_add_u32 m0, s90, 0x1400\n"
      "s_nop 0\n"
      "global_load_lds_dwordx4 v60, s[46:47]\n"
      "s_add_u32 s46, s92, 0x1800\n"
      "s_addc_u32 s47, s93, 0\n"
      "s_add_u32 m0, s90, 0x1800\n"
      "s_nop 0\n"
      "global_load_lds_dwordx4 v60, s[46:47]\n"
      "s_add_u32 s46, s92, 0x1900\n"
      "s_addc_u32 s47, s93, 0\n"
      "s_add_u32 m0, s90, 0x1c00\n"
      "s_nop 0\n"
      "global_load_lds_dwordx4 v60, s[46:47]\n"
      "s_mov_b64 s[92:93], %[vc]\n"
      "s_add_u32 s92, s92, s88\n"
      "s_addc_u32 s93, s93, 0\n"
      "s_add_u32 s46, s92, 0x0\n"
      "s_addc_u32 s47, s93, 0\n"
      "s_add_u32 m0, s90, 0x2000\n"
      "s_nop 0\n"
      "global_load_lds_dwordx4 v61, s[46:47]\n"
      "s_add_u32 s46, s92, 0x80\n"
      "s_addc_u32 s47, s93, 0\n"
      "s_add_u32 m0, s90, 0x2400\n"
      "s_nop 0\n"
      "global_load_lds_dwordx4 v61, s[46:47]\n"
      "s_add_u32 s46, s92, 0x100\n"
      "s_addc_u32 s47, s93, 0\n"
      "s_add_u32 m0, s90, 0x2800\n"
      "s_nop 0\n"
      "global_load_lds_dwordx4 v61, s[46:47]\n"
      "s_add_u32 s46, s92, 0x180\n"
      "s_addc_u32 s47, s93, 0\n"
      "s_add_u32 m0, s90, 0x2c00\n"
      "s_nop 0\n"
      "global_load_lds_dwordx4 v61, s[46:47]\n"
      "s_add_u32 s46, s92, 0x800\n"
      "s_addc_u32 s47, s93, 0\n"
      "s_add_u32 m0, s90, 0x3000\n"
      "s_nop 0\n"
      "global_load_lds_dwordx4 v61, s[46:47]\n"
      "s_add_u32 s46, s92, 0x880\n"
      "s_addc_u32 s47, s93, 0\n"
      "s_add_u32 m0, s90, 0x3400\n"
      "s_nop 0\n"
      "global_load_lds_dwordx4 v61, s[46:47]\n"
      "s_add_u32 s46, s92, 0x900\n"
      "s_addc_u32 s47, s93, 0\n"
      "s_add_u32 m0, s90, 0x3800\n"
      "s_nop 0\n"
      "global_load_lds_dwordx4 v61, s[46:47]\n"
      "s_add_u32 s46, s92, 0x980\n"
      "s_addc_u32 s47, s93, 0\n"
      "s_add_u32 m0, s90, 0x3c00\n"
      "s_nop 0\n"
      "global_load_lds_dwordx4 v61, s[46:47]\n"
      "s_mov_b32 s83, 2\n"
      "s_getpc_b64 s[88:89]\n"
      "XPC_40_%=:\n"
      "s_add_u32 s88, s88, C_EXTRA_%=-XPC_40_%=\n"
      "s_addc_u32 s89, s89, 0\n"
      "s_nop 0\n"
      "s_swappc_b64 s[98:99], s[88:89]\n"
      "s_mov_b32 s42, 2\n"
      "v_mov_b32_e32 v96, -1\n"
      "v_mov_b32_e32 v97, -1\n"
      "v_mov_b32_e32 v98, -1\n"
      "v_mov_b32_e32 v99, -1\n"
      "v_mov_b32_e32 v100, -1\n"
      "v_mov_b32_e32 v101, -1\n"
      "v_mov_b32_e32 v102, -1\n"
      "v_mov_b32_e32 v103, -1\n"
      "v_mov_b32_e32 v104, -1\n"
      "v_mov_b32_e32 v105, -1\n"
      "v_mov_b32_e32 v106, -1\n"
      "v_mov_b32_e32 v107, -1\n"
      "v_mov_b32_e32 v108, -1\n"
      "v_mov_b32_e32 v109, -1\n"
      "v_mov_b32_e32 v110, -1\n"
      "v_mov_b32_e32 v111, -1\n"
      "v_mov_b32_e32 v112, -1\n"
      "v_mov_b32_e32 v113, -1\n"
      "v_mov_b32_e32 v114, -1\n"
      "v_mov_b32_e32 v115, -1\n"
      "v_mov_b32_e32 v116, -1\n"
      "v_mov_b32_e32 v117, -1\n"
      "v_mov_b32_e32 v118, -1\n"
      "v_mov_b32_e32 v119, -1\n"
      "v_mov_b32_e32 v124, -1\n"
      "v_mov_b32_e32 v125, -1\n"
      "v_mov_b32_e32 v126, -1\n"
      "v_mov_b32_e32 v127, -1\n"
      "v_mov_b32_e32 v128, -1\n"
      "v_mov_b32_e32 v129, -1\n"
      "v_mov_b32_e32 v130, -1\n"
      "v_mov_b32_e32 v131, -1\n"
      "v_mov_b32_e32 v132, -1\n"
      "v_mov_b32_e32 v133, -1\n"
      "v_mov_b32_e32 v134, -1\n"
      "v_mov_b32_e32 v135, -1\n"
      "v_mov_b32_e32 v136, -1\n"
      "v_mov_b32_e32 v137, -1\n"
      "v_mov_b32_e32 v138, -1\n"
      "v_mov_b32_e32 v139, -1\n"
      "v_mbcnt_lo_u32_b32 v56, -1, 0\n"
      "v_mbcnt_hi_u32_b32 v56, -1, v56\n"
      "v_lshrrev_b32_e32 v57, 4, v56\n"
      "v_lshlrev_b32_e32 v93, 5, v57\n"
      "v_lshlrev_b32_e32 v92, 10, v57\n"
      "v_and_b32_e32 v57, 15, v56\n"
      "v_lshl_add_u32 v92, v57, 4, v92\n"
      "s_lshl_b32 s88, s38, 8\n"
      "v_add_u32_e32 v92, s88, v92\n"
      "v_bfe_u32 v57, v56, 3, 1\n"
      "v_lshlrev_b32_e32 v94, 6, v57\n"
      "v_lshrrev_b32_e32 v57, 4, v56\n"
      "v_lshl_add_u32 v94, v57, 4, v94\n"
      "v_and_b32_e32 v57, 7, v56\n"
      "v_lshl_add_u32 v94, v57, 1, v94\n"
      "s_lshl_b32 s88, s38, 7\n"
      "s_add_u32 s88, s88, 0x1e800\n"
      "v_add_u32_e32 v94, s88, v94\n"
      "s_mov_b32 s88, 0xb0800\n"
      "s_mul_i32 s89, s42, 262144\n"
      "s_add_u32 s88, s88, s89\n"
      "s_mul_i32 s89, s40, 16384\n"
      "s_add_u32 s88, s88, s89\n"
      "s_add_u32 s44, s36, s88\n"
      "s_addc_u32 s45, s37, 0\n"
      "s_mov_b32 s88, 0x80800\n"
      "s_mul_i32 s89, s42, 2048\n"
      "s_add_u32 s88, s88, s89\n"
      "s_mul_i32 s89, s40, 128\n"
      "s_add_u32 s88, s88, s89\n"
      "s_add_u32 s86, s36, s88\n"
      "s_addc_u32 s87, s37, 0\n"
      "s_mov_b64 s[92:93], -1\n"
      "s_mov_b64 s[48:49], -1\n"
      "s_mov_b64 s[50:51], -1\n"
      "s_mov_b64 s[52:53], -1\n"
      "s_mov_b64 s[54:55], -1\n"
      "s_mov_b64 s[56:57], -1\n"
      "s_mov_b32 s43, 0\n"
      "s_nop 0\n"
      "A3_41_%=:\n"
      "s_mov_b64 exec, s[48:49]\n"
      "s_nop 0\n"
      "s_cbranch_execz AS_43_%=\n"
      "s_add_u32 s46, s44, 0x0\n"
      "s_addc_u32 s47, s45, 0\n"
      "global_load_dwordx4 v[96:99], v92, s[46:47] sc1\n"
      "AS_43_%=:\n"
      "s_mov_b64 exec, s[50:51]\n"
      "s_nop 0\n"
      "s_cbranch_execz AS_44_%=\n"
      "s_add_u32 s46, s44, 0x1000\n"
      "s_addc_u32 s47, s45, 0\n"
      "global_load_dwordx4 v[100:103], v92, s[46:47] sc1\n"
      "AS_44_%=:\n"
      "s_mov_b64 exec, s[52:53]\n"
      "s_nop 0\n"
      "s_cbranch_execz AS_45_%=\n"
      "s_add_u32 s46, s44, 0x2000\n"
      "s_addc_u32 s47, s45, 0\n"
      "global_load_dwordx4 v[104:107], v92, s[46:47] sc1\n"
      "AS_45_%=:\n"
      "s_mov_b64 exec, s[54:55]\n"
      "s_nop 0\n"
      "s_cbranch_execz AS_46_%=\n"
      "s_add_u32 s46, s44, 0x3000\n"
      "s_addc_u32 s47, s45, 0\n"
      "global_load_dwordx4 v[108:111], v92, s[46:47] sc1\n"
      "AS_46_%=:\n"
      "s_mov_b64 exec, -1\n"
      "s_cmp_eq_u64 s[56:57], 0\n"
      "s_nop 0\n"
      "s_cbranch_scc1 AS_47_%=\n"
      "global_load_dwordx4 v[112:115], v93, s[86:87] sc1\n"
      "global_load_dwordx4 v[116:119], v93, s[86:87] offset:16 sc1\n"
      "AS_47_%=:\n"
      "s_sleep 1\n"
      "v_max3_u32 v57, v96, v97, v98\n"
      "v_max_u32_e32 v57, v57, v99\n"
      "v_cmp_eq_u32_e64 s[48:49], -1, v57\n"
      "v_max3_u32 v58, v100, v101, v102\n"
      "v_max_u32_e32 v58, v58, v103\n"
      "v_cmp_eq_u32_e64 s[50:51], -1, v58\n"
      "v_max3_u32 v57, v104, v105, v106\n"
      "v_max_u32_e32 v57, v57, v107\n"
      "v_cmp_eq_u32_e64 s[52:53], -1, v57\n"
      "v_max3_u32 v58, v108, v109, v110\n"
      "v_max_u32_e32 v58, v58, v111\n"
      "v_cmp_eq_u32_e64 s[54:55], -1, v58\n"
      "v_max3_u32 v57, v112, v113, v114\n"
      "v_max3_u32 v58, v115, v116, v117\n"
      "v_max3_u32 v57, v57, v118, v119\n"
      "v_max_u32_e32 v57, v57, v58\n"
      "v_cmp_eq_u32_e64 s[56:57], -1, v57\n"
      "s_mov_b64 s[88:89], s[48:49]\n"
      "s_or_b64 s[88:89], s[88:89], s[50:51]\n"
      "s_or_b64 s[88:89], s[88:89], s[52:53]\n"
      "s_or_b64 s[88:89], s[88:89], s[54:55]\n"
      "s_or_b64 s[88:89], s[88:89], s[56:57]\n"
      "s_cmp_eq_u64 s[88:89], 0\n"
      "s_nop 0\n"
      "s_cbranch_scc1 A4_42_%=\n"
      "s_mov_b32 s90, 0x00010001\n"
      "s_mov_b32 s91, 0x00010001\n"
      "s_mov_b32 s88, 0\n"
      "v_cmp_ne_u32_e64 s[58:59], -1, v112\n"
      "v_cmp_ne_u32_e64 s[60:61], -1, v114\n"
      "v_cmp_ne_u32_e64 s[98:99], -1, v116\n"
      "v_cmp_ne_u32_e64 s[76:77], -1, v118\n"
      "s_and_b64 s[84:85], s[58:59], s[90:91]\n"
      "s_bcnt1_i32_b64 s89, s[84:85]\n"
      "s_add_u32 s88, s88, s89\n"
      "s_and_b64 s[84:85], s[60:61], s[90:91]\n"
      "s_bcnt1_i32_b64 s89, s[84:85]\n"
      "s_add_u32 s88, s88, s89\n"
      "s_and_b64 s[84:85], s[98:99], s[90:91]\n"
      "s_bcnt1_i32_b64 s89, s[84:85]\n"
      "s_add_u32 s88, s88, s89\n"
      "s_and_b64 s[84:85], s[76:77], s[90:91]\n"
      "s_bcnt1_i32_b64 s89, s[84:85]\n"
      "s_add_u32 s88, s88, s89\n"
      "s_cmp_ge_u32 s88, 0\n"
      "s_cselect_b64 s[92:93], -1, 0\n"
      "s_or_b64 s[58:59], s[58:59], s[92:93]\n"
      "s_and_b64 s[48:49], s[48:49], s[58:59]\n"
      "s_or_b64 s[60:61], s[60:61], s[92:93]\n"
      "s_and_b64 s[50:51], s[50:51], s[60:61]\n"
      "s_or_b64 s[98:99], s[98:99], s[92:93]\n"
      "s_and_b64 s[52:53], s[52:53], s[98:99]\n"
      "s_or_b64 s[76:77], s[76:77], s[92:93]\n"
      "s_and_b64 s[54:55], s[54:55], s[76:77]\n"
      "s_cmp_ge_u32 s88, 0\n"
      "s_nop 0\n"
      "s_cbranch_scc1 AF_48_%=\n"
      "s_sleep 4\n"
      "AF_48_%=:\n"
      "s_add_u32 s43, s43, 1\n"
      "s_cmp_lt_u32 s43, 0x80000\n"
      "s_waitcnt vmcnt(6)\n"
      "s_nop 0\n"
      "s_cbranch_scc1 A3_41_%=\n"
      "s_nop 0\n"
      "A4_42_%=:\n"
      "v_max3_f32 v56, v112, v114, v116\n"
      "v_max_f32_e32 v56, v56, v118\n"
      "v_mov_b32_e32 v57, v56\n"
      "s_nop 1\n"
      "v_permlane16_swap_b32_e32 v57, v56\n"
      "v_max_f32_e32 v56, v56, v57\n"
      "v_mov_b32_e32 v57, v56\n"
      "s_nop 1\n"
      "v_permlane32_swap_b32_e32 v57, v56\n"
      "v_max_f32_e32 v56, v56, v57\n"
      "v_sub_f32_e32 v60, v112, v56\n"
      "v_sub_f32_e32 v61, v114, v56\n"
      "v_sub_f32_e32 v62, v116, v56\n"
      "v_sub_f32_e32 v63, v118, v56\n"
      "v_mul_f32_e32 v60, s34, v60\n"
      "v_mul_f32_e32 v61, s34, v61\n"
      "v_mul_f32_e32 v62, s34, v62\n"
      "v_mul_f32_e32 v63, s34, v63\n"
      "v_exp_f32_e32 v60, v60\n"
      "v_exp_f32_e32 v61, v61\n"
      "v_exp_f32_e32 v62, v62\n"
      "v_exp_f32_e32 v63, v63\n"
      "v_mul_f32_e32 v59, v60, v113\n"
      "v_mul_f32_e32 v64, v60, v96\n"
      "v_mul_f32_e32 v65, v60, v97\n"
      "v_mul_f32_e32 v66, v60, v98\n"
      "v_mul_f32_e32 v67, v60, v99\n"
      "v_fmac_f32_e32 v59, v61, v115\n"
      "v_fmac_f32_e32 v64, v61, v100\n"
      "v_fmac_f32_e32 v65, v61, v101\n"
      "v_fmac_f32_e32 v66, v61, v102\n"
      "v_fmac_f32_e32 v67, v61, v103\n"
      "v_fmac_f32_e32 v59, v62, v117\n"
      "v_fmac_f32_e32 v64, v62, v104\n"
      "v_fmac_f32_e32 v65, v62, v105\n"
      "v_fmac_f32_e32 v66, v62, v106\n"
      "v_fmac_f32_e32 v67, v62, v107\n"
      "v_fmac_f32_e32 v59, v63, v119\n"
      "v_fmac_f32_e32 v64, v63, v108\n"
      "v_fmac_f32_e32 v65, v63, v109\n"
      "v_fmac_f32_e32 v66, v63, v110\n"
      "v_fmac_f32_e32 v67, v63, v111\n"
      "v_mov_b32_e32 v57, v59\n"
      "v_permlane32_swap_b32_e32 v64, v66\n"
      "v_permlane32_swap_b32_e32 v65, v67\n"
      "v_permlane32_swap_b32_e32 v57, v59\n"
      "v_add_f32_e32 v64, v64, v66\n"
      "v_add_f32_e32 v65, v65, v67\n"
      "v_add_f32_e32 v59, v59, v57\n"
      "s_nop 0\n"
      "v_permlane16_swap_b32_e32 v64, v65\n"
      "v_mov_b32_e32 v57, v59\n"
      "v_add_f32_e32 v64, v64, v65\n"
      "s_nop 0\n"
      "v_permlane16_swap_b32_e32 v57, v59\n"
      "v_add_f32_e32 v59, v59, v57\n"
      "v_rcp_f32_e32 v59, v59\n"
      "s_nop 0\n"
      "v_mul_f32_e32 v64, v64, v59\n"
      "v_cvt_pk_bf16_f32 v64, v64, v64\n"
      "ds_write_b16 v94, v64\n"
      "s_mov_b32 s88, 0x82800\n"
      "s_mul_i32 s89, s42, 14336\n"
      "s_add_u32 s88, s88, s89\n"
      "s_mul_i32 s89, s40, 896\n"
      "s_add_u32 s88, s88, s89\n"
      "s_mul_i32 s89, s41, 448\n"
      "s_add_u32 s88, s88, s89\n"
      "s_mul_i32 s89, s38, 128\n"
      "s_add_u32 s88, s88, s89\n"
      "s_add_u32 s44, s36, s88\n"
      "s_addc_u32 s45, s37, 0\n"
      "s_cmp_eq_u32 s38, 3\n"
      "s_cselect_b32 s88, 32, 0\n"
      "s_sub_u32 s44, s44, s88\n"
      "s_subb_u32 s45, s45, 0\n"
      "v_mbcnt_lo_u32_b32 v93, -1, 0\n"
      "v_mbcnt_hi_u32_b32 v93, -1, v93\n"
      "v_and_b32_e32 v92, 15, v93\n"
      "v_lshlrev_b32_e32 v92, 5, v92\n"
      "v_lshrrev_b32_e32 v93, 4, v93\n"
      "v_lshl_add_u32 v92, v93, 3, v92\n"
      "s_waitcnt lgkmcnt(0)\n"
      "s_barrier\n"
      "ds_read_b128 v[0:3], v156 offset:2048\n"
      "ds_read_b128 v[4:7], v156 offset:2112\n"
      "ds_read_b128 v[8:11], v156 offset:2176\n"
      "ds_read_b128 v[12:15], v156 offset:2240\n"
      "ds_read_b128 v[16:19], v156 offset:2304\n"
      "ds_read_b128 v[20:23], v156 offset:2368\n"
      "ds_read_b128 v[24:27], v156 offset:2432\n"
      "ds_read_b128 v[28:31], v156 offset:2496\n"
      "s_waitcnt lgkmcnt(7)\n"
      "v_mfma_f32_16x16x32_bf16 v[64:67], v[160:163], v[0:3], 0\n"
      "v_mfma_f32_16x16x32_bf16 v[68:71], v[164:167], v[0:3], 0\n"
      "v_mfma_f32_16x16x32_bf16 v[72:75], v[168:171], v[0:3], 0\n"
      "v_mfma_f32_16x16x32_bf16 v[76:79], v[172:175], v[0:3], 0\n"
      "s_waitcnt lgkmcnt(6)\n"
      "v_mfma_f32_16x16x32_bf16 v[64:67], v[176:179], v[4:7], v[64:67]\n"
      "v_mfma_f32_16x16x32_bf16 v[68:71], v[180:183], v[4:7], v[68:71]\n"
      "v_mfma_f32_16x16x32_bf16 v[72:75], v[184:187], v[4:7], v[72:75]\n"
      "v_mfma_f32_16x16x32_bf16 v[76:79], v[188:191], v[4:7], v[76:79]\n"
      "s_waitcnt lgkmcnt(5)\n"
      "v_mfma_f32_16x16x32_bf16 v[64:67], v[192:195], v[8:11], v[64:67]\n"
      "v_mfma_f32_16x16x32_bf16 v[68:71], v[196:199], v[8:11], v[68:71]\n"
      "v_mfma_f32_16x16x32_bf16 v[72:75], v[200:203], v[8:11], v[72:75]\n"
      "v_mfma_f32_16x16x32_bf16 v[76:79], v[204:207], v[8:11], v[76:79]\n"
      "s_waitcnt lgkmcnt(4)\n"
      "v_mfma_f32_16x16x32_bf16 v[64:67], v[208:211], v[12:15], v[64:67]\n"
      "v_mfma_f32_16x16x32_bf16 v[68:71], v[212:215], v[12:15], v[68:71]\n"
      "v_mfma_f32_16x16x32_bf16 v[72:75], v[216:219], v[12:15], v[72:75]\n"
      "v_mfma_f32_16x16x32_bf16 v[76:79], v[220:223], v[12:15], v[76:79]\n"
      "s_waitcnt lgkmcnt(3)\n"
      "v_mfma_f32_16x16x32_bf16 v[64:67], v[224:227], v[16:19], v[64:67]\n"
      "v_mfma_f32_16x16x32_bf16 v[68:71], v[228:231], v[16:19], v[68:71]\n"
      "v_mfma_f32_16x16x32_bf16 v[72:75], v[232:235], v[16:19], v[72:75]\n"
      "v_mfma_f32_16x16x32_bf16 v[76:79], v[236:239], v[16:19], v[76:79]\n"
      "s_waitcnt lgkmcnt(2)\n"
      "v_mfma_f32_16x16x32_bf16 v[64:67], v[240:243], v[20:23], v[64:67]\n"
      "v_mfma_f32_16x16x32_bf16 v[68:71], v[244:247], v[20:23], v[68:71]\n"
      "v_mfma_f32_16x16x32_bf16 v[72:75], v[248:251], v[20:23], v[72:75]\n"
      "v_mfma_f32_16x16x32_bf16 v[76:79], v[252:255], v[20:23], v[76:79]\n"
      "s_waitcnt lgkmcnt(1)\n"
      "v_mfma_f32_16x16x32_bf16 v[64:67], a[0:3], v[24:27], v[64:67]\n"
      "v_mfma_f32_16x16x32_bf16 v[68:71], a[4:7], v[24:27], v[68:71]\n"
      "v_mfma_f32_16x16x32_bf16 v[72:75], a[8:11], v[24:27], v[72:75]\n"
      "v_mfma_f32_16x16x32_bf16 v[76:79], a[12:15], v[24:27], v[76:79]\n"
      "s_waitcnt lgkmcnt(0)\n"
      "v_mfma_f32_16x16x32_bf16 v[64:67], a[16:19], v[28:31], v[64:67]\n"
      "v_mfma_f32_16x16x32_bf16 v[68:71], a[20:23], v[28:31], v[68:71]\n"
      "v_mfma_f32_16x16x32_bf16 v[72:75], a[24:27], v[28:31], v[72:75]\n"
      "v_mfma_f32_16x16x32_bf16 v[76:79], a[28:31], v[28:31], v[76:79]\n"
      "s_nop 4\n"
      "v_cvt_pk_bf16_f32 v64, v64, v65\n"
      "v_cvt_pk_bf16_f32 v65, v66, v67\n"
      "v_min_u32_e32 v64, -2, v64\n"
      "v_min_u32_e32 v65, -2, v65\n"
      "v_cvt_pk_bf16_f32 v82, v68, v69\n"
      "v_cvt_pk_bf16_f32 v83, v70, v71\n"
      "v_min_u32_e32 v82, -2, v82\n"
      "v_min_u32_e32 v83, -2, v83\n"
      "s_mov_b32 s46, 0x20002\n"
      "s_mov_b32 s47, 0x20002\n"
      "v_cndmask_b32_e64 v64, v64, v82, s[46:47]\n"
      "v_cndmask_b32_e64 v65, v65, v83, s[46:47]\n"
      "v_cvt_pk_bf16_f32 v84, v72, v73\n"
      "v_cvt_pk_bf16_f32 v85, v74, v75\n"
      "v_min_u32_e32 v84, -2, v84\n"
      "v_min_u32_e32 v85, -2, v85\n"
      "s_mov_b32 s46, 0x40004\n"
      "s_mov_b32 s47, 0x40004\n"
      "v_cndmask_b32_e64 v64, v64, v84, s[46:47]\n"
      "v_cndmask_b32_e64 v65, v65, v85, s[46:47]\n"
      "v_cvt_pk_bf16_f32 v86, v76, v77\n"
      "v_cvt_pk_bf16_f32 v87, v78, v79\n"
      "v_min_u32_e32 v86, -2, v86\n"
      "v_min_u32_e32 v87, -2, v87\n"
      "s_mov_b32 s46, 0x80008\n"
      "s_mov_b32 s47, 0x80008\n"
      "v_cndmask_b32_e64 v64, v64, v86, s[46:47]\n"
      "v_cndmask_b32_e64 v65, v65, v87, s[46:47]\n"
      "s_mov_b32 s46, 0x000f000f\n"
      "s_cmp_ge_u32 s38, 2\n"
      "s_cselect_b32 exec_lo, 0x00070007, s46\n"
      "s_mov_b32 exec_hi, exec_lo\n"
      "global_store_dwordx2 v92, v[64:65], s[44:45] sc1\n"
      "s_mov_b64 exec, -1\n"
      "v_mov_b32_e32 v56, v140\n"
      "v_mov_b32_e32 v140, v142\n"
      "v_mov_b32_e32 v142, v56\n"
      "v_mov_b32_e32 v56, v141\n"
      "v_mov_b32_e32 v141, v143\n"
      "v_mov_b32_e32 v143, v56\n"
      "s_mov_b32 s84, 0xcccccccc\n"
      "s_mov_b32 s85, 0xcccccccc\n"
      "v_cndmask_b32_e64 v84, 0, v159, s[64:65]\n"
      "v_cndmask_b32_e64 v85, 0, v159, s[66:67]\n"
      "v_cndmask_b32_e64 v86, 0, v159, s[68:69]\n"
      "v_cndmask_b32_e64 v87, 0, v159, s[70:71]\n"
      "s_mov_b32 s88, 0x82800\n"
      "s_mul_i32 s89, s42, 14336\n"
      "s_add_u32 s88, s88, s89\n"
      "s_add_u32 s44, s36, s88\n"
      "s_addc_u32 s45, s37, 0\n"
      "s_mov_b32 s94, 0x3fff3fff\n"
      "s_mov_b32 s95, 0x3fff3fff\n"
      "s_mov_b64 s[48:49], s[94:95]\n"
      "s_mov_b64 s[50:51], s[94:95]\n"
      "s_mov_b64 s[52:53], s[94:95]\n"
      "s_mov_b64 s[54:55], s[94:95]\n"
      "s_mov_b32 s43, 0\n"
      "s_sleep 8\n"
      "s_nop 0\n"
      "GT_49_%=:\n"
      "s_mov_b64 exec, s[48:49]\n"
      "s_nop 0\n"
      "s_cbranch_execz GS_52_%=\n"
      "s_add_u32 s46, s44, 0x0\n"
      "s_addc_u32 s47, s45, 0\n"
      "global_load_dwordx4 v[124:127], v154, s[46:47] sc1\n"
      "GS_52_%=:\n"
      "s_mov_b64 exec, s[50:51]\n"
      "s_nop 0\n"
      "s_cbranch_execz GS_53_%=\n"
      "s_add_u32 s46, s44, 0xe00\n"
      "s_addc_u32 s47, s45, 0\n"
      "global_load_dwordx4 v[128:131], v154, s[46:47] sc1\n"
      "GS_53_%=:\n"
      "s_mov_b64 exec, s[52:53]\n"
      "s_nop 0\n"
      "s_cbranch_execz GS_54_%=\n"
      "s_add_u32 s46, s44, 0x1c00\n"
      "s_addc_u32 s47, s45, 0\n"
      "global_load_dwordx4 v[132:135], v154, s[46:47] sc1\n"
      "GS_54_%=:\n"
      "s_mov_b64 exec, s[54:55]\n"
      "s_nop 0\n"
      "s_cbranch_execz GS_55_%=\n"
      "s_add_u32 s46, s44, 0x2a00\n"
      "s_addc_u32 s47, s45, 0\n"
      "global_load_dwordx4 v[136:139], v154, s[46:47] sc1\n"
      "GS_55_%=:\n"
      "s_mov_b64 exec, -1\n"
      "s_sleep 1\n"
      "v_max3_u32 v56, v124, v125, v126\n"
      "v_max_u32_e32 v56, v56, v127\n"
      "v_cmp_eq_u32_e64 s[48:49], -1, v56\n"
      "v_max3_u32 v57, v128, v129, v130\n"
      "v_max_u32_e32 v57, v57, v131\n"
      "v_cmp_eq_u32_e64 s[50:51], -1, v57\n"
      "v_max3_u32 v56, v132, v133, v134\n"
      "v_max_u32_e32 v56, v56, v135\n"
      "v_cmp_eq_u32_e64 s[52:53], -1, v56\n"
      "v_max3_u32 v57, v136, v137, v138\n"
      "v_max_u32_e32 v57, v57, v139\n"
      "v_cmp_eq_u32_e64 s[54:55], -1, v57\n"
      "s_and_b64 s[48:49], s[48:49], s[94:95]\n"
      "s_and_b64 s[50:51], s[50:51], s[94:95]\n"
      "s_and_b64 s[52:53], s[52:53], s[94:95]\n"
      "s_and_b64 s[54:55], s[54:55], s[94:95]\n"
      "s_mov_b64 s[88:89], s[48:49]\n"
      "s_or_b64 s[88:89], s[88:89], s[50:51]\n"
      "s_or_b64 s[88:89], s[88:89], s[52:53]\n"
      "s_or_b64 s[88:89], s[88:89], s[54:55]\n"
      "s_cmp_eq_u64 s[88:89], 0\n"
      "s_nop 0\n"
      "s_cbranch_scc1 GD_50_%=\n"
      "s_add_u32 s43, s43, 1\n"
      "s_cmp_lt_u32 s43, 0x80000\n"
      "s_waitcnt vmcnt(4)\n"
      "s_nop 0\n"
      "s_cbranch_scc1 GT_49_%=\n"
      "s_nop 0\n"
      "GD_50_%=:\n"
      "v_mfma_f32_16x16x32_bf16 v[88:91], v[124:127], v[84:87], 0\n"
      "v_mfma_f32_16x16x32_bf16 v[88:91], v[128:131], v[84:87], v[88:91]\n"
      "v_mfma_f32_16x16x32_bf16 v[88:91], v[132:135], v[84:87], v[88:91]\n"
      "v_mfma_f32_16x16x32_bf16 v[88:91], v[136:139], v[84:87], v[88:91]\n"
      "s_nop 7\n"
      "v_cndmask_b32_e64 v56, v88, v90, s[62:63]\n"
      "v_cndmask_b32_e64 v57, v89, v91, s[62:63]\n"
      "v_cvt_pk_bf16_f32 v58, v56, v57\n"
      "v_lshlrev_b32_e32 v56, 16, v58\n"
      "v_and_b32_e32 v57, 0xffff0000, v58\n"
      "v_add_f32_e32 v56, v140, v56\n"
      "v_add_f32_e32 v57, v141, v57\n"
      "v_cvt_pk_bf16_f32 v58, v56, v57\n"
      "s_cmp_ge_u32 s39, 28\n"
      "v_lshlrev_b32_e32 v140, 16, v58\n"
      "v_and_b32_e32 v141, 0xffff0000, v58\n"
      "s_nop 4\n"
      "s_cbranch_scc1 NOBC_56_%=\n"
      "s_mov_b32 exec_lo, -1\n"
      "s_mov_b32 exec_hi, 0x00ffffff\n"
      "ds_write_b32 v155, v58 offset:0\n"
      "s_mov_b64 exec, -1\n"
      "s_waitcnt lgkmcnt(0)\n"
      "s_barrier\n"
      "v_mbcnt_lo_u32_b32 v81, -1, 0\n"
      "v_mbcnt_hi_u32_b32 v81, -1, v81\n"
      "v_lshlrev_b32_e32 v81, 4, v81\n"
      "v_add_u32_e32 v76, 0x1e000, v81\n"
      "ds_read_b128 v[60:63], v76\n"
      "ds_read_b128 v[0:3], v156 offset:0\n"
      "ds_read_b128 v[4:7], v156 offset:64\n"
      "ds_read_b128 v[8:11], v156 offset:128\n"
      "ds_read_b128 v[12:15], v156 offset:256\n"
      "ds_read_b128 v[16:19], v156 offset:320\n"
      "ds_read_b128 v[20:23], v156 offset:384\n"
      "ds_read_b128 v[24:27], v156 offset:512\n"
      "ds_read_b128 v[28:31], v156 offset:576\n"
      "ds_read_b128 v[32:35], v156 offset:640\n"
      "ds_read_b128 v[36:39], v156 offset:768\n"
      "ds_read_b128 v[40:43], v156 offset:832\n"
      "ds_read_b128 v[44:47], v156 offset:896\n"
      "ds_read_b128 v[48:51], v157 offset:192\n"
      "ds_read_b128 v[52:55], v157 offset:704\n"
      "s_mul_i32 s88, s38, 14336\n"
      "v_add_u32_e32 v82, s88, v81\n"
      "s_mov_b32 s89, 0x3f803f80\n"
      "s_waitcnt lgkmcnt(13)\n"
      "v_mfma_f32_16x16x32_bf16 v[64:67], a[144:147], v[0:3], 0\n"
      "v_dot2_f32_bf16 v72, v60, s89, 0\n"
      "v_dot2_f32_bf16 v74, v60, v60, 0\n"
      "v_mfma_f32_16x16x32_bf16 v[68:71], a[200:203], v[0:3], 0\n"
      "v_dot2_f32_bf16 v73, v62, s89, 0\n"
      "v_dot2_f32_bf16 v75, v62, v62, 0\n"
      "ds_read_b128 v[0:3], v82 offset:0\n"
      "s_waitcnt lgkmcnt(13)\n"
      "v_mfma_f32_16x16x32_bf16 v[64:67], a[148:151], v[4:7], v[64:67]\n"
      "v_dot2_f32_bf16 v72, v61, s89, v72\n"
      "v_dot2_f32_bf16 v74, v61, v61, v74\n"
      "v_mfma_f32_16x16x32_bf16 v[68:71], a[204:207], v[4:7], v[68:71]\n"
      "v_dot2_f32_bf16 v73, v63, s89, v73\n"
      "v_dot2_f32_bf16 v75, v63, v63, v75\n"
      "ds_read_b128 v[4:7], v82 offset:1024\n"
      "s_waitcnt lgkmcnt(13)\n"
      "v_mfma_f32_16x16x32_bf16 v[64:67], a[152:155], v[8:11], v[64:67]\n"
      "s_nop 0\n"
      "s_nop 0\n"
      "v_mfma_f32_16x16x32_bf16 v[68:71], a[208:211], v[8:11], v[68:71]\n"
      "v_add_f32_e32 v72, v72, v73\n"
      "v_add_f32_e32 v74, v74, v75\n"
      "ds_read_b128 v[8:11], v82 offset:2048\n"
      "s_waitcnt lgkmcnt(13)\n"
      "v_mfma_f32_16x16x32_bf16 v[64:67], a[156:159], v[12:15], v[64:67]\n"
      "v_add_f32_dpp v72, v72, v72 row_ror:8 row_mask:0xf bank_mask:0xf\n"
      "v_add_f32_dpp v74, v74, v74 row_ror:8 row_mask:0xf bank_mask:0xf\n"
      "v_mfma_f32_16x16x32_bf16 v[68:71], a[212:215], v[12:15], v[68:71]\n"
      "v_add_f32_dpp v72, v72, v72 row_ror:4 row_mask:0xf bank_mask:0xf\n"
      "v_add_f32_dpp v74, v74, v74 row_ror:4 row_mask:0xf bank_mask:0xf\n"
      "ds_read_b128 v[12:15], v82 offset:3072\n"
      "s_waitcnt lgkmcnt(13)\n"
      "v_mfma_f32_16x16x32_bf16 v[64:67], a[160:163], v[16:19], v[64:67]\n"
      "v_add_f32_dpp v72, v72, v72 row_ror:2 row_mask:0xf bank_mask:0xf\n"
      "v_add_f32_dpp v74, v74, v74 row_ror:2 row_mask:0xf bank_mask:0xf\n"
      "v_mfma_f32_16x16x32_bf16 v[68:71], a[216:219], v[16:19], v[68:71]\n"
      "v_add_f32_dpp v72, v72, v72 row_ror:1 row_mask:0xf bank_mask:0xf\n"
      "v_add_f32_dpp v74, v74, v74 row_ror:1 row_mask:0xf bank_mask:0xf\n"
      "ds_read_b128 v[16:19], v82 offset:4096\n"
      "s_waitcnt lgkmcnt(13)\n"
      "v_mfma_f32_16x16x32_bf16 v[64:67], a[164:167], v[20:23], v[64:67]\n"
      "v_mov_b32_e32 v76, v72\n"
      "v_mov_b32_e32 v77, v74\n"
      "v_mfma_f32_16x16x32_bf16 v[68:71], a[220:223], v[20:23], v[68:71]\n"
      "v_permlane16_swap_b32_e32 v76, v72\n"
      "v_permlane16_swap_b32_e32 v77, v74\n"
      "ds_read_b128 v[20:23], v82 offset:5120\n"
      "s_waitcnt lgkmcnt(13)\n"
      "v_mfma_f32_16x16x32_bf16 v[64:67], a[168:171], v[24:27], v[64:67]\n"
      "v_add_f32_e32 v72, v72, v76\n"
      "v_add_f32_e32 v74, v74, v77\n"
      "v_mfma_f32_16x16x32_bf16 v[68:71], a[224:227], v[24:27], v[68:71]\n"
      "v_mov_b32_e32 v76, v72\n"
      "v_mov_b32_e32 v77, v74\n"
      "ds_read_b128 v[24:27], v82 offset:6144\n"
      "s_waitcnt lgkmcnt(13)\n"
      "v_mfma_f32_16x16x32_bf16 v[64:67], a[172:175], v[28:31], v[64:67]\n"
      "v_permlane32_swap_b32_e32 v76, v72\n"
      "v_permlane32_swap_b32_e32 v77, v74\n"
      "v_mfma_f32_16x16x32_bf16 v[68:71], a[228:231], v[28:31], v[68:71]\n"
      "v_add_f32_e32 v72, v72, v76\n"
      "v_add_f32_e32 v74, v74, v77\n"
      "ds_read_b128 v[28:31], v82 offset:7168\n"
      "s_waitcnt lgkmcnt(13)\n"
      "v_mfma_f32_16x16x32_bf16 v[64:67], a[176:179], v[32:35], v[64:67]\n"
      "v_mul_f32_e32 v78, 0x3b124925, v72\n"
      "v_mul_f32_e32 v74, 0x3b124925, v74\n"
      "v_mfma_f32_16x16x32_bf16 v[68:71], a[232:235], v[32:35], v[68:71]\n"
      "v_fma_f32 v74, -v78, v78, v74\n"
      "v_cvt_pk_bf16_f32 v79, v78, v78\n"
      "ds_read_b128 v[32:35], v82 offset:8192\n"
      "s_waitcnt lgkmcnt(13)\n"
      "v_mfma_f32_16x16x32_bf16 v[64:67], a[180:183], v[36:39], v[64:67]\n"
      "v_max_f32_e32 v74, 0, v74\n"
      "v_and_b32_e32 v79, 0xffff0000, v79\n"
      "v_mfma_f32_16x16x32_bf16 v[68:71], a[236:239], v[36:39], v[68:71]\n"
      "v_cvt_pk_bf16_f32 v74, v74, v74\n"
      "v_xor_b32_e32 v79, 0x80000000, v79\n"
      "ds_read_b128 v[36:39], v82 offset:9216\n"
      "s_waitcnt lgkmcnt(13)\n"
      "v_mfma_f32_16x16x32_bf16 v[64:67], a[184:187], v[40:43], v[64:67]\n"
      "v_and_b32_e32 v74, 0xffff0000, v74\n"
      "v_add_f32_e32 v74, 0x3727c5ac, v74\n"
      "v_mfma_f32_16x16x32_bf16 v[68:71], a[240:243], v[40:43], v[68:71]\n"
      "v_cvt_pk_bf16_f32 v74, v74, v74\n"
      "v_and_b32_e32 v74, 0xffff0000, v74\n"
      "ds_read_b128 v[40:43], v82 offset:10240\n"
      "s_waitcnt lgkmcnt(13)\n"
      "v_mfma_f32_16x16x32_bf16 v[64:67], a[188:191], v[44:47], v[64:67]\n"
      "v_sqrt_f32_e32 v74, v74\n"
      "s_nop 0\n"
      "v_mfma_f32_16x16x32_bf16 v[68:71], a[244:247], v[44:47], v[68:71]\n"
      "v_cvt_pk_bf16_f32 v74, v74, v74\n"
      "v_and_b32_e32 v74, 0xffff0000, v74\n"
      "ds_read_b128 v[44:47], v82 offset:11264\n"
      "s_waitcnt lgkmcnt(13)\n"
      "v_mfma_f32_16x16x32_bf16 v[64:67], a[192:195], v[48:51], v[64:67]\n"
      "v_rcp_f32_e32 v80, v74\n"
      "v_mfma_f32_16x16x32_bf16 v[68:71], a[248:251], v[48:51], v[68:71]\n"
      "ds_read_b128 v[48:51], v82 offset:12288\n"
      "s_waitcnt lgkmcnt(13)\n"
      "v_mfma_f32_16x16x32_bf16 v[64:67], a[196:199], v[52:55], v[64:67]\n"
      "v_mfma_f32_16x16x32_bf16 v[68:71], a[252:255], v[52:55], v[68:71]\n"
      "ds_read_b128 v[52:55], v82 offset:13312\n"
      "v_mbcnt_lo_u32_b32 v57, -1, 0\n"
      "v_mbcnt_hi_u32_b32 v57, -1, v57\n"
      "v_lshrrev_b32_e32 v83, 4, v57\n"
      "v_lshlrev_b32_e32 v83, 3, v83\n"
      "v_and_b32_e32 v57, 3, v57\n"
      "v_lshl_add_u32 v83, v57, 1, v83\n"
      "s_lshl_b32 s88, s38, 5\n"
      "s_add_u32 s88, s88, 0x1ea00\n"
      "v_add_u32_e32 v83, s88, v83\n"
      "v_cndmask_b32_e64 v56, v64, v65, s[96:97]\n"
      "v_cndmask_b32_e64 v57, v66, v67, s[96:97]\n"
      "v_cndmask_b32_e64 v58, v68, v69, s[96:97]\n"
      "v_cndmask_b32_e64 v59, v70, v71, s[96:97]\n"
      "v_cndmask_b32_e64 v56, v56, v57, s[84:85]\n"
      "v_cndmask_b32_e64 v58, v58, v59, s[84:85]\n"
      "v_fma_f32 v56, v79, v152, v56\n"
      "v_fma_f32 v58, v79, v153, v58\n"
      "v_mul_f32_e32 v56, v80, v56\n"
      "v_mul_f32_e32 v58, v80, v58\n"
      "v_cvt_pk_bf16_f32 v57, v56, v58\n"
      "v_lshlrev_b32_e32 v56, 16, v57\n"
      "v_and_b32_e32 v58, 0xffff0000, v57\n"
      "v_mul_f32_e32 v56, v56, v58\n"
      "v_cvt_pk_bf16_f32 v56, v56, v56\n"
      "s_mov_b32 exec_lo, 0xf000f\n"
      "s_mov_b32 exec_hi, 0xf000f\n"
      "ds_write_b16 v83, v56\n"
      "s_mov_b64 exec, -1\n"
      "s_mov_b32 s88, 0x90800\n"
      "s_mul_i32 s89, s42, 28672\n"
      "s_add_u32 s88, s88, s89\n"
      "s_mul_i32 s89, s39, 896\n"
      "s_add_u32 s88, s88, s89\n"
      "s_mul_i32 s89, s38, 224\n"
      "s_add_u32 s88, s88, s89\n"
      "s_add_u32 s44, s36, s88\n"
      "s_addc_u32 s45, s37, 0\n"
      "v_mbcnt_lo_u32_b32 v93, -1, 0\n"
      "v_mbcnt_hi_u32_b32 v93, -1, v93\n"
      "v_and_b32_e32 v92, 15, v93\n"
      "v_lshlrev_b32_e32 v92, 5, v92\n"
      "v_lshrrev_b32_e32 v93, 4, v93\n"
      "v_lshl_add_u32 v92, v93, 3, v92\n"
      "s_waitcnt lgkmcnt(0)\n"
      "s_barrier\n"
      "ds_read_b128 v[56:59], v156 offset:2560\n"
      "ds_read_b128 v[60:63], v156 offset:2624\n"
      "s_waitcnt lgkmcnt(1)\n"
      "v_mfma_f32_16x16x32_bf16 v[64:67], v[0:3], v[56:59], 0\n"
      "v_mfma_f32_16x16x32_bf16 v[68:71], v[8:11], v[56:59], 0\n"
      "s_waitcnt lgkmcnt(0)\n"
      "v_mfma_f32_16x16x32_bf16 v[64:67], v[4:7], v[60:63], v[64:67]\n"
      "v_mfma_f32_16x16x32_bf16 v[72:75], v[16:19], v[56:59], 0\n"
      "v_mfma_f32_16x16x32_bf16 v[68:71], v[12:15], v[60:63], v[68:71]\n"
      "v_mfma_f32_16x16x32_bf16 v[76:79], v[24:27], v[56:59], 0\n"
      "s_nop 4\n"
      "v_cvt_pk_bf16_f32 v64, v64, v65\n"
      "v_cvt_pk_bf16_f32 v65, v66, v67\n"
      "v_min_u32_e32 v64, -2, v64\n"
      "v_min_u32_e32 v65, -2, v65\n"
      "v_mfma_f32_16x16x32_bf16 v[72:75], v[20:23], v[60:63], v[72:75]\n"
      "v_mfma_f32_16x16x32_bf16 v[80:83], v[32:35], v[56:59], 0\n"
      "v_cvt_pk_bf16_f32 v94, v68, v69\n"
      "v_cvt_pk_bf16_f32 v95, v70, v71\n"
      "v_min_u32_e32 v94, -2, v94\n"
      "v_min_u32_e32 v95, -2, v95\n"
      "s_mov_b32 s46, 0x20002\n"
      "s_mov_b32 s47, 0x20002\n"
      "v_cndmask_b32_e64 v64, v64, v94, s[46:47]\n"
      "v_cndmask_b32_e64 v65, v65, v95, s[46:47]\n"
      "v_mfma_f32_16x16x32_bf16 v[76:79], v[28:31], v[60:63], v[76:79]\n"
      "v_mfma_f32_16x16x32_bf16 v[84:87], v[40:43], v[56:59], 0\n"
      "v_cvt_pk_bf16_f32 v94, v72, v73\n"
      "v_cvt_pk_bf16_f32 v95, v74, v75\n"
      "v_min_u32_e32 v94, -2, v94\n"
      "v_min_u32_e32 v95, -2, v95\n"
      "s_mov_b32 s46, 0x40004\n"
      "s_mov_b32 s47, 0x40004\n"
      "v_cndmask_b32_e64 v64, v64, v94, s[46:47]\n"
      "v_cndmask_b32_e64 v65, v65, v95, s[46:47]\n"
      "v_mfma_f32_16x16x32_bf16 v[80:83], v[36:39], v[60:63], v[80:83]\n"
      "v_mfma_f32_16x16x32_bf16 v[88:91], v[48:51], v[56:59], 0\n"
      "v_cvt_pk_bf16_f32 v94, v76, v77\n"
      "v_cvt_pk_bf16_f32 v95, v78, v79\n"
      "v_min_u32_e32 v94, -2, v94\n"
      "v_min_u32_e32 v95, -2, v95\n"
      "s_mov_b32 s46, 0x80008\n"
      "s_mov_b32 s47, 0x80008\n"
      "v_cndmask_b32_e64 v64, v64, v94, s[46:47]\n"
      "v_cndmask_b32_e64 v65, v65, v95, s[46:47]\n"
      "v_mfma_f32_16x16x32_bf16 v[84:87], v[44:47], v[60:63], v[84:87]\n"
      "v_mfma_f32_16x16x32_bf16 v[88:91], v[52:55], v[60:63], v[88:91]\n"
      "v_cvt_pk_bf16_f32 v94, v80, v81\n"
      "v_cvt_pk_bf16_f32 v95, v82, v83\n"
      "v_min_u32_e32 v94, -2, v94\n"
      "v_min_u32_e32 v95, -2, v95\n"
      "s_mov_b32 s46, 0x100010\n"
      "s_mov_b32 s47, 0x100010\n"
      "v_cndmask_b32_e64 v64, v64, v94, s[46:47]\n"
      "v_cndmask_b32_e64 v65, v65, v95, s[46:47]\n"
      "v_cvt_pk_bf16_f32 v94, v84, v85\n"
      "v_cvt_pk_bf16_f32 v95, v86, v87\n"
      "v_min_u32_e32 v94, -2, v94\n"
      "v_min_u32_e32 v95, -2, v95\n"
      "s_mov_b32 s46, 0x200020\n"
      "s_mov_b32 s47, 0x200020\n"
      "v_cndmask_b32_e64 v64, v64, v94, s[46:47]\n"
      "v_cndmask_b32_e64 v65, v65, v95, s[46:47]\n"
      "v_cvt_pk_bf16_f32 v94, v88, v89\n"
      "v_cvt_pk_bf16_f32 v95, v90, v91\n"
      "v_min_u32_e32 v94, -2, v94\n"
      "v_min_u32_e32 v95, -2, v95\n"
      "s_mov_b32 s46, 0x400040\n"
      "s_mov_b32 s47, 0x400040\n"
      "v_cndmask_b32_e64 v64, v64, v94, s[46:47]\n"
      "v_cndmask_b32_e64 v65, v65, v95, s[46:47]\n"
      "s_mov_b32 exec_lo, 0x7f007f\n"
      "s_mov_b32 exec_hi, 0x7f007f\n"
      "global_store_dwordx2 v92, v[64:65], s[44:45] sc1\n"
      "s_mov_b64 exec, -1\n"
      "s_nop 0\n"
      "NOBC_56_%=:\n"
      "v_mov_b32_e32 v96, -1\n"
      "v_mov_b32_e32 v97, -1\n"
      "v_mov_b32_e32 v98, -1\n"
      "v_mov_b32_e32 v99, -1\n"
      "v_mov_b32_e32 v100, -1\n"
      "v_mov_b32_e32 v101, -1\n"
      "v_mov_b32_e32 v102, -1\n"
      "v_mov_b32_e32 v103, -1\n"
      "v_mov_b32_e32 v104, -1\n"
      "v_mov_b32_e32 v105, -1\n"
      "v_mov_b32_e32 v106, -1\n"
      "v_mov_b32_e32 v107, -1\n"
      "v_mov_b32_e32 v108, -1\n"
      "v_mov_b32_e32 v109, -1\n"
      "v_mov_b32_e32 v110, -1\n"
      "v_mov_b32_e32 v111, -1\n"
      "v_mov_b32_e32 v112, -1\n"
      "v_mov_b32_e32 v113, -1\n"
      "v_mov_b32_e32 v114, -1\n"
      "v_mov_b32_e32 v115, -1\n"
      "v_mov_b32_e32 v116, -1\n"
      "v_mov_b32_e32 v117, -1\n"
      "v_mov_b32_e32 v118, -1\n"
      "v_mov_b32_e32 v119, -1\n"
      "v_mov_b32_e32 v120, -1\n"
      "v_mov_b32_e32 v121, -1\n"
      "v_mov_b32_e32 v122, -1\n"
      "v_mov_b32_e32 v123, -1\n"
      "v_cndmask_b32_e64 v84, 0, v159, s[64:65]\n"
      "v_cndmask_b32_e64 v85, 0, v159, s[66:67]\n"
      "v_cndmask_b32_e64 v86, 0, v159, s[68:69]\n"
      "v_cndmask_b32_e64 v87, 0, v159, s[70:71]\n"
      "s_mov_b32 s88, 0x90800\n"
      "s_mul_i32 s89, s42, 28672\n"
      "s_add_u32 s88, s88, s89\n"
      "s_add_u32 s44, s36, s88\n"
      "s_addc_u32 s45, s37, 0\n"
      "s_mov_b32 s94, 0x3fff3fff\n"
      "s_mov_b32 s95, 0x3fff3fff\n"
      "s_mov_b64 s[48:49], s[94:95]\n"
      "s_mov_b64 s[50:51], s[94:95]\n"
      "s_mov_b64 s[52:53], s[94:95]\n"
      "s_mov_b64 s[54:55], s[94:95]\n"
      "s_mov_b64 s[56:57], s[94:95]\n"
      "s_mov_b64 s[58:59], s[94:95]\n"
      "s_mov_b64 s[60:61], s[94:95]\n"
      "s_mov_b32 s43, 0\n"
      "s_sleep 8\n"
      "s_nop 0\n"
      "GT_57_%=:\n"
      "s_mov_b64 exec, s[48:49]\n"
      "s_nop 0\n"
      "s_cbranch_execz GS_60_%=\n"
      "s_add_u32 s46, s44, 0x0\n"
      "s_addc_u32 s47, s45, 0\n"
      "global_load_dwordx4 v[96:99], v154, s[46:47] sc1\n"
      "GS_60_%=:\n"
      "s_mov_b64 exec, s[50:51]\n"
      "s_nop 0\n"
      "s_cbranch_execz GS_61_%=\n"
      "s_add_u32 s46, s44, 0xe00\n"
      "s_addc_u32 s47, s45, 0\n"
      "global_load_dwordx4 v[100:103], v154, s[46:47] sc1\n"
      "GS_61_%=:\n"
      "s_mov_b64 exec, s[52:53]\n"
      "s_nop 0\n"
      "s_cbranch_execz GS_62_%=\n"
      "s_add_u32 s46, s44, 0x1c00\n"
      "s_addc_u32 s47, s45, 0\n"
      "global_load_dwordx4 v[104:107], v154, s[46:47] sc1\n"
      "GS_62_%=:\n"
      "s_mov_b64 exec, s[54:55]\n"
      "s_nop 0\n"
      "s_cbranch_execz GS_63_%=\n"
      "s_add_u32 s46, s44, 0x2a00\n"
      "s_addc_u32 s47, s45, 0\n"
      "global_load_dwordx4 v[108:111], v154, s[46:47] sc1\n"
      "GS_63_%=:\n"
      "s_mov_b64 exec, s[56:57]\n"
      "s_nop 0\n"
      "s_cbranch_execz GS_64_%=\n"
      "s_add_u32 s46, s44, 0x3800\n"
      "s_addc_u32 s47, s45, 0\n"
      "global_load_dwordx4 v[112:115], v154, s[46:47] sc1\n"
      "GS_64_%=:\n"
      "s_mov_b64 exec, s[58:59]\n"
      "s_nop 0\n"
      "s_cbranch_execz GS_65_%=\n"
      "s_add_u32 s46, s44, 0x4600\n"
      "s_addc_u32 s47, s45, 0\n"
      "global_load_dwordx4 v[116:119], v154, s[46:47] sc1\n"
      "GS_65_%=:\n"
      "s_mov_b64 exec, s[60:61]\n"
      "s_nop 0\n"
      "s_cbranch_execz GS_66_%=\n"
      "s_add_u32 s46, s44, 0x5400\n"
      "s_addc_u32 s47, s45, 0\n"
      "global_load_dwordx4 v[120:123], v154, s[46:47] sc1\n"
      "GS_66_%=:\n"
      "s_mov_b64 exec, -1\n"
      "s_sleep 1\n"
      "v_max3_u32 v56, v96, v97, v98\n"
      "v_max_u32_e32 v56, v56, v99\n"
      "v_cmp_eq_u32_e64 s[48:49], -1, v56\n"
      "v_max3_u32 v57, v100, v101, v102\n"
      "v_max_u32_e32 v57, v57, v103\n"
      "v_cmp_eq_u32_e64 s[50:51], -1, v57\n"
      "v_max3_u32 v56, v104, v105, v106\n"
      "v_max_u32_e32 v56, v56, v107\n"
      "v_cmp_eq_u32_e64 s[52:53], -1, v56\n"
      "v_max3_u32 v57, v108, v109, v110\n"
      "v_max_u32_e32 v57, v57, v111\n"
      "v_cmp_eq_u32_e64 s[54:55], -1, v57\n"
      "v_max3_u32 v56, v112, v113, v114\n"
      "v_max_u32_e32 v56, v56, v115\n"
      "v_cmp_eq_u32_e64 s[56:57], -1, v56\n"
      "v_max3_u32 v57, v116, v117, v118\n"
      "v_max_u32_e32 v57, v57, v119\n"
      "v_cmp_eq_u32_e64 s[58:59], -1, v57\n"
      "v_max3_u32 v56, v120, v121, v122\n"
      "v_max_u32_e32 v56, v56, v123\n"
      "v_cmp_eq_u32_e64 s[60:61], -1, v56\n"
      "s_and_b64 s[48:49], s[48:49], s[94:95]\n"
      "s_and_b64 s[50:51], s[50:51], s[94:95]\n"
      "s_and_b64 s[52:53], s[52:53], s[94:95]\n"
      "s_and_b64 s[54:55], s[54:55], s[94:95]\n"
      "s_and_b64 s[56:57], s[56:57], s[94:95]\n"
      "s_and_b64 s[58:59], s[58:59], s[94:95]\n"
      "s_and_b64 s[60:61], s[60:61], s[94:95]\n"
      "s_mov_b64 s[88:89], s[48:49]\n"
      "s_or_b64 s[88:89], s[88:89], s[50:51]\n"
      "s_or_b64 s[88:89], s[88:89], s[52:53]\n"
      "s_or_b64 s[88:89], s[88:89], s[54:55]\n"
      "s_or_b64 s[88:89], s[88:89], s[56:57]\n"
      "s_or_b64 s[88:89], s[88:89], s[58:59]\n"
      "s_or_b64 s[88:89], s[88:89], s[60:61]\n"
      "s_cmp_eq_u64 s[88:89], 0\n"
      "s_nop 0\n"
      "s_cbranch_scc1 GD_58_%=\n"
      "s_add_u32 s43, s43, 1\n"
      "s_cmp_lt_u32 s43, 0x80000\n"
      "s_waitcnt vmcnt(7)\n"
      "s_nop 0\n"
      "s_cbranch_scc1 GT_57_%=\n"
      "s_nop 0\n"
      "GD_58_%=:\n"
      "v_mfma_f32_16x16x32_bf16 v[88:91], v[96:99], v[84:87], 0\n"
      "v_mfma_f32_16x16x32_bf16 v[88:91], v[100:103], v[84:87], v[88:91]\n"
      "v_mfma_f32_16x16x32_bf16 v[88:91], v[104:107], v[84:87], v[88:91]\n"
      "v_mfma_f32_16x16x32_bf16 v[88:91], v[108:111], v[84:87], v[88:91]\n"
      "v_mfma_f32_16x16x32_bf16 v[88:91], v[112:115], v[84:87], v[88:91]\n"
      "v_mfma_f32_16x16x32_bf16 v[88:91], v[116:119], v[84:87], v[88:91]\n"
      "v_mfma_f32_16x16x32_bf16 v[88:91], v[120:123], v[84:87], v[88:91]\n"
      "s_nop 7\n"
      "v_cndmask_b32_e64 v56, v88, v90, s[62:63]\n"
      "v_cndmask_b32_e64 v57, v89, v91, s[62:63]\n"
      "v_cvt_pk_bf16_f32 v58, v56, v57\n"
      "v_lshlrev_b32_e32 v56, 16, v58\n"
      "v_and_b32_e32 v57, 0xffff0000, v58\n"
      "v_add_f32_e32 v56, v140, v56\n"
      "v_add_f32_e32 v57, v141, v57\n"
      "v_cvt_pk_bf16_f32 v58, v56, v57\n"
      "s_mov_b32 exec_lo, -1\n"
      "s_mov_b32 exec_hi, 0x00ffffff\n"
      "ds_write_b32 v155, v58 offset:0\n"
      "s_mov_b64 exec, -1\n"
      "v_lshlrev_b32_e32 v140, 16, v58\n"
      "v_and_b32_e32 v141, 0xffff0000, v58\n"
      "v_mbcnt_lo_u32_b32 v93, -1, 0\n"
      "v_mbcnt_hi_u32_b32 v93, -1, v93\n"
      "v_and_b32_e32 v92, 15, v93\n"
      "v_lshlrev_b32_e32 v92, 6, v92\n"
      "v_and_b32_e32 v93, 48, v93\n"
      "v_add_u32_e32 v92, v92, v93\n"
      "s_add_u32 s88, s42, 1\n"
      "s_lshl_b32 s88, s88, 17\n"
      "s_lshl_b32 s89, s40, 13\n"
      "s_add_u32 s88, s88, s89\n"
      "s_lshl_b32 s89, s41, 10\n"
      "s_add_u32 s88, s88, s89\n"
      "s_lshr_b32 s89, s38, 1\n"
      "s_lshl_b32 s89, s89, 9\n"
      "s_add_u32 s88, s88, s89\n"
      "s_and_b32 s89, s38, 1\n"
      "s_lshl_b32 s89, s89, 12\n"
      "s_add_u32 s88, s88, s89\n"
      "s_add_u32 s88, s88, 0x800\n"
      "s_add_u32 s46, s36, s88\n"
      "s_addc_u32 s47, s37, 0\n"
      "s_mov_b64 s[44:45], s[46:47]\n"
      "s_waitcnt lgkmcnt(0)\n"
      "s_barrier\n"
      "v_mbcnt_lo_u32_b32 v81, -1, 0\n"
      "v_mbcnt_hi_u32_b32 v81, -1, v81\n"
      "v_lshlrev_b32_e32 v81, 4, v81\n"
      "v_add_u32_e32 v76, 0x1e000, v81\n"
      "ds_read_b128 v[60:63], v76\n"
      "ds_read_b128 v[0:3], v156 offset:0\n"
      "ds_read_b128 v[4:7], v156 offset:64\n"
      "ds_read_b128 v[8:11], v156 offset:128\n"
      "ds_read_b128 v[12:15], v156 offset:256\n"
      "ds_read_b128 v[16:19], v156 offset:320\n"
      "ds_read_b128 v[20:23], v156 offset:384\n"
      "ds_read_b128 v[24:27], v156 offset:512\n"
      "ds_read_b128 v[28:31], v156 offset:576\n"
      "ds_read_b128 v[32:35], v156 offset:640\n"
      "ds_read_b128 v[36:39], v156 offset:768\n"
      "ds_read_b128 v[40:43], v156 offset:832\n"
      "ds_read_b128 v[44:47], v156 offset:896\n"
      "ds_read_b128 v[48:51], v157 offset:192\n"
      "ds_read_b128 v[52:55], v157 offset:704\n"
      "s_mov_b32 s89, 0x3f803f80\n"
      "s_waitcnt lgkmcnt(13)\n"
      "v_mfma_f32_16x16x32_bf16 v[64:67], a[32:35], v[0:3], 0\n"
      "v_dot2_f32_bf16 v72, v60, s89, 0\n"
      "v_dot2_f32_bf16 v74, v60, v60, 0\n"
      "v_mfma_f32_16x16x32_bf16 v[68:71], a[88:91], v[0:3], 0\n"
      "v_dot2_f32_bf16 v73, v62, s89, 0\n"
      "v_dot2_f32_bf16 v75, v62, v62, 0\n"
      "s_waitcnt lgkmcnt(12)\n"
      "v_mfma_f32_16x16x32_bf16 v[64:67], a[36:39], v[4:7], v[64:67]\n"
      "v_dot2_f32_bf16 v72, v61, s89, v72\n"
      "v_dot2_f32_bf16 v74, v61, v61, v74\n"
      "v_mfma_f32_16x16x32_bf16 v[68:71], a[92:95], v[4:7], v[68:71]\n"
      "v_dot2_f32_bf16 v73, v63, s89, v73\n"
      "v_dot2_f32_bf16 v75, v63, v63, v75\n"
      "s_waitcnt lgkmcnt(11)\n"
      "v_mfma_f32_16x16x32_bf16 v[64:67], a[40:43], v[8:11], v[64:67]\n"
      "s_nop 0\n"
      "s_nop 0\n"
      "v_mfma_f32_16x16x32_bf16 v[68:71], a[96:99], v[8:11], v[68:71]\n"
      "v_add_f32_e32 v72, v72, v73\n"
      "v_add_f32_e32 v74, v74, v75\n"
      "s_waitcnt lgkmcnt(10)\n"
      "v_mfma_f32_16x16x32_bf16 v[64:67], a[44:47], v[12:15], v[64:67]\n"
      "v_add_f32_dpp v72, v72, v72 row_ror:8 row_mask:0xf bank_mask:0xf\n"
      "v_add_f32_dpp v74, v74, v74 row_ror:8 row_mask:0xf bank_mask:0xf\n"
      "v_mfma_f32_16x16x32_bf16 v[68:71], a[100:103], v[12:15], v[68:71]\n"
      "v_add_f32_dpp v72, v72, v72 row_ror:4 row_mask:0xf bank_mask:0xf\n"
      "v_add_f32_dpp v74, v74, v74 row_ror:4 row_mask:0xf bank_mask:0xf\n"
      "s_waitcnt lgkmcnt(9)\n"
      "v_mfma_f32_16x16x32_bf16 v[64:67], a[48:51], v[16:19], v[64:67]\n"
      "v_add_f32_dpp v72, v72, v72 row_ror:2 row_mask:0xf bank_mask:0xf\n"
      "v_add_f32_dpp v74, v74, v74 row_ror:2 row_mask:0xf bank_mask:0xf\n"
      "v_mfma_f32_16x16x32_bf16 v[68:71], a[104:107], v[16:19], v[68:71]\n"
      "v_add_f32_dpp v72, v72, v72 row_ror:1 row_mask:0xf bank_mask:0xf\n"
      "v_add_f32_dpp v74, v74, v74 row_ror:1 row_mask:0xf bank_mask:0xf\n"
      "s_waitcnt lgkmcnt(8)\n"
      "v_mfma_f32_16x16x32_bf16 v[64:67], a[52:55], v[20:23], v[64:67]\n"
      "v_mov_b32_e32 v76, v72\n"
      "v_mov_b32_e32 v77, v74\n"
      "v_mfma_f32_16x16x32_bf16 v[68:71], a[108:111], v[20:23], v[68:71]\n"
      "v_permlane16_swap_b32_e32 v76, v72\n"
      "v_permlane16_swap_b32_e32 v77, v74\n"
      "s_waitcnt lgkmcnt(7)\n"
      "v_mfma_f32_16x16x32_bf16 v[64:67], a[56:59], v[24:27], v[64:67]\n"
      "v_add_f32_e32 v72, v72, v76\n"
      "v_add_f32_e32 v74, v74, v77\n"
      "v_mfma_f32_16x16x32_bf16 v[68:71], a[112:115], v[24:27], v[68:71]\n"
      "v_mov_b32_e32 v76, v72\n"
      "v_mov_b32_e32 v77, v74\n"
      "s_waitcnt lgkmcnt(6)\n"
      "v_mfma_f32_16x16x32_bf16 v[64:67], a[60:63], v[28:31], v[64:67]\n"
      "v_permlane32_swap_b32_e32 v76, v72\n"
      "v_permlane32_swap_b32_e32 v77, v74\n"
      "v_mfma_f32_16x16x32_bf16 v[68:71], a[116:119], v[28:31], v[68:71]\n"
      "v_add_f32_e32 v72, v72, v76\n"
      "v_add_f32_e32 v74, v74, v77\n"
      "s_waitcnt lgkmcnt(5)\n"
      "v_mfma_f32_16x16x32_bf16 v[64:67], a[64:67], v[32:35], v[64:67]\n"
      "v_mul_f32_e32 v78, 0x3b124925, v72\n"
      "v_mul_f32_e32 v74, 0x3b124925, v74\n"
      "v_mfma_f32_16x16x32_bf16 v[68:71], a[120:123], v[32:35], v[68:71]\n"
      "v_fma_f32 v74, -v78, v78, v74\n"
      "v_cvt_pk_bf16_f32 v79, v78, v78\n"
      "s_waitcnt lgkmcnt(4)\n"
      "v_mfma_f32_16x16x32_bf16 v[64:67], a[68:71], v[36:39], v[64:67]\n"
      "v_max_f32_e32 v74, 0, v74\n"
      "v_and_b32_e32 v79, 0xffff0000, v79\n"
      "v_mfma_f32_16x16x32_bf16 v[68:71], a[124:127], v[36:39], v[68:71]\n"
      "v_cvt_pk_bf16_f32 v74, v74, v74\n"
      "v_xor_b32_e32 v79, 0x80000000, v79\n"
      "s_waitcnt lgkmcnt(3)\n"
      "v_mfma_f32_16x16x32_bf16 v[64:67], a[72:75], v[40:43], v[64:67]\n"
      "v_and_b32_e32 v74, 0xffff0000, v74\n"
      "v_add_f32_e32 v74, 0x3727c5ac, v74\n"
      "v_mfma_f32_16x16x32_bf16 v[68:71], a[128:131], v[40:43], v[68:71]\n"
      "v_cvt_pk_bf16_f32 v74, v74, v74\n"
      "v_and_b32_e32 v74, 0xffff0000, v74\n"
      "s_waitcnt lgkmcnt(2)\n"
      "v_mfma_f32_16x16x32_bf16 v[64:67], a[76:79], v[44:47], v[64:67]\n"
      "v_sqrt_f32_e32 v74, v74\n"
      "s_nop 0\n"
      "v_mfma_f32_16x16x32_bf16 v[68:71], a[132:135], v[44:47], v[68:71]\n"
      "v_cvt_pk_bf16_f32 v74, v74, v74\n"
      "v_and_b32_e32 v74, 0xffff0000, v74\n"
      "s_waitcnt lgkmcnt(1)\n"
      "v_mfma_f32_16x16x32_bf16 v[64:67], a[80:83], v[48:51], v[64:67]\n"
      "v_rcp_f32_e32 v80, v74\n"
      "v_mfma_f32_16x16x32_bf16 v[68:71], a[136:139], v[48:51], v[68:71]\n"
      "s_waitcnt lgkmcnt(0)\n"
      "v_mfma_f32_16x16x32_bf16 v[64:67], a[84:87], v[52:55], v[64:67]\n"
      "v_mfma_f32_16x16x32_bf16 v[68:71], a[140:143], v[52:55], v[68:71]\n"
      "s_nop 6\n"
      "v_fma_f32 v64, v79, v144, v64\n"
      "v_fma_f32 v65, v79, v145, v65\n"
      "v_fma_f32 v66, v79, v146, v66\n"
      "v_fma_f32 v67, v79, v147, v67\n"
      "v_mul_f32_e32 v64, v80, v64\n"
      "v_mul_f32_e32 v65, v80, v65\n"
      "v_mul_f32_e32 v66, v80, v66\n"
      "v_mul_f32_e32 v67, v80, v67\n"
      "v_cvt_pk_bf16_f32 v84, v64, v65\n"
      "v_cvt_pk_bf16_f32 v86, v66, v67\n"
      "v_mov_b32_e32 v85, s35\n"
      "v_mov_b32_e32 v87, s35\n"
      "v_fma_f32 v68, v79, v148, v68\n"
      "v_fma_f32 v69, v79, v149, v69\n"
      "v_fma_f32 v70, v79, v150, v70\n"
      "v_fma_f32 v71, v79, v151, v71\n"
      "v_mul_f32_e32 v68, v80, v68\n"
      "v_mul_f32_e32 v69, v80, v69\n"
      "v_mul_f32_e32 v70, v80, v70\n"
      "v_mul_f32_e32 v71, v80, v71\n"
      "v_cvt_pk_bf16_f32 v88, v68, v69\n"
      "v_cvt_pk_bf16_f32 v90, v70, v71\n"
      "v_mov_b32_e32 v89, s35\n"
      "v_mov_b32_e32 v91, s35\n"
      "s_mov_b32 s46, 0x00020002\n"
      "s_mov_b32 s47, 0x00020002\n"
      "v_cndmask_b32_e64 v84, v84, v88, s[46:47]\n"
      "v_cndmask_b32_e64 v86, v86, v90, s[46:47]\n"
      "s_mov_b32 exec_lo, 0x30003\n"
      "s_mov_b32 exec_hi, 0x30003\n"
      "global_store_dwordx4 v92, v[84:87], s[44:45] sc1\n"
      "s_mov_b64 exec, -1\n"
      "s_mov_b32 s42, 1\n"
      "s_waitcnt vmcnt(1)\n"
      "v_mov_b32_e32 v96, -1\n"
      "v_mov_b32_e32 v97, -1\n"
      "v_mov_b32_e32 v98, -1\n"
      "v_mov_b32_e32 v99, -1\n"
      "v_mov_b32_e32 v100, -1\n"
      "v_mov_b32_e32 v101, -1\n"
      "v_mov_b32_e32 v102, -1\n"
      "v_mov_b32_e32 v103, -1\n"
      "v_mov_b32_e32 v104, -1\n"
      "v_mov_b32_e32 v105, -1\n"
      "v_mov_b32_e32 v106, -1\n"
      "v_mov_b32_e32 v107, -1\n"
      "v_mov_b32_e32 v108, -1\n"
      "v_mov_b32_e32 v109, -1\n"
      "v_mov_b32_e32 v110, -1\n"
      "v_mov_b32_e32 v111, -1\n"
      "v_mov_b32_e32 v112, -1\n"
      "v_mov_b32_e32 v113, -1\n"
      "v_mov_b32_e32 v114, -1\n"
      "v_mov_b32_e32 v115, -1\n"
      "v_mov_b32_e32 v116, -1\n"
      "v_mov_b32_e32 v117, -1\n"
      "v_mov_b32_e32 v118, -1\n"
      "v_mov_b32_e32 v119, -1\n"
      "v_mbcnt_lo_u32_b32 v56, -1, 0\n"
      "v_mbcnt_hi_u32_b32 v56, -1, v56\n"
      "v_lshrrev_b32_e32 v57, 4, v56\n"
      "v_lshlrev_b32_e32 v93, 5, v57\n"
      "v_lshlrev_b32_e32 v92, 10, v57\n"
      "v_and_b32_e32 v57, 15, v56\n"
      "v_lshl_add_u32 v92, v57, 4, v92\n"
      "s_lshl_b32 s88, s38, 8\n"
      "v_add_u32_e32 v92, s88, v92\n"
      "v_bfe_u32 v57, v56, 3, 1\n"
      "v_lshlrev_b32_e32 v94, 6, v57\n"
      "v_lshrrev_b32_e32 v57, 4, v56\n"
      "v_lshl_add_u32 v94, v57, 4, v94\n"
      "v_and_b32_e32 v57, 7, v56\n"
      "v_lshl_add_u32 v94, v57, 1, v94\n"
      "s_lshl_b32 s88, s38, 7\n"
      "s_add_u32 s88, s88, 0x1e800\n"
      "v_add_u32_e32 v94, s88, v94\n"
      "s_mov_b32 s88, 0xb0800\n"
      "s_mul_i32 s89, s42, 262144\n"
      "s_add_u32 s88, s88, s89\n"
      "s_mul_i32 s89, s40, 16384\n"
      "s_add_u32 s88, s88, s89\n"
      "s_add_u32 s44, s36, s88\n"
      "s_addc_u32 s45, s37, 0\n"
      "s_mov_b32 s88, 0x80800\n"
      "s_mul_i32 s89, s42, 2048\n"
      "s_add_u32 s88, s88, s89\n"
      "s_mul_i32 s89, s40, 128\n"
      "s_add_u32 s88, s88, s89\n"
      "s_add_u32 s86, s36, s88\n"
      "s_addc_u32 s87, s37, 0\n"
      "s_mov_b64 s[92:93], -1\n"
      "s_mov_b64 s[48:49], -1\n"
      "s_mov_b64 s[50:51], -1\n"
      "s_mov_b64 s[52:53], -1\n"
      "s_mov_b64 s[54:55], -1\n"
      "s_mov_b64 s[56:57], -1\n"
      "s_mov_b32 s43, 0\n"
      "s_nop 0\n"
      "A3_67_%=:\n"
      "s_mov_b64 exec, s[48:49]\n"
      "s_nop 0\n"
      "s_cbranch_execz AS_69_%=\n"
      "s_add_u32 s46, s44, 0x0\n"
      "s_addc_u32 s47, s45, 0\n"
      "global_load_dwordx4 v[96:99], v92, s[46:47] sc1\n"
      "AS_69_%=:\n"
      "s_mov_b64 exec, s[50:51]\n"
      "s_nop 0\n"
      "s_cbranch_execz AS_70_%=\n"
      "s_add_u32 s46, s44, 0x1000\n"
      "s_addc_u32 s47, s45, 0\n"
      "global_load_dwordx4 v[100:103], v92, s[46:47] sc1\n"
      "AS_70_%=:\n"
      "s_mov_b64 exec, s[52:53]\n"
      "s_nop 0\n"
      "s_cbranch_execz AS_71_%=\n"
      "s_add_u32 s46, s44, 0x2000\n"
      "s_addc_u32 s47, s45, 0\n"
      "global_load_dwordx4 v[104:107], v92, s[46:47] sc1\n"
      "AS_71_%=:\n"
      "s_mov_b64 exec, s[54:55]\n"
      "s_nop 0\n"
      "s_cbranch_execz AS_72_%=\n"
      "s_add_u32 s46, s44, 0x3000\n"
      "s_addc_u32 s47, s45, 0\n"
      "global_load_dwordx4 v[108:111], v92, s[46:47] sc1\n"
      "AS_72_%=:\n"
      "s_mov_b64 exec, -1\n"
      "s_cmp_eq_u64 s[56:57], 0\n"
      "s_nop 0\n"
      "s_cbranch_scc1 AS_73_%=\n"
      "global_load_dwordx4 v[112:115], v93, s[86:87] sc1\n"
      "global_load_dwordx4 v[116:119], v93, s[86:87] offset:16 sc1\n"
      "AS_73_%=:\n"
      "s_sleep 1\n"
      "v_max3_u32 v57, v96, v97, v98\n"
      "v_max_u32_e32 v57, v57, v99\n"
      "v_cmp_eq_u32_e64 s[48:49], -1, v57\n"
      "v_max3_u32 v58, v100, v101, v102\n"
      "v_max_u32_e32 v58, v58, v103\n"
      "v_cmp_eq_u32_e64 s[50:51], -1, v58\n"
      "v_max3_u32 v57, v104, v105, v106\n"
      "v_max_u32_e32 v57, v57, v107\n"
      "v_cmp_eq_u32_e64 s[52:53], -1, v57\n"
      "v_max3_u32 v58, v108, v109, v110\n"
      "v_max_u32_e32 v58, v58, v111\n"
      "v_cmp_eq_u32_e64 s[54:55], -1, v58\n"
      "v_max3_u32 v57, v112, v113, v114\n"
      "v_max3_u32 v58, v115, v116, v117\n"
      "v_max3_u32 v57, v57, v118, v119\n"
      "v_max_u32_e32 v57, v57, v58\n"
      "v_cmp_eq_u32_e64 s[56:57], -1, v57\n"
      "s_mov_b64 s[88:89], s[48:49]\n"
      "s_or_b64 s[88:89], s[88:89], s[50:51]\n"
      "s_or_b64 s[88:89], s[88:89], s[52:53]\n"
      "s_or_b64 s[88:89], s[88:89], s[54:55]\n"
      "s_or_b64 s[88:89], s[88:89], s[56:57]\n"
      "s_cmp_eq_u64 s[88:89], 0\n"
      "s_nop 0\n"
      "s_cbranch_scc1 A4_68_%=\n"
      "s_mov_b32 s90, 0x00010001\n"
      "s_mov_b32 s91, 0x00010001\n"
      "s_mov_b32 s88, 0\n"
      "v_cmp_ne_u32_e64 s[58:59], -1, v112\n"
      "v_cmp_ne_u32_e64 s[60:61], -1, v114\n"
      "v_cmp_ne_u32_e64 s[98:99], -1, v116\n"
      "v_cmp_ne_u32_e64 s[76:77], -1, v118\n"
      "s_and_b64 s[84:85], s[58:59], s[90:91]\n"
      "s_bcnt1_i32_b64 s89, s[84:85]\n"
      "s_add_u32 s88, s88, s89\n"
      "s_and_b64 s[84:85], s[60:61], s[90:91]\n"
      "s_bcnt1_i32_b64 s89, s[84:85]\n"
      "s_add_u32 s88, s88, s89\n"
      "s_and_b64 s[84:85], s[98:99], s[90:91]\n"
      "s_bcnt1_i32_b64 s89, s[84:85]\n"
      "s_add_u32 s88, s88, s89\n"
      "s_and_b64 s[84:85], s[76:77], s[90:91]\n"
      "s_bcnt1_i32_b64 s89, s[84:85]\n"
      "s_add_u32 s88, s88, s89\n"
      "s_cmp_ge_u32 s88, 0\n"
      "s_cselect_b64 s[92:93], -1, 0\n"
      "s_or_b64 s[58:59], s[58:59], s[92:93]\n"
      "s_and_b64 s[48:49], s[48:49], s[58:59]\n"
      "s_or_b64 s[60:61], s[60:61], s[92:93]\n"
      "s_and_b64 s[50:51], s[50:51], s[60:61]\n"
      "s_or_b64 s[98:99], s[98:99], s[92:93]\n"
      "s_and_b64 s[52:53], s[52:53], s[98:99]\n"
      "s_or_b64 s[76:77], s[76:77], s[92:93]\n"
      "s_and_b64 s[54:55], s[54:55], s[76:77]\n"
      "s_cmp_ge_u32 s88, 0\n"
      "s_nop 0\n"
      "s_cbranch_scc1 AF_74_%=\n"
      "s_sleep 4\n"
      "AF_74_%=:\n"
      "s_add_u32 s43, s43, 1\n"
      "s_cmp_lt_u32 s43, 0x80000\n"
      "s_waitcnt vmcnt(6)\n"
      "s_nop 0\n"
      "s_cbranch_scc1 A3_67_%=\n"
      "s_nop 0\n"
      "A4_68_%=:\n"
      "v_mov_b32_e32 v124, -1\n"
      "v_mov_b32_e32 v125, -1\n"
      "v_mov_b32_e32 v126, -1\n"
      "v_mov_b32_e32 v127, -1\n"
      "v_mov_b32_e32 v128, -1\n"
      "v_mov_b32_e32 v129, -1\n"
      "v_mov_b32_e32 v130, -1\n"
      "v_mov_b32_e32 v131, -1\n"
      "v_mov_b32_e32 v132, -1\n"
      "v_mov_b32_e32 v133, -1\n"
      "v_mov_b32_e32 v134, -1\n"
      "v_mov_b32_e32 v135, -1\n"
      "v_mov_b32_e32 v136, -1\n"
      "v_mov_b32_e32 v137, -1\n"
      "v_mov_b32_e32 v138, -1\n"
      "v_mov_b32_e32 v139, -1\n"
      "v_max3_f32 v56, v112, v114, v116\n"
      "v_max_f32_e32 v56, v56, v118\n"
      "v_mov_b32_e32 v57, v56\n"
      "s_nop 1\n"
      "v_permlane16_swap_b32_e32 v57, v56\n"
      "v_max_f32_e32 v56, v56, v57\n"
      "v_mov_b32_e32 v57, v56\n"
      "s_nop 1\n"
      "v_permlane32_swap_b32_e32 v57, v56\n"
      "v_max_f32_e32 v56, v56, v57\n"
      "v_sub_f32_e32 v60, v112, v56\n"
      "v_sub_f32_e32 v61, v114, v56\n"
      "v_sub_f32_e32 v62, v116, v56\n"
      "v_sub_f32_e32 v63, v118, v56\n"
      "v_mul_f32_e32 v60, s34, v60\n"
      "v_mul_f32_e32 v61, s34, v61\n"
      "v_mul_f32_e32 v62, s34, v62\n"
      "v_mul_f32_e32 v63, s34, v63\n"
      "v_exp_f32_e32 v60, v60\n"
      "v_exp_f32_e32 v61, v61\n"
      "v_exp_f32_e32 v62, v62\n"
      "v_exp_f32_e32 v63, v63\n"
      "v_mul_f32_e32 v59, v60, v113\n"
      "v_mul_f32_e32 v64, v60, v96\n"
      "v_mul_f32_e32 v65, v60, v97\n"
      "v_mul_f32_e32 v66, v60, v98\n"
      "v_mul_f32_e32 v67, v60, v99\n"
      "v_fmac_f32_e32 v59, v61, v115\n"
      "v_fmac_f32_e32 v64, v61, v100\n"
      "v_fmac_f32_e32 v65, v61, v101\n"
      "v_fmac_f32_e32 v66, v61, v102\n"
      "v_fmac_f32_e32 v67, v61, v103\n"
      "v_fmac_f32_e32 v59, v62, v117\n"
      "v_fmac_f32_e32 v64, v62, v104\n"
      "v_fmac_f32_e32 v65, v62, v105\n"
      "v_fmac_f32_e32 v66, v62, v106\n"
      "v_fmac_f32_e32 v67, v62, v107\n"
      "v_fmac_f32_e32 v59, v63, v119\n"
      "v_fmac_f32_e32 v64, v63, v108\n"
      "v_fmac_f32_e32 v65, v63, v109\n"
      "v_fmac_f32_e32 v66, v63, v110\n"
      "v_fmac_f32_e32 v67, v63, v111\n"
      "v_mov_b32_e32 v57, v59\n"
      "v_permlane32_swap_b32_e32 v64, v66\n"
      "v_permlane32_swap_b32_e32 v65, v67\n"
      "v_permlane32_swap_b32_e32 v57, v59\n"
      "v_add_f32_e32 v64, v64, v66\n"
      "v_add_f32_e32 v65, v65, v67\n"
      "v_add_f32_e32 v59, v59, v57\n"
      "s_nop 0\n"
      "v_permlane16_swap_b32_e32 v64, v65\n"
      "v_mov_b32_e32 v57, v59\n"
      "v_add_f32_e32 v64, v64, v65\n"
      "s_nop 0\n"
      "v_permlane16_swap_b32_e32 v57, v59\n"
      "v_add_f32_e32 v59, v59, v57\n"
      "v_rcp_f32_e32 v59, v59\n"
      "s_nop 0\n"
      "v_mul_f32_e32 v64, v64, v59\n"
      "v_cvt_pk_bf16_f32 v64, v64, v64\n"
      "ds_write_b16 v94, v64\n"
      "s_mov_b32 s88, 0x82800\n"
      "s_mul_i32 s89, s42, 14336\n"
      "s_add_u32 s88, s88, s89\n"
      "s_mul_i32 s89, s40, 896\n"
      "s_add_u32 s88, s88, s89\n"
      "s_mul_i32 s89, s41, 448\n"
      "s_add_u32 s88, s88, s89\n"
      "s_mul_i32 s89, s38, 128\n"
      "s_add_u32 s88, s88, s89\n"
      "s_add_u32 s44, s36, s88\n"
      "s_addc_u32 s45, s37, 0\n"
      "s_cmp_eq_u32 s38, 3\n"
      "s_cselect_b32 s88, 32, 0\n"
      "s_sub_u32 s44, s44, s88\n"
      "s_subb_u32 s45, s45, 0\n"
      "v_mbcnt_lo_u32_b32 v93, -1, 0\n"
      "v_mbcnt_hi_u32_b32 v93, -1, v93\n"
      "v_and_b32_e32 v92, 15, v93\n"
      "v_lshlrev_b32_e32 v92, 5, v92\n"
      "v_lshrrev_b32_e32 v93, 4, v93\n"
      "v_lshl_add_u32 v92, v93, 3, v92\n"
      "s_waitcnt lgkmcnt(0)\n"
      "s_barrier\n"
      "ds_read_b128 v[0:3], v156 offset:2048\n"
      "ds_read_b128 v[4:7], v156 offset:2112\n"
      "ds_read_b128 v[8:11], v156 offset:2176\n"
      "ds_read_b128 v[12:15], v156 offset:2240\n"
      "ds_read_b128 v[16:19], v156 offset:2304\n"
      "ds_read_b128 v[20:23], v156 offset:2368\n"
      "ds_read_b128 v[24:27], v156 offset:2432\n"
      "ds_read_b128 v[28:31], v156 offset:2496\n"
      "s_waitcnt lgkmcnt(7)\n"
      "v_mfma_f32_16x16x32_bf16 v[64:67], v[160:163], v[0:3], 0\n"
      "v_mfma_f32_16x16x32_bf16 v[68:71], v[164:167], v[0:3], 0\n"
      "v_mfma_f32_16x16x32_bf16 v[72:75], v[168:171], v[0:3], 0\n"
      "v_mfma_f32_16x16x32_bf16 v[76:79], v[172:175], v[0:3], 0\n"
      "s_waitcnt lgkmcnt(6)\n"
      "v_mfma_f32_16x16x32_bf16 v[64:67], v[176:179], v[4:7], v[64:67]\n"
      "v_mfma_f32_16x16x32_bf16 v[68:71], v[180:183], v[4:7], v[68:71]\n"
      "v_mfma_f32_16x16x32_bf16 v[72:75], v[184:187], v[4:7], v[72:75]\n"
      "v_mfma_f32_16x16x32_bf16 v[76:79], v[188:191], v[4:7], v[76:79]\n"
      "s_waitcnt lgkmcnt(5)\n"
      "v_mfma_f32_16x16x32_bf16 v[64:67], v[192:195], v[8:11], v[64:67]\n"
      "v_mfma_f32_16x16x32_bf16 v[68:71], v[196:199], v[8:11], v[68:71]\n"
      "v_mfma_f32_16x16x32_bf16 v[72:75], v[200:203], v[8:11], v[72:75]\n"
      "v_mfma_f32_16x16x32_bf16 v[76:79], v[204:207], v[8:11], v[76:79]\n"
      "s_waitcnt lgkmcnt(4)\n"
      "v_mfma_f32_16x16x32_bf16 v[64:67], v[208:211], v[12:15], v[64:67]\n"
      "v_mfma_f32_16x16x32_bf16 v[68:71], v[212:215], v[12:15], v[68:71]\n"
      "v_mfma_f32_16x16x32_bf16 v[72:75], v[216:219], v[12:15], v[72:75]\n"
      "v_mfma_f32_16x16x32_bf16 v[76:79], v[220:223], v[12:15], v[76:79]\n"
      "s_waitcnt lgkmcnt(3)\n"
      "v_mfma_f32_16x16x32_bf16 v[64:67], v[224:227], v[16:19], v[64:67]\n"
      "v_mfma_f32_16x16x32_bf16 v[68:71], v[228:231], v[16:19], v[68:71]\n"
      "v_mfma_f32_16x16x32_bf16 v[72:75], v[232:235], v[16:19], v[72:75]\n"
      "v_mfma_f32_16x16x32_bf16 v[76:79], v[236:239], v[16:19], v[76:79]\n"
      "s_waitcnt lgkmcnt(2)\n"
      "v_mfma_f32_16x16x32_bf16 v[64:67], v[240:243], v[20:23], v[64:67]\n"
      "v_mfma_f32_16x16x32_bf16 v[68:71], v[244:247], v[20:23], v[68:71]\n"
      "v_mfma_f32_16x16x32_bf16 v[72:75], v[248:251], v[20:23], v[72:75]\n"
      "v_mfma_f32_16x16x32_bf16 v[76:79], v[252:255], v[20:23], v[76:79]\n"
      "s_waitcnt lgkmcnt(1)\n"
      "v_mfma_f32_16x16x32_bf16 v[64:67], a[0:3], v[24:27], v[64:67]\n"
      "v_mfma_f32_16x16x32_bf16 v[68:71], a[4:7], v[24:27], v[68:71]\n"
      "v_mfma_f32_16x16x32_bf16 v[72:75], a[8:11], v[24:27], v[72:75]\n"
      "v_mfma_f32_16x16x32_bf16 v[76:79], a[12:15], v[24:27], v[76:79]\n"
      "s_waitcnt lgkmcnt(0)\n"
      "v_mfma_f32_16x16x32_bf16 v[64:67], a[16:19], v[28:31], v[64:67]\n"
      "v_mfma_f32_16x16x32_bf16 v[68:71], a[20:23], v[28:31], v[68:71]\n"
      "v_mfma_f32_16x16x32_bf16 v[72:75], a[24:27], v[28:31], v[72:75]\n"
      "v_mfma_f32_16x16x32_bf16 v[76:79], a[28:31], v[28:31], v[76:79]\n"
      "s_nop 4\n"
      "v_cvt_pk_bf16_f32 v64, v64, v65\n"
      "v_cvt_pk_bf16_f32 v65, v66, v67\n"
      "v_min_u32_e32 v64, -2, v64\n"
      "v_min_u32_e32 v65, -2, v65\n"
      "v_cvt_pk_bf16_f32 v82, v68, v69\n"
      "v_cvt_pk_bf16_f32 v83, v70, v71\n"
      "v_min_u32_e32 v82, -2, v82\n"
      "v_min_u32_e32 v83, -2, v83\n"
      "s_mov_b32 s46, 0x20002\n"
      "s_mov_b32 s47, 0x20002\n"
      "v_cndmask_b32_e64 v64, v64, v82, s[46:47]\n"
      "v_cndmask_b32_e64 v65, v65, v83, s[46:47]\n"
      "v_cvt_pk_bf16_f32 v84, v72, v73\n"
      "v_cvt_pk_bf16_f32 v85, v74, v75\n"
      "v_min_u32_e32 v84, -2, v84\n"
      "v_min_u32_e32 v85, -2, v85\n"
      "s_mov_b32 s46, 0x40004\n"
      "s_mov_b32 s47, 0x40004\n"
      "v_cndmask_b32_e64 v64, v64, v84, s[46:47]\n"
      "v_cndmask_b32_e64 v65, v65, v85, s[46:47]\n"
      "v_cvt_pk_bf16_f32 v86, v76, v77\n"
      "v_cvt_pk_bf16_f32 v87, v78, v79\n"
      "v_min_u32_e32 v86, -2, v86\n"
      "v_min_u32_e32 v87, -2, v87\n"
      "s_mov_b32 s46, 0x80008\n"
      "s_mov_b32 s47, 0x80008\n"
      "v_cndmask_b32_e64 v64, v64, v86, s[46:47]\n"
      "v_cndmask_b32_e64 v65, v65, v87, s[46:47]\n"
      "s_mov_b32 s46, 0x000f000f\n"
      "s_cmp_ge_u32 s38, 2\n"
      "s_cselect_b32 exec_lo, 0x00070007, s46\n"
      "s_mov_b32 exec_hi, exec_lo\n"
      "global_store_dwordx2 v92, v[64:65], s[44:45] sc1\n"
      "s_mov_b64 exec, -1\n"
      "s_mov_b32 s42, 2\n"
      "s_mov_b32 s83, 3\n"
      "s_getpc_b64 s[88:89]\n"
      "XPC_75_%=:\n"
      "s_add_u32 s88, s88, C_EXTRA_%=-XPC_75_%=\n"
      "s_addc_u32 s89, s89, 0\n"
      "s_nop 0\n"
      "s_swappc_b64 s[98:99], s[88:89]\n"
      "s_mov_b32 s88, 0x86000\n"
      "s_add_u32 s44, s36, s88\n"
      "s_addc_u32 s45, s37, 0\n"
      "s_mov_b32 exec_lo, 0x3fff3fff\n"
      "s_mov_b32 exec_hi, 0x3fff3fff\n"
      "s_add_u32 s46, s44, 0x0\n"
      "s_addc_u32 s47, s45, 0\n"
      "global_load_dwordx4 v[124:127], v154, s[46:47] sc1\n"
      "s_add_u32 s46, s44, 0xe00\n"
      "s_addc_u32 s47, s45, 0\n"
      "global_load_dwordx4 v[128:131], v154, s[46:47] sc1\n"
      "s_add_u32 s46, s44, 0x1c00\n"
      "s_addc_u32 s47, s45, 0\n"
      "global_load_dwordx4 v[132:135], v154, s[46:47] sc1\n"
      "s_add_u32 s46, s44, 0x2a00\n"
      "s_addc_u32 s47, s45, 0\n"
      "global_load_dwordx4 v[136:139], v154, s[46:47] sc1\n"
      "s_mov_b64 exec, -1\n"
      "s_mov_b32 s42, 3\n"
      "v_mov_b32_e32 v96, -1\n"
      "v_mov_b32_e32 v97, -1\n"
      "v_mov_b32_e32 v98, -1\n"
      "v_mov_b32_e32 v99, -1\n"
      "v_mov_b32_e32 v100, -1\n"
      "v_mov_b32_e32 v101, -1\n"
      "v_mov_b32_e32 v102, -1\n"
      "v_mov_b32_e32 v103, -1\n"
      "v_mov_b32_e32 v104, -1\n"
      "v_mov_b32_e32 v105, -1\n"
      "v_mov_b32_e32 v106, -1\n"
      "v_mov_b32_e32 v107, -1\n"
      "v_mov_b32_e32 v108, -1\n"
      "v_mov_b32_e32 v109, -1\n"
      "v_mov_b32_e32 v110, -1\n"
      "v_mov_b32_e32 v111, -1\n"
      "v_mov_b32_e32 v112, -1\n"
      "v_mov_b32_e32 v113, -1\n"
      "v_mov_b32_e32 v114, -1\n"
      "v_mov_b32_e32 v115, -1\n"
      "v_mov_b32_e32 v116, -1\n"
      "v_mov_b32_e32 v117, -1\n"
      "v_mov_b32_e32 v118, -1\n"
      "v_mov_b32_e32 v119, -1\n"
      "v_mbcnt_lo_u32_b32 v56, -1, 0\n"
      "v_mbcnt_hi_u32_b32 v56, -1, v56\n"
      "v_lshrrev_b32_e32 v57, 4, v56\n"
      "v_lshlrev_b32_e32 v93, 5, v57\n"
      "v_lshlrev_b32_e32 v92, 10, v57\n"
      "v_and_b32_e32 v57, 15, v56\n"
      "v_lshl_add_u32 v92, v57, 4, v92\n"
      "s_lshl_b32 s88, s38, 8\n"
      "v_add_u32_e32 v92, s88, v92\n"
      "v_bfe_u32 v57, v56, 3, 1\n"
      "v_lshlrev_b32_e32 v94, 6, v57\n"
      "v_lshrrev_b32_e32 v57, 4, v56\n"
      "v_lshl_add_u32 v94, v57, 4, v94\n"
      "v_and_b32_e32 v57, 7, v56\n"
      "v_lshl_add_u32 v94, v57, 1, v94\n"
      "s_lshl_b32 s88, s38, 7\n"
      "s_add_u32 s88, s88, 0x1e800\n"
      "v_add_u32_e32 v94, s88, v94\n"
      "s_mov_b32 s88, 0xb0800\n"
      "s_mul_i32 s89, s42, 262144\n"
      "s_add_u32 s88, s88, s89\n"
      "s_mul_i32 s89, s40, 16384\n"
      "s_add_u32 s88, s88, s89\n"
      "s_add_u32 s44, s36, s88\n"
      "s_addc_u32 s45, s37, 0\n"
      "s_mov_b32 s88, 0x80800\n"
      "s_mul_i32 s89, s42, 2048\n"
      "s_add_u32 s88, s88, s89\n"
      "s_mul_i32 s89, s40, 128\n"
      "s_add_u32 s88, s88, s89\n"
      "s_add_u32 s86, s36, s88\n"
      "s_addc_u32 s87, s37, 0\n"
      "s_mov_b64 s[92:93], -1\n"
      "s_mov_b64 s[48:49], -1\n"
      "s_mov_b64 s[50:51], -1\n"
      "s_mov_b64 s[52:53], -1\n"
      "s_mov_b64 s[54:55], -1\n"
      "s_mov_b64 s[56:57], -1\n"
      "s_mov_b32 s43, 0\n"
      "s_nop 0\n"
      "A3_76_%=:\n"
      "s_mov_b64 exec, s[48:49]\n"
      "s_nop 0\n"
      "s_cbranch_execz AS_78_%=\n"
      "s_add_u32 s46, s44, 0x0\n"
      "s_addc_u32 s47, s45, 0\n"
      "global_load_dwordx4 v[96:99], v92, s[46:47] sc1\n"
      "AS_78_%=:\n"
      "s_mov_b64 exec, s[50:51]\n"
      "s_nop 0\n"
      "s_cbranch_execz AS_79_%=\n"
      "s_add_u32 s46, s44, 0x1000\n"
      "s_addc_u32 s47, s45, 0\n"
      "global_load_dwordx4 v[100:103], v92, s[46:47] sc1\n"
      "AS_79_%=:\n"
      "s_mov_b64 exec, s[52:53]\n"
      "s_nop 0\n"
      "s_cbranch_execz AS_80_%=\n"
      "s_add_u32 s46, s44, 0x2000\n"
      "s_addc_u32 s47, s45, 0\n"
      "global_load_dwordx4 v[104:107], v92, s[46:47] sc1\n"
      "AS_80_%=:\n"
      "s_mov_b64 exec, s[54:55]\n"
      "s_nop 0\n"
      "s_cbranch_execz AS_81_%=\n"
      "s_add_u32 s46, s44, 0x3000\n"
      "s_addc_u32 s47, s45, 0\n"
      "global_load_dwordx4 v[108:111], v92, s[46:47] sc1\n"
      "AS_81_%=:\n"
      "s_mov_b64 exec, -1\n"
      "s_cmp_eq_u64 s[56:57], 0\n"
      "s_nop 0\n"
      "s_cbranch_scc1 AS_82_%=\n"
      "global_load_dwordx4 v[112:115], v93, s[86:87] sc1\n"
      "global_load_dwordx4 v[116:119], v93, s[86:87] offset:16 sc1\n"
      "AS_82_%=:\n"
      "s_sleep 1\n"
      "v_max3_u32 v57, v96, v97, v98\n"
      "v_max_u32_e32 v57, v57, v99\n"
      "v_cmp_eq_u32_e64 s[48:49], -1, v57\n"
      "v_max3_u32 v58, v100, v101, v102\n"
      "v_max_u32_e32 v58, v58, v103\n"
      "v_cmp_eq_u32_e64 s[50:51], -1, v58\n"
      "v_max3_u32 v57, v104, v105, v106\n"
      "v_max_u32_e32 v57, v57, v107\n"
      "v_cmp_eq_u32_e64 s[52:53], -1, v57\n"
      "v_max3_u32 v58, v108, v109, v110\n"
      "v_max_u32_e32 v58, v58, v111\n"
      "v_cmp_eq_u32_e64 s[54:55], -1, v58\n"
      "v_max3_u32 v57, v112, v113, v114\n"
      "v_max3_u32 v58, v115, v116, v117\n"
      "v_max3_u32 v57, v57, v118, v119\n"
      "v_max_u32_e32 v57, v57, v58\n"
      "v_cmp_eq_u32_e64 s[56:57], -1, v57\n"
      "s_mov_b64 s[88:89], s[48:49]\n"
      "s_or_b64 s[88:89], s[88:89], s[50:51]\n"
      "s_or_b64 s[88:89], s[88:89], s[52:53]\n"
      "s_or_b64 s[88:89], s[88:89], s[54:55]\n"
      "s_or_b64 s[88:89], s[88:89], s[56:57]\n"
      "s_cmp_eq_u64 s[88:89], 0\n"
      "s_nop 0\n"
      "s_cbranch_scc1 A4_77_%=\n"
      "s_mov_b32 s90, 0x00010001\n"
      "s_mov_b32 s91, 0x00010001\n"
      "s_mov_b32 s88, 0\n"
      "v_cmp_ne_u32_e64 s[58:59], -1, v112\n"
      "v_cmp_ne_u32_e64 s[60:61], -1, v114\n"
      "v_cmp_ne_u32_e64 s[98:99], -1, v116\n"
      "v_cmp_ne_u32_e64 s[76:77], -1, v118\n"
      "s_and_b64 s[84:85], s[58:59], s[90:91]\n"
      "s_bcnt1_i32_b64 s89, s[84:85]\n"
      "s_add_u32 s88, s88, s89\n"
      "s_and_b64 s[84:85], s[60:61], s[90:91]\n"
      "s_bcnt1_i32_b64 s89, s[84:85]\n"
      "s_add_u32 s88, s88, s89\n"
      "s_and_b64 s[84:85], s[98:99], s[90:91]\n"
      "s_bcnt1_i32_b64 s89, s[84:85]\n"
      "s_add_u32 s88, s88, s89\n"
      "s_and_b64 s[84:85], s[76:77], s[90:91]\n"
      "s_bcnt1_i32_b64 s89, s[84:85]\n"
      "s_add_u32 s88, s88, s89\n"
      "s_cmp_ge_u32 s88, 0\n"
      "s_cselect_b64 s[92:93], -1, 0\n"
      "s_or_b64 s[58:59], s[58:59], s[92:93]\n"
      "s_and_b64 s[48:49], s[48:49], s[58:59]\n"
      "s_or_b64 s[60:61], s[60:61], s[92:93]\n"
      "s_and_b64 s[50:51], s[50:51], s[60:61]\n"
      "s_or_b64 s[98:99], s[98:99], s[92:93]\n"
      "s_and_b64 s[52:53], s[52:53], s[98:99]\n"
      "s_or_b64 s[76:77], s[76:77], s[92:93]\n"
      "s_and_b64 s[54:55], s[54:55], s[76:77]\n"
      "s_cmp_ge_u32 s88, 0\n"
      "s_nop 0\n"
      "s_cbranch_scc1 AF_83_%=\n"
      "s_sleep 4\n"
      "AF_83_%=:\n"
      "s_add_u32 s43, s43, 1\n"
      "s_cmp_lt_u32 s43, 0x80000\n"
      "s_waitcnt vmcnt(6)\n"
      "s_nop 0\n"
      "s_cbranch_scc1 A3_76_%=\n"
      "s_nop 0\n"
      "A4_77_%=:\n"
      "v_max3_f32 v56, v112, v114, v116\n"
      "v_max_f32_e32 v56, v56, v118\n"
      "v_mov_b32_e32 v57, v56\n"
      "s_nop 1\n"
      "v_permlane16_swap_b32_e32 v57, v56\n"
      "v_max_f32_e32 v56, v56, v57\n"
      "v_mov_b32_e32 v57, v56\n"
      "s_nop 1\n"
      "v_permlane32_swap_b32_e32 v57, v56\n"
      "v_max_f32_e32 v56, v56, v57\n"
      "v_sub_f32_e32 v60, v112, v56\n"
      "v_sub_f32_e32 v61, v114, v56\n"
      "v_sub_f32_e32 v62, v116, v56\n"
      "v_sub_f32_e32 v63, v118, v56\n"
      "v_mul_f32_e32 v60, s34, v60\n"
      "v_mul_f32_e32 v61, s34, v61\n"
      "v_mul_f32_e32 v62, s34, v62\n"
      "v_mul_f32_e32 v63, s34, v63\n"
      "v_exp_f32_e32 v60, v60\n"
      "v_exp_f32_e32 v61, v61\n"
      "v_exp_f32_e32 v62, v62\n"
      "v_exp_f32_e32 v63, v63\n"
      "v_mul_f32_e32 v59, v60, v113\n"
      "v_mul_f32_e32 v64, v60, v96\n"
      "v_mul_f32_e32 v65, v60, v97\n"
      "v_mul_f32_e32 v66, v60, v98\n"
      "v_mul_f32_e32 v67, v60, v99\n"
      "v_fmac_f32_e32 v59, v61, v115\n"
      "v_fmac_f32_e32 v64, v61, v100\n"
      "v_fmac_f32_e32 v65, v61, v101\n"
      "v_fmac_f32_e32 v66, v61, v102\n"
      "v_fmac_f32_e32 v67, v61, v103\n"
      "v_fmac_f32_e32 v59, v62, v117\n"
      "v_fmac_f32_e32 v64, v62, v104\n"
      "v_fmac_f32_e32 v65, v62, v105\n"
      "v_fmac_f32_e32 v66, v62, v106\n"
      "v_fmac_f32_e32 v67, v62, v107\n"
      "v_fmac_f32_e32 v59, v63, v119\n"
      "v_fmac_f32_e32 v64, v63, v108\n"
      "v_fmac_f32_e32 v65, v63, v109\n"
      "v_fmac_f32_e32 v66, v63, v110\n"
      "v_fmac_f32_e32 v67, v63, v111\n"
      "v_mov_b32_e32 v57, v59\n"
      "v_permlane32_swap_b32_e32 v64, v66\n"
      "v_permlane32_swap_b32_e32 v65, v67\n"
      "v_permlane32_swap_b32_e32 v57, v59\n"
      "v_add_f32_e32 v64, v64, v66\n"
      "v_add_f32_e32 v65, v65, v67\n"
      "v_add_f32_e32 v59, v59, v57\n"
      "s_nop 0\n"
      "v_permlane16_swap_b32_e32 v64, v65\n"
      "v_mov_b32_e32 v57, v59\n"
      "v_add_f32_e32 v64, v64, v65\n"
      "s_nop 0\n"
      "v_permlane16_swap_b32_e32 v57, v59\n"
      "v_add_f32_e32 v59, v59, v57\n"
      "v_rcp_f32_e32 v59, v59\n"
      "s_nop 0\n"
      "v_mul_f32_e32 v64, v64, v59\n"
      "v_cvt_pk_bf16_f32 v64, v64, v64\n"
      "ds_write_b16 v94, v64\n"
      "s_mov_b32 s88, 0x82800\n"
      "s_mul_i32 s89, s42, 14336\n"
      "s_add_u32 s88, s88, s89\n"
      "s_mul_i32 s89, s40, 896\n"
      "s_add_u32 s88, s88, s89\n"
      "s_mul_i32 s89, s41, 448\n"
      "s_add_u32 s88, s88, s89\n"
      "s_mul_i32 s89, s38, 128\n"
      "s_add_u32 s88, s88, s89\n"
      "s_add_u32 s44, s36, s88\n"
      "s_addc_u32 s45, s37, 0\n"
      "s_cmp_eq_u32 s38, 3\n"
      "s_cselect_b32 s88, 32, 0\n"
      "s_sub_u32 s44, s44, s88\n"
      "s_subb_u32 s45, s45, 0\n"
      "v_mbcnt_lo_u32_b32 v93, -1, 0\n"
      "v_mbcnt_hi_u32_b32 v93, -1, v93\n"
      "v_and_b32_e32 v92, 15, v93\n"
      "v_lshlrev_b32_e32 v92, 5, v92\n"
      "v_lshrrev_b32_e32 v93, 4, v93\n"
      "v_lshl_add_u32 v92, v93, 3, v92\n"
      "s_waitcnt lgkmcnt(0)\n"
      "s_barrier\n"
      "ds_read_b128 v[0:3], v156 offset:2048\n"
      "ds_read_b128 v[4:7], v156 offset:2112\n"
      "ds_read_b128 v[8:11], v156 offset:2176\n"
      "ds_read_b128 v[12:15], v156 offset:2240\n"
      "ds_read_b128 v[16:19], v156 offset:2304\n"
      "ds_read_b128 v[20:23], v156 offset:2368\n"
      "ds_read_b128 v[24:27], v156 offset:2432\n"
      "ds_read_b128 v[28:31], v156 offset:2496\n"
      "s_waitcnt lgkmcnt(7)\n"
      "v_mfma_f32_16x16x32_bf16 v[64:67], v[160:163], v[0:3], 0\n"
      "v_mfma_f32_16x16x32_bf16 v[68:71], v[164:167], v[0:3], 0\n"
      "v_mfma_f32_16x16x32_bf16 v[72:75], v[168:171], v[0:3], 0\n"
      "v_mfma_f32_16x16x32_bf16 v[76:79], v[172:175], v[0:3], 0\n"
      "s_waitcnt lgkmcnt(6)\n"
      "v_mfma_f32_16x16x32_bf16 v[64:67], v[176:179], v[4:7], v[64:67]\n"
      "v_mfma_f32_16x16x32_bf16 v[68:71], v[180:183], v[4:7], v[68:71]\n"
      "v_mfma_f32_16x16x32_bf16 v[72:75], v[184:187], v[4:7], v[72:75]\n"
      "v_mfma_f32_16x16x32_bf16 v[76:79], v[188:191], v[4:7], v[76:79]\n"
      "s_waitcnt lgkmcnt(5)\n"
      "v_mfma_f32_16x16x32_bf16 v[64:67], v[192:195], v[8:11], v[64:67]\n"
      "v_mfma_f32_16x16x32_bf16 v[68:71], v[196:199], v[8:11], v[68:71]\n"
      "v_mfma_f32_16x16x32_bf16 v[72:75], v[200:203], v[8:11], v[72:75]\n"
      "v_mfma_f32_16x16x32_bf16 v[76:79], v[204:207], v[8:11], v[76:79]\n"
      "s_waitcnt lgkmcnt(4)\n"
      "v_mfma_f32_16x16x32_bf16 v[64:67], v[208:211], v[12:15], v[64:67]\n"
      "v_mfma_f32_16x16x32_bf16 v[68:71], v[212:215], v[12:15], v[68:71]\n"
      "v_mfma_f32_16x16x32_bf16 v[72:75], v[216:219], v[12:15], v[72:75]\n"
      "v_mfma_f32_16x16x32_bf16 v[76:79], v[220:223], v[12:15], v[76:79]\n"
      "s_waitcnt lgkmcnt(3)\n"
      "v_mfma_f32_16x16x32_bf16 v[64:67], v[224:227], v[16:19], v[64:67]\n"
      "v_mfma_f32_16x16x32_bf16 v[68:71], v[228:231], v[16:19], v[68:71]\n"
      "v_mfma_f32_16x16x32_bf16 v[72:75], v[232:235], v[16:19], v[72:75]\n"
      "v_mfma_f32_16x16x32_bf16 v[76:79], v[236:239], v[16:19], v[76:79]\n"
      "s_waitcnt lgkmcnt(2)\n"
      "v_mfma_f32_16x16x32_bf16 v[64:67], v[240:243], v[20:23], v[64:67]\n"
      "v_mfma_f32_16x16x32_bf16 v[68:71], v[244:247], v[20:23], v[68:71]\n"
      "v_mfma_f32_16x16x32_bf16 v[72:75], v[248:251], v[20:23], v[72:75]\n"
      "v_mfma_f32_16x16x32_bf16 v[76:79], v[252:255], v[20:23], v[76:79]\n"
      "s_waitcnt lgkmcnt(1)\n"
      "v_mfma_f32_16x16x32_bf16 v[64:67], a[0:3], v[24:27], v[64:67]\n"
      "v_mfma_f32_16x16x32_bf16 v[68:71], a[4:7], v[24:27], v[68:71]\n"
      "v_mfma_f32_16x16x32_bf16 v[72:75], a[8:11], v[24:27], v[72:75]\n"
      "v_mfma_f32_16x16x32_bf16 v[76:79], a[12:15], v[24:27], v[76:79]\n"
      "s_waitcnt lgkmcnt(0)\n"
      "v_mfma_f32_16x16x32_bf16 v[64:67], a[16:19], v[28:31], v[64:67]\n"
      "v_mfma_f32_16x16x32_bf16 v[68:71], a[20:23], v[28:31], v[68:71]\n"
      "v_mfma_f32_16x16x32_bf16 v[72:75], a[24:27], v[28:31], v[72:75]\n"
      "v_mfma_f32_16x16x32_bf16 v[76:79], a[28:31], v[28:31], v[76:79]\n"
      "s_nop 4\n"
      "v_cvt_pk_bf16_f32 v64, v64, v65\n"
      "v_cvt_pk_bf16_f32 v65, v66, v67\n"
      "v_min_u32_e32 v64, -2, v64\n"
      "v_min_u32_e32 v65, -2, v65\n"
      "v_cvt_pk_bf16_f32 v82, v68, v69\n"
      "v_cvt_pk_bf16_f32 v83, v70, v71\n"
      "v_min_u32_e32 v82, -2, v82\n"
      "v_min_u32_e32 v83, -2, v83\n"
      "s_mov_b32 s46, 0x20002\n"
      "s_mov_b32 s47, 0x20002\n"
      "v_cndmask_b32_e64 v64, v64, v82, s[46:47]\n"
      "v_cndmask_b32_e64 v65, v65, v83, s[46:47]\n"
      "v_cvt_pk_bf16_f32 v84, v72, v73\n"
      "v_cvt_pk_bf16_f32 v85, v74, v75\n"
      "v_min_u32_e32 v84, -2, v84\n"
      "v_min_u32_e32 v85, -2, v85\n"
      "s_mov_b32 s46, 0x40004\n"
      "s_mov_b32 s47, 0x40004\n"
      "v_cndmask_b32_e64 v64, v64, v84, s[46:47]\n"
      "v_cndmask_b32_e64 v65, v65, v85, s[46:47]\n"
      "v_cvt_pk_bf16_f32 v86, v76, v77\n"
      "v_cvt_pk_bf16_f32 v87, v78, v79\n"
      "v_min_u32_e32 v86, -2, v86\n"
      "v_min_u32_e32 v87, -2, v87\n"
      "s_mov_b32 s46, 0x80008\n"
      "s_mov_b32 s47, 0x80008\n"
      "v_cndmask_b32_e64 v64, v64, v86, s[46:47]\n"
      "v_cndmask_b32_e64 v65, v65, v87, s[46:47]\n"
      "s_mov_b32 s46, 0x000f000f\n"
      "s_cmp_ge_u32 s38, 2\n"
      "s_cselect_b32 exec_lo, 0x00070007, s46\n"
      "s_mov_b32 exec_hi, exec_lo\n"
      "global_store_dwordx2 v92, v[64:65], s[44:45] sc1\n"
      "s_mov_b64 exec, -1\n"
      "s_mov_b32 s42, 1\n"
      "v_mov_b32_e32 v56, v140\n"
      "v_mov_b32_e32 v140, v142\n"
      "v_mov_b32_e32 v142, v56\n"
      "v_mov_b32_e32 v56, v141\n"
      "v_mov_b32_e32 v141, v143\n"
      "v_mov_b32_e32 v143, v56\n"
      "s_mov_b32 s84, 0xcccccccc\n"
      "s_mov_b32 s85, 0xcccccccc\n"
      "v_cndmask_b32_e64 v84, 0, v159, s[64:65]\n"
      "v_cndmask_b32_e64 v85, 0, v159, s[66:67]\n"
      "v_cndmask_b32_e64 v86, 0, v159, s[68:69]\n"
      "v_cndmask_b32_e64 v87, 0, v159, s[70:71]\n"
      "s_mov_b32 s88, 0x82800\n"
      "s_mul_i32 s89, s42, 14336\n"
      "s_add_u32 s88, s88, s89\n"
      "s_add_u32 s44, s36, s88\n"
      "s_addc_u32 s45, s37, 0\n"
      "s_mov_b32 s94, 0x3fff3fff\n"
      "s_mov_b32 s95, 0x3fff3fff\n"
      "s_mov_b64 s[48:49], s[94:95]\n"
      "s_mov_b64 s[50:51], s[94:95]\n"
      "s_mov_b64 s[52:53], s[94:95]\n"
      "s_mov_b64 s[54:55], s[94:95]\n"
      "s_mov_b32 s43, 0\n"
      "s_nop 0\n"
      "s_branch GC_86_%=\n"
      "s_nop 0\n"
      "GT_84_%=:\n"
      "s_mov_b64 exec, s[48:49]\n"
      "s_nop 0\n"
      "s_cbranch_execz GS_87_%=\n"
      "s_add_u32 s46, s44, 0x0\n"
      "s_addc_u32 s47, s45, 0\n"
      "global_load_dwordx4 v[124:127], v154, s[46:47] sc1\n"
      "GS_87_%=:\n"
      "s_mov_b64 exec, s[50:51]\n"
      "s_nop 0\n"
      "s_cbranch_execz GS_88_%=\n"
      "s_add_u32 s46, s44, 0xe00\n"
      "s_addc_u32 s47, s45, 0\n"
      "global_load_dwordx4 v[128:131], v154, s[46:47] sc1\n"
      "GS_88_%=:\n"
      "s_mov_b64 exec, s[52:53]\n"
      "s_nop 0\n"
      "s_cbranch_execz GS_89_%=\n"
      "s_add_u32 s46, s44, 0x1c00\n"
      "s_addc_u32 s47, s45, 0\n"
      "global_load_dwordx4 v[132:135], v154, s[46:47] sc1\n"
      "GS_89_%=:\n"
      "s_mov_b64 exec, s[54:55]\n"
      "s_nop 0\n"
      "s_cbranch_execz GS_90_%=\n"
      "s_add_u32 s46, s44, 0x2a00\n"
      "s_addc_u32 s47, s45, 0\n"
      "global_load_dwordx4 v[136:139], v154, s[46:47] sc1\n"
      "GS_90_%=:\n"
      "s_mov_b64 exec, -1\n"
      "s_sleep 1\n"
      "s_nop 0\n"
      "GC_86_%=:\n"
      "v_max3_u32 v56, v124, v125, v126\n"
      "v_max_u32_e32 v56, v56, v127\n"
      "v_cmp_eq_u32_e64 s[48:49], -1, v56\n"
      "v_max3_u32 v57, v128, v129, v130\n"
      "v_max_u32_e32 v57, v57, v131\n"
      "v_cmp_eq_u32_e64 s[50:51], -1, v57\n"
      "v_max3_u32 v56, v132, v133, v134\n"
      "v_max_u32_e32 v56, v56, v135\n"
      "v_cmp_eq_u32_e64 s[52:53], -1, v56\n"
      "v_max3_u32 v57, v136, v137, v138\n"
      "v_max_u32_e32 v57, v57, v139\n"
      "v_cmp_eq_u32_e64 s[54:55], -1, v57\n"
      "s_and_b64 s[48:49], s[48:49], s[94:95]\n"
      "s_and_b64 s[50:51], s[50:51], s[94:95]\n"
      "s_and_b64 s[52:53], s[52:53], s[94:95]\n"
      "s_and_b64 s[54:55], s[54:55], s[94:95]\n"
      "s_mov_b64 s[88:89], s[48:49]\n"
      "s_or_b64 s[88:89], s[88:89], s[50:51]\n"
      "s_or_b64 s[88:89], s[88:89], s[52:53]\n"
      "s_or_b64 s[88:89], s[88:89], s[54:55]\n"
      "s_cmp_eq_u64 s[88:89], 0\n"
      "s_nop 0\n"
      "s_cbranch_scc1 GD_85_%=\n"
      "s_add_u32 s43, s43, 1\n"
      "s_cmp_lt_u32 s43, 0x80000\n"
      "s_waitcnt vmcnt(4)\n"
      "s_nop 0\n"
      "s_cbranch_scc1 GT_84_%=\n"
      "s_nop 0\n"
      "GD_85_%=:\n"
      "v_mfma_f32_16x16x32_bf16 v[88:91], v[124:127], v[84:87], 0\n"
      "v_mfma_f32_16x16x32_bf16 v[88:91], v[128:131], v[84:87], v[88:91]\n"
      "v_mfma_f32_16x16x32_bf16 v[88:91], v[132:135], v[84:87], v[88:91]\n"
      "v_mfma_f32_16x16x32_bf16 v[88:91], v[136:139], v[84:87], v[88:91]\n"
      "s_nop 7\n"
      "v_cndmask_b32_e64 v56, v88, v90, s[62:63]\n"
      "v_cndmask_b32_e64 v57, v89, v91, s[62:63]\n"
      "v_cvt_pk_bf16_f32 v58, v56, v57\n"
      "v_lshlrev_b32_e32 v56, 16, v58\n"
      "v_and_b32_e32 v57, 0xffff0000, v58\n"
      "v_add_f32_e32 v56, v140, v56\n"
      "v_add_f32_e32 v57, v141, v57\n"
      "v_cvt_pk_bf16_f32 v58, v56, v57\n"
      "s_cmp_ge_u32 s39, 28\n"
      "v_lshlrev_b32_e32 v140, 16, v58\n"
      "v_and_b32_e32 v141, 0xffff0000, v58\n"
      "s_nop 4\n"
      "s_cbranch_scc1 NOBC_91_%=\n"
      "s_mov_b32 exec_lo, -1\n"
      "s_mov_b32 exec_hi, 0x00ffffff\n"
      "ds_write_b32 v155, v58 offset:0\n"
      "s_mov_b64 exec, -1\n"
      "s_waitcnt lgkmcnt(0)\n"
      "s_barrier\n"
      "v_mbcnt_lo_u32_b32 v81, -1, 0\n"
      "v_mbcnt_hi_u32_b32 v81, -1, v81\n"
      "v_lshlrev_b32_e32 v81, 4, v81\n"
      "v_add_u32_e32 v76, 0x1e000, v81\n"
      "ds_read_b128 v[60:63], v76\n"
      "ds_read_b128 v[0:3], v156 offset:0\n"
      "ds_read_b128 v[4:7], v156 offset:64\n"
      "ds_read_b128 v[8:11], v156 offset:128\n"
      "ds_read_b128 v[12:15], v156 offset:256\n"
      "ds_read_b128 v[16:19], v156 offset:320\n"
      "ds_read_b128 v[20:23], v156 offset:384\n"
      "ds_read_b128 v[24:27], v156 offset:512\n"
      "ds_read_b128 v[28:31], v156 offset:576\n"
      "ds_read_b128 v[32:35], v156 offset:640\n"
      "ds_read_b128 v[36:39], v156 offset:768\n"
      "ds_read_b128 v[40:43], v156 offset:832\n"
      "ds_read_b128 v[44:47], v156 offset:896\n"
      "ds_read_b128 v[48:51], v157 offset:192\n"
      "ds_read_b128 v[52:55], v157 offset:704\n"
      "s_mul_i32 s88, s38, 14336\n"
      "v_add_u32_e32 v82, s88, v81\n"
      "s_mov_b32 s89, 0x3f803f80\n"
      "s_waitcnt lgkmcnt(13)\n"
      "v_mfma_f32_16x16x32_bf16 v[64:67], a[144:147], v[0:3], 0\n"
      "v_dot2_f32_bf16 v72, v60, s89, 0\n"
      "v_dot2_f32_bf16 v74, v60, v60, 0\n"
      "v_mfma_f32_16x16x32_bf16 v[68:71], a[200:203], v[0:3], 0\n"
      "v_dot2_f32_bf16 v73, v62, s89, 0\n"
      "v_dot2_f32_bf16 v75, v62, v62, 0\n"
      "ds_read_b128 v[0:3], v82 offset:0\n"
      "s_waitcnt lgkmcnt(13)\n"
      "v_mfma_f32_16x16x32_bf16 v[64:67], a[148:151], v[4:7], v[64:67]\n"
      "v_dot2_f32_bf16 v72, v61, s89, v72\n"
      "v_dot2_f32_bf16 v74, v61, v61, v74\n"
      "v_mfma_f32_16x16x32_bf16 v[68:71], a[204:207], v[4:7], v[68:71]\n"
      "v_dot2_f32_bf16 v73, v63, s89, v73\n"
      "v_dot2_f32_bf16 v75, v63, v63, v75\n"
      "ds_read_b128 v[4:7], v82 offset:1024\n"
      "s_waitcnt lgkmcnt(13)\n"
      "v_mfma_f32_16x16x32_bf16 v[64:67], a[152:155], v[8:11], v[64:67]\n"
      "s_nop 0\n"
      "s_nop 0\n"
      "v_mfma_f32_16x16x32_bf16 v[68:71], a[208:211], v[8:11], v[68:71]\n"
      "v_add_f32_e32 v72, v72, v73\n"
      "v_add_f32_e32 v74, v74, v75\n"
      "ds_read_b128 v[8:11], v82 offset:2048\n"
      "s_waitcnt lgkmcnt(13)\n"
      "v_mfma_f32_16x16x32_bf16 v[64:67], a[156:159], v[12:15], v[64:67]\n"
      "v_add_f32_dpp v72, v72, v72 row_ror:8 row_mask:0xf bank_mask:0xf\n"
      "v_add_f32_dpp v74, v74, v74 row_ror:8 row_mask:0xf bank_mask:0xf\n"
      "v_mfma_f32_16x16x32_bf16 v[68:71], a[212:215], v[12:15], v[68:71]\n"
      "v_add_f32_dpp v72, v72, v72 row_ror:4 row_mask:0xf bank_mask:0xf\n"
      "v_add_f32_dpp v74, v74, v74 row_ror:4 row_mask:0xf bank_mask:0xf\n"
      "ds_read_b128 v[12:15], v82 offset:3072\n"
      "s_waitcnt lgkmcnt(13)\n"
      "v_mfma_f32_16x16x32_bf16 v[64:67], a[160:163], v[16:19], v[64:67]\n"
      "v_add_f32_dpp v72, v72, v72 row_ror:2 row_mask:0xf bank_mask:0xf\n"
      "v_add_f32_dpp v74, v74, v74 row_ror:2 row_mask:0xf bank_mask:0xf\n"
      "v_mfma_f32_16x16x32_bf16 v[68:71], a[216:219], v[16:19], v[68:71]\n"
      "v_add_f32_dpp v72, v72, v72 row_ror:1 row_mask:0xf bank_mask:0xf\n"
      "v_add_f32_dpp v74, v74, v74 row_ror:1 row_mask:0xf bank_mask:0xf\n"
      "ds_read_b128 v[16:19], v82 offset:4096\n"
      "s_waitcnt lgkmcnt(13)\n"
      "v_mfma_f32_16x16x32_bf16 v[64:67], a[164:167], v[20:23], v[64:67]\n"
      "v_mov_b32_e32 v76, v72\n"
      "v_mov_b32_e32 v77, v74\n"
      "v_mfma_f32_16x16x32_bf16 v[68:71], a[220:223], v[20:23], v[68:71]\n"
      "v_permlane16_swap_b32_e32 v76, v72\n"
      "v_permlane16_swap_b32_e32 v77, v74\n"
      "ds_read_b128 v[20:23], v82 offset:5120\n"
      "s_waitcnt lgkmcnt(13)\n"
      "v_mfma_f32_16x16x32_bf16 v[64:67], a[168:171], v[24:27], v[64:67]\n"
      "v_add_f32_e32 v72, v72, v76\n"
      "v_add_f32_e32 v74, v74, v77\n"
      "v_mfma_f32_16x16x32_bf16 v[68:71], a[224:227], v[24:27], v[68:71]\n"
      "v_mov_b32_e32 v76, v72\n"
      "v_mov_b32_e32 v77, v74\n"
      "ds_read_b128 v[24:27], v82 offset:6144\n"
      "s_waitcnt lgkmcnt(13)\n"
      "v_mfma_f32_16x16x32_bf16 v[64:67], a[172:175], v[28:31], v[64:67]\n"
      "v_permlane32_swap_b32_e32 v76, v72\n"
      "v_permlane32_swap_b32_e32 v77, v74\n"
      "v_mfma_f32_16x16x32_bf16 v[68:71], a[228:231], v[28:31], v[68:71]\n"
      "v_add_f32_e32 v72, v72, v76\n"
      "v_add_f32_e32 v74, v74, v77\n"
      "ds_read_b128 v[28:31], v82 offset:7168\n"
      "s_waitcnt lgkmcnt(13)\n"
      "v_mfma_f32_16x16x32_bf16 v[64:67], a[176:179], v[32:35], v[64:67]\n"
      "v_mul_f32_e32 v78, 0x3b124925, v72\n"
      "v_mul_f32_e32 v74, 0x3b124925, v74\n"
      "v_mfma_f32_16x16x32_bf16 v[68:71], a[232:235], v[32:35], v[68:71]\n"
      "v_fma_f32 v74, -v78, v78, v74\n"
      "v_cvt_pk_bf16_f32 v79, v78, v78\n"
      "ds_read_b128 v[32:35], v82 offset:8192\n"
      "s_waitcnt lgkmcnt(13)\n"
      "v_mfma_f32_16x16x32_bf16 v[64:67], a[180:183], v[36:39], v[64:67]\n"
      "v_max_f32_e32 v74, 0, v74\n"
      "v_and_b32_e32 v79, 0xffff0000, v79\n"
      "v_mfma_f32_16x16x32_bf16 v[68:71], a[236:239], v[36:39], v[68:71]\n"
      "v_cvt_pk_bf16_f32 v74, v74, v74\n"
      "v_xor_b32_e32 v79, 0x80000000, v79\n"
      "ds_read_b128 v[36:39], v82 offset:9216\n"
      "s_waitcnt lgkmcnt(13)\n"
      "v_mfma_f32_16x16x32_bf16 v[64:67], a[184:187], v[40:43], v[64:67]\n"
      "v_and_b32_e32 v74, 0xffff0000, v74\n"
      "v_add_f32_e32 v74, 0x3727c5ac, v74\n"
      "v_mfma_f32_16x16x32_bf16 v[68:71], a[240:243], v[40:43], v[68:71]\n"
      "v_cvt_pk_bf16_f32 v74, v74, v74\n"
      "v_and_b32_e32 v74, 0xffff0000, v74\n"
      "ds_read_b128 v[40:43], v82 offset:10240\n"
      "s_waitcnt lgkmcnt(13)\n"
      "v_mfma_f32_16x16x32_bf16 v[64:67], a[188:191], v[44:47], v[64:67]\n"
      "v_sqrt_f32_e32 v74, v74\n"
      "s_nop 0\n"
      "v_mfma_f32_16x16x32_bf16 v[68:71], a[244:247], v[44:47], v[68:71]\n"
      "v_cvt_pk_bf16_f32 v74, v74, v74\n"
      "v_and_b32_e32 v74, 0xffff0000, v74\n"
      "ds_read_b128 v[44:47], v82 offset:11264\n"
      "s_waitcnt lgkmcnt(13)\n"
      "v_mfma_f32_16x16x32_bf16 v[64:67], a[192:195], v[48:51], v[64:67]\n"
      "v_rcp_f32_e32 v80, v74\n"
      "v_mfma_f32_16x16x32_bf16 v[68:71], a[248:251], v[48:51], v[68:71]\n"
      "ds_read_b128 v[48:51], v82 offset:12288\n"
      "s_waitcnt lgkmcnt(13)\n"
      "v_mfma_f32_16x16x32_bf16 v[64:67], a[196:199], v[52:55], v[64:67]\n"
      "v_mfma_f32_16x16x32_bf16 v[68:71], a[252:255], v[52:55], v[68:71]\n"
      "ds_read_b128 v[52:55], v82 offset:13312\n"
      "s_waitcnt vmcnt(1)\n"
      "v_mov_b32_e32 v96, -1\n"
      "v_mov_b32_e32 v97, -1\n"
      "v_mov_b32_e32 v98, -1\n"
      "v_mov_b32_e32 v99, -1\n"
      "v_mov_b32_e32 v100, -1\n"
      "v_mov_b32_e32 v101, -1\n"
      "v_mov_b32_e32 v102, -1\n"
      "v_mov_b32_e32 v103, -1\n"
      "v_mov_b32_e32 v104, -1\n"
      "v_mov_b32_e32 v105, -1\n"
      "v_mov_b32_e32 v106, -1\n"
      "v_mov_b32_e32 v107, -1\n"
      "v_mov_b32_e32 v108, -1\n"
      "v_mov_b32_e32 v109, -1\n"
      "v_mov_b32_e32 v110, -1\n"
      "v_mov_b32_e32 v111, -1\n"
      "v_mbcnt_lo_u32_b32 v57, -1, 0\n"
      "v_mbcnt_hi_u32_b32 v57, -1, v57\n"
      "v_lshrrev_b32_e32 v83, 4, v57\n"
      "v_lshlrev_b32_e32 v83, 3, v83\n"
      "v_and_b32_e32 v57, 3, v57\n"
      "v_lshl_add_u32 v83, v57, 1, v83\n"
      "s_lshl_b32 s88, s38, 5\n"
      "s_add_u32 s88, s88, 0x1ea00\n"
      "v_add_u32_e32 v83, s88, v83\n"
      "v_cndmask_b32_e64 v56, v64, v65, s[96:97]\n"
      "v_cndmask_b32_e64 v57, v66, v67, s[96:97]\n"
      "v_cndmask_b32_e64 v58, v68, v69, s[96:97]\n"
      "v_cndmask_b32_e64 v59, v70, v71, s[96:97]\n"
      "v_cndmask_b32_e64 v56, v56, v57, s[84:85]\n"
      "v_cndmask_b32_e64 v58, v58, v59, s[84:85]\n"
      "v_fma_f32 v56, v79, v152, v56\n"
      "v_fma_f32 v58, v79, v153, v58\n"
      "v_mul_f32_e32 v56, v80, v56\n"
      "v_mul_f32_e32 v58, v80, v58\n"
      "v_cvt_pk_bf16_f32 v57, v56, v58\n"
      "v_lshlrev_b32_e32 v56, 16, v57\n"
      "v_and_b32_e32 v58, 0xffff0000, v57\n"
      "v_mul_f32_e32 v56, v56, v58\n"
      "v_cvt_pk_bf16_f32 v56, v56, v56\n"
      "s_mov_b32 exec_lo, 0xf000f\n"
      "s_mov_b32 exec_hi, 0xf000f\n"
      "ds_write_b16 v83, v56\n"
      "s_mov_b64 exec, -1\n"
      "s_mov_b32 s88, 0x8d000\n"
      "s_add_u32 s44, s36, s88\n"
      "s_addc_u32 s45, s37, 0\n"
      "s_mov_b32 exec_lo, 0x3fff3fff\n"
      "s_mov_b32 exec_hi, 0x3fff3fff\n"
      "s_add_u32 s46, s44, 0x0\n"
      "s_addc_u32 s47, s45, 0\n"
      "global_load_dwordx4 v[96:99], v154, s[46:47] sc1\n"
      "s_add_u32 s46, s44, 0xe00\n"
      "s_addc_u32 s47, s45, 0\n"
      "global_load_dwordx4 v[100:103], v154, s[46:47] sc1\n"
      "s_add_u32 s46, s44, 0x1c00\n"
      "s_addc_u32 s47, s45, 0\n"
      "global_load_dwordx4 v[104:107], v154, s[46:47] sc1\n"
      "s_add_u32 s46, s44, 0x2a00\n"
      "s_addc_u32 s47, s45, 0\n"
      "global_load_dwordx4 v[108:111], v154, s[46:47] sc1\n"
      "s_mov_b64 exec, -1\n"
      "s_mov_b32 s88, 0x90800\n"
      "s_mul_i32 s89, s42, 28672\n"
      "s_add_u32 s88, s88, s89\n"
      "s_mul_i32 s89, s39, 896\n"
      "s_add_u32 s88, s88, s89\n"
      "s_mul_i32 s89, s38, 224\n"
      "s_add_u32 s88, s88, s89\n"
      "s_add_u32 s44, s36, s88\n"
      "s_addc_u32 s45, s37, 0\n"
      "v_mbcnt_lo_u32_b32 v93, -1, 0\n"
      "v_mbcnt_hi_u32_b32 v93, -1, v93\n"
      "v_and_b32_e32 v92, 15, v93\n"
      "v_lshlrev_b32_e32 v92, 5, v92\n"
      "v_lshrrev_b32_e32 v93, 4, v93\n"
      "v_lshl_add_u32 v92, v93, 3, v92\n"
      "s_waitcnt lgkmcnt(0)\n"
      "s_barrier\n"
      "ds_read_b128 v[56:59], v156 offset:2560\n"
      "ds_read_b128 v[60:63], v156 offset:2624\n"
      "s_waitcnt lgkmcnt(1)\n"
      "v_mfma_f32_16x16x32_bf16 v[64:67], v[0:3], v[56:59], 0\n"
      "v_mfma_f32_16x16x32_bf16 v[68:71], v[8:11], v[56:59], 0\n"
      "s_waitcnt lgkmcnt(0)\n"
      "v_mfma_f32_16x16x32_bf16 v[64:67], v[4:7], v[60:63], v[64:67]\n"
      "v_mfma_f32_16x16x32_bf16 v[72:75], v[16:19], v[56:59], 0\n"
      "v_mfma_f32_16x16x32_bf16 v[68:71], v[12:15], v[60:63], v[68:71]\n"
      "v_mfma_f32_16x16x32_bf16 v[76:79], v[24:27], v[56:59], 0\n"
      "s_nop 4\n"
      "v_cvt_pk_bf16_f32 v64, v64, v65\n"
      "v_cvt_pk_bf16_f32 v65, v66, v67\n"
      "v_min_u32_e32 v64, -2, v64\n"
      "v_min_u32_e32 v65, -2, v65\n"
      "v_mfma_f32_16x16x32_bf16 v[72:75], v[20:23], v[60:63], v[72:75]\n"
      "v_mfma_f32_16x16x32_bf16 v[80:83], v[32:35], v[56:59], 0\n"
      "v_cvt_pk_bf16_f32 v94, v68, v69\n"
      "v_cvt_pk_bf16_f32 v95, v70, v71\n"
      "v_min_u32_e32 v94, -2, v94\n"
      "v_min_u32_e32 v95, -2, v95\n"
      "s_mov_b32 s46, 0x20002\n"
      "s_mov_b32 s47, 0x20002\n"
      "v_cndmask_b32_e64 v64, v64, v94, s[46:47]\n"
      "v_cndmask_b32_e64 v65, v65, v95, s[46:47]\n"
      "v_mfma_f32_16x16x32_bf16 v[76:79], v[28:31], v[60:63], v[76:79]\n"
      "v_mfma_f32_16x16x32_bf16 v[84:87], v[40:43], v[56:59], 0\n"
      "v_cvt_pk_bf16_f32 v94, v72, v73\n"
      "v_cvt_pk_bf16_f32 v95, v74, v75\n"
      "v_min_u32_e32 v94, -2, v94\n"
      "v_min_u32_e32 v95, -2, v95\n"
      "s_mov_b32 s46, 0x40004\n"
      "s_mov_b32 s47, 0x40004\n"
      "v_cndmask_b32_e64 v64, v64, v94, s[46:47]\n"
      "v_cndmask_b32_e64 v65, v65, v95, s[46:47]\n"
      "v_mfma_f32_16x16x32_bf16 v[80:83], v[36:39], v[60:63], v[80:83]\n"
      "v_mfma_f32_16x16x32_bf16 v[88:91], v[48:51], v[56:59], 0\n"
      "v_cvt_pk_bf16_f32 v94, v76, v77\n"
      "v_cvt_pk_bf16_f32 v95, v78, v79\n"
      "v_min_u32_e32 v94, -2, v94\n"
      "v_min_u32_e32 v95, -2, v95\n"
      "s_mov_b32 s46, 0x80008\n"
      "s_mov_b32 s47, 0x80008\n"
      "v_cndmask_b32_e64 v64, v64, v94, s[46:47]\n"
      "v_cndmask_b32_e64 v65, v65, v95, s[46:47]\n"
      "v_mfma_f32_16x16x32_bf16 v[84:87], v[44:47], v[60:63], v[84:87]\n"
      "v_mfma_f32_16x16x32_bf16 v[88:91], v[52:55], v[60:63], v[88:91]\n"
      "v_cvt_pk_bf16_f32 v94, v80, v81\n"
      "v_cvt_pk_bf16_f32 v95, v82, v83\n"
      "v_min_u32_e32 v94, -2, v94\n"
      "v_min_u32_e32 v95, -2, v95\n"
      "s_mov_b32 s46, 0x100010\n"
      "s_mov_b32 s47, 0x100010\n"
      "v_cndmask_b32_e64 v64, v64, v94, s[46:47]\n"
      "v_cndmask_b32_e64 v65, v65, v95, s[46:47]\n"
      "v_cvt_pk_bf16_f32 v94, v84, v85\n"
      "v_cvt_pk_bf16_f32 v95, v86, v87\n"
      "v_min_u32_e32 v94, -2, v94\n"
      "v_min_u32_e32 v95, -2, v95\n"
      "s_mov_b32 s46, 0x200020\n"
      "s_mov_b32 s47, 0x200020\n"
      "v_cndmask_b32_e64 v64, v64, v94, s[46:47]\n"
      "v_cndmask_b32_e64 v65, v65, v95, s[46:47]\n"
      "v_cvt_pk_bf16_f32 v94, v88, v89\n"
      "v_cvt_pk_bf16_f32 v95, v90, v91\n"
      "v_min_u32_e32 v94, -2, v94\n"
      "v_min_u32_e32 v95, -2, v95\n"
      "s_mov_b32 s46, 0x400040\n"
      "s_mov_b32 s47, 0x400040\n"
      "v_cndmask_b32_e64 v64, v64, v94, s[46:47]\n"
      "v_cndmask_b32_e64 v65, v65, v95, s[46:47]\n"
      "s_mov_b32 exec_lo, 0x7f007f\n"
      "s_mov_b32 exec_hi, 0x7f007f\n"
      "global_store_dwordx2 v92, v[64:65], s[44:45] sc1\n"
      "s_mov_b64 exec, -1\n"
      "s_nop 0\n"
      "s_branch BCJ_92_%=\n"
      "s_nop 0\n"
      "NOBC_91_%=:\n"
      "s_waitcnt vmcnt(0)\n"
      "v_mov_b32_e32 v96, -1\n"
      "v_mov_b32_e32 v97, -1\n"
      "v_mov_b32_e32 v98, -1\n"
      "v_mov_b32_e32 v99, -1\n"
      "v_mov_b32_e32 v100, -1\n"
      "v_mov_b32_e32 v101, -1\n"
      "v_mov_b32_e32 v102, -1\n"
      "v_mov_b32_e32 v103, -1\n"
      "v_mov_b32_e32 v104, -1\n"
      "v_mov_b32_e32 v105, -1\n"
      "v_mov_b32_e32 v106, -1\n"
      "v_mov_b32_e32 v107, -1\n"
      "v_mov_b32_e32 v108, -1\n"
      "v_mov_b32_e32 v109, -1\n"
      "v_mov_b32_e32 v110, -1\n"
      "v_mov_b32_e32 v111, -1\n"
      "s_nop 4\n"
      "BCJ_92_%=:\n"
      "s_mov_b32 s42, 3\n"
      "v_mov_b32_e32 v56, v140\n"
      "v_mov_b32_e32 v140, v142\n"
      "v_mov_b32_e32 v142, v56\n"
      "v_mov_b32_e32 v56, v141\n"
      "v_mov_b32_e32 v141, v143\n"
      "v_mov_b32_e32 v143, v56\n"
      "s_mov_b32 s84, 0xcccccccc\n"
      "s_mov_b32 s85, 0xcccccccc\n"
      "v_cndmask_b32_e64 v84, 0, v159, s[64:65]\n"
      "v_cndmask_b32_e64 v85, 0, v159, s[66:67]\n"
      "v_cndmask_b32_e64 v86, 0, v159, s[68:69]\n"
      "v_cndmask_b32_e64 v87, 0, v159, s[70:71]\n"
      "s_mov_b32 s88, 0x82800\n"
      "s_mul_i32 s89, s42, 14336\n"
      "s_add_u32 s88, s88, s89\n"
      "s_add_u32 s44, s36, s88\n"
      "s_addc_u32 s45, s37, 0\n"
      "s_waitcnt vmcnt(1)\n"
      "s_mov_b32 s94, 0x3fff3fff\n"
      "s_mov_b32 s95, 0x3fff3fff\n"
      "s_mov_b64 s[48:49], s[94:95]\n"
      "s_mov_b64 s[50:51], s[94:95]\n"
      "s_mov_b64 s[52:53], s[94:95]\n"
      "s_mov_b64 s[54:55], s[94:95]\n"
      "s_mov_b32 s43, 0\n"
      "s_nop 0\n"
      "s_branch GC_95_%=\n"
      "s_nop 0\n"
      "GT_93_%=:\n"
      "s_mov_b64 exec, s[48:49]\n"
      "s_nop 0\n"
      "s_cbranch_execz GS_96_%=\n"
      "s_add_u32 s46, s44, 0x0\n"
      "s_addc_u32 s47, s45, 0\n"
      "global_load_dwordx4 v[96:99], v154, s[46:47] sc1\n"
      "GS_96_%=:\n"
      "s_mov_b64 exec, s[50:51]\n"
      "s_nop 0\n"
      "s_cbranch_execz GS_97_%=\n"
      "s_add_u32 s46, s44, 0xe00\n"
      "s_addc_u32 s47, s45, 0\n"
      "global_load_dwordx4 v[100:103], v154, s[46:47] sc1\n"
      "GS_97_%=:\n"
      "s_mov_b64 exec, s[52:53]\n"
      "s_nop 0\n"
      "s_cbranch_execz GS_98_%=\n"
      "s_add_u32 s46, s44, 0x1c00\n"
      "s_addc_u32 s47, s45, 0\n"
      "global_load_dwordx4 v[104:107], v154, s[46:47] sc1\n"
      "GS_98_%=:\n"
      "s_mov_b64 exec, s[54:55]\n"
      "s_nop 0\n"
      "s_cbranch_execz GS_99_%=\n"
      "s_add_u32 s46, s44, 0x2a00\n"
      "s_addc_u32 s47, s45, 0\n"
      "global_load_dwordx4 v[108:111], v154, s[46:47] sc1\n"
      "GS_99_%=:\n"
      "s_mov_b64 exec, -1\n"
      "s_sleep 1\n"
      "s_nop 0\n"
      "GC_95_%=:\n"
      "v_max3_u32 v56, v96, v97, v98\n"
      "v_max_u32_e32 v56, v56, v99\n"
      "v_cmp_eq_u32_e64 s[48:49], -1, v56\n"
      "v_max3_u32 v57, v100, v101, v102\n"
      "v_max_u32_e32 v57, v57, v103\n"
      "v_cmp_eq_u32_e64 s[50:51], -1, v57\n"
      "v_max3_u32 v56, v104, v105, v106\n"
      "v_max_u32_e32 v56, v56, v107\n"
      "v_cmp_eq_u32_e64 s[52:53], -1, v56\n"
      "v_max3_u32 v57, v108, v109, v110\n"
      "v_max_u32_e32 v57, v57, v111\n"
      "v_cmp_eq_u32_e64 s[54:55], -1, v57\n"
      "s_and_b64 s[48:49], s[48:49], s[94:95]\n"
      "s_and_b64 s[50:51], s[50:51], s[94:95]\n"
      "s_and_b64 s[52:53], s[52:53], s[94:95]\n"
      "s_and_b64 s[54:55], s[54:55], s[94:95]\n"
      "s_mov_b64 s[88:89], s[48:49]\n"
      "s_or_b64 s[88:89], s[88:89], s[50:51]\n"
      "s_or_b64 s[88:89], s[88:89], s[52:53]\n"
      "s_or_b64 s[88:89], s[88:89], s[54:55]\n"
      "s_cmp_eq_u64 s[88:89], 0\n"
      "s_nop 0\n"
      "s_cbranch_scc1 GD_94_%=\n"
      "s_add_u32 s43, s43, 1\n"
      "s_cmp_lt_u32 s43, 0x80000\n"
      "s_waitcnt vmcnt(4)\n"
      "s_nop 0\n"
      "s_cbranch_scc1 GT_93_%=\n"
      "s_nop 0\n"
      "GD_94_%=:\n"
      "v_mfma_f32_16x16x32_bf16 v[88:91], v[96:99], v[84:87], 0\n"
      "v_mfma_f32_16x16x32_bf16 v[88:91], v[100:103], v[84:87], v[88:91]\n"
      "v_mfma_f32_16x16x32_bf16 v[88:91], v[104:107], v[84:87], v[88:91]\n"
      "v_mfma_f32_16x16x32_bf16 v[88:91], v[108:111], v[84:87], v[88:91]\n"
      "s_nop 7\n"
      "v_cndmask_b32_e64 v56, v88, v90, s[62:63]\n"
      "v_cndmask_b32_e64 v57, v89, v91, s[62:63]\n"
      "v_cvt_pk_bf16_f32 v58, v56, v57\n"
      "v_lshlrev_b32_e32 v56, 16, v58\n"
      "v_and_b32_e32 v57, 0xffff0000, v58\n"
      "v_add_f32_e32 v56, v140, v56\n"
      "v_add_f32_e32 v57, v141, v57\n"
      "v_cvt_pk_bf16_f32 v58, v56, v57\n"
      "s_cmp_ge_u32 s39, 28\n"
      "v_lshlrev_b32_e32 v140, 16, v58\n"
      "v_and_b32_e32 v141, 0xffff0000, v58\n"
      "s_nop 4\n"
      "s_cbranch_scc1 NOBC_100_%=\n"
      "s_mov_b32 exec_lo, -1\n"
      "s_mov_b32 exec_hi, 0x00ffffff\n"
      "ds_write_b32 v155, v58 offset:0\n"
      "s_mov_b64 exec, -1\n"
      "s_waitcnt lgkmcnt(0)\n"
      "s_barrier\n"
      "v_mbcnt_lo_u32_b32 v81, -1, 0\n"
      "v_mbcnt_hi_u32_b32 v81, -1, v81\n"
      "v_lshlrev_b32_e32 v81, 4, v81\n"
      "v_add_u32_e32 v76, 0x1e000, v81\n"
      "ds_read_b128 v[60:63], v76\n"
      "ds_read_b128 v[0:3], v156 offset:0\n"
      "ds_read_b128 v[4:7], v156 offset:64\n"
      "ds_read_b128 v[8:11], v156 offset:128\n"
      "ds_read_b128 v[12:15], v156 offset:256\n"
      "ds_read_b128 v[16:19], v156 offset:320\n"
      "ds_read_b128 v[20:23], v156 offset:384\n"
      "ds_read_b128 v[24:27], v156 offset:512\n"
      "ds_read_b128 v[28:31], v156 offset:576\n"
      "ds_read_b128 v[32:35], v156 offset:640\n"
      "ds_read_b128 v[36:39], v156 offset:768\n"
      "ds_read_b128 v[40:43], v156 offset:832\n"
      "ds_read_b128 v[44:47], v156 offset:896\n"
      "ds_read_b128 v[48:51], v157 offset:192\n"
      "ds_read_b128 v[52:55], v157 offset:704\n"
      "s_mul_i32 s88, s38, 14336\n"
      "v_add_u32_e32 v82, s88, v81\n"
      "s_mov_b32 s89, 0x3f803f80\n"
      "s_waitcnt lgkmcnt(13)\n"
      "v_mfma_f32_16x16x32_bf16 v[64:67], a[144:147], v[0:3], 0\n"
      "v_dot2_f32_bf16 v72, v60, s89, 0\n"
      "v_dot2_f32_bf16 v74, v60, v60, 0\n"
      "v_mfma_f32_16x16x32_bf16 v[68:71], a[200:203], v[0:3], 0\n"
      "v_dot2_f32_bf16 v73, v62, s89, 0\n"
      "v_dot2_f32_bf16 v75, v62, v62, 0\n"
      "ds_read_b128 v[0:3], v82 offset:0\n"
      "s_waitcnt lgkmcnt(13)\n"
      "v_mfma_f32_16x16x32_bf16 v[64:67], a[148:151], v[4:7], v[64:67]\n"
      "v_dot2_f32_bf16 v72, v61, s89, v72\n"
      "v_dot2_f32_bf16 v74, v61, v61, v74\n"
      "v_mfma_f32_16x16x32_bf16 v[68:71], a[204:207], v[4:7], v[68:71]\n"
      "v_dot2_f32_bf16 v73, v63, s89, v73\n"
      "v_dot2_f32_bf16 v75, v63, v63, v75\n"
      "ds_read_b128 v[4:7], v82 offset:1024\n"
      "s_waitcnt lgkmcnt(13)\n"
      "v_mfma_f32_16x16x32_bf16 v[64:67], a[152:155], v[8:11], v[64:67]\n"
      "s_nop 0\n"
      "s_nop 0\n"
      "v_mfma_f32_16x16x32_bf16 v[68:71], a[208:211], v[8:11], v[68:71]\n"
      "v_add_f32_e32 v72, v72, v73\n"
      "v_add_f32_e32 v74, v74, v75\n"
      "ds_read_b128 v[8:11], v82 offset:2048\n"
      "s_waitcnt lgkmcnt(13)\n"
      "v_mfma_f32_16x16x32_bf16 v[64:67], a[156:159], v[12:15], v[64:67]\n"
      "v_add_f32_dpp v72, v72, v72 row_ror:8 row_mask:0xf bank_mask:0xf\n"
      "v_add_f32_dpp v74, v74, v74 row_ror:8 row_mask:0xf bank_mask:0xf\n"
      "v_mfma_f32_16x16x32_bf16 v[68:71], a[212:215], v[12:15], v[68:71]\n"
      "v_add_f32_dpp v72, v72, v72 row_ror:4 row_mask:0xf bank_mask:0xf\n"
      "v_add_f32_dpp v74, v74, v74 row_ror:4 row_mask:0xf bank_mask:0xf\n"
      "ds_read_b128 v[12:15], v82 offset:3072\n"
      "s_waitcnt lgkmcnt(13)\n"
      "v_mfma_f32_16x16x32_bf16 v[64:67], a[160:163], v[16:19], v[64:67]\n"
      "v_add_f32_dpp v72, v72, v72 row_ror:2 row_mask:0xf bank_mask:0xf\n"
      "v_add_f32_dpp v74, v74, v74 row_ror:2 row_mask:0xf bank_mask:0xf\n"
      "v_mfma_f32_16x16x32_bf16 v[68:71], a[216:219], v[16:19], v[68:71]\n"
      "v_add_f32_dpp v72, v72, v72 row_ror:1 row_mask:0xf bank_mask:0xf\n"
      "v_add_f32_dpp v74, v74, v74 row_ror:1 row_mask:0xf bank_mask:0xf\n"
      "ds_read_b128 v[16:19], v82 offset:4096\n"
      "s_waitcnt lgkmcnt(13)\n"
      "v_mfma_f32_16x16x32_bf16 v[64:67], a[164:167], v[20:23], v[64:67]\n"
      "v_mov_b32_e32 v76, v72\n"
      "v_mov_b32_e32 v77, v74\n"
      "v_mfma_f32_16x16x32_bf16 v[68:71], a[220:223], v[20:23], v[68:71]\n"
      "v_permlane16_swap_b32_e32 v76, v72\n"
      "v_permlane16_swap_b32_e32 v77, v74\n"
      "ds_read_b128 v[20:23], v82 offset:5120\n"
      "s_waitcnt lgkmcnt(13)\n"
      "v_mfma_f32_16x16x32_bf16 v[64:67], a[168:171], v[24:27], v[64:67]\n"
      "v_add_f32_e32 v72, v72, v76\n"
      "v_add_f32_e32 v74, v74, v77\n"
      "v_mfma_f32_16x16x32_bf16 v[68:71], a[224:227], v[24:27], v[68:71]\n"
      "v_mov_b32_e32 v76, v72\n"
      "v_mov_b32_e32 v77, v74\n"
      "ds_read_b128 v[24:27], v82 offset:6144\n"
      "s_waitcnt lgkmcnt(13)\n"
      "v_mfma_f32_16x16x32_bf16 v[64:67], a[172:175], v[28:31], v[64:67]\n"
      "v_permlane32_swap_b32_e32 v76, v72\n"
      "v_permlane32_swap_b32_e32 v77, v74\n"
      "v_mfma_f32_16x16x32_bf16 v[68:71], a[228:231], v[28:31], v[68:71]\n"
      "v_add_f32_e32 v72, v72, v76\n"
      "v_add_f32_e32 v74, v74, v77\n"
      "ds_read_b128 v[28:31], v82 offset:7168\n"
      "s_waitcnt lgkmcnt(13)\n"
      "v_mfma_f32_16x16x32_bf16 v[64:67], a[176:179], v[32:35], v[64:67]\n"
      "v_mul_f32_e32 v78, 0x3b124925, v72\n"
      "v_mul_f32_e32 v74, 0x3b124925, v74\n"
      "v_mfma_f32_16x16x32_bf16 v[68:71], a[232:235], v[32:35], v[68:71]\n"
      "v_fma_f32 v74, -v78, v78, v74\n"
      "v_cvt_pk_bf16_f32 v79, v78, v78\n"
      "ds_read_b128 v[32:35], v82 offset:8192\n"
      "s_waitcnt lgkmcnt(13)\n"
      "v_mfma_f32_16x16x32_bf16 v[64:67], a[180:183], v[36:39], v[64:67]\n"
      "v_max_f32_e32 v74, 0, v74\n"
      "v_and_b32_e32 v79, 0xffff0000, v79\n"
      "v_mfma_f32_16x16x32_bf16 v[68:71], a[236:239], v[36:39], v[68:71]\n"
      "v_cvt_pk_bf16_f32 v74, v74, v74\n"
      "v_xor_b32_e32 v79, 0x80000000, v79\n"
      "ds_read_b128 v[36:39], v82 offset:9216\n"
      "s_waitcnt lgkmcnt(13)\n"
      "v_mfma_f32_16x16x32_bf16 v[64:67], a[184:187], v[40:43], v[64:67]\n"
      "v_and_b32_e32 v74, 0xffff0000, v74\n"
      "v_add_f32_e32 v74, 0x3727c5ac, v74\n"
      "v_mfma_f32_16x16x32_bf16 v[68:71], a[240:243], v[40:43], v[68:71]\n"
      "v_cvt_pk_bf16_f32 v74, v74, v74\n"
      "v_and_b32_e32 v74, 0xffff0000, v74\n"
      "ds_read_b128 v[40:43], v82 offset:10240\n"
      "s_waitcnt lgkmcnt(13)\n"
      "v_mfma_f32_16x16x32_bf16 v[64:67], a[188:191], v[44:47], v[64:67]\n"
      "v_sqrt_f32_e32 v74, v74\n"
      "s_nop 0\n"
      "v_mfma_f32_16x16x32_bf16 v[68:71], a[244:247], v[44:47], v[68:71]\n"
      "v_cvt_pk_bf16_f32 v74, v74, v74\n"
      "v_and_b32_e32 v74, 0xffff0000, v74\n"
      "ds_read_b128 v[44:47], v82 offset:11264\n"
      "s_waitcnt lgkmcnt(13)\n"
      "v_mfma_f32_16x16x32_bf16 v[64:67], a[192:195], v[48:51], v[64:67]\n"
      "v_rcp_f32_e32 v80, v74\n"
      "v_mfma_f32_16x16x32_bf16 v[68:71], a[248:251], v[48:51], v[68:71]\n"
      "ds_read_b128 v[48:51], v82 offset:12288\n"
      "s_waitcnt lgkmcnt(13)\n"
      "v_mfma_f32_16x16x32_bf16 v[64:67], a[196:199], v[52:55], v[64:67]\n"
      "v_mfma_f32_16x16x32_bf16 v[68:71], a[252:255], v[52:55], v[68:71]\n"
      "ds_read_b128 v[52:55], v82 offset:13312\n"
      "v_mbcnt_lo_u32_b32 v57, -1, 0\n"
      "v_mbcnt_hi_u32_b32 v57, -1, v57\n"
      "v_lshrrev_b32_e32 v83, 4, v57\n"
      "v_lshlrev_b32_e32 v83, 3, v83\n"
      "v_and_b32_e32 v57, 3, v57\n"
      "v_lshl_add_u32 v83, v57, 1, v83\n"
      "s_lshl_b32 s88, s38, 5\n"
      "s_add_u32 s88, s88, 0x1ea00\n"
      "v_add_u32_e32 v83, s88, v83\n"
      "v_cndmask_b32_e64 v56, v64, v65, s[96:97]\n"
      "v_cndmask_b32_e64 v57, v66, v67, s[96:97]\n"
      "v_cndmask_b32_e64 v58, v68, v69, s[96:97]\n"
      "v_cndmask_b32_e64 v59, v70, v71, s[96:97]\n"
      "v_cndmask_b32_e64 v56, v56, v57, s[84:85]\n"
      "v_cndmask_b32_e64 v58, v58, v59, s[84:85]\n"
      "v_fma_f32 v56, v79, v152, v56\n"
      "v_fma_f32 v58, v79, v153, v58\n"
      "v_mul_f32_e32 v56, v80, v56\n"
      "v_mul_f32_e32 v58, v80, v58\n"
      "v_cvt_pk_bf16_f32 v57, v56, v58\n"
      "v_lshlrev_b32_e32 v56, 16, v57\n"
      "v_and_b32_e32 v58, 0xffff0000, v57\n"
      "v_mul_f32_e32 v56, v56, v58\n"
      "v_cvt_pk_bf16_f32 v56, v56, v56\n"
      "s_mov_b32 exec_lo, 0xf000f\n"
      "s_mov_b32 exec_hi, 0xf000f\n"
      "ds_write_b16 v83, v56\n"
      "s_mov_b64 exec, -1\n"
      "s_mov_b32 s88, 0x90800\n"
      "s_mul_i32 s89, s42, 28672\n"
      "s_add_u32 s88, s88, s89\n"
      "s_mul_i32 s89, s39, 896\n"
      "s_add_u32 s88, s88, s89\n"
      "s_mul_i32 s89, s38, 224\n"
      "s_add_u32 s88, s88, s89\n"
      "s_add_u32 s44, s36, s88\n"
      "s_addc_u32 s45, s37, 0\n"
      "v_mbcnt_lo_u32_b32 v93, -1, 0\n"
      "v_mbcnt_hi_u32_b32 v93, -1, v93\n"
      "v_and_b32_e32 v92, 15, v93\n"
      "v_lshlrev_b32_e32 v92, 5, v92\n"
      "v_lshrrev_b32_e32 v93, 4, v93\n"
      "v_lshl_add_u32 v92, v93, 3, v92\n"
      "s_waitcnt lgkmcnt(0)\n"
      "s_barrier\n"
      "ds_read_b128 v[56:59], v156 offset:2560\n"
      "ds_read_b128 v[60:63], v156 offset:2624\n"
      "s_waitcnt lgkmcnt(1)\n"
      "v_mfma_f32_16x16x32_bf16 v[64:67], v[0:3], v[56:59], 0\n"
      "v_mfma_f32_16x16x32_bf16 v[68:71], v[8:11], v[56:59], 0\n"
      "s_waitcnt lgkmcnt(0)\n"
      "v_mfma_f32_16x16x32_bf16 v[64:67], v[4:7], v[60:63], v[64:67]\n"
      "v_mfma_f32_16x16x32_bf16 v[72:75], v[16:19], v[56:59], 0\n"
      "v_mfma_f32_16x16x32_bf16 v[68:71], v[12:15], v[60:63], v[68:71]\n"
      "v_mfma_f32_16x16x32_bf16 v[76:79], v[24:27], v[56:59], 0\n"
      "s_nop 4\n"
      "v_cvt_pk_bf16_f32 v64, v64, v65\n"
      "v_cvt_pk_bf16_f32 v65, v66, v67\n"
      "v_min_u32_e32 v64, -2, v64\n"
      "v_min_u32_e32 v65, -2, v65\n"
      "v_mfma_f32_16x16x32_bf16 v[72:75], v[20:23], v[60:63], v[72:75]\n"
      "v_mfma_f32_16x16x32_bf16 v[80:83], v[32:35], v[56:59], 0\n"
      "v_cvt_pk_bf16_f32 v94, v68, v69\n"
      "v_cvt_pk_bf16_f32 v95, v70, v71\n"
      "v_min_u32_e32 v94, -2, v94\n"
      "v_min_u32_e32 v95, -2, v95\n"
      "s_mov_b32 s46, 0x20002\n"
      "s_mov_b32 s47, 0x20002\n"
      "v_cndmask_b32_e64 v64, v64, v94, s[46:47]\n"
      "v_cndmask_b32_e64 v65, v65, v95, s[46:47]\n"
      "v_mfma_f32_16x16x32_bf16 v[76:79], v[28:31], v[60:63], v[76:79]\n"
      "v_mfma_f32_16x16x32_bf16 v[84:87], v[40:43], v[56:59], 0\n"
      "v_cvt_pk_bf16_f32 v94, v72, v73\n"
      "v_cvt_pk_bf16_f32 v95, v74, v75\n"
      "v_min_u32_e32 v94, -2, v94\n"
      "v_min_u32_e32 v95, -2, v95\n"
      "s_mov_b32 s46, 0x40004\n"
      "s_mov_b32 s47, 0x40004\n"
      "v_cndmask_b32_e64 v64, v64, v94, s[46:47]\n"
      "v_cndmask_b32_e64 v65, v65, v95, s[46:47]\n"
      "v_mfma_f32_16x16x32_bf16 v[80:83], v[36:39], v[60:63], v[80:83]\n"
      "v_mfma_f32_16x16x32_bf16 v[88:91], v[48:51], v[56:59], 0\n"
      "v_cvt_pk_bf16_f32 v94, v76, v77\n"
      "v_cvt_pk_bf16_f32 v95, v78, v79\n"
      "v_min_u32_e32 v94, -2, v94\n"
      "v_min_u32_e32 v95, -2, v95\n"
      "s_mov_b32 s46, 0x80008\n"
      "s_mov_b32 s47, 0x80008\n"
      "v_cndmask_b32_e64 v64, v64, v94, s[46:47]\n"
      "v_cndmask_b32_e64 v65, v65, v95, s[46:47]\n"
      "v_mfma_f32_16x16x32_bf16 v[84:87], v[44:47], v[60:63], v[84:87]\n"
      "v_mfma_f32_16x16x32_bf16 v[88:91], v[52:55], v[60:63], v[88:91]\n"
      "v_cvt_pk_bf16_f32 v94, v80, v81\n"
      "v_cvt_pk_bf16_f32 v95, v82, v83\n"
      "v_min_u32_e32 v94, -2, v94\n"
      "v_min_u32_e32 v95, -2, v95\n"
      "s_mov_b32 s46, 0x100010\n"
      "s_mov_b32 s47, 0x100010\n"
      "v_cndmask_b32_e64 v64, v64, v94, s[46:47]\n"
      "v_cndmask_b32_e64 v65, v65, v95, s[46:47]\n"
      "v_cvt_pk_bf16_f32 v94, v84, v85\n"
      "v_cvt_pk_bf16_f32 v95, v86, v87\n"
      "v_min_u32_e32 v94, -2, v94\n"
      "v_min_u32_e32 v95, -2, v95\n"
      "s_mov_b32 s46, 0x200020\n"
      "s_mov_b32 s47, 0x200020\n"
      "v_cndmask_b32_e64 v64, v64, v94, s[46:47]\n"
      "v_cndmask_b32_e64 v65, v65, v95, s[46:47]\n"
      "v_cvt_pk_bf16_f32 v94, v88, v89\n"
      "v_cvt_pk_bf16_f32 v95, v90, v91\n"
      "v_min_u32_e32 v94, -2, v94\n"
      "v_min_u32_e32 v95, -2, v95\n"
      "s_mov_b32 s46, 0x400040\n"
      "s_mov_b32 s47, 0x400040\n"
      "v_cndmask_b32_e64 v64, v64, v94, s[46:47]\n"
      "v_cndmask_b32_e64 v65, v65, v95, s[46:47]\n"
      "s_mov_b32 exec_lo, 0x7f007f\n"
      "s_mov_b32 exec_hi, 0x7f007f\n"
      "global_store_dwordx2 v92, v[64:65], s[44:45] sc1\n"
      "s_mov_b64 exec, -1\n"
      "s_waitcnt vmcnt(1)\n"
      "s_nop 0\n"
      "s_branch BCJ_101_%=\n"
      "s_nop 0\n"
      "NOBC_100_%=:\n"
      "s_waitcnt vmcnt(0)\n"
      "s_nop 0\n"
      "BCJ_101_%=:\n"
      "v_mov_b32_e32 v96, -1\n"
      "v_mov_b32_e32 v97, -1\n"
      "v_mov_b32_e32 v98, -1\n"
      "v_mov_b32_e32 v99, -1\n"
      "v_mov_b32_e32 v100, -1\n"
      "v_mov_b32_e32 v101, -1\n"
      "v_mov_b32_e32 v102, -1\n"
      "v_mov_b32_e32 v103, -1\n"
      "v_mov_b32_e32 v104, -1\n"
      "v_mov_b32_e32 v105, -1\n"
      "v_mov_b32_e32 v106, -1\n"
      "v_mov_b32_e32 v107, -1\n"
      "v_mov_b32_e32 v108, -1\n"
      "v_mov_b32_e32 v109, -1\n"
      "v_mov_b32_e32 v110, -1\n"
      "v_mov_b32_e32 v111, -1\n"
      "v_mov_b32_e32 v112, -1\n"
      "v_mov_b32_e32 v113, -1\n"
      "v_mov_b32_e32 v114, -1\n"
      "v_mov_b32_e32 v115, -1\n"
      "v_mov_b32_e32 v116, -1\n"
      "v_mov_b32_e32 v117, -1\n"
      "v_mov_b32_e32 v118, -1\n"
      "v_mov_b32_e32 v119, -1\n"
      "v_mov_b32_e32 v120, -1\n"
      "v_mov_b32_e32 v121, -1\n"
      "v_mov_b32_e32 v122, -1\n"
      "v_mov_b32_e32 v123, -1\n"
      "s_mov_b32 s42, 1\n"
      "v_mov_b32_e32 v56, v140\n"
      "v_mov_b32_e32 v140, v142\n"
      "v_mov_b32_e32 v142, v56\n"
      "v_mov_b32_e32 v56, v141\n"
      "v_mov_b32_e32 v141, v143\n"
      "v_mov_b32_e32 v143, v56\n"
      "s_cmp_ge_u32 s39, 28\n"
      "s_nop 3\n"
      "s_cbranch_scc1 NOF_102_%=\n"
      "s_mul_i32 s88, s39, 37\n"
      "s_lshr_b32 s88, s88, 8\n"
      "s_add_u32 s89, s88, 1\n"
      "s_and_b32 s89, s89, 3\n"
      "s_cmp_lg_u32 s89, s38\n"
      "s_nop 0\n"
      "s_cbranch_scc1 NOF_102_%=\n"
      "s_mul_i32 s88, s88, 7\n"
      "s_sub_u32 s88, s39, s88\n"
      "s_lshl_b32 s83, s88, 1\n"
      "s_lshl_b32 s82, 0x00030003, s83\n"
      "v_cndmask_b32_e64 v84, 0, v159, s[64:65]\n"
      "v_cndmask_b32_e64 v85, 0, v159, s[66:67]\n"
      "v_cndmask_b32_e64 v86, 0, v159, s[68:69]\n"
      "v_cndmask_b32_e64 v87, 0, v159, s[70:71]\n"
      "v_mbcnt_lo_u32_b32 v93, -1, 0\n"
      "v_mbcnt_hi_u32_b32 v93, -1, v93\n"
      "v_lshrrev_b32_e32 v92, 3, v93\n"
      "v_lshlrev_b32_e32 v92, 5, v92\n"
      "v_and_b32_e32 v93, 7, v93\n"
      "v_lshl_add_u32 v92, v93, 1, v92\n"
      "s_mul_i32 s88, s38, 224\n"
      "v_add_u32_e32 v92, s88, v92\n"
      "s_mov_b32 s88, 0x90800\n"
      "s_mul_i32 s89, s42, 28672\n"
      "s_add_u32 s88, s88, s89\n"
      "s_add_u32 s44, s36, s88\n"
      "s_addc_u32 s45, s37, 0\n"
      "s_mov_b32 s94, s82\n"
      "s_mov_b32 s95, s82\n"
      "s_mov_b64 s[48:49], s[94:95]\n"
      "s_mov_b64 s[50:51], s[94:95]\n"
      "s_mov_b64 s[52:53], s[94:95]\n"
      "s_mov_b64 s[54:55], s[94:95]\n"
      "s_mov_b64 s[56:57], s[94:95]\n"
      "s_mov_b64 s[58:59], s[94:95]\n"
      "s_mov_b64 s[60:61], s[94:95]\n"
      "s_mov_b32 s43, 0\n"
      "s_nop 0\n"
      "GT_103_%=:\n"
      "s_mov_b64 exec, s[48:49]\n"
      "s_nop 0\n"
      "s_cbranch_execz GS_106_%=\n"
      "s_add_u32 s46, s44, 0x0\n"
      "s_addc_u32 s47, s45, 0\n"
      "global_load_dwordx4 v[96:99], v154, s[46:47] sc1\n"
      "GS_106_%=:\n"
      "s_mov_b64 exec, s[50:51]\n"
      "s_nop 0\n"
      "s_cbranch_execz GS_107_%=\n"
      "s_add_u32 s46, s44, 0xe00\n"
      "s_addc_u32 s47, s45, 0\n"
      "global_load_dwordx4 v[100:103], v154, s[46:47] sc1\n"
      "GS_107_%=:\n"
      "s_mov_b64 exec, s[52:53]\n"
      "s_nop 0\n"
      "s_cbranch_execz GS_108_%=\n"
      "s_add_u32 s46, s44, 0x1c00\n"
      "s_addc_u32 s47, s45, 0\n"
      "global_load_dwordx4 v[104:107], v154, s[46:47] sc1\n"
      "GS_108_%=:\n"
      "s_mov_b64 exec, s[54:55]\n"
      "s_nop 0\n"
      "s_cbranch_execz GS_109_%=\n"
      "s_add_u32 s46, s44, 0x2a00\n"
      "s_addc_u32 s47, s45, 0\n"
      "global_load_dwordx4 v[108:111], v154, s[46:47] sc1\n"
      "GS_109_%=:\n"
      "s_mov_b64 exec, s[56:57]\n"
      "s_nop 0\n"
      "s_cbranch_execz GS_110_%=\n"
      "s_add_u32 s46, s44, 0x3800\n"
      "s_addc_u32 s47, s45, 0\n"
      "global_load_dwordx4 v[112:115], v154, s[46:47] sc1\n"
      "GS_110_%=:\n"
      "s_mov_b64 exec, s[58:59]\n"
      "s_nop 0\n"
      "s_cbranch_execz GS_111_%=\n"
      "s_add_u32 s46, s44, 0x4600\n"
      "s_addc_u32 s47, s45, 0\n"
      "global_load_dwordx4 v[116:119], v154, s[46:47] sc1\n"
      "GS_111_%=:\n"
      "s_mov_b64 exec, s[60:61]\n"
      "s_nop 0\n"
      "s_cbranch_execz GS_112_%=\n"
      "s_add_u32 s46, s44, 0x5400\n"
      "s_addc_u32 s47, s45, 0\n"
      "global_load_dwordx4 v[120:123], v154, s[46:47] sc1\n"
      "GS_112_%=:\n"
      "s_mov_b64 exec, -1\n"
      "s_sleep 1\n"
      "v_max3_u32 v56, v96, v97, v98\n"
      "v_max_u32_e32 v56, v56, v99\n"
      "v_cmp_eq_u32_e64 s[48:49], -1, v56\n"
      "v_max3_u32 v57, v100, v101, v102\n"
      "v_max_u32_e32 v57, v57, v103\n"
      "v_cmp_eq_u32_e64 s[50:51], -1, v57\n"
      "v_max3_u32 v56, v104, v105, v106\n"
      "v_max_u32_e32 v56, v56, v107\n"
      "v_cmp_eq_u32_e64 s[52:53], -1, v56\n"
      "v_max3_u32 v57, v108, v109, v110\n"
      "v_max_u32_e32 v57, v57, v111\n"
      "v_cmp_eq_u32_e64 s[54:55], -1, v57\n"
      "v_max3_u32 v56, v112, v113, v114\n"
      "v_max_u32_e32 v56, v56, v115\n"
      "v_cmp_eq_u32_e64 s[56:57], -1, v56\n"
      "v_max3_u32 v57, v116, v117, v118\n"
      "v_max_u32_e32 v57, v57, v119\n"
      "v_cmp_eq_u32_e64 s[58:59], -1, v57\n"
      "v_max3_u32 v56, v120, v121, v122\n"
      "v_max_u32_e32 v56, v56, v123\n"
      "v_cmp_eq_u32_e64 s[60:61], -1, v56\n"
      "s_and_b64 s[48:49], s[48:49], s[94:95]\n"
      "s_and_b64 s[50:51], s[50:51], s[94:95]\n"
      "s_and_b64 s[52:53], s[52:53], s[94:95]\n"
      "s_and_b64 s[54:55], s[54:55], s[94:95]\n"
      "s_and_b64 s[56:57], s[56:57], s[94:95]\n"
      "s_and_b64 s[58:59], s[58:59], s[94:95]\n"
      "s_and_b64 s[60:61], s[60:61], s[94:95]\n"
      "s_mov_b64 s[88:89], s[48:49]\n"
      "s_or_b64 s[88:89], s[88:89], s[50:51]\n"
      "s_or_b64 s[88:89], s[88:89], s[52:53]\n"
      "s_or_b64 s[88:89], s[88:89], s[54:55]\n"
      "s_or_b64 s[88:89], s[88:89], s[56:57]\n"
      "s_or_b64 s[88:89], s[88:89], s[58:59]\n"
      "s_or_b64 s[88:89], s[88:89], s[60:61]\n"
      "s_cmp_eq_u64 s[88:89], 0\n"
      "s_nop 0\n"
      "s_cbranch_scc1 GD_104_%=\n"
      "s_add_u32 s43, s43, 1\n"
      "s_cmp_lt_u32 s43, 0x80000\n"
      "s_waitcnt vmcnt(7)\n"
      "s_nop 0\n"
      "s_cbranch_scc1 GT_103_%=\n"
      "s_nop 0\n"
      "GD_104_%=:\n"
      "v_mfma_f32_16x16x32_bf16 v[88:91], v[96:99], v[84:87], 0\n"
      "v_mfma_f32_16x16x32_bf16 v[88:91], v[100:103], v[84:87], v[88:91]\n"
      "v_mfma_f32_16x16x32_bf16 v[88:91], v[104:107], v[84:87], v[88:91]\n"
      "v_mfma_f32_16x16x32_bf16 v[88:91], v[108:111], v[84:87], v[88:91]\n"
      "v_mfma_f32_16x16x32_bf16 v[88:91], v[112:115], v[84:87], v[88:91]\n"
      "v_mfma_f32_16x16x32_bf16 v[88:91], v[116:119], v[84:87], v[88:91]\n"
      "v_mfma_f32_16x16x32_bf16 v[88:91], v[120:123], v[84:87], v[88:91]\n"
      "s_nop 7\n"
      "v_cndmask_b32_e64 v56, v88, v90, s[62:63]\n"
      "v_cndmask_b32_e64 v57, v89, v91, s[62:63]\n"
      "v_cvt_pk_bf16_f32 v58, v56, v57\n"
      "v_lshlrev_b32_e32 v56, 16, v58\n"
      "v_and_b32_e32 v57, 0xffff0000, v58\n"
      "v_add_f32_e32 v56, v140, v56\n"
      "v_add_f32_e32 v57, v141, v57\n"
      "v_cvt_pk_bf16_f32 v58, v56, v57\n"
      "s_lshr_b32 s88, s42, 1\n"
      "s_mul_i32 s88, s88, 896\n"
      "s_mov_b64 s[46:47], %[out]\n"
      "s_add_u32 s46, s46, s88\n"
      "s_addc_u32 s47, s47, 0\n"
      "s_lshl_b32 s88, s83, 2\n"
      "s_lshl_b64 exec, 0xff, s88\n"
      "global_store_short v92, v58, s[46:47]\n"
      "global_store_short_d16_hi v92, v58, s[46:47] offset:16\n"
      "s_mov_b64 exec, -1\n"
      "s_nop 0\n"
      "NOF_102_%=:\n"
      "s_mov_b32 s42, 3\n"
      "v_mov_b32_e32 v56, v140\n"
      "v_mov_b32_e32 v140, v142\n"
      "v_mov_b32_e32 v142, v56\n"
      "v_mov_b32_e32 v56, v141\n"
      "v_mov_b32_e32 v141, v143\n"
      "v_mov_b32_e32 v143, v56\n"
      "s_cmp_ge_u32 s39, 28\n"
      "s_nop 3\n"
      "s_cbranch_scc1 NOF_113_%=\n"
      "s_mul_i32 s88, s39, 37\n"
      "s_lshr_b32 s88, s88, 8\n"
      "s_cmp_lg_u32 s88, s38\n"
      "s_nop 0\n"
      "s_cbranch_scc1 NOF_113_%=\n"
      "s_mul_i32 s88, s88, 7\n"
      "s_sub_u32 s88, s39, s88\n"
      "s_lshl_b32 s83, s88, 1\n"
      "s_lshl_b32 s82, 0x00030003, s83\n"
      "v_cndmask_b32_e64 v84, 0, v159, s[64:65]\n"
      "v_cndmask_b32_e64 v85, 0, v159, s[66:67]\n"
      "v_cndmask_b32_e64 v86, 0, v159, s[68:69]\n"
      "v_cndmask_b32_e64 v87, 0, v159, s[70:71]\n"
      "v_mbcnt_lo_u32_b32 v93, -1, 0\n"
      "v_mbcnt_hi_u32_b32 v93, -1, v93\n"
      "v_lshrrev_b32_e32 v92, 3, v93\n"
      "v_lshlrev_b32_e32 v92, 5, v92\n"
      "v_and_b32_e32 v93, 7, v93\n"
      "v_lshl_add_u32 v92, v93, 1, v92\n"
      "s_mul_i32 s88, s38, 224\n"
      "v_add_u32_e32 v92, s88, v92\n"
      "s_mov_b32 s88, 0x90800\n"
      "s_mul_i32 s89, s42, 28672\n"
      "s_add_u32 s88, s88, s89\n"
      "s_add_u32 s44, s36, s88\n"
      "s_addc_u32 s45, s37, 0\n"
      "s_mov_b32 s94, s82\n"
      "s_mov_b32 s95, s82\n"
      "s_mov_b64 s[48:49], s[94:95]\n"
      "s_mov_b64 s[50:51], s[94:95]\n"
      "s_mov_b64 s[52:53], s[94:95]\n"
      "s_mov_b64 s[54:55], s[94:95]\n"
      "s_mov_b64 s[56:57], s[94:95]\n"
      "s_mov_b64 s[58:59], s[94:95]\n"
      "s_mov_b64 s[60:61], s[94:95]\n"
      "s_mov_b32 s43, 0\n"
      "s_sleep 8\n"
      "s_nop 0\n"
      "GT_114_%=:\n"
      "s_mov_b64 exec, s[48:49]\n"
      "s_nop 0\n"
      "s_cbranch_execz GS_117_%=\n"
      "s_add_u32 s46, s44, 0x0\n"
      "s_addc_u32 s47, s45, 0\n"
      "global_load_dwordx4 v[96:99], v154, s[46:47] sc1\n"
      "GS_117_%=:\n"
      "s_mov_b64 exec, s[50:51]\n"
      "s_nop 0\n"
      "s_cbranch_execz GS_118_%=\n"
      "s_add_u32 s46, s44, 0xe00\n"
      "s_addc_u32 s47, s45, 0\n"
      "global_load_dwordx4 v[100:103], v154, s[46:47] sc1\n"
      "GS_118_%=:\n"
      "s_mov_b64 exec, s[52:53]\n"
      "s_nop 0\n"
      "s_cbranch_execz GS_119_%=\n"
      "s_add_u32 s46, s44, 0x1c00\n"
      "s_addc_u32 s47, s45, 0\n"
      "global_load_dwordx4 v[104:107], v154, s[46:47] sc1\n"
      "GS_119_%=:\n"
      "s_mov_b64 exec, s[54:55]\n"
      "s_nop 0\n"
      "s_cbranch_execz GS_120_%=\n"
      "s_add_u32 s46, s44, 0x2a00\n"
      "s_addc_u32 s47, s45, 0\n"
      "global_load_dwordx4 v[108:111], v154, s[46:47] sc1\n"
      "GS_120_%=:\n"
      "s_mov_b64 exec, s[56:57]\n"
      "s_nop 0\n"
      "s_cbranch_execz GS_121_%=\n"
      "s_add_u32 s46, s44, 0x3800\n"
      "s_addc_u32 s47, s45, 0\n"
      "global_load_dwordx4 v[112:115], v154, s[46:47] sc1\n"
      "GS_121_%=:\n"
      "s_mov_b64 exec, s[58:59]\n"
      "s_nop 0\n"
      "s_cbranch_execz GS_122_%=\n"
      "s_add_u32 s46, s44, 0x4600\n"
      "s_addc_u32 s47, s45, 0\n"
      "global_load_dwordx4 v[116:119], v154, s[46:47] sc1\n"
      "GS_122_%=:\n"
      "s_mov_b64 exec, s[60:61]\n"
      "s_nop 0\n"
      "s_cbranch_execz GS_123_%=\n"
      "s_add_u32 s46, s44, 0x5400\n"
      "s_addc_u32 s47, s45, 0\n"
      "global_load_dwordx4 v[120:123], v154, s[46:47] sc1\n"
      "GS_123_%=:\n"
      "s_mov_b64 exec, -1\n"
      "s_sleep 1\n"
      "v_max3_u32 v56, v96, v97, v98\n"
      "v_max_u32_e32 v56, v56, v99\n"
      "v_cmp_eq_u32_e64 s[48:49], -1, v56\n"
      "v_max3_u32 v57, v100, v101, v102\n"
      "v_max_u32_e32 v57, v57, v103\n"
      "v_cmp_eq_u32_e64 s[50:51], -1, v57\n"
      "v_max3_u32 v56, v104, v105, v106\n"
      "v_max_u32_e32 v56, v56, v107\n"
      "v_cmp_eq_u32_e64 s[52:53], -1, v56\n"
      "v_max3_u32 v57, v108, v109, v110\n"
      "v_max_u32_e32 v57, v57, v111\n"
      "v_cmp_eq_u32_e64 s[54:55], -1, v57\n"
      "v_max3_u32 v56, v112, v113, v114\n"
      "v_max_u32_e32 v56, v56, v115\n"
      "v_cmp_eq_u32_e64 s[56:57], -1, v56\n"
      "v_max3_u32 v57, v116, v117, v118\n"
      "v_max_u32_e32 v57, v57, v119\n"
      "v_cmp_eq_u32_e64 s[58:59], -1, v57\n"
      "v_max3_u32 v56, v120, v121, v122\n"
      "v_max_u32_e32 v56, v56, v123\n"
      "v_cmp_eq_u32_e64 s[60:61], -1, v56\n"
      "s_and_b64 s[48:49], s[48:49], s[94:95]\n"
      "s_and_b64 s[50:51], s[50:51], s[94:95]\n"
      "s_and_b64 s[52:53], s[52:53], s[94:95]\n"
      "s_and_b64 s[54:55], s[54:55], s[94:95]\n"
      "s_and_b64 s[56:57], s[56:57], s[94:95]\n"
      "s_and_b64 s[58:59], s[58:59], s[94:95]\n"
      "s_and_b64 s[60:61], s[60:61], s[94:95]\n"
      "s_mov_b64 s[88:89], s[48:49]\n"
      "s_or_b64 s[88:89], s[88:89], s[50:51]\n"
      "s_or_b64 s[88:89], s[88:89], s[52:53]\n"
      "s_or_b64 s[88:89], s[88:89], s[54:55]\n"
      "s_or_b64 s[88:89], s[88:89], s[56:57]\n"
      "s_or_b64 s[88:89], s[88:89], s[58:59]\n"
      "s_or_b64 s[88:89], s[88:89], s[60:61]\n"
      "s_cmp_eq_u64 s[88:89], 0\n"
      "s_nop 0\n"
      "s_cbranch_scc1 GD_115_%=\n"
      "s_add_u32 s43, s43, 1\n"
      "s_cmp_lt_u32 s43, 0x80000\n"
      "s_waitcnt vmcnt(7)\n"
      "s_nop 0\n"
      "s_cbranch_scc1 GT_114_%=\n"
      "s_nop 0\n"
      "GD_115_%=:\n"
      "v_mfma_f32_16x16x32_bf16 v[88:91], v[96:99], v[84:87], 0\n"
      "v_mfma_f32_16x16x32_bf16 v[88:91], v[100:103], v[84:87], v[88:91]\n"
      "v_mfma_f32_16x16x32_bf16 v[88:91], v[104:107], v[84:87], v[88:91]\n"
      "v_mfma_f32_16x16x32_bf16 v[88:91], v[108:111], v[84:87], v[88:91]\n"
      "v_mfma_f32_16x16x32_bf16 v[88:91], v[112:115], v[84:87], v[88:91]\n"
      "v_mfma_f32_16x16x32_bf16 v[88:91], v[116:119], v[84:87], v[88:91]\n"
      "v_mfma_f32_16x16x32_bf16 v[88:91], v[120:123], v[84:87], v[88:91]\n"
      "s_nop 7\n"
      "v_cndmask_b32_e64 v56, v88, v90, s[62:63]\n"
      "v_cndmask_b32_e64 v57, v89, v91, s[62:63]\n"
      "v_cvt_pk_bf16_f32 v58, v56, v57\n"
      "v_lshlrev_b32_e32 v56, 16, v58\n"
      "v_and_b32_e32 v57, 0xffff0000, v58\n"
      "v_add_f32_e32 v56, v140, v56\n"
      "v_add_f32_e32 v57, v141, v57\n"
      "v_cvt_pk_bf16_f32 v58, v56, v57\n"
      "s_lshr_b32 s88, s42, 1\n"
      "s_mul_i32 s88, s88, 896\n"
      "s_mov_b64 s[46:47], %[out]\n"
      "s_add_u32 s46, s46, s88\n"
      "s_addc_u32 s47, s47, 0\n"
      "s_lshl_b32 s88, s83, 2\n"
      "s_lshl_b64 exec, 0xff, s88\n"
      "global_store_short v92, v58, s[46:47]\n"
      "global_store_short_d16_hi v92, v58, s[46:47] offset:16\n"
      "s_mov_b64 exec, -1\n"
      "s_nop 0\n"
      "NOF_113_%=:\n"
      "s_branch CE_12_%=\n"
      "s_mov_b32 s42, 0\n"
      "s_nop 0\n"
      "CL_124_%=:\n"
      "v_mov_b32_e32 v96, -1\n"
      "v_mov_b32_e32 v97, -1\n"
      "v_mov_b32_e32 v98, -1\n"
      "v_mov_b32_e32 v99, -1\n"
      "v_mov_b32_e32 v100, -1\n"
      "v_mov_b32_e32 v101, -1\n"
      "v_mov_b32_e32 v102, -1\n"
      "v_mov_b32_e32 v103, -1\n"
      "v_mov_b32_e32 v104, -1\n"
      "v_mov_b32_e32 v105, -1\n"
      "v_mov_b32_e32 v106, -1\n"
      "v_mov_b32_e32 v107, -1\n"
      "v_mov_b32_e32 v108, -1\n"
      "v_mov_b32_e32 v109, -1\n"
      "v_mov_b32_e32 v110, -1\n"
      "v_mov_b32_e32 v111, -1\n"
      "v_mov_b32_e32 v112, -1\n"
      "v_mov_b32_e32 v113, -1\n"
      "v_mov_b32_e32 v114, -1\n"
      "v_mov_b32_e32 v115, -1\n"
      "v_mov_b32_e32 v116, -1\n"
      "v_mov_b32_e32 v117, -1\n"
      "v_mov_b32_e32 v118, -1\n"
      "v_mov_b32_e32 v119, -1\n"
      "v_mov_b32_e32 v124, -1\n"
      "v_mov_b32_e32 v125, -1\n"
      "v_mov_b32_e32 v126, -1\n"
      "v_mov_b32_e32 v127, -1\n"
      "v_mov_b32_e32 v128, -1\n"
      "v_mov_b32_e32 v129, -1\n"
      "v_mov_b32_e32 v130, -1\n"
      "v_mov_b32_e32 v131, -1\n"
      "v_mov_b32_e32 v132, -1\n"
      "v_mov_b32_e32 v133, -1\n"
      "v_mov_b32_e32 v134, -1\n"
      "v_mov_b32_e32 v135, -1\n"
      "v_mov_b32_e32 v136, -1\n"
      "v_mov_b32_e32 v137, -1\n"
      "v_mov_b32_e32 v138, -1\n"
      "v_mov_b32_e32 v139, -1\n"
      "v_mbcnt_lo_u32_b32 v56, -1, 0\n"
      "v_mbcnt_hi_u32_b32 v56, -1, v56\n"
      "v_lshrrev_b32_e32 v57, 4, v56\n"
      "v_lshlrev_b32_e32 v93, 5, v57\n"
      "v_lshlrev_b32_e32 v92, 10, v57\n"
      "v_and_b32_e32 v57, 15, v56\n"
      "v_lshl_add_u32 v92, v57, 4, v92\n"
      "s_lshl_b32 s88, s38, 8\n"
      "v_add_u32_e32 v92, s88, v92\n"
      "v_bfe_u32 v57, v56, 3, 1\n"
      "v_lshlrev_b32_e32 v94, 6, v57\n"
      "v_lshrrev_b32_e32 v57, 4, v56\n"
      "v_lshl_add_u32 v94, v57, 4, v94\n"
      "v_and_b32_e32 v57, 7, v56\n"
      "v_lshl_add_u32 v94, v57, 1, v94\n"
      "s_lshl_b32 s88, s38, 7\n"
      "s_add_u32 s88, s88, 0x1e800\n"
      "v_add_u32_e32 v94, s88, v94\n"
      "s_mov_b32 s88, 0xb0800\n"
      "s_mul_i32 s89, s42, 262144\n"
      "s_add_u32 s88, s88, s89\n"
      "s_mul_i32 s89, s40, 16384\n"
      "s_add_u32 s88, s88, s89\n"
      "s_add_u32 s44, s36, s88\n"
      "s_addc_u32 s45, s37, 0\n"
      "s_mov_b32 s88, 0x80800\n"
      "s_mul_i32 s89, s42, 2048\n"
      "s_add_u32 s88, s88, s89\n"
      "s_mul_i32 s89, s40, 128\n"
      "s_add_u32 s88, s88, s89\n"
      "s_add_u32 s86, s36, s88\n"
      "s_addc_u32 s87, s37, 0\n"
      "s_mov_b64 s[92:93], -1\n"
      "s_mov_b64 s[48:49], -1\n"
      "s_mov_b64 s[50:51], -1\n"
      "s_mov_b64 s[52:53], -1\n"
      "s_mov_b64 s[54:55], -1\n"
      "s_mov_b64 s[56:57], -1\n"
      "s_mov_b32 s43, 0\n"
      "s_nop 0\n"
      "A3_127_%=:\n"
      "s_mov_b64 exec, s[48:49]\n"
      "s_nop 0\n"
      "s_cbranch_execz AS_129_%=\n"
      "s_add_u32 s46, s44, 0x0\n"
      "s_addc_u32 s47, s45, 0\n"
      "global_load_dwordx4 v[96:99], v92, s[46:47] sc1\n"
      "AS_129_%=:\n"
      "s_mov_b64 exec, s[50:51]\n"
      "s_nop 0\n"
      "s_cbranch_execz AS_130_%=\n"
      "s_add_u32 s46, s44, 0x1000\n"
      "s_addc_u32 s47, s45, 0\n"
      "global_load_dwordx4 v[100:103], v92, s[46:47] sc1\n"
      "AS_130_%=:\n"
      "s_mov_b64 exec, s[52:53]\n"
      "s_nop 0\n"
      "s_cbranch_execz AS_131_%=\n"
      "s_add_u32 s46, s44, 0x2000\n"
      "s_addc_u32 s47, s45, 0\n"
      "global_load_dwordx4 v[104:107], v92, s[46:47] sc1\n"
      "AS_131_%=:\n"
      "s_mov_b64 exec, s[54:55]\n"
      "s_nop 0\n"
      "s_cbranch_execz AS_132_%=\n"
      "s_add_u32 s46, s44, 0x3000\n"
      "s_addc_u32 s47, s45, 0\n"
      "global_load_dwordx4 v[108:111], v92, s[46:47] sc1\n"
      "AS_132_%=:\n"
      "s_mov_b64 exec, -1\n"
      "s_cmp_eq_u64 s[56:57], 0\n"
      "s_nop 0\n"
      "s_cbranch_scc1 AS_133_%=\n"
      "global_load_dwordx4 v[112:115], v93, s[86:87] sc1\n"
      "global_load_dwordx4 v[116:119], v93, s[86:87] offset:16 sc1\n"
      "AS_133_%=:\n"
      "s_sleep 1\n"
      "v_max3_u32 v57, v96, v97, v98\n"
      "v_max_u32_e32 v57, v57, v99\n"
      "v_cmp_eq_u32_e64 s[48:49], -1, v57\n"
      "v_max3_u32 v58, v100, v101, v102\n"
      "v_max_u32_e32 v58, v58, v103\n"
      "v_cmp_eq_u32_e64 s[50:51], -1, v58\n"
      "v_max3_u32 v57, v104, v105, v106\n"
      "v_max_u32_e32 v57, v57, v107\n"
      "v_cmp_eq_u32_e64 s[52:53], -1, v57\n"
      "v_max3_u32 v58, v108, v109, v110\n"
      "v_max_u32_e32 v58, v58, v111\n"
      "v_cmp_eq_u32_e64 s[54:55], -1, v58\n"
      "v_max3_u32 v57, v112, v113, v114\n"
      "v_max3_u32 v58, v115, v116, v117\n"
      "v_max3_u32 v57, v57, v118, v119\n"
      "v_max_u32_e32 v57, v57, v58\n"
      "v_cmp_eq_u32_e64 s[56:57], -1, v57\n"
      "s_mov_b64 s[88:89], s[48:49]\n"
      "s_or_b64 s[88:89], s[88:89], s[50:51]\n"
      "s_or_b64 s[88:89], s[88:89], s[52:53]\n"
      "s_or_b64 s[88:89], s[88:89], s[54:55]\n"
      "s_or_b64 s[88:89], s[88:89], s[56:57]\n"
      "s_cmp_eq_u64 s[88:89], 0\n"
      "s_nop 0\n"
      "s_cbranch_scc1 A4_128_%=\n"
      "s_mov_b32 s90, 0x00010001\n"
      "s_mov_b32 s91, 0x00010001\n"
      "s_mov_b32 s88, 0\n"
      "v_cmp_ne_u32_e64 s[58:59], -1, v112\n"
      "v_cmp_ne_u32_e64 s[60:61], -1, v114\n"
      "v_cmp_ne_u32_e64 s[98:99], -1, v116\n"
      "v_cmp_ne_u32_e64 s[76:77], -1, v118\n"
      "s_and_b64 s[84:85], s[58:59], s[90:91]\n"
      "s_bcnt1_i32_b64 s89, s[84:85]\n"
      "s_add_u32 s88, s88, s89\n"
      "s_and_b64 s[84:85], s[60:61], s[90:91]\n"
      "s_bcnt1_i32_b64 s89, s[84:85]\n"
      "s_add_u32 s88, s88, s89\n"
      "s_and_b64 s[84:85], s[98:99], s[90:91]\n"
      "s_bcnt1_i32_b64 s89, s[84:85]\n"
      "s_add_u32 s88, s88, s89\n"
      "s_and_b64 s[84:85], s[76:77], s[90:91]\n"
      "s_bcnt1_i32_b64 s89, s[84:85]\n"
      "s_add_u32 s88, s88, s89\n"
      "s_cmp_ge_u32 s88, 0\n"
      "s_cselect_b64 s[92:93], -1, 0\n"
      "s_or_b64 s[58:59], s[58:59], s[92:93]\n"
      "s_and_b64 s[48:49], s[48:49], s[58:59]\n"
      "s_or_b64 s[60:61], s[60:61], s[92:93]\n"
      "s_and_b64 s[50:51], s[50:51], s[60:61]\n"
      "s_or_b64 s[98:99], s[98:99], s[92:93]\n"
      "s_and_b64 s[52:53], s[52:53], s[98:99]\n"
      "s_or_b64 s[76:77], s[76:77], s[92:93]\n"
      "s_and_b64 s[54:55], s[54:55], s[76:77]\n"
      "s_cmp_ge_u32 s88, 0\n"
      "s_nop 0\n"
      "s_cbranch_scc1 AF_134_%=\n"
      "s_sleep 4\n"
      "AF_134_%=:\n"
      "s_add_u32 s43, s43, 1\n"
      "s_cmp_lt_u32 s43, 0x80000\n"
      "s_waitcnt vmcnt(6)\n"
      "s_nop 0\n"
      "s_cbranch_scc1 A3_127_%=\n"
      "s_nop 0\n"
      "A4_128_%=:\n"
      "v_max3_f32 v56, v112, v114, v116\n"
      "v_max_f32_e32 v56, v56, v118\n"
      "v_mov_b32_e32 v57, v56\n"
      "s_nop 1\n"
      "v_permlane16_swap_b32_e32 v57, v56\n"
      "v_max_f32_e32 v56, v56, v57\n"
      "v_mov_b32_e32 v57, v56\n"
      "s_nop 1\n"
      "v_permlane32_swap_b32_e32 v57, v56\n"
      "v_max_f32_e32 v56, v56, v57\n"
      "v_sub_f32_e32 v60, v112, v56\n"
      "v_sub_f32_e32 v61, v114, v56\n"
      "v_sub_f32_e32 v62, v116, v56\n"
      "v_sub_f32_e32 v63, v118, v56\n"
      "v_mul_f32_e32 v60, s34, v60\n"
      "v_mul_f32_e32 v61, s34, v61\n"
      "v_mul_f32_e32 v62, s34, v62\n"
      "v_mul_f32_e32 v63, s34, v63\n"
      "v_exp_f32_e32 v60, v60\n"
      "v_exp_f32_e32 v61, v61\n"
      "v_exp_f32_e32 v62, v62\n"
      "v_exp_f32_e32 v63, v63\n"
      "v_mul_f32_e32 v59, v60, v113\n"
      "v_mul_f32_e32 v64, v60, v96\n"
      "v_mul_f32_e32 v65, v60, v97\n"
      "v_mul_f32_e32 v66, v60, v98\n"
      "v_mul_f32_e32 v67, v60, v99\n"
      "v_fmac_f32_e32 v59, v61, v115\n"
      "v_fmac_f32_e32 v64, v61, v100\n"
      "v_fmac_f32_e32 v65, v61, v101\n"
      "v_fmac_f32_e32 v66, v61, v102\n"
      "v_fmac_f32_e32 v67, v61, v103\n"
      "v_fmac_f32_e32 v59, v62, v117\n"
      "v_fmac_f32_e32 v64, v62, v104\n"
      "v_fmac_f32_e32 v65, v62, v105\n"
      "v_fmac_f32_e32 v66, v62, v106\n"
      "v_fmac_f32_e32 v67, v62, v107\n"
      "v_fmac_f32_e32 v59, v63, v119\n"
      "v_fmac_f32_e32 v64, v63, v108\n"
      "v_fmac_f32_e32 v65, v63, v109\n"
      "v_fmac_f32_e32 v66, v63, v110\n"
      "v_fmac_f32_e32 v67, v63, v111\n"
      "v_mov_b32_e32 v57, v59\n"
      "v_permlane32_swap_b32_e32 v64, v66\n"
      "v_permlane32_swap_b32_e32 v65, v67\n"
      "v_permlane32_swap_b32_e32 v57, v59\n"
      "v_add_f32_e32 v64, v64, v66\n"
      "v_add_f32_e32 v65, v65, v67\n"
      "v_add_f32_e32 v59, v59, v57\n"
      "s_nop 0\n"
      "v_permlane16_swap_b32_e32 v64, v65\n"
      "v_mov_b32_e32 v57, v59\n"
      "v_add_f32_e32 v64, v64, v65\n"
      "s_nop 0\n"
      "v_permlane16_swap_b32_e32 v57, v59\n"
      "v_add_f32_e32 v59, v59, v57\n"
      "v_rcp_f32_e32 v59, v59\n"
      "s_nop 0\n"
      "v_mul_f32_e32 v64, v64, v59\n"
      "v_cvt_pk_bf16_f32 v64, v64, v64\n"
      "ds_write_b16 v94, v64\n"
      "s_mov_b32 s88, 0x82800\n"
      "s_mul_i32 s89, s42, 14336\n"
      "s_add_u32 s88, s88, s89\n"
      "s_mul_i32 s89, s40, 896\n"
      "s_add_u32 s88, s88, s89\n"
      "s_mul_i32 s89, s41, 448\n"
      "s_add_u32 s88, s88, s89\n"
      "s_mul_i32 s89, s38, 128\n"
      "s_add_u32 s88, s88, s89\n"
      "s_add_u32 s44, s36, s88\n"
      "s_addc_u32 s45, s37, 0\n"
      "s_cmp_eq_u32 s38, 3\n"
      "s_cselect_b32 s88, 32, 0\n"
      "s_sub_u32 s44, s44, s88\n"
      "s_subb_u32 s45, s45, 0\n"
      "v_mbcnt_lo_u32_b32 v93, -1, 0\n"
      "v_mbcnt_hi_u32_b32 v93, -1, v93\n"
      "v_and_b32_e32 v92, 15, v93\n"
      "v_lshlrev_b32_e32 v92, 5, v92\n"
      "v_lshrrev_b32_e32 v93, 4, v93\n"
      "v_lshl_add_u32 v92, v93, 3, v92\n"
      "s_waitcnt lgkmcnt(0)\n"
      "s_barrier\n"
      "ds_read_b128 v[0:3], v156 offset:2048\n"
      "ds_read_b128 v[4:7], v156 offset:2112\n"
      "ds_read_b128 v[8:11], v156 offset:2176\n"
      "ds_read_b128 v[12:15], v156 offset:2240\n"
      "ds_read_b128 v[16:19], v156 offset:2304\n"
      "ds_read_b128 v[20:23], v156 offset:2368\n"
      "ds_read_b128 v[24:27], v156 offset:2432\n"
      "ds_read_b128 v[28:31], v156 offset:2496\n"
      "s_waitcnt lgkmcnt(7)\n"
      "v_mfma_f32_16x16x32_bf16 v[64:67], v[160:163], v[0:3], 0\n"
      "v_mfma_f32_16x16x32_bf16 v[68:71], v[164:167], v[0:3], 0\n"
      "v_mfma_f32_16x16x32_bf16 v[72:75], v[168:171], v[0:3], 0\n"
      "v_mfma_f32_16x16x32_bf16 v[76:79], v[172:175], v[0:3], 0\n"
      "s_waitcnt lgkmcnt(6)\n"
      "v_mfma_f32_16x16x32_bf16 v[64:67], v[176:179], v[4:7], v[64:67]\n"
      "v_mfma_f32_16x16x32_bf16 v[68:71], v[180:183], v[4:7], v[68:71]\n"
      "v_mfma_f32_16x16x32_bf16 v[72:75], v[184:187], v[4:7], v[72:75]\n"
      "v_mfma_f32_16x16x32_bf16 v[76:79], v[188:191], v[4:7], v[76:79]\n"
      "s_waitcnt lgkmcnt(5)\n"
      "v_mfma_f32_16x16x32_bf16 v[64:67], v[192:195], v[8:11], v[64:67]\n"
      "v_mfma_f32_16x16x32_bf16 v[68:71], v[196:199], v[8:11], v[68:71]\n"
      "v_mfma_f32_16x16x32_bf16 v[72:75], v[200:203], v[8:11], v[72:75]\n"
      "v_mfma_f32_16x16x32_bf16 v[76:79], v[204:207], v[8:11], v[76:79]\n"
      "s_waitcnt lgkmcnt(4)\n"
      "v_mfma_f32_16x16x32_bf16 v[64:67], v[208:211], v[12:15], v[64:67]\n"
      "v_mfma_f32_16x16x32_bf16 v[68:71], v[212:215], v[12:15], v[68:71]\n"
      "v_mfma_f32_16x16x32_bf16 v[72:75], v[216:219], v[12:15], v[72:75]\n"
      "v_mfma_f32_16x16x32_bf16 v[76:79], v[220:223], v[12:15], v[76:79]\n"
      "s_waitcnt lgkmcnt(3)\n"
      "v_mfma_f32_16x16x32_bf16 v[64:67], v[224:227], v[16:19], v[64:67]\n"
      "v_mfma_f32_16x16x32_bf16 v[68:71], v[228:231], v[16:19], v[68:71]\n"
      "v_mfma_f32_16x16x32_bf16 v[72:75], v[232:235], v[16:19], v[72:75]\n"
      "v_mfma_f32_16x16x32_bf16 v[76:79], v[236:239], v[16:19], v[76:79]\n"
      "s_waitcnt lgkmcnt(2)\n"
      "v_mfma_f32_16x16x32_bf16 v[64:67], v[240:243], v[20:23], v[64:67]\n"
      "v_mfma_f32_16x16x32_bf16 v[68:71], v[244:247], v[20:23], v[68:71]\n"
      "v_mfma_f32_16x16x32_bf16 v[72:75], v[248:251], v[20:23], v[72:75]\n"
      "v_mfma_f32_16x16x32_bf16 v[76:79], v[252:255], v[20:23], v[76:79]\n"
      "s_waitcnt lgkmcnt(1)\n"
      "v_mfma_f32_16x16x32_bf16 v[64:67], a[0:3], v[24:27], v[64:67]\n"
      "v_mfma_f32_16x16x32_bf16 v[68:71], a[4:7], v[24:27], v[68:71]\n"
      "v_mfma_f32_16x16x32_bf16 v[72:75], a[8:11], v[24:27], v[72:75]\n"
      "v_mfma_f32_16x16x32_bf16 v[76:79], a[12:15], v[24:27], v[76:79]\n"
      "s_waitcnt lgkmcnt(0)\n"
      "v_mfma_f32_16x16x32_bf16 v[64:67], a[16:19], v[28:31], v[64:67]\n"
      "v_mfma_f32_16x16x32_bf16 v[68:71], a[20:23], v[28:31], v[68:71]\n"
      "v_mfma_f32_16x16x32_bf16 v[72:75], a[24:27], v[28:31], v[72:75]\n"
      "v_mfma_f32_16x16x32_bf16 v[76:79], a[28:31], v[28:31], v[76:79]\n"
      "s_nop 4\n"
      "v_cvt_pk_bf16_f32 v64, v64, v65\n"
      "v_cvt_pk_bf16_f32 v65, v66, v67\n"
      "v_min_u32_e32 v64, -2, v64\n"
      "v_min_u32_e32 v65, -2, v65\n"
      "v_cvt_pk_bf16_f32 v82, v68, v69\n"
      "v_cvt_pk_bf16_f32 v83, v70, v71\n"
      "v_min_u32_e32 v82, -2, v82\n"
      "v_min_u32_e32 v83, -2, v83\n"
      "s_mov_b32 s46, 0x20002\n"
      "s_mov_b32 s47, 0x20002\n"
      "v_cndmask_b32_e64 v64, v64, v82, s[46:47]\n"
      "v_cndmask_b32_e64 v65, v65, v83, s[46:47]\n"
      "v_cvt_pk_bf16_f32 v84, v72, v73\n"
      "v_cvt_pk_bf16_f32 v85, v74, v75\n"
      "v_min_u32_e32 v84, -2, v84\n"
      "v_min_u32_e32 v85, -2, v85\n"
      "s_mov_b32 s46, 0x40004\n"
      "s_mov_b32 s47, 0x40004\n"
      "v_cndmask_b32_e64 v64, v64, v84, s[46:47]\n"
      "v_cndmask_b32_e64 v65, v65, v85, s[46:47]\n"
      "v_cvt_pk_bf16_f32 v86, v76, v77\n"
      "v_cvt_pk_bf16_f32 v87, v78, v79\n"
      "v_min_u32_e32 v86, -2, v86\n"
      "v_min_u32_e32 v87, -2, v87\n"
      "s_mov_b32 s46, 0x80008\n"
      "s_mov_b32 s47, 0x80008\n"
      "v_cndmask_b32_e64 v64, v64, v86, s[46:47]\n"
      "v_cndmask_b32_e64 v65, v65, v87, s[46:47]\n"
      "s_mov_b32 s46, 0x000f000f\n"
      "s_cmp_ge_u32 s38, 2\n"
      "s_cselect_b32 exec_lo, 0x00070007, s46\n"
      "s_mov_b32 exec_hi, exec_lo\n"
      "global_store_dwordx2 v92, v[64:65], s[44:45] sc1\n"
      "s_mov_b64 exec, -1\n"
      "s_mov_b32 s84, 0xcccccccc\n"
      "s_mov_b32 s85, 0xcccccccc\n"
      "v_cndmask_b32_e64 v84, 0, v159, s[64:65]\n"
      "v_cndmask_b32_e64 v85, 0, v159, s[66:67]\n"
      "v_cndmask_b32_e64 v86, 0, v159, s[68:69]\n"
      "v_cndmask_b32_e64 v87, 0, v159, s[70:71]\n"
      "s_mov_b32 s88, 0x82800\n"
      "s_mul_i32 s89, s42, 14336\n"
      "s_add_u32 s88, s88, s89\n"
      "s_add_u32 s44, s36, s88\n"
      "s_addc_u32 s45, s37, 0\n"
      "s_mov_b32 s94, 0x3fff3fff\n"
      "s_mov_b32 s95, 0x3fff3fff\n"
      "s_mov_b64 s[48:49], s[94:95]\n"
      "s_mov_b64 s[50:51], s[94:95]\n"
      "s_mov_b64 s[52:53], s[94:95]\n"
      "s_mov_b64 s[54:55], s[94:95]\n"
      "s_mov_b32 s43, 0\n"
      "s_sleep 8\n"
      "s_nop 0\n"
      "GT_135_%=:\n"
      "s_mov_b64 exec, s[48:49]\n"
      "s_nop 0\n"
      "s_cbranch_execz GS_138_%=\n"
      "s_add_u32 s46, s44, 0x0\n"
      "s_addc_u32 s47, s45, 0\n"
      "global_load_dwordx4 v[124:127], v154, s[46:47] sc1\n"
      "GS_138_%=:\n"
      "s_mov_b64 exec, s[50:51]\n"
      "s_nop 0\n"
      "s_cbranch_execz GS_139_%=\n"
      "s_add_u32 s46, s44, 0xe00\n"
      "s_addc_u32 s47, s45, 0\n"
      "global_load_dwordx4 v[128:131], v154, s[46:47] sc1\n"
      "GS_139_%=:\n"
      "s_mov_b64 exec, s[52:53]\n"
      "s_nop 0\n"
      "s_cbranch_execz GS_140_%=\n"
      "s_add_u32 s46, s44, 0x1c00\n"
      "s_addc_u32 s47, s45, 0\n"
      "global_load_dwordx4 v[132:135], v154, s[46:47] sc1\n"
      "GS_140_%=:\n"
      "s_mov_b64 exec, s[54:55]\n"
      "s_nop 0\n"
      "s_cbranch_execz GS_141_%=\n"
      "s_add_u32 s46, s44, 0x2a00\n"
      "s_addc_u32 s47, s45, 0\n"
      "global_load_dwordx4 v[136:139], v154, s[46:47] sc1\n"
      "GS_141_%=:\n"
      "s_mov_b64 exec, -1\n"
      "s_sleep 1\n"
      "v_max3_u32 v56, v124, v125, v126\n"
      "v_max_u32_e32 v56, v56, v127\n"
      "v_cmp_eq_u32_e64 s[48:49], -1, v56\n"
      "v_max3_u32 v57, v128, v129, v130\n"
      "v_max_u32_e32 v57, v57, v131\n"
      "v_cmp_eq_u32_e64 s[50:51], -1, v57\n"
      "v_max3_u32 v56, v132, v133, v134\n"
      "v_max_u32_e32 v56, v56, v135\n"
      "v_cmp_eq_u32_e64 s[52:53], -1, v56\n"
      "v_max3_u32 v57, v136, v137, v138\n"
      "v_max_u32_e32 v57, v57, v139\n"
      "v_cmp_eq_u32_e64 s[54:55], -1, v57\n"
      "s_and_b64 s[48:49], s[48:49], s[94:95]\n"
      "s_and_b64 s[50:51], s[50:51], s[94:95]\n"
      "s_and_b64 s[52:53], s[52:53], s[94:95]\n"
      "s_and_b64 s[54:55], s[54:55], s[94:95]\n"
      "s_mov_b64 s[88:89], s[48:49]\n"
      "s_or_b64 s[88:89], s[88:89], s[50:51]\n"
      "s_or_b64 s[88:89], s[88:89], s[52:53]\n"
      "s_or_b64 s[88:89], s[88:89], s[54:55]\n"
      "s_cmp_eq_u64 s[88:89], 0\n"
      "s_nop 0\n"
      "s_cbranch_scc1 GD_136_%=\n"
      "s_add_u32 s43, s43, 1\n"
      "s_cmp_lt_u32 s43, 0x80000\n"
      "s_waitcnt vmcnt(4)\n"
      "s_nop 0\n"
      "s_cbranch_scc1 GT_135_%=\n"
      "s_nop 0\n"
      "GD_136_%=:\n"
      "v_mfma_f32_16x16x32_bf16 v[88:91], v[124:127], v[84:87], 0\n"
      "v_mfma_f32_16x16x32_bf16 v[88:91], v[128:131], v[84:87], v[88:91]\n"
      "v_mfma_f32_16x16x32_bf16 v[88:91], v[132:135], v[84:87], v[88:91]\n"
      "v_mfma_f32_16x16x32_bf16 v[88:91], v[136:139], v[84:87], v[88:91]\n"
      "s_nop 7\n"
      "v_cndmask_b32_e64 v56, v88, v90, s[62:63]\n"
      "v_cndmask_b32_e64 v57, v89, v91, s[62:63]\n"
      "v_cvt_pk_bf16_f32 v58, v56, v57\n"
      "v_lshlrev_b32_e32 v56, 16, v58\n"
      "v_and_b32_e32 v57, 0xffff0000, v58\n"
      "v_add_f32_e32 v56, v140, v56\n"
      "v_add_f32_e32 v57, v141, v57\n"
      "v_cvt_pk_bf16_f32 v58, v56, v57\n"
      "s_cmp_ge_u32 s39, 28\n"
      "v_lshlrev_b32_e32 v140, 16, v58\n"
      "v_and_b32_e32 v141, 0xffff0000, v58\n"
      "s_nop 4\n"
      "s_cbranch_scc1 NOBC_142_%=\n"
      "s_mov_b32 exec_lo, -1\n"
      "s_mov_b32 exec_hi, 0x00ffffff\n"
      "ds_write_b32 v155, v58 offset:0\n"
      "s_mov_b64 exec, -1\n"
      "s_waitcnt lgkmcnt(0)\n"
      "s_barrier\n"
      "v_mbcnt_lo_u32_b32 v81, -1, 0\n"
      "v_mbcnt_hi_u32_b32 v81, -1, v81\n"
      "v_lshlrev_b32_e32 v81, 4, v81\n"
      "v_add_u32_e32 v76, 0x1e000, v81\n"
      "ds_read_b128 v[60:63], v76\n"
      "ds_read_b128 v[0:3], v156 offset:0\n"
      "ds_read_b128 v[4:7], v156 offset:64\n"
      "ds_read_b128 v[8:11], v156 offset:128\n"
      "ds_read_b128 v[12:15], v156 offset:256\n"
      "ds_read_b128 v[16:19], v156 offset:320\n"
      "ds_read_b128 v[20:23], v156 offset:384\n"
      "ds_read_b128 v[24:27], v156 offset:512\n"
      "ds_read_b128 v[28:31], v156 offset:576\n"
      "ds_read_b128 v[32:35], v156 offset:640\n"
      "ds_read_b128 v[36:39], v156 offset:768\n"
      "ds_read_b128 v[40:43], v156 offset:832\n"
      "ds_read_b128 v[44:47], v156 offset:896\n"
      "ds_read_b128 v[48:51], v157 offset:192\n"
      "ds_read_b128 v[52:55], v157 offset:704\n"
      "s_mul_i32 s88, s38, 14336\n"
      "v_add_u32_e32 v82, s88, v81\n"
      "s_mov_b32 s89, 0x3f803f80\n"
      "s_waitcnt lgkmcnt(13)\n"
      "v_mfma_f32_16x16x32_bf16 v[64:67], a[144:147], v[0:3], 0\n"
      "v_dot2_f32_bf16 v72, v60, s89, 0\n"
      "v_dot2_f32_bf16 v74, v60, v60, 0\n"
      "v_mfma_f32_16x16x32_bf16 v[68:71], a[200:203], v[0:3], 0\n"
      "v_dot2_f32_bf16 v73, v62, s89, 0\n"
      "v_dot2_f32_bf16 v75, v62, v62, 0\n"
      "ds_read_b128 v[0:3], v82 offset:0\n"
      "s_waitcnt lgkmcnt(13)\n"
      "v_mfma_f32_16x16x32_bf16 v[64:67], a[148:151], v[4:7], v[64:67]\n"
      "v_dot2_f32_bf16 v72, v61, s89, v72\n"
      "v_dot2_f32_bf16 v74, v61, v61, v74\n"
      "v_mfma_f32_16x16x32_bf16 v[68:71], a[204:207], v[4:7], v[68:71]\n"
      "v_dot2_f32_bf16 v73, v63, s89, v73\n"
      "v_dot2_f32_bf16 v75, v63, v63, v75\n"
      "ds_read_b128 v[4:7], v82 offset:1024\n"
      "s_waitcnt lgkmcnt(13)\n"
      "v_mfma_f32_16x16x32_bf16 v[64:67], a[152:155], v[8:11], v[64:67]\n"
      "s_nop 0\n"
      "s_nop 0\n"
      "v_mfma_f32_16x16x32_bf16 v[68:71], a[208:211], v[8:11], v[68:71]\n"
      "v_add_f32_e32 v72, v72, v73\n"
      "v_add_f32_e32 v74, v74, v75\n"
      "ds_read_b128 v[8:11], v82 offset:2048\n"
      "s_waitcnt lgkmcnt(13)\n"
      "v_mfma_f32_16x16x32_bf16 v[64:67], a[156:159], v[12:15], v[64:67]\n"
      "v_add_f32_dpp v72, v72, v72 row_ror:8 row_mask:0xf bank_mask:0xf\n"
      "v_add_f32_dpp v74, v74, v74 row_ror:8 row_mask:0xf bank_mask:0xf\n"
      "v_mfma_f32_16x16x32_bf16 v[68:71], a[212:215], v[12:15], v[68:71]\n"
      "v_add_f32_dpp v72, v72, v72 row_ror:4 row_mask:0xf bank_mask:0xf\n"
      "v_add_f32_dpp v74, v74, v74 row_ror:4 row_mask:0xf bank_mask:0xf\n"
      "ds_read_b128 v[12:15], v82 offset:3072\n"
      "s_waitcnt lgkmcnt(13)\n"
      "v_mfma_f32_16x16x32_bf16 v[64:67], a[160:163], v[16:19], v[64:67]\n"
      "v_add_f32_dpp v72, v72, v72 row_ror:2 row_mask:0xf bank_mask:0xf\n"
      "v_add_f32_dpp v74, v74, v74 row_ror:2 row_mask:0xf bank_mask:0xf\n"
      "v_mfma_f32_16x16x32_bf16 v[68:71], a[216:219], v[16:19], v[68:71]\n"
      "v_add_f32_dpp v72, v72, v72 row_ror:1 row_mask:0xf bank_mask:0xf\n"
      "v_add_f32_dpp v74, v74, v74 row_ror:1 row_mask:0xf bank_mask:0xf\n"
      "ds_read_b128 v[16:19], v82 offset:4096\n"
      "s_waitcnt lgkmcnt(13)\n"
      "v_mfma_f32_16x16x32_bf16 v[64:67], a[164:167], v[20:23], v[64:67]\n"
      "v_mov_b32_e32 v76, v72\n"
      "v_mov_b32_e32 v77, v74\n"
      "v_mfma_f32_16x16x32_bf16 v[68:71], a[220:223], v[20:23], v[68:71]\n"
      "v_permlane16_swap_b32_e32 v76, v72\n"
      "v_permlane16_swap_b32_e32 v77, v74\n"
      "ds_read_b128 v[20:23], v82 offset:5120\n"
      "s_waitcnt lgkmcnt(13)\n"
      "v_mfma_f32_16x16x32_bf16 v[64:67], a[168:171], v[24:27], v[64:67]\n"
      "v_add_f32_e32 v72, v72, v76\n"
      "v_add_f32_e32 v74, v74, v77\n"
      "v_mfma_f32_16x16x32_bf16 v[68:71], a[224:227], v[24:27], v[68:71]\n"
      "v_mov_b32_e32 v76, v72\n"
      "v_mov_b32_e32 v77, v74\n"
      "ds_read_b128 v[24:27], v82 offset:6144\n"
      "s_waitcnt lgkmcnt(13)\n"
      "v_mfma_f32_16x16x32_bf16 v[64:67], a[172:175], v[28:31], v[64:67]\n"
      "v_permlane32_swap_b32_e32 v76, v72\n"
      "v_permlane32_swap_b32_e32 v77, v74\n"
      "v_mfma_f32_16x16x32_bf16 v[68:71], a[228:231], v[28:31], v[68:71]\n"
      "v_add_f32_e32 v72, v72, v76\n"
      "v_add_f32_e32 v74, v74, v77\n"
      "ds_read_b128 v[28:31], v82 offset:7168\n"
      "s_waitcnt lgkmcnt(13)\n"
      "v_mfma_f32_16x16x32_bf16 v[64:67], a[176:179], v[32:35], v[64:67]\n"
      "v_mul_f32_e32 v78, 0x3b124925, v72\n"
      "v_mul_f32_e32 v74, 0x3b124925, v74\n"
      "v_mfma_f32_16x16x32_bf16 v[68:71], a[232:235], v[32:35], v[68:71]\n"
      "v_fma_f32 v74, -v78, v78, v74\n"
      "v_cvt_pk_bf16_f32 v79, v78, v78\n"
      "ds_read_b128 v[32:35], v82 offset:8192\n"
      "s_waitcnt lgkmcnt(13)\n"
      "v_mfma_f32_16x16x32_bf16 v[64:67], a[180:183], v[36:39], v[64:67]\n"
      "v_max_f32_e32 v74, 0, v74\n"
      "v_and_b32_e32 v79, 0xffff0000, v79\n"
      "v_mfma_f32_16x16x32_bf16 v[68:71], a[236:239], v[36:39], v[68:71]\n"
      "v_cvt_pk_bf16_f32 v74, v74, v74\n"
      "v_xor_b32_e32 v79, 0x80000000, v79\n"
      "ds_read_b128 v[36:39], v82 offset:9216\n"
      "s_waitcnt lgkmcnt(13)\n"
      "v_mfma_f32_16x16x32_bf16 v[64:67], a[184:187], v[40:43], v[64:67]\n"
      "v_and_b32_e32 v74, 0xffff0000, v74\n"
      "v_add_f32_e32 v74, 0x3727c5ac, v74\n"
      "v_mfma_f32_16x16x32_bf16 v[68:71], a[240:243], v[40:43], v[68:71]\n"
      "v_cvt_pk_bf16_f32 v74, v74, v74\n"
      "v_and_b32_e32 v74, 0xffff0000, v74\n"
      "ds_read_b128 v[40:43], v82 offset:10240\n"
      "s_waitcnt lgkmcnt(13)\n"
      "v_mfma_f32_16x16x32_bf16 v[64:67], a[188:191], v[44:47], v[64:67]\n"
      "v_sqrt_f32_e32 v74, v74\n"
      "s_nop 0\n"
      "v_mfma_f32_16x16x32_bf16 v[68:71], a[244:247], v[44:47], v[68:71]\n"
      "v_cvt_pk_bf16_f32 v74, v74, v74\n"
      "v_and_b32_e32 v74, 0xffff0000, v74\n"
      "ds_read_b128 v[44:47], v82 offset:11264\n"
      "s_waitcnt lgkmcnt(13)\n"
      "v_mfma_f32_16x16x32_bf16 v[64:67], a[192:195], v[48:51], v[64:67]\n"
      "v_rcp_f32_e32 v80, v74\n"
      "v_mfma_f32_16x16x32_bf16 v[68:71], a[248:251], v[48:51], v[68:71]\n"
      "ds_read_b128 v[48:51], v82 offset:12288\n"
      "s_waitcnt lgkmcnt(13)\n"
      "v_mfma_f32_16x16x32_bf16 v[64:67], a[196:199], v[52:55], v[64:67]\n"
      "v_mfma_f32_16x16x32_bf16 v[68:71], a[252:255], v[52:55], v[68:71]\n"
      "ds_read_b128 v[52:55], v82 offset:13312\n"
      "v_mbcnt_lo_u32_b32 v57, -1, 0\n"
      "v_mbcnt_hi_u32_b32 v57, -1, v57\n"
      "v_lshrrev_b32_e32 v83, 4, v57\n"
      "v_lshlrev_b32_e32 v83, 3, v83\n"
      "v_and_b32_e32 v57, 3, v57\n"
      "v_lshl_add_u32 v83, v57, 1, v83\n"
      "s_lshl_b32 s88, s38, 5\n"
      "s_add_u32 s88, s88, 0x1ea00\n"
      "v_add_u32_e32 v83, s88, v83\n"
      "v_cndmask_b32_e64 v56, v64, v65, s[96:97]\n"
      "v_cndmask_b32_e64 v57, v66, v67, s[96:97]\n"
      "v_cndmask_b32_e64 v58, v68, v69, s[96:97]\n"
      "v_cndmask_b32_e64 v59, v70, v71, s[96:97]\n"
      "v_cndmask_b32_e64 v56, v56, v57, s[84:85]\n"
      "v_cndmask_b32_e64 v58, v58, v59, s[84:85]\n"
      "v_fma_f32 v56, v79, v152, v56\n"
      "v_fma_f32 v58, v79, v153, v58\n"
      "v_mul_f32_e32 v56, v80, v56\n"
      "v_mul_f32_e32 v58, v80, v58\n"
      "v_cvt_pk_bf16_f32 v57, v56, v58\n"
      "v_lshlrev_b32_e32 v56, 16, v57\n"
      "v_and_b32_e32 v58, 0xffff0000, v57\n"
      "v_mul_f32_e32 v56, v56, v58\n"
      "v_cvt_pk_bf16_f32 v56, v56, v56\n"
      "s_mov_b32 exec_lo, 0xf000f\n"
      "s_mov_b32 exec_hi, 0xf000f\n"
      "ds_write_b16 v83, v56\n"
      "s_mov_b64 exec, -1\n"
      "s_mov_b32 s88, 0x90800\n"
      "s_mul_i32 s89, s42, 28672\n"
      "s_add_u32 s88, s88, s89\n"
      "s_mul_i32 s89, s39, 896\n"
      "s_add_u32 s88, s88, s89\n"
      "s_mul_i32 s89, s38, 224\n"
      "s_add_u32 s88, s88, s89\n"
      "s_add_u32 s44, s36, s88\n"
      "s_addc_u32 s45, s37, 0\n"
      "v_mbcnt_lo_u32_b32 v93, -1, 0\n"
      "v_mbcnt_hi_u32_b32 v93, -1, v93\n"
      "v_and_b32_e32 v92, 15, v93\n"
      "v_lshlrev_b32_e32 v92, 5, v92\n"
      "v_lshrrev_b32_e32 v93, 4, v93\n"
      "v_lshl_add_u32 v92, v93, 3, v92\n"
      "s_waitcnt lgkmcnt(0)\n"
      "s_barrier\n"
      "ds_read_b128 v[56:59], v156 offset:2560\n"
      "ds_read_b128 v[60:63], v156 offset:2624\n"
      "s_waitcnt lgkmcnt(1)\n"
      "v_mfma_f32_16x16x32_bf16 v[64:67], v[0:3], v[56:59], 0\n"
      "v_mfma_f32_16x16x32_bf16 v[68:71], v[8:11], v[56:59], 0\n"
      "s_waitcnt lgkmcnt(0)\n"
      "v_mfma_f32_16x16x32_bf16 v[64:67], v[4:7], v[60:63], v[64:67]\n"
      "v_mfma_f32_16x16x32_bf16 v[72:75], v[16:19], v[56:59], 0\n"
      "v_mfma_f32_16x16x32_bf16 v[68:71], v[12:15], v[60:63], v[68:71]\n"
      "v_mfma_f32_16x16x32_bf16 v[76:79], v[24:27], v[56:59], 0\n"
      "s_nop 4\n"
      "v_cvt_pk_bf16_f32 v64, v64, v65\n"
      "v_cvt_pk_bf16_f32 v65, v66, v67\n"
      "v_min_u32_e32 v64, -2, v64\n"
      "v_min_u32_e32 v65, -2, v65\n"
      "v_mfma_f32_16x16x32_bf16 v[72:75], v[20:23], v[60:63], v[72:75]\n"
      "v_mfma_f32_16x16x32_bf16 v[80:83], v[32:35], v[56:59], 0\n"
      "v_cvt_pk_bf16_f32 v94, v68, v69\n"
      "v_cvt_pk_bf16_f32 v95, v70, v71\n"
      "v_min_u32_e32 v94, -2, v94\n"
      "v_min_u32_e32 v95, -2, v95\n"
      "s_mov_b32 s46, 0x20002\n"
      "s_mov_b32 s47, 0x20002\n"
      "v_cndmask_b32_e64 v64, v64, v94, s[46:47]\n"
      "v_cndmask_b32_e64 v65, v65, v95, s[46:47]\n"
      "v_mfma_f32_16x16x32_bf16 v[76:79], v[28:31], v[60:63], v[76:79]\n"
      "v_mfma_f32_16x16x32_bf16 v[84:87], v[40:43], v[56:59], 0\n"
      "v_cvt_pk_bf16_f32 v94, v72, v73\n"
      "v_cvt_pk_bf16_f32 v95, v74, v75\n"
      "v_min_u32_e32 v94, -2, v94\n"
      "v_min_u32_e32 v95, -2, v95\n"
      "s_mov_b32 s46, 0x40004\n"
      "s_mov_b32 s47, 0x40004\n"
      "v_cndmask_b32_e64 v64, v64, v94, s[46:47]\n"
      "v_cndmask_b32_e64 v65, v65, v95, s[46:47]\n"
      "v_mfma_f32_16x16x32_bf16 v[80:83], v[36:39], v[60:63], v[80:83]\n"
      "v_mfma_f32_16x16x32_bf16 v[88:91], v[48:51], v[56:59], 0\n"
      "v_cvt_pk_bf16_f32 v94, v76, v77\n"
      "v_cvt_pk_bf16_f32 v95, v78, v79\n"
      "v_min_u32_e32 v94, -2, v94\n"
      "v_min_u32_e32 v95, -2, v95\n"
      "s_mov_b32 s46, 0x80008\n"
      "s_mov_b32 s47, 0x80008\n"
      "v_cndmask_b32_e64 v64, v64, v94, s[46:47]\n"
      "v_cndmask_b32_e64 v65, v65, v95, s[46:47]\n"
      "v_mfma_f32_16x16x32_bf16 v[84:87], v[44:47], v[60:63], v[84:87]\n"
      "v_mfma_f32_16x16x32_bf16 v[88:91], v[52:55], v[60:63], v[88:91]\n"
      "v_cvt_pk_bf16_f32 v94, v80, v81\n"
      "v_cvt_pk_bf16_f32 v95, v82, v83\n"
      "v_min_u32_e32 v94, -2, v94\n"
      "v_min_u32_e32 v95, -2, v95\n"
      "s_mov_b32 s46, 0x100010\n"
      "s_mov_b32 s47, 0x100010\n"
      "v_cndmask_b32_e64 v64, v64, v94, s[46:47]\n"
      "v_cndmask_b32_e64 v65, v65, v95, s[46:47]\n"
      "v_cvt_pk_bf16_f32 v94, v84, v85\n"
      "v_cvt_pk_bf16_f32 v95, v86, v87\n"
      "v_min_u32_e32 v94, -2, v94\n"
      "v_min_u32_e32 v95, -2, v95\n"
      "s_mov_b32 s46, 0x200020\n"
      "s_mov_b32 s47, 0x200020\n"
      "v_cndmask_b32_e64 v64, v64, v94, s[46:47]\n"
      "v_cndmask_b32_e64 v65, v65, v95, s[46:47]\n"
      "v_cvt_pk_bf16_f32 v94, v88, v89\n"
      "v_cvt_pk_bf16_f32 v95, v90, v91\n"
      "v_min_u32_e32 v94, -2, v94\n"
      "v_min_u32_e32 v95, -2, v95\n"
      "s_mov_b32 s46, 0x400040\n"
      "s_mov_b32 s47, 0x400040\n"
      "v_cndmask_b32_e64 v64, v64, v94, s[46:47]\n"
      "v_cndmask_b32_e64 v65, v65, v95, s[46:47]\n"
      "s_mov_b32 exec_lo, 0x7f007f\n"
      "s_mov_b32 exec_hi, 0x7f007f\n"
      "global_store_dwordx2 v92, v[64:65], s[44:45] sc1\n"
      "s_mov_b64 exec, -1\n"
      "s_nop 0\n"
      "NOBC_142_%=:\n"
      "v_mov_b32_e32 v96, -1\n"
      "v_mov_b32_e32 v97, -1\n"
      "v_mov_b32_e32 v98, -1\n"
      "v_mov_b32_e32 v99, -1\n"
      "v_mov_b32_e32 v100, -1\n"
      "v_mov_b32_e32 v101, -1\n"
      "v_mov_b32_e32 v102, -1\n"
      "v_mov_b32_e32 v103, -1\n"
      "v_mov_b32_e32 v104, -1\n"
      "v_mov_b32_e32 v105, -1\n"
      "v_mov_b32_e32 v106, -1\n"
      "v_mov_b32_e32 v107, -1\n"
      "v_mov_b32_e32 v108, -1\n"
      "v_mov_b32_e32 v109, -1\n"
      "v_mov_b32_e32 v110, -1\n"
      "v_mov_b32_e32 v111, -1\n"
      "v_mov_b32_e32 v112, -1\n"
      "v_mov_b32_e32 v113, -1\n"
      "v_mov_b32_e32 v114, -1\n"
      "v_mov_b32_e32 v115, -1\n"
      "v_mov_b32_e32 v116, -1\n"
      "v_mov_b32_e32 v117, -1\n"
      "v_mov_b32_e32 v118, -1\n"
      "v_mov_b32_e32 v119, -1\n"
      "v_mov_b32_e32 v120, -1\n"
      "v_mov_b32_e32 v121, -1\n"
      "v_mov_b32_e32 v122, -1\n"
      "v_mov_b32_e32 v123, -1\n"
      "s_bitcmp1_b32 s42, 0\n"
      "s_nop 3\n"
      "s_cbranch_scc1 CO_125_%=\n"
      "v_cndmask_b32_e64 v84, 0, v159, s[64:65]\n"
      "v_cndmask_b32_e64 v85, 0, v159, s[66:67]\n"
      "v_cndmask_b32_e64 v86, 0, v159, s[68:69]\n"
      "v_cndmask_b32_e64 v87, 0, v159, s[70:71]\n"
      "s_mov_b32 s88, 0x90800\n"
      "s_mul_i32 s89, s42, 28672\n"
      "s_add_u32 s88, s88, s89\n"
      "s_add_u32 s44, s36, s88\n"
      "s_addc_u32 s45, s37, 0\n"
      "s_mov_b32 s94, 0x3fff3fff\n"
      "s_mov_b32 s95, 0x3fff3fff\n"
      "s_mov_b64 s[48:49], s[94:95]\n"
      "s_mov_b64 s[50:51], s[94:95]\n"
      "s_mov_b64 s[52:53], s[94:95]\n"
      "s_mov_b64 s[54:55], s[94:95]\n"
      "s_mov_b64 s[56:57], s[94:95]\n"
      "s_mov_b64 s[58:59], s[94:95]\n"
      "s_mov_b64 s[60:61], s[94:95]\n"
      "s_mov_b32 s43, 0\n"
      "s_sleep 8\n"
      "s_nop 0\n"
      "GT_143_%=:\n"
      "s_mov_b64 exec, s[48:49]\n"
      "s_nop 0\n"
      "s_cbranch_execz GS_146_%=\n"
      "s_add_u32 s46, s44, 0x0\n"
      "s_addc_u32 s47, s45, 0\n"
      "global_load_dwordx4 v[96:99], v154, s[46:47] sc1\n"
      "GS_146_%=:\n"
      "s_mov_b64 exec, s[50:51]\n"
      "s_nop 0\n"
      "s_cbranch_execz GS_147_%=\n"
      "s_add_u32 s46, s44, 0xe00\n"
      "s_addc_u32 s47, s45, 0\n"
      "global_load_dwordx4 v[100:103], v154, s[46:47] sc1\n"
      "GS_147_%=:\n"
      "s_mov_b64 exec, s[52:53]\n"
      "s_nop 0\n"
      "s_cbranch_execz GS_148_%=\n"
      "s_add_u32 s46, s44, 0x1c00\n"
      "s_addc_u32 s47, s45, 0\n"
      "global_load_dwordx4 v[104:107], v154, s[46:47] sc1\n"
      "GS_148_%=:\n"
      "s_mov_b64 exec, s[54:55]\n"
      "s_nop 0\n"
      "s_cbranch_execz GS_149_%=\n"
      "s_add_u32 s46, s44, 0x2a00\n"
      "s_addc_u32 s47, s45, 0\n"
      "global_load_dwordx4 v[108:111], v154, s[46:47] sc1\n"
      "GS_149_%=:\n"
      "s_mov_b64 exec, s[56:57]\n"
      "s_nop 0\n"
      "s_cbranch_execz GS_150_%=\n"
      "s_add_u32 s46, s44, 0x3800\n"
      "s_addc_u32 s47, s45, 0\n"
      "global_load_dwordx4 v[112:115], v154, s[46:47] sc1\n"
      "GS_150_%=:\n"
      "s_mov_b64 exec, s[58:59]\n"
      "s_nop 0\n"
      "s_cbranch_execz GS_151_%=\n"
      "s_add_u32 s46, s44, 0x4600\n"
      "s_addc_u32 s47, s45, 0\n"
      "global_load_dwordx4 v[116:119], v154, s[46:47] sc1\n"
      "GS_151_%=:\n"
      "s_mov_b64 exec, s[60:61]\n"
      "s_nop 0\n"
      "s_cbranch_execz GS_152_%=\n"
      "s_add_u32 s46, s44, 0x5400\n"
      "s_addc_u32 s47, s45, 0\n"
      "global_load_dwordx4 v[120:123], v154, s[46:47] sc1\n"
      "GS_152_%=:\n"
      "s_mov_b64 exec, -1\n"
      "s_sleep 1\n"
      "v_max3_u32 v56, v96, v97, v98\n"
      "v_max_u32_e32 v56, v56, v99\n"
      "v_cmp_eq_u32_e64 s[48:49], -1, v56\n"
      "v_max3_u32 v57, v100, v101, v102\n"
      "v_max_u32_e32 v57, v57, v103\n"
      "v_cmp_eq_u32_e64 s[50:51], -1, v57\n"
      "v_max3_u32 v56, v104, v105, v106\n"
      "v_max_u32_e32 v56, v56, v107\n"
      "v_cmp_eq_u32_e64 s[52:53], -1, v56\n"
      "v_max3_u32 v57, v108, v109, v110\n"
      "v_max_u32_e32 v57, v57, v111\n"
      "v_cmp_eq_u32_e64 s[54:55], -1, v57\n"
      "v_max3_u32 v56, v112, v113, v114\n"
      "v_max_u32_e32 v56, v56, v115\n"
      "v_cmp_eq_u32_e64 s[56:57], -1, v56\n"
      "v_max3_u32 v57, v116, v117, v118\n"
      "v_max_u32_e32 v57, v57, v119\n"
      "v_cmp_eq_u32_e64 s[58:59], -1, v57\n"
      "v_max3_u32 v56, v120, v121, v122\n"
      "v_max_u32_e32 v56, v56, v123\n"
      "v_cmp_eq_u32_e64 s[60:61], -1, v56\n"
      "s_and_b64 s[48:49], s[48:49], s[94:95]\n"
      "s_and_b64 s[50:51], s[50:51], s[94:95]\n"
      "s_and_b64 s[52:53], s[52:53], s[94:95]\n"
      "s_and_b64 s[54:55], s[54:55], s[94:95]\n"
      "s_and_b64 s[56:57], s[56:57], s[94:95]\n"
      "s_and_b64 s[58:59], s[58:59], s[94:95]\n"
      "s_and_b64 s[60:61], s[60:61], s[94:95]\n"
      "s_mov_b64 s[88:89], s[48:49]\n"
      "s_or_b64 s[88:89], s[88:89], s[50:51]\n"
      "s_or_b64 s[88:89], s[88:89], s[52:53]\n"
      "s_or_b64 s[88:89], s[88:89], s[54:55]\n"
      "s_or_b64 s[88:89], s[88:89], s[56:57]\n"
      "s_or_b64 s[88:89], s[88:89], s[58:59]\n"
      "s_or_b64 s[88:89], s[88:89], s[60:61]\n"
      "s_cmp_eq_u64 s[88:89], 0\n"
      "s_nop 0\n"
      "s_cbranch_scc1 GD_144_%=\n"
      "s_add_u32 s43, s43, 1\n"
      "s_cmp_lt_u32 s43, 0x80000\n"
      "s_waitcnt vmcnt(7)\n"
      "s_nop 0\n"
      "s_cbranch_scc1 GT_143_%=\n"
      "s_nop 0\n"
      "GD_144_%=:\n"
      "v_mfma_f32_16x16x32_bf16 v[88:91], v[96:99], v[84:87], 0\n"
      "v_mfma_f32_16x16x32_bf16 v[88:91], v[100:103], v[84:87], v[88:91]\n"
      "v_mfma_f32_16x16x32_bf16 v[88:91], v[104:107], v[84:87], v[88:91]\n"
      "v_mfma_f32_16x16x32_bf16 v[88:91], v[108:111], v[84:87], v[88:91]\n"
      "v_mfma_f32_16x16x32_bf16 v[88:91], v[112:115], v[84:87], v[88:91]\n"
      "v_mfma_f32_16x16x32_bf16 v[88:91], v[116:119], v[84:87], v[88:91]\n"
      "v_mfma_f32_16x16x32_bf16 v[88:91], v[120:123], v[84:87], v[88:91]\n"
      "s_nop 7\n"
      "v_cndmask_b32_e64 v56, v88, v90, s[62:63]\n"
      "v_cndmask_b32_e64 v57, v89, v91, s[62:63]\n"
      "v_cvt_pk_bf16_f32 v58, v56, v57\n"
      "v_lshlrev_b32_e32 v56, 16, v58\n"
      "v_and_b32_e32 v57, 0xffff0000, v58\n"
      "v_add_f32_e32 v56, v140, v56\n"
      "v_add_f32_e32 v57, v141, v57\n"
      "v_cvt_pk_bf16_f32 v58, v56, v57\n"
      "s_mov_b32 exec_lo, -1\n"
      "s_mov_b32 exec_hi, 0x00ffffff\n"
      "ds_write_b32 v155, v58 offset:0\n"
      "s_mov_b64 exec, -1\n"
      "v_lshlrev_b32_e32 v140, 16, v58\n"
      "v_and_b32_e32 v141, 0xffff0000, v58\n"
      "v_mbcnt_lo_u32_b32 v93, -1, 0\n"
      "v_mbcnt_hi_u32_b32 v93, -1, v93\n"
      "v_and_b32_e32 v92, 15, v93\n"
      "v_lshlrev_b32_e32 v92, 6, v92\n"
      "v_and_b32_e32 v93, 48, v93\n"
      "v_add_u32_e32 v92, v92, v93\n"
      "s_add_u32 s88, s42, 1\n"
      "s_lshl_b32 s88, s88, 17\n"
      "s_lshl_b32 s89, s40, 13\n"
      "s_add_u32 s88, s88, s89\n"
      "s_lshl_b32 s89, s41, 10\n"
      "s_add_u32 s88, s88, s89\n"
      "s_lshr_b32 s89, s38, 1\n"
      "s_lshl_b32 s89, s89, 9\n"
      "s_add_u32 s88, s88, s89\n"
      "s_and_b32 s89, s38, 1\n"
      "s_lshl_b32 s89, s89, 12\n"
      "s_add_u32 s88, s88, s89\n"
      "s_add_u32 s88, s88, 0x800\n"
      "s_add_u32 s46, s36, s88\n"
      "s_addc_u32 s47, s37, 0\n"
      "s_mov_b64 s[44:45], s[46:47]\n"
      "s_waitcnt lgkmcnt(0)\n"
      "s_barrier\n"
      "v_mbcnt_lo_u32_b32 v81, -1, 0\n"
      "v_mbcnt_hi_u32_b32 v81, -1, v81\n"
      "v_lshlrev_b32_e32 v81, 4, v81\n"
      "v_add_u32_e32 v76, 0x1e000, v81\n"
      "ds_read_b128 v[60:63], v76\n"
      "ds_read_b128 v[0:3], v156 offset:0\n"
      "ds_read_b128 v[4:7], v156 offset:64\n"
      "ds_read_b128 v[8:11], v156 offset:128\n"
      "ds_read_b128 v[12:15], v156 offset:256\n"
      "ds_read_b128 v[16:19], v156 offset:320\n"
      "ds_read_b128 v[20:23], v156 offset:384\n"
      "ds_read_b128 v[24:27], v156 offset:512\n"
      "ds_read_b128 v[28:31], v156 offset:576\n"
      "ds_read_b128 v[32:35], v156 offset:640\n"
      "ds_read_b128 v[36:39], v156 offset:768\n"
      "ds_read_b128 v[40:43], v156 offset:832\n"
      "ds_read_b128 v[44:47], v156 offset:896\n"
      "ds_read_b128 v[48:51], v157 offset:192\n"
      "ds_read_b128 v[52:55], v157 offset:704\n"
      "s_mov_b32 s89, 0x3f803f80\n"
      "s_waitcnt lgkmcnt(13)\n"
      "v_mfma_f32_16x16x32_bf16 v[64:67], a[32:35], v[0:3], 0\n"
      "v_dot2_f32_bf16 v72, v60, s89, 0\n"
      "v_dot2_f32_bf16 v74, v60, v60, 0\n"
      "v_mfma_f32_16x16x32_bf16 v[68:71], a[88:91], v[0:3], 0\n"
      "v_dot2_f32_bf16 v73, v62, s89, 0\n"
      "v_dot2_f32_bf16 v75, v62, v62, 0\n"
      "s_waitcnt lgkmcnt(12)\n"
      "v_mfma_f32_16x16x32_bf16 v[64:67], a[36:39], v[4:7], v[64:67]\n"
      "v_dot2_f32_bf16 v72, v61, s89, v72\n"
      "v_dot2_f32_bf16 v74, v61, v61, v74\n"
      "v_mfma_f32_16x16x32_bf16 v[68:71], a[92:95], v[4:7], v[68:71]\n"
      "v_dot2_f32_bf16 v73, v63, s89, v73\n"
      "v_dot2_f32_bf16 v75, v63, v63, v75\n"
      "s_waitcnt lgkmcnt(11)\n"
      "v_mfma_f32_16x16x32_bf16 v[64:67], a[40:43], v[8:11], v[64:67]\n"
      "s_nop 0\n"
      "s_nop 0\n"
      "v_mfma_f32_16x16x32_bf16 v[68:71], a[96:99], v[8:11], v[68:71]\n"
      "v_add_f32_e32 v72, v72, v73\n"
      "v_add_f32_e32 v74, v74, v75\n"
      "s_waitcnt lgkmcnt(10)\n"
      "v_mfma_f32_16x16x32_bf16 v[64:67], a[44:47], v[12:15], v[64:67]\n"
      "v_add_f32_dpp v72, v72, v72 row_ror:8 row_mask:0xf bank_mask:0xf\n"
      "v_add_f32_dpp v74, v74, v74 row_ror:8 row_mask:0xf bank_mask:0xf\n"
      "v_mfma_f32_16x16x32_bf16 v[68:71], a[100:103], v[12:15], v[68:71]\n"
      "v_add_f32_dpp v72, v72, v72 row_ror:4 row_mask:0xf bank_mask:0xf\n"
      "v_add_f32_dpp v74, v74, v74 row_ror:4 row_mask:0xf bank_mask:0xf\n"
      "s_waitcnt lgkmcnt(9)\n"
      "v_mfma_f32_16x16x32_bf16 v[64:67], a[48:51], v[16:19], v[64:67]\n"
      "v_add_f32_dpp v72, v72, v72 row_ror:2 row_mask:0xf bank_mask:0xf\n"
      "v_add_f32_dpp v74, v74, v74 row_ror:2 row_mask:0xf bank_mask:0xf\n"
      "v_mfma_f32_16x16x32_bf16 v[68:71], a[104:107], v[16:19], v[68:71]\n"
      "v_add_f32_dpp v72, v72, v72 row_ror:1 row_mask:0xf bank_mask:0xf\n"
      "v_add_f32_dpp v74, v74, v74 row_ror:1 row_mask:0xf bank_mask:0xf\n"
      "s_waitcnt lgkmcnt(8)\n"
      "v_mfma_f32_16x16x32_bf16 v[64:67], a[52:55], v[20:23], v[64:67]\n"
      "v_mov_b32_e32 v76, v72\n"
      "v_mov_b32_e32 v77, v74\n"
      "v_mfma_f32_16x16x32_bf16 v[68:71], a[108:111], v[20:23], v[68:71]\n"
      "v_permlane16_swap_b32_e32 v76, v72\n"
      "v_permlane16_swap_b32_e32 v77, v74\n"
      "s_waitcnt lgkmcnt(7)\n"
      "v_mfma_f32_16x16x32_bf16 v[64:67], a[56:59], v[24:27], v[64:67]\n"
      "v_add_f32_e32 v72, v72, v76\n"
      "v_add_f32_e32 v74, v74, v77\n"
      "v_mfma_f32_16x16x32_bf16 v[68:71], a[112:115], v[24:27], v[68:71]\n"
      "v_mov_b32_e32 v76, v72\n"
      "v_mov_b32_e32 v77, v74\n"
      "s_waitcnt lgkmcnt(6)\n"
      "v_mfma_f32_16x16x32_bf16 v[64:67], a[60:63], v[28:31], v[64:67]\n"
      "v_permlane32_swap_b32_e32 v76, v72\n"
      "v_permlane32_swap_b32_e32 v77, v74\n"
      "v_mfma_f32_16x16x32_bf16 v[68:71], a[116:119], v[28:31], v[68:71]\n"
      "v_add_f32_e32 v72, v72, v76\n"
      "v_add_f32_e32 v74, v74, v77\n"
      "s_waitcnt lgkmcnt(5)\n"
      "v_mfma_f32_16x16x32_bf16 v[64:67], a[64:67], v[32:35], v[64:67]\n"
      "v_mul_f32_e32 v78, 0x3b124925, v72\n"
      "v_mul_f32_e32 v74, 0x3b124925, v74\n"
      "v_mfma_f32_16x16x32_bf16 v[68:71], a[120:123], v[32:35], v[68:71]\n"
      "v_fma_f32 v74, -v78, v78, v74\n"
      "v_cvt_pk_bf16_f32 v79, v78, v78\n"
      "s_waitcnt lgkmcnt(4)\n"
      "v_mfma_f32_16x16x32_bf16 v[64:67], a[68:71], v[36:39], v[64:67]\n"
      "v_max_f32_e32 v74, 0, v74\n"
      "v_and_b32_e32 v79, 0xffff0000, v79\n"
      "v_mfma_f32_16x16x32_bf16 v[68:71], a[124:127], v[36:39], v[68:71]\n"
      "v_cvt_pk_bf16_f32 v74, v74, v74\n"
      "v_xor_b32_e32 v79, 0x80000000, v79\n"
      "s_waitcnt lgkmcnt(3)\n"
      "v_mfma_f32_16x16x32_bf16 v[64:67], a[72:75], v[40:43], v[64:67]\n"
      "v_and_b32_e32 v74, 0xffff0000, v74\n"
      "v_add_f32_e32 v74, 0x3727c5ac, v74\n"
      "v_mfma_f32_16x16x32_bf16 v[68:71], a[128:131], v[40:43], v[68:71]\n"
      "v_cvt_pk_bf16_f32 v74, v74, v74\n"
      "v_and_b32_e32 v74, 0xffff0000, v74\n"
      "s_waitcnt lgkmcnt(2)\n"
      "v_mfma_f32_16x16x32_bf16 v[64:67], a[76:79], v[44:47], v[64:67]\n"
      "v_sqrt_f32_e32 v74, v74\n"
      "s_nop 0\n"
      "v_mfma_f32_16x16x32_bf16 v[68:71], a[132:135], v[44:47], v[68:71]\n"
      "v_cvt_pk_bf16_f32 v74, v74, v74\n"
      "v_and_b32_e32 v74, 0xffff0000, v74\n"
      "s_waitcnt lgkmcnt(1)\n"
      "v_mfma_f32_16x16x32_bf16 v[64:67], a[80:83], v[48:51], v[64:67]\n"
      "v_rcp_f32_e32 v80, v74\n"
      "v_mfma_f32_16x16x32_bf16 v[68:71], a[136:139], v[48:51], v[68:71]\n"
      "s_waitcnt lgkmcnt(0)\n"
      "v_mfma_f32_16x16x32_bf16 v[64:67], a[84:87], v[52:55], v[64:67]\n"
      "v_mfma_f32_16x16x32_bf16 v[68:71], a[140:143], v[52:55], v[68:71]\n"
      "s_nop 6\n"
      "v_fma_f32 v64, v79, v144, v64\n"
      "v_fma_f32 v65, v79, v145, v65\n"
      "v_fma_f32 v66, v79, v146, v66\n"
      "v_fma_f32 v67, v79, v147, v67\n"
      "v_mul_f32_e32 v64, v80, v64\n"
      "v_mul_f32_e32 v65, v80, v65\n"
      "v_mul_f32_e32 v66, v80, v66\n"
      "v_mul_f32_e32 v67, v80, v67\n"
      "v_cvt_pk_bf16_f32 v84, v64, v65\n"
      "v_cvt_pk_bf16_f32 v86, v66, v67\n"
      "v_mov_b32_e32 v85, s35\n"
      "v_mov_b32_e32 v87, s35\n"
      "v_fma_f32 v68, v79, v148, v68\n"
      "v_fma_f32 v69, v79, v149, v69\n"
      "v_fma_f32 v70, v79, v150, v70\n"
      "v_fma_f32 v71, v79, v151, v71\n"
      "v_mul_f32_e32 v68, v80, v68\n"
      "v_mul_f32_e32 v69, v80, v69\n"
      "v_mul_f32_e32 v70, v80, v70\n"
      "v_mul_f32_e32 v71, v80, v71\n"
      "v_cvt_pk_bf16_f32 v88, v68, v69\n"
      "v_cvt_pk_bf16_f32 v90, v70, v71\n"
      "v_mov_b32_e32 v89, s35\n"
      "v_mov_b32_e32 v91, s35\n"
      "s_mov_b32 s46, 0x00020002\n"
      "s_mov_b32 s47, 0x00020002\n"
      "v_cndmask_b32_e64 v84, v84, v88, s[46:47]\n"
      "v_cndmask_b32_e64 v86, v86, v90, s[46:47]\n"
      "s_mov_b32 exec_lo, 0x30003\n"
      "s_mov_b32 exec_hi, 0x30003\n"
      "global_store_dwordx4 v92, v[84:87], s[44:45] sc1\n"
      "s_mov_b64 exec, -1\n"
      "s_add_u32 s83, s42, 1\n"
      "s_getpc_b64 s[88:89]\n"
      "XPC_153_%=:\n"
      "s_add_u32 s88, s88, C_EXTRA_%=-XPC_153_%=\n"
      "s_addc_u32 s89, s89, 0\n"
      "s_nop 0\n"
      "s_swappc_b64 s[98:99], s[88:89]\n"
      "s_cmp_lg_u32 s42, 0\n"
      "s_nop 0\n"
      "s_cbranch_scc1 CN_126_%=\n"
      "v_mbcnt_lo_u32_b32 v76, -1, 0\n"
      "v_mbcnt_hi_u32_b32 v76, -1, v76\n"
      "v_bfe_u32 v77, v76, 2, 2\n"
      "v_lshlrev_b32_e32 v60, 9, v77\n"
      "v_lshrrev_b32_e32 v77, 4, v76\n"
      "v_lshl_add_u32 v60, v77, 6, v60\n"
      "v_lshlrev_b32_e32 v61, 9, v77\n"
      "v_and_b32_e32 v78, 3, v76\n"
      "v_lshl_add_u32 v60, v78, 4, v60\n"
      "v_bfe_u32 v78, v76, 3, 1\n"
      "v_lshl_add_u32 v61, v78, 12, v61\n"
      "v_and_b32_e32 v78, 7, v76\n"
      "v_lshl_add_u32 v61, v78, 4, v61\n"
      "s_lshl_b32 s88, s100, 22\n"
      "s_lshl_b32 s89, s101, 2\n"
      "s_add_u32 s89, s89, s38\n"
      "s_add_u32 s89, s89, 504\n"
      "s_lshl_b32 s89, s89, 13\n"
      "s_add_u32 s88, s88, s89\n"
      "s_add_u32 s88, s88, 0x4000000\n"
      "s_lshl_b32 s90, s38, 14\n"
      "s_add_u32 s90, s90, 0xe000\n"
      "s_mov_b64 s[92:93], %[kc]\n"
      "s_add_u32 s92, s92, s88\n"
      "s_addc_u32 s93, s93, 0\n"
      "s_add_u32 s46, s92, 0x0\n"
      "s_addc_u32 s47, s93, 0\n"
      "s_add_u32 m0, s90, 0x0\n"
      "s_nop 0\n"
      "global_load_lds_dwordx4 v60, s[46:47]\n"
      "s_add_u32 s46, s92, 0x100\n"
      "s_addc_u32 s47, s93, 0\n"
      "s_add_u32 m0, s90, 0x400\n"
      "s_nop 0\n"
      "global_load_lds_dwordx4 v60, s[46:47]\n"
      "s_add_u32 s46, s92, 0x800\n"
      "s_addc_u32 s47, s93, 0\n"
      "s_add_u32 m0, s90, 0x800\n"
      "s_nop 0\n"
      "global_load_lds_dwordx4 v60, s[46:47]\n"
      "s_add_u32 s46, s92, 0x900\n"
      "s_addc_u32 s47, s93, 0\n"
      "s_add_u32 m0, s90, 0xc00\n"
      "s_nop 0\n"
      "global_load_lds_dwordx4 v60, s[46:47]\n"
      "s_add_u32 s46, s92, 0x1000\n"
      "s_addc_u32 s47, s93, 0\n"
      "s_add_u32 m0, s90, 0x1000\n"
      "s_nop 0\n"
      "global_load_lds_dwordx4 v60, s[46:47]\n"
      "s_add_u32 s46, s92, 0x1100\n"
      "s_addc_u32 s47, s93, 0\n"
      "s_add_u32 m0, s90, 0x1400\n"
      "s_nop 0\n"
      "global_load_lds_dwordx4 v60, s[46:47]\n"
      "s_add_u32 s46, s92, 0x1800\n"
      "s_addc_u32 s47, s93, 0\n"
      "s_add_u32 m0, s90, 0x1800\n"
      "s_nop 0\n"
      "global_load_lds_dwordx4 v60, s[46:47]\n"
      "s_add_u32 s46, s92, 0x1900\n"
      "s_addc_u32 s47, s93, 0\n"
      "s_add_u32 m0, s90, 0x1c00\n"
      "s_nop 0\n"
      "global_load_lds_dwordx4 v60, s[46:47]\n"
      "s_mov_b64 s[92:93], %[vc]\n"
      "s_add_u32 s92, s92, s88\n"
      "s_addc_u32 s93, s93, 0\n"
      "s_add_u32 s46, s92, 0x0\n"
      "s_addc_u32 s47, s93, 0\n"
      "s_add_u32 m0, s90, 0x2000\n"
      "s_nop 0\n"
      "global_load_lds_dwordx4 v61, s[46:47]\n"
      "s_add_u32 s46, s92, 0x80\n"
      "s_addc_u32 s47, s93, 0\n"
      "s_add_u32 m0, s90, 0x2400\n"
      "s_nop 0\n"
      "global_load_lds_dwordx4 v61, s[46:47]\n"
      "s_add_u32 s46, s92, 0x100\n"
      "s_addc_u32 s47, s93, 0\n"
      "s_add_u32 m0, s90, 0x2800\n"
      "s_nop 0\n"
      "global_load_lds_dwordx4 v61, s[46:47]\n"
      "s_add_u32 s46, s92, 0x180\n"
      "s_addc_u32 s47, s93, 0\n"
      "s_add_u32 m0, s90, 0x2c00\n"
      "s_nop 0\n"
      "global_load_lds_dwordx4 v61, s[46:47]\n"
      "s_add_u32 s46, s92, 0x800\n"
      "s_addc_u32 s47, s93, 0\n"
      "s_add_u32 m0, s90, 0x3000\n"
      "s_nop 0\n"
      "global_load_lds_dwordx4 v61, s[46:47]\n"
      "s_add_u32 s46, s92, 0x880\n"
      "s_addc_u32 s47, s93, 0\n"
      "s_add_u32 m0, s90, 0x3400\n"
      "s_nop 0\n"
      "global_load_lds_dwordx4 v61, s[46:47]\n"
      "s_add_u32 s46, s92, 0x900\n"
      "s_addc_u32 s47, s93, 0\n"
      "s_add_u32 m0, s90, 0x3800\n"
      "s_nop 0\n"
      "global_load_lds_dwordx4 v61, s[46:47]\n"
      "s_add_u32 s46, s92, 0x980\n"
      "s_addc_u32 s47, s93, 0\n"
      "s_add_u32 m0, s90, 0x3c00\n"
      "s_nop 0\n"
      "global_load_lds_dwordx4 v61, s[46:47]\n"
      "s_mov_b32 s83, 2\n"
      "s_getpc_b64 s[88:89]\n"
      "XPC_154_%=:\n"
      "s_add_u32 s88, s88, C_EXTRA_%=-XPC_154_%=\n"
      "s_addc_u32 s89, s89, 0\n"
      "s_nop 0\n"
      "s_swappc_b64 s[98:99], s[88:89]\n"
      "s_branch CN_126_%=\n"
      "s_nop 0\n"
      "CO_125_%=:\n"
      "s_cmp_ge_u32 s39, 28\n"
      "s_nop 0\n"
      "s_cbranch_scc1 NOF_155_%=\n"
      "s_mul_i32 s88, s39, 37\n"
      "s_lshr_b32 s88, s88, 8\n"
      "s_cmp_lg_u32 s88, s38\n"
      "s_nop 0\n"
      "s_cbranch_scc1 NOF_155_%=\n"
      "s_mul_i32 s88, s88, 7\n"
      "s_sub_u32 s88, s39, s88\n"
      "s_lshl_b32 s83, s88, 1\n"
      "s_lshl_b32 s82, 0x00030003, s83\n"
      "v_cndmask_b32_e64 v84, 0, v159, s[64:65]\n"
      "v_cndmask_b32_e64 v85, 0, v159, s[66:67]\n"
      "v_cndmask_b32_e64 v86, 0, v159, s[68:69]\n"
      "v_cndmask_b32_e64 v87, 0, v159, s[70:71]\n"
      "v_mbcnt_lo_u32_b32 v93, -1, 0\n"
      "v_mbcnt_hi_u32_b32 v93, -1, v93\n"
      "v_lshrrev_b32_e32 v92, 3, v93\n"
      "v_lshlrev_b32_e32 v92, 5, v92\n"
      "v_and_b32_e32 v93, 7, v93\n"
      "v_lshl_add_u32 v92, v93, 1, v92\n"
      "s_mul_i32 s88, s38, 224\n"
      "v_add_u32_e32 v92, s88, v92\n"
      "s_mov_b32 s88, 0x90800\n"
      "s_mul_i32 s89, s42, 28672\n"
      "s_add_u32 s88, s88, s89\n"
      "s_add_u32 s44, s36, s88\n"
      "s_addc_u32 s45, s37, 0\n"
      "s_mov_b32 s94, s82\n"
      "s_mov_b32 s95, s82\n"
      "s_mov_b64 s[48:49], s[94:95]\n"
      "s_mov_b64 s[50:51], s[94:95]\n"
      "s_mov_b64 s[52:53], s[94:95]\n"
      "s_mov_b64 s[54:55], s[94:95]\n"
      "s_mov_b64 s[56:57], s[94:95]\n"
      "s_mov_b64 s[58:59], s[94:95]\n"
      "s_mov_b64 s[60:61], s[94:95]\n"
      "s_mov_b32 s43, 0\n"
      "s_sleep 8\n"
      "s_nop 0\n"
      "GT_156_%=:\n"
      "s_mov_b64 exec, s[48:49]\n"
      "s_nop 0\n"
      "s_cbranch_execz GS_159_%=\n"
      "s_add_u32 s46, s44, 0x0\n"
      "s_addc_u32 s47, s45, 0\n"
      "global_load_dwordx4 v[96:99], v154, s[46:47] sc1\n"
      "GS_159_%=:\n"
      "s_mov_b64 exec, s[50:51]\n"
      "s_nop 0\n"
      "s_cbranch_execz GS_160_%=\n"
      "s_add_u32 s46, s44, 0xe00\n"
      "s_addc_u32 s47, s45, 0\n"
      "global_load_dwordx4 v[100:103], v154, s[46:47] sc1\n"
      "GS_160_%=:\n"
      "s_mov_b64 exec, s[52:53]\n"
      "s_nop 0\n"
      "s_cbranch_execz GS_161_%=\n"
      "s_add_u32 s46, s44, 0x1c00\n"
      "s_addc_u32 s47, s45, 0\n"
      "global_load_dwordx4 v[104:107], v154, s[46:47] sc1\n"
      "GS_161_%=:\n"
      "s_mov_b64 exec, s[54:55]\n"
      "s_nop 0\n"
      "s_cbranch_execz GS_162_%=\n"
      "s_add_u32 s46, s44, 0x2a00\n"
      "s_addc_u32 s47, s45, 0\n"
      "global_load_dwordx4 v[108:111], v154, s[46:47] sc1\n"
      "GS_162_%=:\n"
      "s_mov_b64 exec, s[56:57]\n"
      "s_nop 0\n"
      "s_cbranch_execz GS_163_%=\n"
      "s_add_u32 s46, s44, 0x3800\n"
      "s_addc_u32 s47, s45, 0\n"
      "global_load_dwordx4 v[112:115], v154, s[46:47] sc1\n"
      "GS_163_%=:\n"
      "s_mov_b64 exec, s[58:59]\n"
      "s_nop 0\n"
      "s_cbranch_execz GS_164_%=\n"
      "s_add_u32 s46, s44, 0x4600\n"
      "s_addc_u32 s47, s45, 0\n"
      "global_load_dwordx4 v[116:119], v154, s[46:47] sc1\n"
      "GS_164_%=:\n"
      "s_mov_b64 exec, s[60:61]\n"
      "s_nop 0\n"
      "s_cbranch_execz GS_165_%=\n"
      "s_add_u32 s46, s44, 0x5400\n"
      "s_addc_u32 s47, s45, 0\n"
      "global_load_dwordx4 v[120:123], v154, s[46:47] sc1\n"
      "GS_165_%=:\n"
      "s_mov_b64 exec, -1\n"
      "s_sleep 1\n"
      "v_max3_u32 v56, v96, v97, v98\n"
      "v_max_u32_e32 v56, v56, v99\n"
      "v_cmp_eq_u32_e64 s[48:49], -1, v56\n"
      "v_max3_u32 v57, v100, v101, v102\n"
      "v_max_u32_e32 v57, v57, v103\n"
      "v_cmp_eq_u32_e64 s[50:51], -1, v57\n"
      "v_max3_u32 v56, v104, v105, v106\n"
      "v_max_u32_e32 v56, v56, v107\n"
      "v_cmp_eq_u32_e64 s[52:53], -1, v56\n"
      "v_max3_u32 v57, v108, v109, v110\n"
      "v_max_u32_e32 v57, v57, v111\n"
      "v_cmp_eq_u32_e64 s[54:55], -1, v57\n"
      "v_max3_u32 v56, v112, v113, v114\n"
      "v_max_u32_e32 v56, v56, v115\n"
      "v_cmp_eq_u32_e64 s[56:57], -1, v56\n"
      "v_max3_u32 v57, v116, v117, v118\n"
      "v_max_u32_e32 v57, v57, v119\n"
      "v_cmp_eq_u32_e64 s[58:59], -1, v57\n"
      "v_max3_u32 v56, v120, v121, v122\n"
      "v_max_u32_e32 v56, v56, v123\n"
      "v_cmp_eq_u32_e64 s[60:61], -1, v56\n"
      "s_and_b64 s[48:49], s[48:49], s[94:95]\n"
      "s_and_b64 s[50:51], s[50:51], s[94:95]\n"
      "s_and_b64 s[52:53], s[52:53], s[94:95]\n"
      "s_and_b64 s[54:55], s[54:55], s[94:95]\n"
      "s_and_b64 s[56:57], s[56:57], s[94:95]\n"
      "s_and_b64 s[58:59], s[58:59], s[94:95]\n"
      "s_and_b64 s[60:61], s[60:61], s[94:95]\n"
      "s_mov_b64 s[88:89], s[48:49]\n"
      "s_or_b64 s[88:89], s[88:89], s[50:51]\n"
      "s_or_b64 s[88:89], s[88:89], s[52:53]\n"
      "s_or_b64 s[88:89], s[88:89], s[54:55]\n"
      "s_or_b64 s[88:89], s[88:89], s[56:57]\n"
      "s_or_b64 s[88:89], s[88:89], s[58:59]\n"
      "s_or_b64 s[88:89], s[88:89], s[60:61]\n"
      "s_cmp_eq_u64 s[88:89], 0\n"
      "s_nop 0\n"
      "s_cbranch_scc1 GD_157_%=\n"
      "s_add_u32 s43, s43, 1\n"
      "s_cmp_lt_u32 s43, 0x80000\n"
      "s_waitcnt vmcnt(7)\n"
      "s_nop 0\n"
      "s_cbranch_scc1 GT_156_%=\n"
      "s_nop 0\n"
      "GD_157_%=:\n"
      "v_mfma_f32_16x16x32_bf16 v[88:91], v[96:99], v[84:87], 0\n"
      "v_mfma_f32_16x16x32_bf16 v[88:91], v[100:103], v[84:87], v[88:91]\n"
      "v_mfma_f32_16x16x32_bf16 v[88:91], v[104:107], v[84:87], v[88:91]\n"
      "v_mfma_f32_16x16x32_bf16 v[88:91], v[108:111], v[84:87], v[88:91]\n"
      "v_mfma_f32_16x16x32_bf16 v[88:91], v[112:115], v[84:87], v[88:91]\n"
      "v_mfma_f32_16x16x32_bf16 v[88:91], v[116:119], v[84:87], v[88:91]\n"
      "v_mfma_f32_16x16x32_bf16 v[88:91], v[120:123], v[84:87], v[88:91]\n"
      "s_nop 7\n"
      "v_cndmask_b32_e64 v56, v88, v90, s[62:63]\n"
      "v_cndmask_b32_e64 v57, v89, v91, s[62:63]\n"
      "v_cvt_pk_bf16_f32 v58, v56, v57\n"
      "v_lshlrev_b32_e32 v56, 16, v58\n"
      "v_and_b32_e32 v57, 0xffff0000, v58\n"
      "v_add_f32_e32 v56, v140, v56\n"
      "v_add_f32_e32 v57, v141, v57\n"
      "v_cvt_pk_bf16_f32 v58, v56, v57\n"
      "s_lshr_b32 s88, s42, 1\n"
      "s_mul_i32 s88, s88, 896\n"
      "s_mov_b64 s[46:47], %[out]\n"
      "s_add_u32 s46, s46, s88\n"
      "s_addc_u32 s47, s47, 0\n"
      "s_lshl_b32 s88, s83, 2\n"
      "s_lshl_b64 exec, 0xff, s88\n"
      "global_store_short v92, v58, s[46:47]\n"
      "global_store_short_d16_hi v92, v58, s[46:47] offset:16\n"
      "s_mov_b64 exec, -1\n"
      "s_nop 0\n"
      "NOF_155_%=:\n"
      "s_cmp_eq_u32 s42, 3\n"
      "s_nop 0\n"
      "s_cbranch_scc1 CE_12_%=\n"
      "s_waitcnt vmcnt(0)\n"
      "s_nop 0\n"
      "CN_126_%=:\n"
      "v_mov_b32_e32 v56, v140\n"
      "v_mov_b32_e32 v140, v142\n"
      "v_mov_b32_e32 v142, v56\n"
      "v_mov_b32_e32 v56, v141\n"
      "v_mov_b32_e32 v141, v143\n"
      "v_mov_b32_e32 v143, v56\n"
      "s_lshl_b32 s88, s42, 2\n"
      "s_lshr_b32 s42, 0x0132, s88\n"
      "s_and_b32 s42, s42, 3\n"
      "s_nop 1\n"
      "s_branch CL_124_%=\n"
      "s_nop 0\n"
      "CE_12_%=:\n"
      "s_endpgm\n"
      "s_nop 0\n"
      "C_EXTRA_%=:\n"
      "s_waitcnt vmcnt(0)\n"
      "v_mbcnt_lo_u32_b32 v96, -1, 0\n"
      "v_mbcnt_hi_u32_b32 v96, -1, v96\n"
      "v_lshlrev_b32_e32 v97, 4, v96\n"
      "s_mov_b32 s88, 0x800\n"
      "s_mul_i32 s89, s83, 131072\n"
      "s_add_u32 s88, s88, s89\n"
      "s_mul_i32 s89, s100, 8192\n"
      "s_add_u32 s88, s88, s89\n"
      "s_add_u32 s44, s36, s88\n"
      "s_addc_u32 s45, s37, 0\n"
      "v_mbcnt_lo_u32_b32 v96, -1, 0\n"
      "v_mbcnt_hi_u32_b32 v96, -1, v96\n"
      "v_lshrrev_b32_e32 v104, 4, v96\n"
      "v_lshlrev_b32_e32 v104, 9, v104\n"
      "v_bfe_u32 v98, v96, 3, 1\n"
      "v_lshl_add_u32 v104, v98, 12, v104\n"
      "v_and_b32_e32 v98, 7, v96\n"
      "v_lshl_add_u32 v104, v98, 4, v104\n"
      "s_mov_b32 s43, 0\n"
      "s_nop 3\n"
      "XQ_166_%=:\n"
      "global_load_dwordx4 v[100:103], v104, s[44:45] sc1\n"
      "s_waitcnt vmcnt(0)\n"
      "v_cmp_ne_u32_e64 s[88:89], s35, v101\n"
      "v_cmp_ne_u32_e64 s[90:91], s35, v103\n"
      "s_or_b64 s[88:89], s[88:89], s[90:91]\n"
      "s_cmp_eq_u64 s[88:89], 0\n"
      "s_nop 2\n"
      "s_cbranch_scc1 XD_167_%=\n"
      "s_add_u32 s43, s43, 1\n"
      "s_cmp_lt_u32 s43, 0x80000\n"
      "s_nop 0\n"
      "s_cbranch_scc1 XQ_166_%=\n"
      "s_nop 0\n"
      "XD_167_%=:\n"
      "v_mbcnt_lo_u32_b32 v96, -1, 0\n"
      "v_mbcnt_hi_u32_b32 v96, -1, v96\n"
      "s_lshl_b32 s88, s38, 9\n"
      "s_add_u32 s88, s88, 0x1eb00\n"
      "v_mov_b32_e32 v101, v102\n"
      "v_lshlrev_b32_e32 v98, 3, v96\n"
      "v_add_u32_e32 v98, s88, v98\n"
      "ds_write_b64 v98, v[100:101]\n"
      "s_nop 3\n"
      "C_EXTRA2_%=:\n"
      "v_mbcnt_lo_u32_b32 v96, -1, 0\n"
      "v_mbcnt_hi_u32_b32 v96, -1, v96\n"
      "v_lshlrev_b32_e32 v97, 4, v96\n"
      "s_lshl_b32 s89, s38, 14\n"
      "s_add_u32 s89, s89, 0xe000\n"
      "v_add_u32_e32 v97, s89, v97\n"
      "s_waitcnt lgkmcnt(0)\n"
      "s_lshl_b32 s88, s38, 9\n"
      "v_mbcnt_lo_u32_b32 v99, -1, 0\n"
      "v_mbcnt_hi_u32_b32 v99, -1, v99\n"
      "v_lshrrev_b32_e32 v98, 4, v99\n"
      "v_lshlrev_b32_e32 v98, 6, v98\n"
      "v_and_b32_e32 v99, 3, v99\n"
      "v_lshl_add_u32 v98, v99, 4, v98\n"
      "v_add_u32_e32 v98, 0x1eb00, v98\n"
      "v_add_u32_e32 v98, s88, v98\n"
      "s_mov_b32 vcc_lo, 0xf000f\n"
      "s_mov_b32 vcc_hi, 0xf000f\n"
      "v_mov_b32_e32 v99, 0x20440\n"
      "v_cndmask_b32_e32 v99, v99, v98, vcc\n"
      "ds_read_b128 v[0:3], v99 offset:0\n"
      "ds_read_b128 v[4:7], v99 offset:256\n"
      "s_mov_b32 vcc_lo, 0xf000f0\n"
      "s_mov_b32 vcc_hi, 0xf000f0\n"
      "v_mov_b32_e32 v99, 0x20440\n"
      "v_cndmask_b32_e32 v99, v99, v98, vcc\n"
      "ds_read_b128 v[8:11], v99 offset:0\n"
      "ds_read_b128 v[12:15], v99 offset:256\n"
      "s_mov_b32 vcc_lo, 0xf000f00\n"
      "s_mov_b32 vcc_hi, 0xf000f00\n"
      "v_mov_b32_e32 v99, 0x20440\n"
      "v_cndmask_b32_e32 v99, v99, v98, vcc\n"
      "ds_read_b128 v[16:19], v99 offset:0\n"
      "ds_read_b128 v[20:23], v99 offset:256\n"
      "s_mov_b32 vcc_lo, 0xf000f000\n"
      "s_mov_b32 vcc_hi, 0xf000f000\n"
      "v_mov_b32_e32 v99, 0x20440\n"
      "v_cndmask_b32_e32 v99, v99, v98, vcc\n"
      "ds_read_b128 v[24:27], v99 offset:0\n"
      "ds_read_b128 v[28:31], v99 offset:256\n"
      "s_waitcnt lgkmcnt(0)\n"
      "ds_read_b128 v[64:67], v97 offset:0\n"
      "ds_read_b128 v[68:71], v97 offset:1024\n"
      "ds_read_b128 v[72:75], v97 offset:2048\n"
      "ds_read_b128 v[76:79], v97 offset:3072\n"
      "ds_read_b128 v[80:83], v97 offset:4096\n"
      "ds_read_b128 v[84:87], v97 offset:5120\n"
      "ds_read_b128 v[88:91], v97 offset:6144\n"
      "ds_read_b128 v[92:95], v97 offset:7168\n"
      "s_mov_b32 s86, 0xcccccccc\n"
      "s_mov_b32 s87, 0xcccccccc\n"
      "s_mov_b32 s84, 0xf0f0f0f0\n"
      "s_mov_b32 s85, 0xf0f0f0f0\n"
      "v_and_b32_e32 v99, 1, v96\n"
      "v_cmp_eq_u32_e32 vcc, 1, v99\n"
      "v_mov_b32_e32 v63, 0xffff\n"
      "v_mov_b32_e32 v99, 0xffff0000\n"
      "v_cndmask_b32_e32 v63, v63, v99, vcc\n"
      "v_mov_b32_e32 v32, 0\n"
      "v_mov_b32_e32 v33, 0\n"
      "v_mov_b32_e32 v34, 0\n"
      "v_mov_b32_e32 v35, 0\n"
      "v_mov_b32_e32 v36, 0\n"
      "v_mov_b32_e32 v37, 0\n"
      "v_mov_b32_e32 v38, 0\n"
      "v_mov_b32_e32 v39, 0\n"
      "v_mov_b32_e32 v40, 0\n"
      "v_mov_b32_e32 v41, 0\n"
      "v_mov_b32_e32 v42, 0\n"
      "v_mov_b32_e32 v43, 0\n"
      "v_mov_b32_e32 v44, 0\n"
      "v_mov_b32_e32 v45, 0\n"
      "v_mov_b32_e32 v46, 0\n"
      "v_mov_b32_e32 v47, 0\n"
      "v_mov_b32_e32 v62, 0\n"
      "s_mov_b32 s74, 0xff800000\n"
      "s_waitcnt lgkmcnt(0)\n"
      "v_mfma_f32_16x16x32_bf16 v[48:51], v[64:67], v[0:3], 0\n"
      "v_mfma_f32_16x16x32_bf16 v[48:51], v[68:71], v[4:7], v[48:51]\n"
      "v_mfma_f32_16x16x32_bf16 v[48:51], v[72:75], v[8:11], v[48:51]\n"
      "v_mfma_f32_16x16x32_bf16 v[48:51], v[76:79], v[12:15], v[48:51]\n"
      "v_mfma_f32_16x16x32_bf16 v[48:51], v[80:83], v[16:19], v[48:51]\n"
      "v_mfma_f32_16x16x32_bf16 v[48:51], v[84:87], v[20:23], v[48:51]\n"
      "v_mfma_f32_16x16x32_bf16 v[48:51], v[88:91], v[24:27], v[48:51]\n"
      "v_mfma_f32_16x16x32_bf16 v[48:51], v[92:95], v[28:31], v[48:51]\n"
      "ds_read_b128 v[64:67], v97 offset:8192\n"
      "ds_read_b128 v[68:71], v97 offset:9216\n"
      "ds_read_b128 v[72:75], v97 offset:10240\n"
      "ds_read_b128 v[76:79], v97 offset:11264\n"
      "ds_read_b128 v[80:83], v97 offset:12288\n"
      "ds_read_b128 v[84:87], v97 offset:13312\n"
      "ds_read_b128 v[88:91], v97 offset:14336\n"
      "ds_read_b128 v[92:95], v97 offset:15360\n"
      "v_cndmask_b32_e64 v53, v48, v49, s[96:97]\n"
      "v_cndmask_b32_e64 v54, v50, v51, s[96:97]\n"
      "v_cndmask_b32_e64 v52, v53, v54, s[86:87]\n"
      "s_nop 1\n"
      "v_add_f32_dpp v52, v52, v52 quad_perm:[1,0,3,2] row_mask:0xf bank_mask:0xf\n"
      "s_nop 1\n"
      "v_add_f32_dpp v52, v52, v52 quad_perm:[2,3,0,1] row_mask:0xf bank_mask:0xf\n"
      "v_cmp_lt_f32_e32 vcc, s74, v52\n"
      "s_cbranch_vccz NORS_168_%=\n"
      "s_swappc_b64 s[76:77], s[78:79]\n"
      "NORS_168_%=:\n"
      "v_subrev_f32_e32 v52, s74, v52\n"
      "v_mul_f32_e32 v52, 0x3db8aa3b, v52\n"
      "v_exp_f32_e32 v52, v52\n"
      "s_nop 0\n"
      "v_cvt_pk_bf16_f32 v53, v52, v52\n"
      "v_add_f32_e32 v62, v62, v52\n"
      "v_and_b32_e32 v53, v53, v63\n"
      "s_nop 0\n"
      "v_add_f32_dpp v62, v52, v62 row_half_mirror row_mask:0xf bank_mask:0xf\n"
      "v_mov_b32_dpp v56, v52 row_half_mirror row_mask:0xf bank_mask:0xf\n"
      "v_cvt_pk_bf16_f32 v56, v56, v56\n"
      "v_and_b32_e32 v56, v56, v63\n"
      "v_cndmask_b32_e64 v55, v53, v56, s[84:85]\n"
      "v_cndmask_b32_e64 v59, v56, v53, s[84:85]\n"
      "v_cndmask_b32_e64 v52, 0, v55, s[64:65]\n"
      "v_cndmask_b32_e64 v53, 0, v55, s[66:67]\n"
      "v_cndmask_b32_e64 v54, 0, v55, s[68:69]\n"
      "v_cndmask_b32_e64 v55, 0, v55, s[70:71]\n"
      "v_cndmask_b32_e64 v56, 0, v59, s[64:65]\n"
      "v_cndmask_b32_e64 v57, 0, v59, s[66:67]\n"
      "v_cndmask_b32_e64 v58, 0, v59, s[68:69]\n"
      "v_cndmask_b32_e64 v59, 0, v59, s[70:71]\n"
      "s_waitcnt lgkmcnt(0)\n"
      "v_mfma_f32_16x16x32_bf16 v[32:35], v[64:67], v[52:55], v[32:35]\n"
      "v_mfma_f32_16x16x32_bf16 v[36:39], v[68:71], v[52:55], v[36:39]\n"
      "v_mfma_f32_16x16x32_bf16 v[40:43], v[72:75], v[52:55], v[40:43]\n"
      "v_mfma_f32_16x16x32_bf16 v[44:47], v[76:79], v[52:55], v[44:47]\n"
      "v_mfma_f32_16x16x32_bf16 v[32:35], v[80:83], v[56:59], v[32:35]\n"
      "v_mfma_f32_16x16x32_bf16 v[36:39], v[84:87], v[56:59], v[36:39]\n"
      "v_mfma_f32_16x16x32_bf16 v[40:43], v[88:91], v[56:59], v[40:43]\n"
      "v_mfma_f32_16x16x32_bf16 v[44:47], v[92:95], v[56:59], v[44:47]\n"
      "s_mov_b32 s88, 0xb4000\n"
      "s_mul_i32 s89, s83, 262144\n"
      "s_add_u32 s88, s88, s89\n"
      "s_mul_i32 s89, s100, 16384\n"
      "s_add_u32 s88, s88, s89\n"
      "s_mul_i32 s89, s101, 1024\n"
      "s_add_u32 s88, s88, s89\n"
      "s_add_u32 s84, s36, s88\n"
      "s_addc_u32 s85, s37, 0\n"
      "s_mov_b32 s88, 0x80858\n"
      "s_mul_i32 s89, s83, 2048\n"
      "s_add_u32 s88, s88, s89\n"
      "s_mul_i32 s89, s100, 128\n"
      "s_add_u32 s88, s88, s89\n"
      "s_mul_i32 s89, s101, 32\n"
      "s_add_u32 s88, s88, s89\n"
      "s_add_u32 s86, s36, s88\n"
      "s_addc_u32 s87, s37, 0\n"
      "s_mov_b32 s75, 0x1f300\n"
      "s_lshl_b32 s82, s83, 2\n"
      "s_add_u32 s82, s82, 0x20400\n"
      "s_nop 0\n"
      "s_swappc_b64 s[76:77], s[80:81]\n"
      "s_setpc_b64 s[98:99]\n"
      "s_nop 0\n"
      "C_RESC_%=:\n"
      "s_nop 1\n"
      "v_max_f32_dpp v54, v52, v52 row_ror:4 row_mask:0xf bank_mask:0xf\n"
      "s_nop 1\n"
      "v_max_f32_dpp v54, v54, v54 row_ror:8 row_mask:0xf bank_mask:0xf\n"
      "s_nop 1\n"
      "v_readlane_b32 s88, v54, 0\n"
      "v_readlane_b32 s89, v54, 16\n"
      "v_readlane_b32 s90, v54, 32\n"
      "v_readlane_b32 s91, v54, 48\n"
      "v_mov_b32_e32 v53, s88\n"
      "v_max_f32_e32 v53, s89, v53\n"
      "v_max_f32_e32 v53, s90, v53\n"
      "v_max_f32_e32 v53, s91, v53\n"
      "v_sub_f32_e32 v54, s74, v53\n"
      "v_mul_f32_e32 v54, 0x3db8aa3b, v54\n"
      "v_exp_f32_e32 v54, v54\n"
      "v_readfirstlane_b32 s74, v53\n"
      "v_mul_f32_e32 v62, v62, v54\n"
      "v_mul_f32_e32 v32, v32, v54\n"
      "v_mul_f32_e32 v33, v33, v54\n"
      "v_mul_f32_e32 v34, v34, v54\n"
      "v_mul_f32_e32 v35, v35, v54\n"
      "v_mul_f32_e32 v36, v36, v54\n"
      "v_mul_f32_e32 v37, v37, v54\n"
      "v_mul_f32_e32 v38, v38, v54\n"
      "v_mul_f32_e32 v39, v39, v54\n"
      "v_mul_f32_e32 v40, v40, v54\n"
      "v_mul_f32_e32 v41, v41, v54\n"
      "v_mul_f32_e32 v42, v42, v54\n"
      "v_mul_f32_e32 v43, v43, v54\n"
      "v_mul_f32_e32 v44, v44, v54\n"
      "v_mul_f32_e32 v45, v45, v54\n"
      "v_mul_f32_e32 v46, v46, v54\n"
      "v_mul_f32_e32 v47, v47, v54\n"
      "s_nop 4\n"
      "s_setpc_b64 s[76:77]\n"
      "s_nop 0\n"
      "C_FIN_%=:\n"
      "v_mbcnt_lo_u32_b32 v0, -1, 0\n"
      "v_mbcnt_hi_u32_b32 v0, -1, v0\n"
      "v_add_f32_dpp v62, v62, v62 row_ror:8 row_mask:0xf bank_mask:0xf\n"
      "v_permlane32_swap_b32_e32 v32, v40\n"
      "v_permlane32_swap_b32_e32 v33, v41\n"
      "v_permlane32_swap_b32_e32 v34, v42\n"
      "v_permlane32_swap_b32_e32 v35, v43\n"
      "v_permlane32_swap_b32_e32 v36, v44\n"
      "v_permlane32_swap_b32_e32 v37, v45\n"
      "v_permlane32_swap_b32_e32 v38, v46\n"
      "v_permlane32_swap_b32_e32 v39, v47\n"
      "v_add_f32_dpp v32, v40, v32 row_ror:8 row_mask:0xf bank_mask:0xf\n"
      "v_add_f32_dpp v33, v41, v33 row_ror:8 row_mask:0xf bank_mask:0xf\n"
      "v_add_f32_dpp v34, v42, v34 row_ror:8 row_mask:0xf bank_mask:0xf\n"
      "v_add_f32_dpp v35, v43, v35 row_ror:8 row_mask:0xf bank_mask:0xf\n"
      "v_add_f32_dpp v36, v44, v36 row_ror:8 row_mask:0xf bank_mask:0xf\n"
      "v_add_f32_dpp v37, v45, v37 row_ror:8 row_mask:0xf bank_mask:0xf\n"
      "v_add_f32_dpp v38, v46, v38 row_ror:8 row_mask:0xf bank_mask:0xf\n"
      "v_add_f32_dpp v39, v47, v39 row_ror:8 row_mask:0xf bank_mask:0xf\n"
      "v_and_b32_e32 v1, 32, v0\n"
      "v_lshlrev_b32_e32 v1, 4, v1\n"
      "v_and_b32_e32 v2, 16, v0\n"
      "v_lshl_add_u32 v1, v2, 3, v1\n"
      "v_and_b32_e32 v2, 7, v0\n"
      "v_lshl_add_u32 v1, v2, 4, v1\n"
      "s_lshl_b32 s88, s38, 10\n"
      "s_add_u32 s88, s88, s75\n"
      "v_add_u32_e32 v1, s88, v1\n"
      "s_mov_b64 exec, s[72:73]\n"
      "ds_write_b128 v1, v[32:35]\n"
      "ds_write_b128 v1, v[36:39] offset:256\n"
      "s_mov_b32 exec_lo, 0x00010001\n"
      "s_mov_b32 exec_hi, 0x00010001\n"
      "v_lshrrev_b32_e32 v1, 2, v0\n"
      "s_lshl_b32 s88, s38, 4\n"
      "s_add_u32 s88, s88, s75\n"
      "v_add_u32_e32 v1, s88, v1\n"
      "ds_write_b32 v1, v62 offset:4096\n"
      "s_mov_b64 exec, 1\n"
      "s_lshl_b32 s88, s38, 2\n"
      "s_add_u32 s88, s88, s75\n"
      "v_mov_b32_e32 v1, s88\n"
      "v_mov_b32_e32 v2, s74\n"
      "ds_write_b32 v1, v2 offset:4160\n"
      "v_mov_b32_e32 v1, s82\n"
      "v_mov_b32_e32 v2, 1\n"
      "ds_add_rtn_u32 v3, v1, v2\n"
      "s_waitcnt lgkmcnt(0)\n"
      "v_readfirstlane_b32 s88, v3\n"
      "s_mov_b64 exec, -1\n"
      "s_cmp_lg_u32 s88, 3\n"
      "s_cbranch_scc1 FIND_169_%=\n"
      "v_mov_b32_e32 v1, s75\n"
      "ds_read_b128 v[4:7], v1 offset:4160\n"
      "ds_read_b128 v[8:11], v1 offset:4096\n"
      "ds_read_b128 v[12:15], v1 offset:4112\n"
      "ds_read_b128 v[16:19], v1 offset:4128\n"
      "ds_read_b128 v[20:23], v1 offset:4144\n"
      "v_lshlrev_b32_e32 v24, 4, v0\n"
      "v_add_u32_e32 v1, s75, v24\n"
      "ds_read_b128 v[32:35], v1 offset:0\n"
      "ds_read_b128 v[36:39], v1 offset:1024\n"
      "ds_read_b128 v[40:43], v1 offset:2048\n"
      "ds_read_b128 v[44:47], v1 offset:3072\n"
      "s_waitcnt lgkmcnt(4)\n"
      "v_max3_f32 v25, v4, v5, v6\n"
      "v_max_f32_e32 v25, v25, v7\n"
      "v_sub_f32_e32 v4, v4, v25\n"
      "v_sub_f32_e32 v5, v5, v25\n"
      "v_sub_f32_e32 v6, v6, v25\n"
      "v_sub_f32_e32 v7, v7, v25\n"
      "v_mul_f32_e32 v4, 0x3db8aa3b, v4\n"
      "v_mul_f32_e32 v5, 0x3db8aa3b, v5\n"
      "v_mul_f32_e32 v6, 0x3db8aa3b, v6\n"
      "v_mul_f32_e32 v7, 0x3db8aa3b, v7\n"
      "v_exp_f32_e32 v4, v4\n"
      "v_exp_f32_e32 v5, v5\n"
      "v_exp_f32_e32 v6, v6\n"
      "v_exp_f32_e32 v7, v7\n"
      "v_add_f32_e32 v8, v8, v9\n"
      "v_add_f32_e32 v10, v10, v11\n"
      "v_add_f32_e32 v8, v8, v10\n"
      "v_add_f32_e32 v12, v12, v13\n"
      "v_add_f32_e32 v14, v14, v15\n"
      "v_add_f32_e32 v12, v12, v14\n"
      "v_add_f32_e32 v16, v16, v17\n"
      "v_add_f32_e32 v18, v18, v19\n"
      "v_add_f32_e32 v16, v16, v18\n"
      "v_add_f32_e32 v20, v20, v21\n"
      "v_add_f32_e32 v22, v22, v23\n"
      "v_add_f32_e32 v20, v20, v22\n"
      "v_mul_f32_e32 v26, v4, v8\n"
      "v_fmac_f32_e32 v26, v5, v12\n"
      "v_fmac_f32_e32 v26, v6, v16\n"
      "v_fmac_f32_e32 v26, v7, v20\n"
      "s_waitcnt lgkmcnt(0)\n"
      "v_mul_f32_e32 v28, v4, v32\n"
      "v_mul_f32_e32 v29, v4, v33\n"
      "v_mul_f32_e32 v30, v4, v34\n"
      "v_mul_f32_e32 v31, v4, v35\n"
      "v_fmac_f32_e32 v28, v5, v36\n"
      "v_fmac_f32_e32 v29, v5, v37\n"
      "v_fmac_f32_e32 v30, v5, v38\n"
      "v_fmac_f32_e32 v31, v5, v39\n"
      "v_fmac_f32_e32 v28, v6, v40\n"
      "v_fmac_f32_e32 v29, v6, v41\n"
      "v_fmac_f32_e32 v30, v6, v42\n"
      "v_fmac_f32_e32 v31, v6, v43\n"
      "v_fmac_f32_e32 v28, v7, v44\n"
      "v_fmac_f32_e32 v29, v7, v45\n"
      "v_fmac_f32_e32 v30, v7, v46\n"
      "v_fmac_f32_e32 v31, v7, v47\n"
      "v_min_u32_e32 v28, -2, v28\n"
      "v_min_u32_e32 v29, -2, v29\n"
      "v_min_u32_e32 v30, -2, v30\n"
      "v_min_u32_e32 v31, -2, v31\n"
      "v_min_u32_e32 v25, -2, v25\n"
      "v_min_u32_e32 v26, -2, v26\n"
      "global_store_dwordx4 v24, v[28:31], s[84:85] sc1\n"
      "v_mov_b32_e32 v1, 0\n"
      "v_mov_b32_e32 v6, v25\n"
      "v_mov_b32_e32 v7, v26\n"
      "s_mov_b64 exec, 1\n"
      "global_store_dwordx2 v1, v[6:7], s[86:87] sc1\n"
      "s_mov_b64 exec, -1\n"
      "s_nop 1\n"
      "FIND_169_%=:\n"
      "s_setpc_b64 s[76:77]\n"
      :
      : [ws] "s"(ws), [wid] "s"(w), [cu] "s"(cu), [x] "s"(x), [kc] "s"(kc), [vc] "s"(vc), [wq] "s"(wq), [wo] "s"(wo), [wg] "s"(wg), [wu] "s"(wu), [wd] "s"(wd), [out] "s"(out)
      : "v255", "a255", "s34", "s35", "s36", "s37", "s38", "s39", "s40", "s41", "s42", "s43", "s44", "s45", "s46", "s47", "s48", "s49", "s50", "s51", "s52", "s53", "s54", "s55", "s56", "s57", "s58", "s59", "s60", "s61", "s62", "s63", "s64", "s65", "s66", "s67", "s68", "s69", "s70", "s71", "s72", "s73", "s74", "s75", "s76", "s77", "s78", "s79", "s80", "s81", "s82", "s83", "s84", "s85", "s86", "s87", "s88", "s89", "s90", "s91", "s92", "s93", "s94", "s95", "s96", "s97", "s98", "s99", "s100", "s101", "vcc", "scc", "memory");
}

__device__ __forceinline__ void attn_role(int h, int s, const u16* __restrict__ kc, const u16* __restrict__ vc, char* ws, const u16* __restrict__ x, const u16* __restrict__ wq, int xcd, int i) {
  const int w = __builtin_amdgcn_readfirstlane(threadIdx.x >> 6);
  const int sl = w == 0 ? xcd * 28 + i : ((w == 1 && i < 4) ? 224 + xcd * 4 + i : -1);
  const int bo = ((4 * s + w + 112 * h) % 504) * 8192;
  const unsigned long long kb = (unsigned long long)kc + (unsigned long long)h * (8192ull * 512ull);
  const unsigned long long vb = (unsigned long long)vc + (unsigned long long)h * (8192ull * 512ull);
  const unsigned long long wsp = (unsigned long long)ws;
  asm volatile(
      "s_memrealtime s[86:87]\n"
      "s_mov_b32 s94, 1650\n"
      "s_waitcnt lgkmcnt(0)\n"
      "s_lshl_b32 s86, s86, 4\n"
      "s_sub_u32 s93, s86, 0\n"
      "s_mov_b64 s[36:37], %[kb]\n"
      "s_mov_b64 s[38:39], %[vb]\n"
      "s_mov_b64 s[40:41], %[ws]\n"
      "s_mov_b32 s48, %[wid]\n"
      "s_mov_b32 s100, %[bo]\n"
      "s_lshl_b32 s80, %[hd], 13\n"
      "s_add_u32 s80, s80, 0x800\n"
      "s_add_u32 s42, s40, s80\n"
      "s_addc_u32 s43, s41, 0\n"
      "s_lshl_b32 s80, %[hd], 4\n"
      "s_add_u32 s80, s80, %[sp]\n"
      "s_lshl_b32 s80, s80, 10\n"
      "s_add_u32 s80, s80, 0xb0800\n"
      "s_add_u32 s44, s40, s80\n"
      "s_addc_u32 s45, s41, 0\n"
      "s_and_b32 s80, %[sp], 3\n"
      "s_lshl_b32 s80, s80, 5\n"
      "s_lshr_b32 s81, %[sp], 2\n"
      "s_lshl_b32 s81, s81, 3\n"
      "s_add_u32 s80, s80, s81\n"
      "s_lshl_b32 s81, %[hd], 7\n"
      "s_add_u32 s80, s80, s81\n"
      "s_add_u32 s80, s80, 0x80800\n"
      "s_add_u32 s46, s40, s80\n"
      "s_addc_u32 s47, s41, 0\n"
      "s_lshl_b32 s92, s48, 6\n"
      "s_add_u32 s92, s92, 0x27e40\n"
      "v_mbcnt_lo_u32_b32 v0, -1, 0\n"
      "v_mbcnt_hi_u32_b32 v0, -1, v0\n"
      "v_lshlrev_b32_e32 v1, 4, v0\n"
      "v_mov_b32_e32 v4, -1\n"
      "v_mov_b32_e32 v5, -1\n"
      "v_mov_b32_e32 v6, -1\n"
      "v_mov_b32_e32 v7, -1\n"
      "s_lshl_b32 s80, s48, 18\n"
      "s_add_u32 s64, s44, s80\n"
      "s_addc_u32 s65, s45, 0\n"
      "global_store_dwordx4 v1, v[4:7], s[64:65] sc1\n"
      "s_lshl_b32 s80, s48, 11\n"
      "s_add_u32 s64, s46, s80\n"
      "s_addc_u32 s65, s47, 0\n"
      "v_mov_b32_e32 v2, 0\n"
      "s_mov_b64 exec, 1\n"
      "global_store_dwordx2 v2, v[4:5], s[64:65] sc1\n"
      "global_load_dword v3, v2, s[40:41] sc1\n"
      "s_mov_b64 exec, -1\n"
      "v_mbcnt_lo_u32_b32 v48, -1, 0\n"
      "v_mbcnt_hi_u32_b32 v48, -1, v48\n"
      "v_bfe_u32 v49, v48, 2, 2\n"
      "v_lshlrev_b32_e32 v60, 9, v49\n"
      "v_lshrrev_b32_e32 v49, 4, v48\n"
      "v_lshl_add_u32 v60, v49, 6, v60\n"
      "v_lshlrev_b32_e32 v61, 9, v49\n"
      "v_and_b32_e32 v50, 3, v48\n"
      "v_lshl_add_u32 v60, v50, 4, v60\n"
      "v_add_u32_e32 v60, 0x1000, v60\n"
      "v_bfe_u32 v50, v48, 3, 1\n"
      "v_lshl_add_u32 v61, v50, 12, v61\n"
      "v_and_b32_e32 v50, 7, v48\n"
      "v_lshl_add_u32 v61, v50, 4, v61\n"
      "s_mov_b32 s95, %[sl]\n"
      "v_mov_b32_e32 v24, 0\n"
      "v_mov_b32_e32 v25, 0\n"
      "v_mov_b32_e32 v26, 0\n"
      "v_mov_b32_e32 v27, 0\n"
      "v_mov_b32_e32 v28, 0\n"
      "v_mov_b32_e32 v29, 0\n"
      "v_mov_b32_e32 v30, 0\n"
      "v_mov_b32_e32 v31, 0\n"
      "s_cmp_lt_i32 s95, 0\n"
      "s_nop 3\n"
      "s_cbranch_scc1 Q1S_1_%=\n"
      "v_mbcnt_lo_u32_b32 v0, -1, 0\n"
      "v_mbcnt_hi_u32_b32 v0, -1, v0\n"
      "v_lshlrev_b32_e32 v1, 4, v0\n"
      "s_mov_b32 exec_lo, -1\n"
      "s_mov_b32 exec_hi, 0x00ffffff\n"
      "global_load_dwordx4 v[24:27], v1, %[x]\n"
      "global_load_dwordx4 v[28:31], v1, %[x] offset:896\n"
      "s_mov_b64 exec, -1\n"
      "v_lshrrev_b32_e32 v1, 1, v0\n"
      "v_lshlrev_b32_e32 v1, 13, v1\n"
      "v_and_b32_e32 v2, 1, v0\n"
      "v_lshl_add_u32 v1, v2, 4, v1\n"
      "s_mov_b64 s[64:65], %[wq]\n"
      "s_lshl_b32 s80, s95, 5\n"
      "s_add_u32 s64, s64, s80\n"
      "s_addc_u32 s65, s65, 0\n"
      "s_lshl_b32 m0, s48, 15\n"
      "s_nop 0\n"
      "global_load_lds_dwordx4 v1, s[64:65]\n"
      "s_add_u32 s64, s64, 0x40000\n"
      "s_addc_u32 s65, s65, 0\n"
      "s_add_u32 m0, m0, 0x400\n"
      "s_nop 0\n"
      "global_load_lds_dwordx4 v1, s[64:65]\n"
      "s_add_u32 s64, s64, 0x40000\n"
      "s_addc_u32 s65, s65, 0\n"
      "s_add_u32 m0, m0, 0x400\n"
      "s_nop 0\n"
      "global_load_lds_dwordx4 v1, s[64:65]\n"
      "s_add_u32 s64, s64, 0x40000\n"
      "s_addc_u32 s65, s65, 0\n"
      "s_add_u32 m0, m0, 0x400\n"
      "s_nop 0\n"
      "global_load_lds_dwordx4 v1, s[64:65]\n"
      "s_add_u32 s64, s64, 0x40000\n"
      "s_addc_u32 s65, s65, 0\n"
      "s_add_u32 m0, m0, 0x400\n"
      "s_nop 0\n"
      "global_load_lds_dwordx4 v1, s[64:65]\n"
      "s_add_u32 s64, s64, 0x40000\n"
      "s_addc_u32 s65, s65, 0\n"
      "s_add_u32 m0, m0, 0x400\n"
      "s_nop 0\n"
      "global_load_lds_dwordx4 v1, s[64:65]\n"
      "s_add_u32 s64, s64, 0x40000\n"
      "s_addc_u32 s65, s65, 0\n"
      "s_add_u32 m0, m0, 0x400\n"
      "s_nop 0\n"
      "global_load_lds_dwordx4 v1, s[64:65]\n"
      "s_add_u32 s64, s64, 0x40000\n"
      "s_addc_u32 s65, s65, 0\n"
      "s_add_u32 m0, m0, 0x400\n"
      "s_nop 0\n"
      "global_load_lds_dwordx4 v1, s[64:65]\n"
      "s_add_u32 s64, s64, 0x40000\n"
      "s_addc_u32 s65, s65, 0\n"
      "s_add_u32 m0, m0, 0x400\n"
      "s_nop 0\n"
      "global_load_lds_dwordx4 v1, s[64:65]\n"
      "s_add_u32 s64, s64, 0x40000\n"
      "s_addc_u32 s65, s65, 0\n"
      "s_add_u32 m0, m0, 0x400\n"
      "s_nop 0\n"
      "global_load_lds_dwordx4 v1, s[64:65]\n"
      "s_add_u32 s64, s64, 0x40000\n"
      "s_addc_u32 s65, s65, 0\n"
      "s_add_u32 m0, m0, 0x400\n"
      "s_nop 0\n"
      "global_load_lds_dwordx4 v1, s[64:65]\n"
      "s_add_u32 s64, s64, 0x40000\n"
      "s_addc_u32 s65, s65, 0\n"
      "s_add_u32 m0, m0, 0x400\n"
      "s_nop 0\n"
      "global_load_lds_dwordx4 v1, s[64:65]\n"
      "s_add_u32 s64, s64, 0x40000\n"
      "s_addc_u32 s65, s65, 0\n"
      "s_add_u32 m0, m0, 0x400\n"
      "s_nop 0\n"
      "global_load_lds_dwordx4 v1, s[64:65]\n"
      "s_add_u32 s64, s64, 0x40000\n"
      "s_addc_u32 s65, s65, 0\n"
      "s_add_u32 m0, m0, 0x400\n"
      "s_nop 0\n"
      "global_load_lds_dwordx4 v1, s[64:65]\n"
      "Q1S_1_%=:\n"
      "TG_2_%=:\n"
      "s_memrealtime s[86:87]\n"
      "s_waitcnt lgkmcnt(0)\n"
      "s_lshl_b32 s86, s86, 4\n"
      "s_sub_u32 s86, s86, s93\n"
      "s_cmp_ge_i32 s86, 0\n"
      "s_cbranch_scc1 TGO_3_%=\n"
      "s_sleep 1\n"
      "s_branch TG_2_%=\n"
      "TGO_3_%=:\n"
      "s_add_u32 s93, s93, 0\n"
      "s_add_u32 s86, s100, 0x0\n"
      "s_cmp_ge_u32 s86, 0x3f0000\n"
      "s_cselect_b32 s87, 0x3f0000, 0\n"
      "s_sub_u32 s86, s86, s87\n"
      "s_add_u32 s90, s36, s86\n"
      "s_addc_u32 s91, s37, 0\n"
      "global_load_dwordx4 v[64:67], v60, s[90:91] offset:-4096 nt\n"
      "global_load_dwordx4 v[68:71], v60, s[90:91] offset:-3840 nt\n"
      "global_load_dwordx4 v[72:75], v60, s[90:91] offset:-2048 nt\n"
      "global_load_dwordx4 v[76:79], v60, s[90:91] offset:-1792 nt\n"
      "global_load_dwordx4 v[80:83], v60, s[90:91] offset:0 nt\n"
      "global_load_dwordx4 v[84:87], v60, s[90:91] offset:256 nt\n"
      "global_load_dwordx4 v[88:91], v60, s[90:91] offset:2048 nt\n"
      "v_mbcnt_lo_u32_b32 v48, -1, 0\n"
      "v_mbcnt_hi_u32_b32 v48, -1, v48\n"
      "v_and_b32_e32 v49, 1, v48\n"
      "v_cmp_eq_u32_e32 vcc, 1, v49\n"
      "v_mov_b32_e32 v63, 0xffff\n"
      "v_mov_b32_e32 v49, 0xffff0000\n"
      "v_cndmask_b32_e32 v63, v63, v49, vcc\n"
      "s_mov_b32 s52, 0x3030303\n"
      "s_mov_b32 s53, 0x3030303\n"
      "s_mov_b32 s54, 0xc0c0c0c\n"
      "s_mov_b32 s55, 0xc0c0c0c\n"
      "s_mov_b32 s56, 0x30303030\n"
      "s_mov_b32 s57, 0x30303030\n"
      "s_mov_b32 s58, 0xc0c0c0c0\n"
      "s_mov_b32 s59, 0xc0c0c0c0\n"
      "s_mov_b32 s50, 0xff00ff00\n"
      "s_mov_b32 s51, 0xff00ff00\n"
      "s_mov_b32 s96, 0xaaaaaaaa\n"
      "s_mov_b32 s97, 0xaaaaaaaa\n"
      "s_mov_b32 s98, 0xcccccccc\n"
      "s_mov_b32 s99, 0xcccccccc\n"
      "s_mov_b32 s50, 0xf0f0f0f0\n"
      "s_mov_b32 s51, 0xf0f0f0f0\n"
      "s_mov_b32 s88, 0x00ff00ff\n"
      "s_mov_b32 s89, 0x00ff00ff\n"
      "s_mov_b32 s34, 0x3db8aa3b\n"
      "s_getpc_b64 s[68:69]\n"
      "PC_4_%=:\n"
      "s_add_u32 s68, s68, A_RESC_%=-PC_4_%=\n"
      "s_addc_u32 s69, s69, 0\n"
      "s_getpc_b64 s[70:71]\n"
      "PC_5_%=:\n"
      "s_add_u32 s70, s70, A_FIN_%=-PC_5_%=\n"
      "s_addc_u32 s71, s71, 0\n"
      "s_lshl_b32 s49, s48, 15\n"
      "v_mbcnt_lo_u32_b32 v8, -1, 0\n"
      "v_mbcnt_hi_u32_b32 v8, -1, v8\n"
      "v_lshlrev_b32_e32 v10, 4, v8\n"
      "s_lshl_b32 s80, s48, 10\n"
      "s_add_u32 s80, s80, 0x26e40\n"
      "v_add_u32_e32 v10, s80, v10\n"
      "v_lshlrev_b32_e32 v8, 3, v8\n"
      "v_add_u32_e32 v8, 0x260a0, v8\n"
      "v_mov_b32_e32 v16, 0\n"
      "v_mov_b32_e32 v17, 0\n"
      "s_mov_b32 exec_lo, -1\n"
      "s_mov_b32 exec_hi, 0xfffff\n"
      "ds_write_b64 v8, v[16:17]\n"
      "s_mov_b64 exec, -1\n"
      "s_waitcnt vmcnt(7)\n"
      "v_readfirstlane_b32 s62, v3\n"
      "s_add_u32 s62, s62, 1\n"
      "s_nop 0\n"
      "v_not_b32_e32 v12, s62\n"
      "v_not_b32_e32 v13, s62\n"
      "v_not_b32_e32 v14, s62\n"
      "v_not_b32_e32 v15, s62\n"
      "ds_write_b128 v10, v[12:15]\n"
      "s_waitcnt lgkmcnt(0)\n"
      "s_barrier\n"
      "s_mul_i32 s80, %[hd], 14\n"
      "s_add_u32 s80, s80, %[sp]\n"
      "s_lshl_b32 s80, s80, 2\n"
      "s_add_u32 s80, s80, 0x180\n"
      "v_mov_b32_e32 v8, s80\n"
      "v_mov_b32_e32 v9, s62\n"
      "s_mov_b64 exec, 1\n"
      "global_store_dword v8, v9, s[40:41] sc1\n"
      "s_mov_b64 exec, -1\n"
      "global_load_dwordx4 v[92:95], v60, s[90:91] offset:2304 nt\n"
      "TG_6_%=:\n"
      "s_memrealtime s[86:87]\n"
      "s_waitcnt lgkmcnt(0)\n"
      "s_lshl_b32 s86, s86, 4\n"
      "s_sub_u32 s86, s86, s93\n"
      "s_cmp_ge_i32 s86, 0\n"
      "s_cbranch_scc1 TGO_7_%=\n"
      "s_sleep 1\n"
      "s_branch TG_6_%=\n"
      "TGO_7_%=:\n"
      "s_add_u32 s93, s93, 2400\n"
      "s_add_u32 s86, s100, 0x0\n"
      "s_cmp_ge_u32 s86, 0x3f0000\n"
      "s_cselect_b32 s87, 0x3f0000, 0\n"
      "s_sub_u32 s86, s86, s87\n"
      "s_add_u32 s90, s38, s86\n"
      "s_addc_u32 s91, s39, 0\n"
      "global_load_dwordx4 v[96:99], v61, s[90:91] offset:0 nt\n"
      "s_cmp_lt_i32 s95, 0\n"
      "s_nop 0\n"
      "s_cbranch_scc1 Q1C_8_%=\n"
      "v_mbcnt_lo_u32_b32 v16, -1, 0\n"
      "v_mbcnt_hi_u32_b32 v16, -1, v16\n"
      "v_lshlrev_b32_e32 v17, 4, v16\n"
      "v_add_u32_e32 v17, s49, v17\n"
      "v_mov_b32_e32 v52, 0x3f803f80\n"
      "v_mov_b32_e32 v53, 0x3f803f80\n"
      "v_mov_b32_e32 v54, 0x3f803f80\n"
      "v_mov_b32_e32 v55, 0x3f803f80\n"
      "ds_write_b128 v17, v[52:55] offset:18432\n"
      "v_and_b32_e32 v20, 15, v16\n"
      "v_min_u32_e32 v20, 2, v20\n"
      "v_lshlrev_b32_e32 v18, 10, v20\n"
      "v_lshrrev_b32_e32 v20, 4, v16\n"
      "v_lshl_add_u32 v18, v20, 4, v18\n"
      "v_add_u32_e32 v18, s49, v18\n"
      "v_lshlrev_b32_e32 v19, 8, v20\n"
      "v_bfe_u32 v20, v16, 2, 2\n"
      "v_lshl_add_u32 v19, v20, 5, v19\n"
      "v_and_b32_e32 v20, 3, v16\n"
      "v_lshl_add_u32 v19, v20, 3, v19\n"
      "v_add_u32_e32 v19, s49, v19\n"
      "v_lshrrev_b32_e32 v20, 4, v16\n"
      "v_lshlrev_b32_e32 v22, 4, v20\n"
      "v_and_b32_e32 v20, 1, v16\n"
      "v_lshl_add_u32 v22, v20, 18, v22\n"
      "s_lshr_b32 s80, s95, 4\n"
      "s_lshl_b32 s80, s80, 13\n"
      "s_bfe_u32 s81, s95, 0x20002\n"
      "s_lshl_b32 s81, s81, 9\n"
      "s_add_u32 s80, s80, s81\n"
      "s_bfe_u32 s81, s95, 0x10001\n"
      "s_lshl_b32 s81, s81, 12\n"
      "s_add_u32 s80, s80, s81\n"
      "s_and_b32 s81, s95, 1\n"
      "s_lshl_b32 s81, s81, 6\n"
      "s_add_u32 s80, s80, s81\n"
      "s_add_u32 s80, s80, 0x800\n"
      "s_add_u32 s64, s40, s80\n"
      "s_addc_u32 s65, s41, 0\n"
      "s_waitcnt vmcnt(10)\n"
      "s_mov_b32 exec_lo, -1\n"
      "s_mov_b32 exec_hi, 0x00ffffff\n"
      "ds_write_b128 v17, v[24:27] offset:16384\n"
      "ds_write_b128 v17, v[28:31] offset:17408\n"
      "s_mov_b64 exec, -1\n"
      "s_mov_b32 s81, 0x3f803f80\n"
      "v_dot2_f32_bf16 v0, v24, s81, 0\n"
      "v_dot2_f32_bf16 v2, v24, v24, 0\n"
      "v_dot2_f32_bf16 v1, v28, s81, 0\n"
      "v_dot2_f32_bf16 v3, v28, v28, 0\n"
      "v_dot2_f32_bf16 v0, v25, s81, v0\n"
      "v_dot2_f32_bf16 v2, v25, v25, v2\n"
      "v_dot2_f32_bf16 v1, v29, s81, v1\n"
      "v_dot2_f32_bf16 v3, v29, v29, v3\n"
      "v_dot2_f32_bf16 v0, v26, s81, v0\n"
      "v_dot2_f32_bf16 v2, v26, v26, v2\n"
      "v_dot2_f32_bf16 v1, v30, s81, v1\n"
      "v_dot2_f32_bf16 v3, v30, v30, v3\n"
      "v_dot2_f32_bf16 v0, v27, s81, v0\n"
      "v_dot2_f32_bf16 v2, v27, v27, v2\n"
      "v_dot2_f32_bf16 v1, v31, s81, v1\n"
      "v_dot2_f32_bf16 v3, v31, v31, v3\n"
      "v_add_f32_dpp v0, v0, v0 row_ror:8 row_mask:0xf bank_mask:0xf\n"
      "s_nop 0\n"
      "v_add_f32_dpp v1, v1, v1 row_ror:8 row_mask:0xf bank_mask:0xf\n"
      "v_add_f32_dpp v2, v2, v2 row_ror:8 row_mask:0xf bank_mask:0xf\n"
      "v_add_f32_dpp v3, v3, v3 row_ror:8 row_mask:0xf bank_mask:0xf\n"
      "v_add_f32_dpp v0, v0, v0 row_ror:4 row_mask:0xf bank_mask:0xf\n"
      "v_add_f32_dpp v1, v1, v1 row_ror:4 row_mask:0xf bank_mask:0xf\n"
      "v_add_f32_dpp v2, v2, v2 row_ror:4 row_mask:0xf bank_mask:0xf\n"
      "v_add_f32_dpp v3, v3, v3 row_ror:4 row_mask:0xf bank_mask:0xf\n"
      "v_add_f32_dpp v0, v0, v0 row_ror:2 row_mask:0xf bank_mask:0xf\n"
      "v_add_f32_dpp v1, v1, v1 row_ror:2 row_mask:0xf bank_mask:0xf\n"
      "v_add_f32_dpp v2, v2, v2 row_ror:2 row_mask:0xf bank_mask:0xf\n"
      "v_add_f32_dpp v3, v3, v3 row_ror:2 row_mask:0xf bank_mask:0xf\n"
      "v_add_f32_dpp v0, v0, v0 row_ror:1 row_mask:0xf bank_mask:0xf\n"
      "v_add_f32_dpp v1, v1, v1 row_ror:1 row_mask:0xf bank_mask:0xf\n"
      "v_add_f32_dpp v2, v2, v2 row_ror:1 row_mask:0xf bank_mask:0xf\n"
      "v_add_f32_dpp v3, v3, v3 row_ror:1 row_mask:0xf bank_mask:0xf\n"
      "v_mov_b32_e32 v4, v0\n"
      "v_mov_b32_e32 v5, v1\n"
      "v_mov_b32_e32 v6, v2\n"
      "v_mov_b32_e32 v7, v3\n"
      "v_permlane16_swap_b32_e32 v4, v0\n"
      "v_permlane16_swap_b32_e32 v5, v1\n"
      "v_permlane16_swap_b32_e32 v6, v2\n"
      "v_permlane16_swap_b32_e32 v7, v3\n"
      "v_add_f32_e32 v0, v0, v4\n"
      "v_add_f32_e32 v1, v1, v5\n"
      "v_add_f32_e32 v2, v2, v6\n"
      "v_add_f32_e32 v3, v3, v7\n"
      "v_mov_b32_e32 v4, v0\n"
      "v_mov_b32_e32 v5, v1\n"
      "v_mov_b32_e32 v6, v2\n"
      "v_mov_b32_e32 v7, v3\n"
      "v_permlane32_swap_b32_e32 v4, v0\n"
      "v_permlane32_swap_b32_e32 v5, v1\n"
      "v_permlane32_swap_b32_e32 v6, v2\n"
      "v_permlane32_swap_b32_e32 v7, v3\n"
      "v_add_f32_e32 v0, v0, v4\n"
      "v_add_f32_e32 v1, v1, v5\n"
      "v_add_f32_e32 v2, v2, v6\n"
      "v_add_f32_e32 v3, v3, v7\n"
      "v_cndmask_b32_e64 v8, v0, v1, s[96:97]\n"
      "v_cndmask_b32_e64 v9, v2, v3, s[96:97]\n"
      "v_mul_f32_e32 v10, 0x3b124925, v8\n"
      "v_mul_f32_e32 v9, 0x3b124925, v9\n"
      "v_fma_f32 v9, -v10, v10, v9\n"
      "v_cvt_pk_bf16_f32 v11, v10, v10\n"
      "v_max_f32_e32 v9, 0, v9\n"
      "v_and_b32_e32 v11, 0xffff0000, v11\n"
      "v_cvt_pk_bf16_f32 v9, v9, v9\n"
      "v_xor_b32_e32 v11, 0x80000000, v11\n"
      "v_and_b32_e32 v9, 0xffff0000, v9\n"
      "v_add_f32_e32 v9, 0x3727c5ac, v9\n"
      "v_cvt_pk_bf16_f32 v9, v9, v9\n"
      "v_and_b32_e32 v9, 0xffff0000, v9\n"
      "v_sqrt_f32_e32 v9, v9\n"
      "s_nop 0\n"
      "v_cvt_pk_bf16_f32 v9, v9, v9\n"
      "v_and_b32_e32 v9, 0xffff0000, v9\n"
      "v_rcp_f32_e32 v12, v9\n"
      "s_waitcnt vmcnt(10)\n"
      "s_waitcnt lgkmcnt(0)\n"
      "ds_read_b64_tr_b16 v[32:33], v19 offset:0\n"
      "ds_read_b64_tr_b16 v[34:35], v19 offset:128\n"
      "ds_read_b128 v[24:27], v18 offset:16384\n"
      "ds_read_b64_tr_b16 v[36:37], v19 offset:1024\n"
      "ds_read_b64_tr_b16 v[38:39], v19 offset:1152\n"
      "ds_read_b128 v[28:31], v18 offset:16448\n"
      "ds_read_b64_tr_b16 v[40:41], v19 offset:2048\n"
      "ds_read_b64_tr_b16 v[42:43], v19 offset:2176\n"
      "ds_read_b128 v[52:55], v18 offset:16512\n"
      "ds_read_b64_tr_b16 v[44:45], v19 offset:3072\n"
      "ds_read_b64_tr_b16 v[46:47], v19 offset:3200\n"
      "ds_read_b128 v[56:59], v18 offset:16576\n"
      "s_waitcnt lgkmcnt(9)\n"
      "v_mfma_f32_16x16x32_bf16 v[48:51], v[32:35], v[24:27], 0\n"
      "s_nop 7\n"
      "ds_read_b64_tr_b16 v[32:33], v19 offset:4096\n"
      "ds_read_b64_tr_b16 v[34:35], v19 offset:4224\n"
      "ds_read_b128 v[24:27], v18 offset:16640\n"
      "s_waitcnt lgkmcnt(9)\n"
      "v_mfma_f32_16x16x32_bf16 v[48:51], v[36:39], v[28:31], v[48:51]\n"
      "s_nop 7\n"
      "ds_read_b64_tr_b16 v[36:37], v19 offset:5120\n"
      "ds_read_b64_tr_b16 v[38:39], v19 offset:5248\n"
      "ds_read_b128 v[28:31], v18 offset:16704\n"
      "s_waitcnt lgkmcnt(9)\n"
      "v_mfma_f32_16x16x32_bf16 v[48:51], v[40:43], v[52:55], v[48:51]\n"
      "s_nop 7\n"
      "ds_read_b64_tr_b16 v[40:41], v19 offset:6144\n"
      "ds_read_b64_tr_b16 v[42:43], v19 offset:6272\n"
      "ds_read_b128 v[52:55], v18 offset:16768\n"
      "s_waitcnt lgkmcnt(9)\n"
      "v_mfma_f32_16x16x32_bf16 v[48:51], v[44:47], v[56:59], v[48:51]\n"
      "s_nop 7\n"
      "ds_read_b64_tr_b16 v[44:45], v19 offset:7168\n"
      "ds_read_b64_tr_b16 v[46:47], v19 offset:7296\n"
      "ds_read_b128 v[56:59], v18 offset:16832\n"
      "s_waitcnt lgkmcnt(9)\n"
      "v_mfma_f32_16x16x32_bf16 v[48:51], v[32:35], v[24:27], v[48:51]\n"
      "s_nop 7\n"
      "ds_read_b64_tr_b16 v[32:33], v19 offset:8192\n"
      "ds_read_b64_tr_b16 v[34:35], v19 offset:8320\n"
      "ds_read_b128 v[24:27], v18 offset:16896\n"
      "s_waitcnt lgkmcnt(9)\n"
      "v_mfma_f32_16x16x32_bf16 v[48:51], v[36:39], v[28:31], v[48:51]\n"
      "s_nop 7\n"
      "ds_read_b64_tr_b16 v[36:37], v19 offset:9216\n"
      "ds_read_b64_tr_b16 v[38:39], v19 offset:9344\n"
      "ds_read_b128 v[28:31], v18 offset:16960\n"
      "s_waitcnt lgkmcnt(9)\n"
      "v_mfma_f32_16x16x32_bf16 v[48:51], v[40:43], v[52:55], v[48:51]\n"
      "s_nop 7\n"
      "ds_read_b64_tr_b16 v[40:41], v19 offset:10240\n"
      "ds_read_b64_tr_b16 v[42:43], v19 offset:10368\n"
      "ds_read_b128 v[52:55], v18 offset:17024\n"
      "s_waitcnt lgkmcnt(9)\n"
      "v_mfma_f32_16x16x32_bf16 v[48:51], v[44:47], v[56:59], v[48:51]\n"
      "s_nop 7\n"
      "ds_read_b64_tr_b16 v[44:45], v19 offset:11264\n"
      "ds_read_b64_tr_b16 v[46:47], v19 offset:11392\n"
      "ds_read_b128 v[56:59], v18 offset:17088\n"
      "s_waitcnt lgkmcnt(9)\n"
      "v_mfma_f32_16x16x32_bf16 v[48:51], v[32:35], v[24:27], v[48:51]\n"
      "s_nop 7\n"
      "ds_read_b64_tr_b16 v[32:33], v19 offset:12288\n"
      "ds_read_b64_tr_b16 v[34:35], v19 offset:12416\n"
      "ds_read_b128 v[24:27], v18 offset:17152\n"
      "s_waitcnt lgkmcnt(9)\n"
      "v_mfma_f32_16x16x32_bf16 v[48:51], v[36:39], v[28:31], v[48:51]\n"
      "s_nop 7\n"
      "ds_read_b64_tr_b16 v[36:37], v19 offset:13312\n"
      "ds_read_b64_tr_b16 v[38:39], v19 offset:13440\n"
      "ds_read_b128 v[28:31], v18 offset:17216\n"
      "s_waitcnt lgkmcnt(9)\n"
      "v_mfma_f32_16x16x32_bf16 v[48:51], v[40:43], v[52:55], v[48:51]\n"
      "s_waitcnt lgkmcnt(6)\n"
      "v_mfma_f32_16x16x32_bf16 v[48:51], v[44:47], v[56:59], v[48:51]\n"
      "s_waitcnt lgkmcnt(3)\n"
      "v_mfma_f32_16x16x32_bf16 v[48:51], v[32:35], v[24:27], v[48:51]\n"
      "s_waitcnt lgkmcnt(0)\n"
      "v_mfma_f32_16x16x32_bf16 v[48:51], v[36:39], v[28:31], v[48:51]\n"
      "s_nop 7\n"
      "v_mov_b32_dpp v0, v48 row_shl:2 row_mask:0xf bank_mask:0xf\n"
      "v_mov_b32_dpp v1, v49 row_shl:2 row_mask:0xf bank_mask:0xf\n"
      "v_mov_b32_dpp v2, v50 row_shl:2 row_mask:0xf bank_mask:0xf\n"
      "v_mov_b32_dpp v3, v51 row_shl:2 row_mask:0xf bank_mask:0xf\n"
      "v_fma_f32 v48, v11, v0, v48\n"
      "v_fma_f32 v49, v11, v1, v49\n"
      "v_fma_f32 v50, v11, v2, v50\n"
      "v_fma_f32 v51, v11, v3, v51\n"
      "v_mul_f32_e32 v48, v12, v48\n"
      "v_mul_f32_e32 v49, v12, v49\n"
      "v_mul_f32_e32 v50, v12, v50\n"
      "v_mul_f32_e32 v51, v12, v51\n"
      "v_cvt_pk_bf16_f32 v4, v48, v49\n"
      "v_cvt_pk_bf16_f32 v6, v50, v51\n"
      "v_mov_b32_e32 v5, s62\n"
      "v_mov_b32_e32 v7, s62\n"
      "s_mov_b32 exec_lo, 0x00030003\n"
      "s_mov_b32 exec_hi, 0x00030003\n"
      "global_store_dwordx4 v22, v[4:7], s[64:65] sc1\n"
      "s_mov_b64 exec, -1\n"
      "s_nop 0\n"
      "Q1C_8_%=:\n"
      "s_waitcnt vmcnt(8)\n"
      "global_load_dwordx4 v[100:103], v61, s[90:91] offset:128 nt\n"
      "global_load_dwordx4 v[104:107], v61, s[90:91] offset:256 nt\n"
      "s_waitcnt vmcnt(8)\n"
      "global_load_dwordx4 v[108:111], v61, s[90:91] offset:384 nt\n"
      "global_load_dwordx4 v[112:115], v61, s[90:91] offset:2048 nt\n"
      "s_waitcnt vmcnt(8)\n"
      "global_load_dwordx4 v[116:119], v61, s[90:91] offset:2176 nt\n"
      "global_load_dwordx4 v[120:123], v61, s[90:91] offset:2304 nt\n"
      "s_waitcnt vmcnt(7)\n"
      "global_load_dwordx4 v[124:127], v61, s[90:91] offset:2432 nt\n"
      "TG_9_%=:\n"
      "s_memrealtime s[86:87]\n"
      "s_waitcnt lgkmcnt(0)\n"
      "s_lshl_b32 s86, s86, 4\n"
      "s_sub_u32 s86, s86, s93\n"
      "s_cmp_ge_i32 s86, 0\n"
      "s_cbranch_scc1 TGO_10_%=\n"
      "s_sleep 1\n"
      "s_branch TG_9_%=\n"
      "TGO_10_%=:\n"
      "s_add_u32 s93, s93, 2100\n"
      "s_add_u32 s86, s100, 0x70000\n"
      "s_cmp_ge_u32 s86, 0x3f0000\n"
      "s_cselect_b32 s87, 0x3f0000, 0\n"
      "s_sub_u32 s86, s86, s87\n"
      "s_add_u32 s90, s36, s86\n"
      "s_addc_u32 s91, s37, 0\n"
      "global_load_dwordx4 v[128:131], v60, s[90:91] offset:-4096 nt\n"
      "global_load_dwordx4 v[132:135], v60, s[90:91] offset:-3840 nt\n"
      "s_waitcnt vmcnt(8)\n"
      "global_load_dwordx4 v[136:139], v60, s[90:91] offset:-2048 nt\n"
      "global_load_dwordx4 v[140:143], v60, s[90:91] offset:-1792 nt\n"
      "s_waitcnt vmcnt(8)\n"
      "global_load_dwordx4 v[144:147], v60, s[90:91] offset:0 nt\n"
      "global_load_dwordx4 v[148:151], v60, s[90:91] offset:256 nt\n"
      "s_waitcnt vmcnt(8)\n"
      "global_load_dwordx4 v[152:155], v60, s[90:91] offset:2048 nt\n"
      "global_load_dwordx4 v[156:159], v60, s[90:91] offset:2304 nt\n"
      "s_waitcnt vmcnt(8)\n"
      "TG_11_%=:\n"
      "s_memrealtime s[86:87]\n"
      "s_waitcnt lgkmcnt(0)\n"
      "s_lshl_b32 s86, s86, 4\n"
      "s_sub_u32 s86, s86, s93\n"
      "s_cmp_ge_i32 s86, 0\n"
      "s_cbranch_scc1 TGO_12_%=\n"
      "s_sleep 1\n"
      "s_branch TG_11_%=\n"
      "TGO_12_%=:\n"
      "s_add_u32 s93, s93, 2100\n"
      "s_add_u32 s86, s100, 0x70000\n"
      "s_cmp_ge_u32 s86, 0x3f0000\n"
      "s_cselect_b32 s87, 0x3f0000, 0\n"
      "s_sub_u32 s86, s86, s87\n"
      "s_add_u32 s90, s38, s86\n"
      "s_addc_u32 s91, s39, 0\n"
      "global_load_dwordx4 v[160:163], v61, s[90:91] offset:0 nt\n"
      "global_load_dwordx4 v[164:167], v61, s[90:91] offset:128 nt\n"
      "s_waitcnt vmcnt(8)\n"
      "global_load_dwordx4 v[168:171], v61, s[90:91] offset:256 nt\n"
      "global_load_dwordx4 v[172:175], v61, s[90:91] offset:384 nt\n"
      "s_waitcnt vmcnt(8)\n"
      "global_load_dwordx4 v[176:179], v61, s[90:91] offset:2048 nt\n"
      "global_load_dwordx4 v[180:183], v61, s[90:91] offset:2176 nt\n"
      "s_waitcnt vmcnt(8)\n"
      "global_load_dwordx4 v[184:187], v61, s[90:91] offset:2304 nt\n"
      "global_load_dwordx4 v[188:191], v61, s[90:91] offset:2432 nt\n"
      "s_waitcnt vmcnt(8)\n"
      "TG_13_%=:\n"
      "s_memrealtime s[86:87]\n"
      "s_waitcnt lgkmcnt(0)\n"
      "s_lshl_b32 s86, s86, 4\n"
      "s_sub_u32 s86, s86, s93\n"
      "s_cmp_ge_i32 s86, 0\n"
      "s_cbranch_scc1 TGO_14_%=\n"
      "s_sleep 1\n"
      "s_branch TG_13_%=\n"
      "TGO_14_%=:\n"
      "s_add_u32 s93, s93, 2100\n"
      "s_add_u32 s86, s100, 0xe0000\n"
      "s_cmp_ge_u32 s86, 0x3f0000\n"
      "s_cselect_b32 s87, 0x3f0000, 0\n"
      "s_sub_u32 s86, s86, s87\n"
      "s_add_u32 s90, s36, s86\n"
      "s_addc_u32 s91, s37, 0\n"
      "global_load_dwordx4 v[192:195], v60, s[90:91] offset:-4096 nt\n"
      "global_load_dwordx4 v[196:199], v60, s[90:91] offset:-3840 nt\n"
      "s_add_u32 s64, s42, 0x0\n"
      "s_addc_u32 s65, s43, 0\n"
      "s_mov_b32 m0, 0x26e40\n"
      "s_nop 0\n"
      "global_load_lds_dwordx4 v61, s[64:65] sc1\n"
      "s_add_u32 s64, s42, 0x40000\n"
      "s_addc_u32 s65, s43, 0\n"
      "s_mov_b32 m0, 0x27640\n"
      "s_nop 0\n"
      "global_load_lds_dwordx4 v61, s[64:65] sc1\n"
      "s_waitcnt vmcnt(8)\n"
      "global_load_dwordx4 v[200:203], v60, s[90:91] offset:-2048 nt\n"
      "global_load_dwordx4 v[204:207], v60, s[90:91] offset:-1792 nt\n"
      "s_waitcnt vmcnt(8)\n"
      "global_load_dwordx4 v[208:211], v60, s[90:91] offset:0 nt\n"
      "global_load_dwordx4 v[212:215], v60, s[90:91] offset:256 nt\n"
      "s_waitcnt vmcnt(8)\n"
      "global_load_dwordx4 v[216:219], v60, s[90:91] offset:2048 nt\n"
      "global_load_dwordx4 v[220:223], v60, s[90:91] offset:2304 nt\n"
      "s_waitcnt vmcnt(8)\n"
      "TG_15_%=:\n"
      "s_memrealtime s[86:87]\n"
      "s_waitcnt lgkmcnt(0)\n"
      "s_lshl_b32 s86, s86, 4\n"
      "s_sub_u32 s86, s86, s93\n"
      "s_cmp_ge_i32 s86, 0\n"
      "s_cbranch_scc1 TGO_16_%=\n"
      "s_sleep 1\n"
      "s_branch TG_15_%=\n"
      "TGO_16_%=:\n"
      "s_add_u32 s93, s93, 2100\n"
      "s_add_u32 s86, s100, 0xe0000\n"
      "s_cmp_ge_u32 s86, 0x3f0000\n"
      "s_cselect_b32 s87, 0x3f0000, 0\n"
      "s_sub_u32 s86, s86, s87\n"
      "s_add_u32 s90, s38, s86\n"
      "s_addc_u32 s91, s39, 0\n"
      "global_load_dwordx4 v[224:227], v61, s[90:91] offset:0 nt\n"
      "global_load_dwordx4 v[228:231], v61, s[90:91] offset:128 nt\n"
      "s_add_u32 s64, s42, 0x0\n"
      "s_addc_u32 s65, s43, 0\n"
      "s_mov_b32 m0, 0x26e40\n"
      "s_nop 0\n"
      "global_load_lds_dwordx4 v61, s[64:65] sc1\n"
      "s_add_u32 s64, s42, 0x40000\n"
      "s_addc_u32 s65, s43, 0\n"
      "s_mov_b32 m0, 0x27640\n"
      "s_nop 0\n"
      "global_load_lds_dwordx4 v61, s[64:65] sc1\n"
      "s_waitcnt vmcnt(8)\n"
      "global_load_dwordx4 v[232:235], v61, s[90:91] offset:256 nt\n"
      "global_load_dwordx4 v[236:239], v61, s[90:91] offset:384 nt\n"
      "s_waitcnt vmcnt(8)\n"
      "global_load_dwordx4 v[240:243], v61, s[90:91] offset:2048 nt\n"
      "global_load_dwordx4 v[244:247], v61, s[90:91] offset:2176 nt\n"
      "s_waitcnt vmcnt(8)\n"
      "global_load_dwordx4 v[248:251], v61, s[90:91] offset:2304 nt\n"
      "global_load_dwordx4 v[252:255], v61, s[90:91] offset:2432 nt\n"
      "s_waitcnt vmcnt(8)\n"
      "TG_17_%=:\n"
      "s_memrealtime s[86:87]\n"
      "s_waitcnt lgkmcnt(0)\n"
      "s_lshl_b32 s86, s86, 4\n"
      "s_sub_u32 s86, s86, s93\n"
      "s_cmp_ge_i32 s86, 0\n"
      "s_cbranch_scc1 TGO_18_%=\n"
      "s_sleep 1\n"
      "s_branch TG_17_%=\n"
      "TGO_18_%=:\n"
      "s_add_u32 s93, s93, 2100\n"
      "s_add_u32 s86, s100, 0x150000\n"
      "s_cmp_ge_u32 s86, 0x3f0000\n"
      "s_cselect_b32 s87, 0x3f0000, 0\n"
      "s_sub_u32 s86, s86, s87\n"
      "s_add_u32 s90, s36, s86\n"
      "s_addc_u32 s91, s37, 0\n"
      "global_load_dwordx4 a[0:3], v60, s[90:91] offset:-4096 nt\n"
      "global_load_dwordx4 a[4:7], v60, s[90:91] offset:-3840 nt\n"
      "s_add_u32 s64, s42, 0x0\n"
      "s_addc_u32 s65, s43, 0\n"
      "s_mov_b32 m0, 0x26e40\n"
      "s_nop 0\n"
      "global_load_lds_dwordx4 v61, s[64:65] sc1\n"
      "s_add_u32 s64, s42, 0x40000\n"
      "s_addc_u32 s65, s43, 0\n"
      "s_mov_b32 m0, 0x27640\n"
      "s_nop 0\n"
      "global_load_lds_dwordx4 v61, s[64:65] sc1\n"
      "s_waitcnt vmcnt(8)\n"
      "global_load_dwordx4 a[8:11], v60, s[90:91] offset:-2048 nt\n"
      "global_load_dwordx4 a[12:15], v60, s[90:91] offset:-1792 nt\n"
      "s_waitcnt vmcnt(8)\n"
      "global_load_dwordx4 a[16:19], v60, s[90:91] offset:0 nt\n"
      "global_load_dwordx4 a[20:23], v60, s[90:91] offset:256 nt\n"
      "s_waitcnt vmcnt(8)\n"
      "global_load_dwordx4 a[24:27], v60, s[90:91] offset:2048 nt\n"
      "global_load_dwordx4 a[28:31], v60, s[90:91] offset:2304 nt\n"
      "s_waitcnt vmcnt(8)\n"
      "TG_19_%=:\n"
      "s_memrealtime s[86:87]\n"
      "s_waitcnt lgkmcnt(0)\n"
      "s_lshl_b32 s86, s86, 4\n"
      "s_sub_u32 s86, s86, s93\n"
      "s_cmp_ge_i32 s86, 0\n"
      "s_cbranch_scc1 TGO_20_%=\n"
      "s_sleep 1\n"
      "s_branch TG_19_%=\n"
      "TGO_20_%=:\n"
      "s_add_u32 s93, s93, 2100\n"
      "s_add_u32 s86, s100, 0x150000\n"
      "s_cmp_ge_u32 s86, 0x3f0000\n"
      "s_cselect_b32 s87, 0x3f0000, 0\n"
      "s_sub_u32 s86, s86, s87\n"
      "s_add_u32 s90, s38, s86\n"
      "s_addc_u32 s91, s39, 0\n"
      "global_load_dwordx4 a[32:35], v61, s[90:91] offset:0 nt\n"
      "global_load_dwordx4 a[36:39], v61, s[90:91] offset:128 nt\n"
      "s_add_u32 s64, s42, 0x0\n"
      "s_addc_u32 s65, s43, 0\n"
      "s_mov_b32 m0, 0x26e40\n"
      "s_nop 0\n"
      "global_load_lds_dwordx4 v61, s[64:65] sc1\n"
      "s_add_u32 s64, s42, 0x40000\n"
      "s_addc_u32 s65, s43, 0\n"
      "s_mov_b32 m0, 0x27640\n"
      "s_nop 0\n"
      "global_load_lds_dwordx4 v61, s[64:65] sc1\n"
      "s_waitcnt vmcnt(8)\n"
      "global_load_dwordx4 a[40:43], v61, s[90:91] offset:256 nt\n"
      "global_load_dwordx4 a[44:47], v61, s[90:91] offset:384 nt\n"
      "s_waitcnt vmcnt(8)\n"
      "global_load_dwordx4 a[48:51], v61, s[90:91] offset:2048 nt\n"
      "global_load_dwordx4 a[52:55], v61, s[90:91] offset:2176 nt\n"
      "s_waitcnt vmcnt(8)\n"
      "global_load_dwordx4 a[56:59], v61, s[90:91] offset:2304 nt\n"
      "global_load_dwordx4 a[60:63], v61, s[90:91] offset:2432 nt\n"
      "s_waitcnt vmcnt(8)\n"
      "TG_21_%=:\n"
      "s_memrealtime s[86:87]\n"
      "s_waitcnt lgkmcnt(0)\n"
      "s_lshl_b32 s86, s86, 4\n"
      "s_sub_u32 s86, s86, s93\n"
      "s_cmp_ge_i32 s86, 0\n"
      "s_cbranch_scc1 TGO_22_%=\n"
      "s_sleep 1\n"
      "s_branch TG_21_%=\n"
      "TGO_22_%=:\n"
      "s_add_u32 s93, s93, 2100\n"
      "s_add_u32 s86, s100, 0x1c0000\n"
      "s_cmp_ge_u32 s86, 0x3f0000\n"
      "s_cselect_b32 s87, 0x3f0000, 0\n"
      "s_sub_u32 s86, s86, s87\n"
      "s_add_u32 s90, s36, s86\n"
      "s_addc_u32 s91, s37, 0\n"
      "global_load_dwordx4 a[64:67], v60, s[90:91] offset:-4096 nt\n"
      "global_load_dwordx4 a[68:71], v60, s[90:91] offset:-3840 nt\n"
      "s_add_u32 s64, s42, 0x0\n"
      "s_addc_u32 s65, s43, 0\n"
      "s_mov_b32 m0, 0x26e40\n"
      "s_nop 0\n"
      "global_load_lds_dwordx4 v61, s[64:65] sc1\n"
      "s_add_u32 s64, s42, 0x40000\n"
      "s_addc_u32 s65, s43, 0\n"
      "s_mov_b32 m0, 0x27640\n"
      "s_nop 0\n"
      "global_load_lds_dwordx4 v61, s[64:65] sc1\n"
      "s_waitcnt vmcnt(8)\n"
      "global_load_dwordx4 a[72:75], v60, s[90:91] offset:-2048 nt\n"
      "global_load_dwordx4 a[76:79], v60, s[90:91] offset:-1792 nt\n"
      "s_waitcnt vmcnt(8)\n"
      "global_load_dwordx4 a[80:83], v60, s[90:91] offset:0 nt\n"
      "global_load_dwordx4 a[84:87], v60, s[90:91] offset:256 nt\n"
      "s_waitcnt vmcnt(8)\n"
      "global_load_dwordx4 a[88:91], v60, s[90:91] offset:2048 nt\n"
      "global_load_dwordx4 a[92:95], v60, s[90:91] offset:2304 nt\n"
      "s_waitcnt vmcnt(8)\n"
      "TG_23_%=:\n"
      "s_memrealtime s[86:87]\n"
      "s_waitcnt lgkmcnt(0)\n"
      "s_lshl_b32 s86, s86, 4\n"
      "s_sub_u32 s86, s86, s93\n"
      "s_cmp_ge_i32 s86, 0\n"
      "s_cbranch_scc1 TGO_24_%=\n"
      "s_sleep 1\n"
      "s_branch TG_23_%=\n"
      "TGO_24_%=:\n"
      "s_add_u32 s93, s93, 2100\n"
      "s_add_u32 s86, s100, 0x1c0000\n"
      "s_cmp_ge_u32 s86, 0x3f0000\n"
      "s_cselect_b32 s87, 0x3f0000, 0\n"
      "s_sub_u32 s86, s86, s87\n"
      "s_add_u32 s90, s38, s86\n"
      "s_addc_u32 s91, s39, 0\n"
      "global_load_dwordx4 a[96:99], v61, s[90:91] offset:0 nt\n"
      "global_load_dwordx4 a[100:103], v61, s[90:91] offset:128 nt\n"
      "s_add_u32 s64, s42, 0x0\n"
      "s_addc_u32 s65, s43, 0\n"
      "s_mov_b32 m0, 0x26e40\n"
      "s_nop 0\n"
      "global_load_lds_dwordx4 v61, s[64:65] sc1\n"
      "s_add_u32 s64, s42, 0x40000\n"
      "s_addc_u32 s65, s43, 0\n"
      "s_mov_b32 m0, 0x27640\n"
      "s_nop 0\n"
      "global_load_lds_dwordx4 v61, s[64:65] sc1\n"
      "s_mov_b32 s79, 0\n"
      "s_nop 0\n"
      "QW_26_%=:\n"
      "v_mbcnt_lo_u32_b32 v52, -1, 0\n"
      "v_mbcnt_hi_u32_b32 v52, -1, v52\n"
      "v_lshlrev_b32_e32 v52, 4, v52\n"
      "v_add_u32_e32 v52, 0x26e40, v52\n"
      "v_mov_b32_e32 v53, 0x260e0\n"
      "ds_read_b128 v[48:51], v52\n"
      "ds_read_b32 v53, v53\n"
      "s_waitcnt lgkmcnt(0)\n"
      "v_cmp_ne_u32_e64 s[80:81], s62, v49\n"
      "v_cmp_ne_u32_e64 s[82:83], s62, v51\n"
      "v_cmp_eq_u32_e32 vcc, 1, v53\n"
      "s_nop 4\n"
      "s_cbranch_vccnz QD_27_%=\n"
      "s_or_b64 s[80:81], s[80:81], s[82:83]\n"
      "s_cmp_eq_u64 s[80:81], 0\n"
      "s_nop 0\n"
      "s_cbranch_scc0 QN_28_%=\n"
      "v_mov_b32_e32 v49, v50\n"
      "v_lshrrev_b32_e32 v52, 1, v52\n"
      "v_add_u32_e32 v52, 0x12f20, v52\n"
      "ds_write_b64 v52, v[48:49]\n"
      "v_mov_b32_e32 v52, 0x260e0\n"
      "v_mov_b32_e32 v53, 1\n"
      "ds_write_b32 v52, v53\n"
      "s_waitcnt lgkmcnt(0)\n"
      "s_nop 2\n"
      "s_branch QD_27_%=\n"
      "s_nop 0\n"
      "QN_28_%=:\n"
      "s_add_u32 s64, s42, 0x0\n"
      "s_addc_u32 s65, s43, 0\n"
      "s_mov_b32 m0, 0x26e40\n"
      "s_nop 0\n"
      "global_load_lds_dwordx4 v61, s[64:65] sc1\n"
      "s_sleep 2\n"
      "s_add_u32 s79, s79, 1\n"
      "s_cmp_lt_u32 s79, 0x100000\n"
      "s_waitcnt vmcnt(40)\n"
      "s_nop 0\n"
      "s_cbranch_scc1 QW_26_%=\n"
      "s_nop 0\n"
      "QD_27_%=:\n"
      "s_mov_b32 s79, 0\n"
      "s_nop 0\n"
      "QW_29_%=:\n"
      "v_mbcnt_lo_u32_b32 v52, -1, 0\n"
      "v_mbcnt_hi_u32_b32 v52, -1, v52\n"
      "v_lshlrev_b32_e32 v52, 4, v52\n"
      "v_add_u32_e32 v52, 0x27640, v52\n"
      "v_mov_b32_e32 v53, 0x26100\n"
      "ds_read_b128 v[48:51], v52\n"
      "ds_read_b32 v53, v53\n"
      "s_waitcnt lgkmcnt(0)\n"
      "v_cmp_ne_u32_e64 s[80:81], s62, v49\n"
      "v_cmp_ne_u32_e64 s[82:83], s62, v51\n"
      "v_cmp_eq_u32_e32 vcc, 1, v53\n"
      "s_nop 4\n"
      "s_cbranch_vccnz QD_30_%=\n"
      "s_or_b64 s[80:81], s[80:81], s[82:83]\n"
      "s_cmp_eq_u64 s[80:81], 0\n"
      "s_nop 0\n"
      "s_cbranch_scc0 QN_31_%=\n"
      "v_mov_b32_e32 v49, v50\n"
      "v_lshrrev_b32_e32 v52, 1, v52\n"
      "v_add_u32_e32 v52, 0x12f20, v52\n"
      "ds_write_b64 v52, v[48:49]\n"
      "v_mov_b32_e32 v52, 0x26100\n"
      "v_mov_b32_e32 v53, 1\n"
      "ds_write_b32 v52, v53\n"
      "s_waitcnt lgkmcnt(0)\n"
      "s_nop 2\n"
      "s_branch QD_30_%=\n"
      "s_nop 0\n"
      "QN_31_%=:\n"
      "s_add_u32 s64, s42, 0x40000\n"
      "s_addc_u32 s65, s43, 0\n"
      "s_mov_b32 m0, 0x27640\n"
      "s_nop 0\n"
      "global_load_lds_dwordx4 v61, s[64:65] sc1\n"
      "s_sleep 2\n"
      "s_add_u32 s79, s79, 1\n"
      "s_cmp_lt_u32 s79, 0x100000\n"
      "s_waitcnt vmcnt(40)\n"
      "s_nop 0\n"
      "s_cbranch_scc1 QW_29_%=\n"
      "s_nop 0\n"
      "QD_30_%=:\n"
      "v_mbcnt_lo_u32_b32 v53, -1, 0\n"
      "v_mbcnt_hi_u32_b32 v53, -1, v53\n"
      "v_lshrrev_b32_e32 v52, 4, v53\n"
      "v_lshlrev_b32_e32 v52, 6, v52\n"
      "v_and_b32_e32 v53, 3, v53\n"
      "v_lshl_add_u32 v52, v53, 4, v52\n"
      "v_add_u32_e32 v52, 0x26640, v52\n"
      "s_mov_b32 vcc_lo, 0xf000f\n"
      "s_mov_b32 vcc_hi, 0xf000f\n"
      "v_mov_b32_e32 v53, 0x26120\n"
      "v_cndmask_b32_e32 v53, v53, v52, vcc\n"
      "ds_read_b128 v[0:3], v53 offset:0\n"
      "ds_read_b128 v[4:7], v53 offset:256\n"
      "s_mov_b32 vcc_lo, 0xf000f0\n"
      "s_mov_b32 vcc_hi, 0xf000f0\n"
      "v_mov_b32_e32 v53, 0x26120\n"
      "v_cndmask_b32_e32 v53, v53, v52, vcc\n"
      "ds_read_b128 v[8:11], v53 offset:0\n"
      "ds_read_b128 v[12:15], v53 offset:256\n"
      "s_mov_b32 vcc_lo, 0xf000f00\n"
      "s_mov_b32 vcc_hi, 0xf000f00\n"
      "v_mov_b32_e32 v53, 0x26120\n"
      "v_cndmask_b32_e32 v53, v53, v52, vcc\n"
      "ds_read_b128 v[16:19], v53 offset:0\n"
      "ds_read_b128 v[20:23], v53 offset:256\n"
      "s_mov_b32 vcc_lo, 0xf000f000\n"
      "s_mov_b32 vcc_hi, 0xf000f000\n"
      "v_mov_b32_e32 v53, 0x26120\n"
      "v_cndmask_b32_e32 v53, v53, v52, vcc\n"
      "ds_read_b128 v[24:27], v53 offset:0\n"
      "ds_read_b128 v[28:31], v53 offset:256\n"
      "s_waitcnt lgkmcnt(0)\n"
      "v_mov_b32_e32 v32, 0\n"
      "v_mov_b32_e32 v33, 0\n"
      "v_mov_b32_e32 v34, 0\n"
      "v_mov_b32_e32 v35, 0\n"
      "v_mov_b32_e32 v36, 0\n"
      "v_mov_b32_e32 v37, 0\n"
      "v_mov_b32_e32 v38, 0\n"
      "v_mov_b32_e32 v39, 0\n"
      "v_mov_b32_e32 v40, 0\n"
      "v_mov_b32_e32 v41, 0\n"
      "v_mov_b32_e32 v42, 0\n"
      "v_mov_b32_e32 v43, 0\n"
      "v_mov_b32_e32 v44, 0\n"
      "v_mov_b32_e32 v45, 0\n"
      "v_mov_b32_e32 v46, 0\n"
      "v_mov_b32_e32 v47, 0\n"
      "v_mov_b32_e32 v62, 0\n"
      "s_mov_b32 s60, 0xff800000\n"
      "v_mfma_f32_16x16x32_bf16 v[48:51], v[64:67], v[0:3], 0\n"
      "v_mfma_f32_16x16x32_bf16 v[48:51], v[68:71], v[4:7], v[48:51]\n"
      "v_mfma_f32_16x16x32_bf16 v[48:51], v[72:75], v[8:11], v[48:51]\n"
      "v_mfma_f32_16x16x32_bf16 v[48:51], v[76:79], v[12:15], v[48:51]\n"
      "v_mfma_f32_16x16x32_bf16 v[48:51], v[80:83], v[16:19], v[48:51]\n"
      "v_mfma_f32_16x16x32_bf16 v[48:51], v[84:87], v[20:23], v[48:51]\n"
      "v_mfma_f32_16x16x32_bf16 v[48:51], v[88:91], v[24:27], v[48:51]\n"
      "v_mfma_f32_16x16x32_bf16 v[48:51], v[92:95], v[28:31], v[48:51]\n"
      "s_nop 7\n"
      "v_cndmask_b32_e64 v53, v48, v49, s[96:97]\n"
      "v_cndmask_b32_e64 v54, v50, v51, s[96:97]\n"
      "v_cndmask_b32_e64 v52, v53, v54, s[98:99]\n"
      "s_nop 1\n"
      "v_add_f32_dpp v52, v52, v52 quad_perm:[1,0,3,2] row_mask:0xf bank_mask:0xf\n"
      "s_nop 1\n"
      "v_add_f32_dpp v52, v52, v52 quad_perm:[2,3,0,1] row_mask:0xf bank_mask:0xf\n"
      "v_cmp_lt_f32_e32 vcc, s60, v52\n"
      "s_cbranch_vccz NORS_32_%=\n"
      "s_swappc_b64 s[66:67], s[68:69]\n"
      "NORS_32_%=:\n"
      "v_subrev_f32_e32 v52, s60, v52\n"
      "v_mul_f32_e32 v52, 0x3db8aa3b, v52\n"
      "v_exp_f32_e32 v52, v52\n"
      "s_nop 0\n"
      "v_cvt_pk_bf16_f32 v53, v52, v52\n"
      "v_add_f32_e32 v62, v62, v52\n"
      "v_and_b32_e32 v53, v53, v63\n"
      "s_nop 0\n"
      "v_add_f32_dpp v62, v52, v62 row_half_mirror row_mask:0xf bank_mask:0xf\n"
      "v_mov_b32_dpp v56, v52 row_half_mirror row_mask:0xf bank_mask:0xf\n"
      "v_cvt_pk_bf16_f32 v56, v56, v56\n"
      "v_and_b32_e32 v56, v56, v63\n"
      "v_cndmask_b32_e64 v55, v53, v56, s[50:51]\n"
      "v_cndmask_b32_e64 v59, v56, v53, s[50:51]\n"
      "v_cndmask_b32_e64 v52, 0, v55, s[52:53]\n"
      "v_cndmask_b32_e64 v53, 0, v55, s[54:55]\n"
      "v_cndmask_b32_e64 v54, 0, v55, s[56:57]\n"
      "v_cndmask_b32_e64 v55, 0, v55, s[58:59]\n"
      "v_cndmask_b32_e64 v56, 0, v59, s[52:53]\n"
      "v_cndmask_b32_e64 v57, 0, v59, s[54:55]\n"
      "v_cndmask_b32_e64 v58, 0, v59, s[56:57]\n"
      "v_cndmask_b32_e64 v59, 0, v59, s[58:59]\n"
      "v_mfma_f32_16x16x32_bf16 v[32:35], v[96:99], v[52:55], v[32:35]\n"
      "v_mfma_f32_16x16x32_bf16 v[36:39], v[100:103], v[52:55], v[36:39]\n"
      "v_mfma_f32_16x16x32_bf16 v[40:43], v[104:107], v[52:55], v[40:43]\n"
      "v_mfma_f32_16x16x32_bf16 v[44:47], v[108:111], v[52:55], v[44:47]\n"
      "v_mfma_f32_16x16x32_bf16 v[32:35], v[112:115], v[56:59], v[32:35]\n"
      "v_mfma_f32_16x16x32_bf16 v[36:39], v[116:119], v[56:59], v[36:39]\n"
      "v_mfma_f32_16x16x32_bf16 v[40:43], v[120:123], v[56:59], v[40:43]\n"
      "v_mfma_f32_16x16x32_bf16 v[44:47], v[124:127], v[56:59], v[44:47]\n"
      "s_waitcnt vmcnt(8)\n"
      "global_load_dwordx4 a[104:107], v61, s[90:91] offset:256 nt\n"
      "global_load_dwordx4 a[108:111], v61, s[90:91] offset:384 nt\n"
      "s_nop 4\n"
      "v_mbcnt_lo_u32_b32 v48, -1, 0\n"
      "v_mbcnt_hi_u32_b32 v48, -1, v48\n"
      "v_lshlrev_b32_e32 v48, 4, v48\n"
      "v_add_u32_e32 v48, s49, v48\n"
      "ds_write_b128 v48, v[64:67] offset:0\n"
      "ds_write_b128 v48, v[68:71] offset:1024\n"
      "ds_write_b128 v48, v[72:75] offset:2048\n"
      "ds_write_b128 v48, v[76:79] offset:3072\n"
      "ds_write_b128 v48, v[80:83] offset:4096\n"
      "ds_write_b128 v48, v[84:87] offset:5120\n"
      "ds_write_b128 v48, v[88:91] offset:6144\n"
      "ds_write_b128 v48, v[92:95] offset:7168\n"
      "s_waitcnt vmcnt(9)\n"
      "global_load_dwordx4 a[112:115], v61, s[90:91] offset:2048 nt\n"
      "ds_write_b128 v48, v[96:99] offset:8192\n"
      "ds_write_b128 v48, v[100:103] offset:9216\n"
      "ds_write_b128 v48, v[104:107] offset:10240\n"
      "ds_write_b128 v48, v[108:111] offset:11264\n"
      "ds_write_b128 v48, v[112:115] offset:12288\n"
      "ds_write_b128 v48, v[116:119] offset:13312\n"
      "ds_write_b128 v48, v[120:123] offset:14336\n"
      "ds_write_b128 v48, v[124:127] offset:15360\n"
      "s_waitcnt lgkmcnt(0)\n"
      "v_mfma_f32_16x16x32_bf16 v[48:51], v[128:131], v[0:3], 0\n"
      "v_mfma_f32_16x16x32_bf16 v[48:51], v[132:135], v[4:7], v[48:51]\n"
      "v_mfma_f32_16x16x32_bf16 v[48:51], v[136:139], v[8:11], v[48:51]\n"
      "v_mfma_f32_16x16x32_bf16 v[48:51], v[140:143], v[12:15], v[48:51]\n"
      "v_mfma_f32_16x16x32_bf16 v[48:51], v[144:147], v[16:19], v[48:51]\n"
      "v_mfma_f32_16x16x32_bf16 v[48:51], v[148:151], v[20:23], v[48:51]\n"
      "v_mfma_f32_16x16x32_bf16 v[48:51], v[152:155], v[24:27], v[48:51]\n"
      "v_mfma_f32_16x16x32_bf16 v[48:51], v[156:159], v[28:31], v[48:51]\n"
      "s_nop 7\n"
      "v_cndmask_b32_e64 v53, v48, v49, s[96:97]\n"
      "v_cndmask_b32_e64 v54, v50, v51, s[96:97]\n"
      "v_cndmask_b32_e64 v52, v53, v54, s[98:99]\n"
      "s_nop 1\n"
      "v_add_f32_dpp v52, v52, v52 quad_perm:[1,0,3,2] row_mask:0xf bank_mask:0xf\n"
      "s_nop 1\n"
      "v_add_f32_dpp v52, v52, v52 quad_perm:[2,3,0,1] row_mask:0xf bank_mask:0xf\n"
      "v_cmp_lt_f32_e32 vcc, s60, v52\n"
      "s_cbranch_vccz NORS_33_%=\n"
      "s_swappc_b64 s[66:67], s[68:69]\n"
      "NORS_33_%=:\n"
      "v_subrev_f32_e32 v52, s60, v52\n"
      "v_mul_f32_e32 v52, 0x3db8aa3b, v52\n"
      "v_exp_f32_e32 v52, v52\n"
      "s_nop 0\n"
      "v_cvt_pk_bf16_f32 v53, v52, v52\n"
      "v_add_f32_e32 v62, v62, v52\n"
      "v_and_b32_e32 v53, v53, v63\n"
      "s_nop 0\n"
      "v_add_f32_dpp v62, v52, v62 row_half_mirror row_mask:0xf bank_mask:0xf\n"
      "v_mov_b32_dpp v56, v52 row_half_mirror row_mask:0xf bank_mask:0xf\n"
      "v_cvt_pk_bf16_f32 v56, v56, v56\n"
      "v_and_b32_e32 v56, v56, v63\n"
      "v_cndmask_b32_e64 v55, v53, v56, s[50:51]\n"
      "v_cndmask_b32_e64 v59, v56, v53, s[50:51]\n"
      "v_cndmask_b32_e64 v52, 0, v55, s[52:53]\n"
      "v_cndmask_b32_e64 v53, 0, v55, s[54:55]\n"
      "v_cndmask_b32_e64 v54, 0, v55, s[56:57]\n"
      "v_cndmask_b32_e64 v55, 0, v55, s[58:59]\n"
      "v_cndmask_b32_e64 v56, 0, v59, s[52:53]\n"
      "v_cndmask_b32_e64 v57, 0, v59, s[54:55]\n"
      "v_cndmask_b32_e64 v58, 0, v59, s[56:57]\n"
      "v_cndmask_b32_e64 v59, 0, v59, s[58:59]\n"
      "v_mfma_f32_16x16x32_bf16 v[32:35], v[160:163], v[52:55], v[32:35]\n"
      "v_mfma_f32_16x16x32_bf16 v[36:39], v[164:167], v[52:55], v[36:39]\n"
      "v_mfma_f32_16x16x32_bf16 v[40:43], v[168:171], v[52:55], v[40:43]\n"
      "v_mfma_f32_16x16x32_bf16 v[44:47], v[172:175], v[52:55], v[44:47]\n"
      "v_mfma_f32_16x16x32_bf16 v[32:35], v[176:179], v[56:59], v[32:35]\n"
      "v_mfma_f32_16x16x32_bf16 v[36:39], v[180:183], v[56:59], v[36:39]\n"
      "v_mfma_f32_16x16x32_bf16 v[40:43], v[184:187], v[56:59], v[40:43]\n"
      "v_mfma_f32_16x16x32_bf16 v[44:47], v[188:191], v[56:59], v[44:47]\n"
      "s_waitcnt vmcnt(8)\n"
      "global_load_dwordx4 a[116:119], v61, s[90:91] offset:2176 nt\n"
      "global_load_dwordx4 a[120:123], v61, s[90:91] offset:2304 nt\n"
      "s_nop 4\n"
      "v_mbcnt_lo_u32_b32 v48, -1, 0\n"
      "v_mbcnt_hi_u32_b32 v48, -1, v48\n"
      "v_lshlrev_b32_e32 v48, 4, v48\n"
      "v_add_u32_e32 v48, s49, v48\n"
      "ds_write_b128 v48, v[128:131] offset:16384\n"
      "ds_write_b128 v48, v[132:135] offset:17408\n"
      "ds_write_b128 v48, v[136:139] offset:18432\n"
      "ds_write_b128 v48, v[140:143] offset:19456\n"
      "ds_write_b128 v48, v[144:147] offset:20480\n"
      "ds_write_b128 v48, v[148:151] offset:21504\n"
      "ds_write_b128 v48, v[152:155] offset:22528\n"
      "ds_write_b128 v48, v[156:159] offset:23552\n"
      "s_waitcnt vmcnt(9)\n"
      "global_load_dwordx4 a[124:127], v61, s[90:91] offset:2432 nt\n"
      "ds_write_b128 v48, v[160:163] offset:24576\n"
      "ds_write_b128 v48, v[164:167] offset:25600\n"
      "ds_write_b128 v48, v[168:171] offset:26624\n"
      "ds_write_b128 v48, v[172:175] offset:27648\n"
      "ds_write_b128 v48, v[176:179] offset:28672\n"
      "ds_write_b128 v48, v[180:183] offset:29696\n"
      "ds_write_b128 v48, v[184:187] offset:30720\n"
      "ds_write_b128 v48, v[188:191] offset:31744\n"
      "s_waitcnt lgkmcnt(0)\n"
      "v_mfma_f32_16x16x32_bf16 v[48:51], v[192:195], v[0:3], 0\n"
      "v_mfma_f32_16x16x32_bf16 v[48:51], v[196:199], v[4:7], v[48:51]\n"
      "v_mfma_f32_16x16x32_bf16 v[48:51], v[200:203], v[8:11], v[48:51]\n"
      "v_mfma_f32_16x16x32_bf16 v[48:51], v[204:207], v[12:15], v[48:51]\n"
      "v_mfma_f32_16x16x32_bf16 v[48:51], v[208:211], v[16:19], v[48:51]\n"
      "v_mfma_f32_16x16x32_bf16 v[48:51], v[212:215], v[20:23], v[48:51]\n"
      "v_mfma_f32_16x16x32_bf16 v[48:51], v[216:219], v[24:27], v[48:51]\n"
      "v_mfma_f32_16x16x32_bf16 v[48:51], v[220:223], v[28:31], v[48:51]\n"
      "s_nop 7\n"
      "v_cndmask_b32_e64 v53, v48, v49, s[96:97]\n"
      "v_cndmask_b32_e64 v54, v50, v51, s[96:97]\n"
      "v_cndmask_b32_e64 v52, v53, v54, s[98:99]\n"
      "s_nop 1\n"
      "v_add_f32_dpp v52, v52, v52 quad_perm:[1,0,3,2] row_mask:0xf bank_mask:0xf\n"
      "s_nop 1\n"
      "v_add_f32_dpp v52, v52, v52 quad_perm:[2,3,0,1] row_mask:0xf bank_mask:0xf\n"
      "v_cmp_lt_f32_e32 vcc, s60, v52\n"
      "s_cbranch_vccz NORS_34_%=\n"
      "s_swappc_b64 s[66:67], s[68:69]\n"
      "NORS_34_%=:\n"
      "v_subrev_f32_e32 v52, s60, v52\n"
      "v_mul_f32_e32 v52, 0x3db8aa3b, v52\n"
      "v_exp_f32_e32 v52, v52\n"
      "s_nop 0\n"
      "v_cvt_pk_bf16_f32 v53, v52, v52\n"
      "v_add_f32_e32 v62, v62, v52\n"
      "v_and_b32_e32 v53, v53, v63\n"
      "s_nop 0\n"
      "v_add_f32_dpp v62, v52, v62 row_half_mirror row_mask:0xf bank_mask:0xf\n"
      "v_mov_b32_dpp v56, v52 row_half_mirror row_mask:0xf bank_mask:0xf\n"
      "v_cvt_pk_bf16_f32 v56, v56, v56\n"
      "v_and_b32_e32 v56, v56, v63\n"
      "v_cndmask_b32_e64 v55, v53, v56, s[50:51]\n"
      "v_cndmask_b32_e64 v59, v56, v53, s[50:51]\n"
      "v_cndmask_b32_e64 v52, 0, v55, s[52:53]\n"
      "v_cndmask_b32_e64 v53, 0, v55, s[54:55]\n"
      "v_cndmask_b32_e64 v54, 0, v55, s[56:57]\n"
      "v_cndmask_b32_e64 v55, 0, v55, s[58:59]\n"
      "v_cndmask_b32_e64 v56, 0, v59, s[52:53]\n"
      "v_cndmask_b32_e64 v57, 0, v59, s[54:55]\n"
      "v_cndmask_b32_e64 v58, 0, v59, s[56:57]\n"
      "v_cndmask_b32_e64 v59, 0, v59, s[58:59]\n"
      "v_mfma_f32_16x16x32_bf16 v[32:35], v[224:227], v[52:55], v[32:35]\n"
      "v_mfma_f32_16x16x32_bf16 v[36:39], v[228:231], v[52:55], v[36:39]\n"
      "v_mfma_f32_16x16x32_bf16 v[40:43], v[232:235], v[52:55], v[40:43]\n"
      "v_mfma_f32_16x16x32_bf16 v[44:47], v[236:239], v[52:55], v[44:47]\n"
      "v_mfma_f32_16x16x32_bf16 v[32:35], v[240:243], v[56:59], v[32:35]\n"
      "v_mfma_f32_16x16x32_bf16 v[36:39], v[244:247], v[56:59], v[36:39]\n"
      "v_mfma_f32_16x16x32_bf16 v[40:43], v[248:251], v[56:59], v[40:43]\n"
      "v_mfma_f32_16x16x32_bf16 v[44:47], v[252:255], v[56:59], v[44:47]\n"
      "s_waitcnt vmcnt(8)\n"
      "TG_35_%=:\n"
      "s_memrealtime s[86:87]\n"
      "s_waitcnt lgkmcnt(0)\n"
      "s_lshl_b32 s86, s86, 4\n"
      "s_sub_u32 s86, s86, s93\n"
      "s_cmp_ge_i32 s86, 0\n"
      "s_cbranch_scc1 TGO_36_%=\n"
      "s_sleep 1\n"
      "s_branch TG_35_%=\n"
      "TGO_36_%=:\n"
      "s_add_u32 s93, s93, 1650\n"
      "s_add_u32 s86, s100, 0x230000\n"
      "s_cmp_ge_u32 s86, 0x3f0000\n"
      "s_cselect_b32 s87, 0x3f0000, 0\n"
      "s_sub_u32 s86, s86, s87\n"
      "s_add_u32 s90, s36, s86\n"
      "s_addc_u32 s91, s37, 0\n"
      "global_load_dwordx4 a[128:131], v60, s[90:91] offset:-4096 nt\n"
      "global_load_dwordx4 a[132:135], v60, s[90:91] offset:-3840 nt\n"
      "v_mfma_f32_16x16x32_bf16 v[48:51], a[0:3], v[0:3], 0\n"
      "v_mfma_f32_16x16x32_bf16 v[48:51], a[4:7], v[4:7], v[48:51]\n"
      "v_mfma_f32_16x16x32_bf16 v[48:51], a[8:11], v[8:11], v[48:51]\n"
      "v_mfma_f32_16x16x32_bf16 v[48:51], a[12:15], v[12:15], v[48:51]\n"
      "v_mfma_f32_16x16x32_bf16 v[48:51], a[16:19], v[16:19], v[48:51]\n"
      "v_mfma_f32_16x16x32_bf16 v[48:51], a[20:23], v[20:23], v[48:51]\n"
      "v_mfma_f32_16x16x32_bf16 v[48:51], a[24:27], v[24:27], v[48:51]\n"
      "v_mfma_f32_16x16x32_bf16 v[48:51], a[28:31], v[28:31], v[48:51]\n"
      "s_nop 7\n"
      "v_cndmask_b32_e64 v53, v48, v49, s[96:97]\n"
      "v_cndmask_b32_e64 v54, v50, v51, s[96:97]\n"
      "v_cndmask_b32_e64 v52, v53, v54, s[98:99]\n"
      "s_nop 1\n"
      "v_add_f32_dpp v52, v52, v52 quad_perm:[1,0,3,2] row_mask:0xf bank_mask:0xf\n"
      "s_nop 1\n"
      "v_add_f32_dpp v52, v52, v52 quad_perm:[2,3,0,1] row_mask:0xf bank_mask:0xf\n"
      "v_cmp_lt_f32_e32 vcc, s60, v52\n"
      "s_cbranch_vccz NORS_37_%=\n"
      "s_swappc_b64 s[66:67], s[68:69]\n"
      "NORS_37_%=:\n"
      "v_subrev_f32_e32 v52, s60, v52\n"
      "v_mul_f32_e32 v52, 0x3db8aa3b, v52\n"
      "v_exp_f32_e32 v52, v52\n"
      "s_nop 0\n"
      "v_cvt_pk_bf16_f32 v53, v52, v52\n"
      "v_add_f32_e32 v62, v62, v52\n"
      "v_and_b32_e32 v53, v53, v63\n"
      "s_nop 0\n"
      "v_add_f32_dpp v62, v52, v62 row_half_mirror row_mask:0xf bank_mask:0xf\n"
      "v_mov_b32_dpp v56, v52 row_half_mirror row_mask:0xf bank_mask:0xf\n"
      "v_cvt_pk_bf16_f32 v56, v56, v56\n"
      "v_and_b32_e32 v56, v56, v63\n"
      "v_cndmask_b32_e64 v55, v53, v56, s[50:51]\n"
      "v_cndmask_b32_e64 v59, v56, v53, s[50:51]\n"
      "v_cndmask_b32_e64 v52, 0, v55, s[52:53]\n"
      "v_cndmask_b32_e64 v53, 0, v55, s[54:55]\n"
      "v_cndmask_b32_e64 v54, 0, v55, s[56:57]\n"
      "v_cndmask_b32_e64 v55, 0, v55, s[58:59]\n"
      "v_cndmask_b32_e64 v56, 0, v59, s[52:53]\n"
      "v_cndmask_b32_e64 v57, 0, v59, s[54:55]\n"
      "v_cndmask_b32_e64 v58, 0, v59, s[56:57]\n"
      "v_cndmask_b32_e64 v59, 0, v59, s[58:59]\n"
      "v_mfma_f32_16x16x32_bf16 v[32:35], a[32:35], v[52:55], v[32:35]\n"
      "v_mfma_f32_16x16x32_bf16 v[36:39], a[36:39], v[52:55], v[36:39]\n"
      "v_mfma_f32_16x16x32_bf16 v[40:43], a[40:43], v[52:55], v[40:43]\n"
      "v_mfma_f32_16x16x32_bf16 v[44:47], a[44:47], v[52:55], v[44:47]\n"
      "v_mfma_f32_16x16x32_bf16 v[32:35], a[48:51], v[56:59], v[32:35]\n"
      "v_mfma_f32_16x16x32_bf16 v[36:39], a[52:55], v[56:59], v[36:39]\n"
      "v_mfma_f32_16x16x32_bf16 v[40:43], a[56:59], v[56:59], v[40:43]\n"
      "v_mfma_f32_16x16x32_bf16 v[44:47], a[60:63], v[56:59], v[44:47]\n"
      "s_waitcnt vmcnt(8)\n"
      "global_load_dwordx4 a[136:139], v60, s[90:91] offset:-2048 nt\n"
      "global_load_dwordx4 a[140:143], v60, s[90:91] offset:-1792 nt\n"
      "v_mfma_f32_16x16x32_bf16 v[48:51], a[64:67], v[0:3], 0\n"
      "v_mfma_f32_16x16x32_bf16 v[48:51], a[68:71], v[4:7], v[48:51]\n"
      "v_mfma_f32_16x16x32_bf16 v[48:51], a[72:75], v[8:11], v[48:51]\n"
      "v_mfma_f32_16x16x32_bf16 v[48:51], a[76:79], v[12:15], v[48:51]\n"
      "v_mfma_f32_16x16x32_bf16 v[48:51], a[80:83], v[16:19], v[48:51]\n"
      "v_mfma_f32_16x16x32_bf16 v[48:51], a[84:87], v[20:23], v[48:51]\n"
      "v_mfma_f32_16x16x32_bf16 v[48:51], a[88:91], v[24:27], v[48:51]\n"
      "v_mfma_f32_16x16x32_bf16 v[48:51], a[92:95], v[28:31], v[48:51]\n"
      "s_nop 7\n"
      "v_cndmask_b32_e64 v53, v48, v49, s[96:97]\n"
      "v_cndmask_b32_e64 v54, v50, v51, s[96:97]\n"
      "v_cndmask_b32_e64 v52, v53, v54, s[98:99]\n"
      "s_nop 1\n"
      "v_add_f32_dpp v52, v52, v52 quad_perm:[1,0,3,2] row_mask:0xf bank_mask:0xf\n"
      "s_nop 1\n"
      "v_add_f32_dpp v52, v52, v52 quad_perm:[2,3,0,1] row_mask:0xf bank_mask:0xf\n"
      "v_cmp_lt_f32_e32 vcc, s60, v52\n"
      "s_cbranch_vccz NORS_38_%=\n"
      "s_swappc_b64 s[66:67], s[68:69]\n"
      "NORS_38_%=:\n"
      "v_subrev_f32_e32 v52, s60, v52\n"
      "v_mul_f32_e32 v52, 0x3db8aa3b, v52\n"
      "v_exp_f32_e32 v52, v52\n"
      "s_nop 0\n"
      "v_cvt_pk_bf16_f32 v53, v52, v52\n"
      "v_add_f32_e32 v62, v62, v52\n"
      "v_and_b32_e32 v53, v53, v63\n"
      "s_nop 0\n"
      "v_add_f32_dpp v62, v52, v62 row_half_mirror row_mask:0xf bank_mask:0xf\n"
      "v_mov_b32_dpp v56, v52 row_half_mirror row_mask:0xf bank_mask:0xf\n"
      "v_cvt_pk_bf16_f32 v56, v56, v56\n"
      "v_and_b32_e32 v56, v56, v63\n"
      "v_cndmask_b32_e64 v55, v53, v56, s[50:51]\n"
      "v_cndmask_b32_e64 v59, v56, v53, s[50:51]\n"
      "v_cndmask_b32_e64 v52, 0, v55, s[52:53]\n"
      "v_cndmask_b32_e64 v53, 0, v55, s[54:55]\n"
      "v_cndmask_b32_e64 v54, 0, v55, s[56:57]\n"
      "v_cndmask_b32_e64 v55, 0, v55, s[58:59]\n"
      "v_cndmask_b32_e64 v56, 0, v59, s[52:53]\n"
      "v_cndmask_b32_e64 v57, 0, v59, s[54:55]\n"
      "v_cndmask_b32_e64 v58, 0, v59, s[56:57]\n"
      "v_cndmask_b32_e64 v59, 0, v59, s[58:59]\n"
      "v_mfma_f32_16x16x32_bf16 v[32:35], a[96:99], v[52:55], v[32:35]\n"
      "v_mfma_f32_16x16x32_bf16 v[36:39], a[100:103], v[52:55], v[36:39]\n"
      "s_waitcnt vmcnt(8)\n"
      "global_load_dwordx4 a[144:147], v60, s[90:91] offset:0 nt\n"
      "global_load_dwordx4 a[148:151], v60, s[90:91] offset:256 nt\n"
      "v_mfma_f32_16x16x32_bf16 v[40:43], a[104:107], v[52:55], v[40:43]\n"
      "v_mfma_f32_16x16x32_bf16 v[44:47], a[108:111], v[52:55], v[44:47]\n"
      "s_waitcnt vmcnt(8)\n"
      "global_load_dwordx4 a[152:155], v60, s[90:91] offset:2048 nt\n"
      "global_load_dwordx4 a[156:159], v60, s[90:91] offset:2304 nt\n"
      "v_mfma_f32_16x16x32_bf16 v[32:35], a[112:115], v[56:59], v[32:35]\n"
      "v_mfma_f32_16x16x32_bf16 v[36:39], a[116:119], v[56:59], v[36:39]\n"
      "s_waitcnt vmcnt(8)\n"
      "TG_39_%=:\n"
      "s_memrealtime s[86:87]\n"
      "s_waitcnt lgkmcnt(0)\n"
      "s_lshl_b32 s86, s86, 4\n"
      "s_sub_u32 s86, s86, s93\n"
      "s_cmp_ge_i32 s86, 0\n"
      "s_cbranch_scc1 TGO_40_%=\n"
      "s_sleep 1\n"
      "s_branch TG_39_%=\n"
      "TGO_40_%=:\n"
      "s_add_u32 s93, s93, 1650\n"
      "s_add_u32 s86, s100, 0x230000\n"
      "s_cmp_ge_u32 s86, 0x3f0000\n"
      "s_cselect_b32 s87, 0x3f0000, 0\n"
      "s_sub_u32 s86, s86, s87\n"
      "s_add_u32 s90, s38, s86\n"
      "s_addc_u32 s91, s39, 0\n"
      "global_load_dwordx4 a[160:163], v61, s[90:91] offset:0 nt\n"
      "global_load_dwordx4 a[164:167], v61, s[90:91] offset:128 nt\n"
      "v_mfma_f32_16x16x32_bf16 v[40:43], a[120:123], v[56:59], v[40:43]\n"
      "v_mfma_f32_16x16x32_bf16 v[44:47], a[124:127], v[56:59], v[44:47]\n"
      "s_waitcnt vmcnt(8)\n"
      "global_load_dwordx4 a[168:171], v61, s[90:91] offset:256 nt\n"
      "global_load_dwordx4 a[172:175], v61, s[90:91] offset:384 nt\n"
      "v_mfma_f32_16x16x32_bf16 v[48:51], a[128:131], v[0:3], 0\n"
      "v_mfma_f32_16x16x32_bf16 v[48:51], a[132:135], v[4:7], v[48:51]\n"
      "s_waitcnt vmcnt(8)\n"
      "global_load_dwordx4 a[176:179], v61, s[90:91] offset:2048 nt\n"
      "global_load_dwordx4 a[180:183], v61, s[90:91] offset:2176 nt\n"
      "v_mfma_f32_16x16x32_bf16 v[48:51], a[136:139], v[8:11], v[48:51]\n"
      "v_mfma_f32_16x16x32_bf16 v[48:51], a[140:143], v[12:15], v[48:51]\n"
      "s_waitcnt vmcnt(8)\n"
      "global_load_dwordx4 a[184:187], v61, s[90:91] offset:2304 nt\n"
      "global_load_dwordx4 a[188:191], v61, s[90:91] offset:2432 nt\n"
      "v_mfma_f32_16x16x32_bf16 v[48:51], a[144:147], v[16:19], v[48:51]\n"
      "v_mfma_f32_16x16x32_bf16 v[48:51], a[148:151], v[20:23], v[48:51]\n"
      "s_waitcnt vmcnt(8)\n"
      "TG_41_%=:\n"
      "s_memrealtime s[86:87]\n"
      "s_waitcnt lgkmcnt(0)\n"
      "s_lshl_b32 s86, s86, 4\n"
      "s_sub_u32 s86, s86, s93\n"
      "s_cmp_ge_i32 s86, 0\n"
      "s_cbranch_scc1 TGO_42_%=\n"
      "s_sleep 1\n"
      "s_branch TG_41_%=\n"
      "TGO_42_%=:\n"
      "s_add_u32 s93, s93, 1650\n"
      "s_add_u32 s86, s100, 0x2a0000\n"
      "s_cmp_ge_u32 s86, 0x3f0000\n"
      "s_cselect_b32 s87, 0x3f0000, 0\n"
      "s_sub_u32 s86, s86, s87\n"
      "s_add_u32 s90, s36, s86\n"
      "s_addc_u32 s91, s37, 0\n"
      "global_load_dwordx4 a[192:195], v60, s[90:91] offset:-4096 nt\n"
      "global_load_dwordx4 a[196:199], v60, s[90:91] offset:-3840 nt\n"
      "v_mfma_f32_16x16x32_bf16 v[48:51], a[152:155], v[24:27], v[48:51]\n"
      "v_mfma_f32_16x16x32_bf16 v[48:51], a[156:159], v[28:31], v[48:51]\n"
      "s_nop 7\n"
      "v_cndmask_b32_e64 v53, v48, v49, s[96:97]\n"
      "v_cndmask_b32_e64 v54, v50, v51, s[96:97]\n"
      "v_cndmask_b32_e64 v52, v53, v54, s[98:99]\n"
      "s_nop 1\n"
      "v_add_f32_dpp v52, v52, v52 quad_perm:[1,0,3,2] row_mask:0xf bank_mask:0xf\n"
      "s_nop 1\n"
      "v_add_f32_dpp v52, v52, v52 quad_perm:[2,3,0,1] row_mask:0xf bank_mask:0xf\n"
      "v_cmp_lt_f32_e32 vcc, s60, v52\n"
      "s_cbranch_vccz NORS_43_%=\n"
      "s_swappc_b64 s[66:67], s[68:69]\n"
      "NORS_43_%=:\n"
      "v_subrev_f32_e32 v52, s60, v52\n"
      "v_mul_f32_e32 v52, 0x3db8aa3b, v52\n"
      "v_exp_f32_e32 v52, v52\n"
      "s_nop 0\n"
      "v_cvt_pk_bf16_f32 v53, v52, v52\n"
      "v_add_f32_e32 v62, v62, v52\n"
      "v_and_b32_e32 v53, v53, v63\n"
      "s_nop 0\n"
      "v_add_f32_dpp v62, v52, v62 row_half_mirror row_mask:0xf bank_mask:0xf\n"
      "v_mov_b32_dpp v56, v52 row_half_mirror row_mask:0xf bank_mask:0xf\n"
      "v_cvt_pk_bf16_f32 v56, v56, v56\n"
      "v_and_b32_e32 v56, v56, v63\n"
      "v_cndmask_b32_e64 v55, v53, v56, s[50:51]\n"
      "v_cndmask_b32_e64 v59, v56, v53, s[50:51]\n"
      "v_cndmask_b32_e64 v52, 0, v55, s[52:53]\n"
      "v_cndmask_b32_e64 v53, 0, v55, s[54:55]\n"
      "v_cndmask_b32_e64 v54, 0, v55, s[56:57]\n"
      "v_cndmask_b32_e64 v55, 0, v55, s[58:59]\n"
      "v_cndmask_b32_e64 v56, 0, v59, s[52:53]\n"
      "v_cndmask_b32_e64 v57, 0, v59, s[54:55]\n"
      "v_cndmask_b32_e64 v58, 0, v59, s[56:57]\n"
      "v_cndmask_b32_e64 v59, 0, v59, s[58:59]\n"
      "s_waitcnt vmcnt(8)\n"
      "global_load_dwordx4 a[200:203], v60, s[90:91] offset:-2048 nt\n"
      "global_load_dwordx4 a[204:207], v60, s[90:91] offset:-1792 nt\n"
      "v_mfma_f32_16x16x32_bf16 v[32:35], a[160:163], v[52:55], v[32:35]\n"
      "v_mfma_f32_16x16x32_bf16 v[36:39], a[164:167], v[52:55], v[36:39]\n"
      "s_waitcnt vmcnt(8)\n"
      "global_load_dwordx4 a[208:211], v60, s[90:91] offset:0 nt\n"
      "global_load_dwordx4 a[212:215], v60, s[90:91] offset:256 nt\n"
      "v_mfma_f32_16x16x32_bf16 v[40:43], a[168:171], v[52:55], v[40:43]\n"
      "v_mfma_f32_16x16x32_bf16 v[44:47], a[172:175], v[52:55], v[44:47]\n"
      "s_waitcnt vmcnt(8)\n"
      "global_load_dwordx4 a[216:219], v60, s[90:91] offset:2048 nt\n"
      "global_load_dwordx4 a[220:223], v60, s[90:91] offset:2304 nt\n"
      "v_mfma_f32_16x16x32_bf16 v[32:35], a[176:179], v[56:59], v[32:35]\n"
      "v_mfma_f32_16x16x32_bf16 v[36:39], a[180:183], v[56:59], v[36:39]\n"
      "s_waitcnt vmcnt(8)\n"
      "TG_44_%=:\n"
      "s_memrealtime s[86:87]\n"
      "s_waitcnt lgkmcnt(0)\n"
      "s_lshl_b32 s86, s86, 4\n"
      "s_sub_u32 s86, s86, s93\n"
      "s_cmp_ge_i32 s86, 0\n"
      "s_cbranch_scc1 TGO_45_%=\n"
      "s_sleep 1\n"
      "s_branch TG_44_%=\n"
      "TGO_45_%=:\n"
      "s_add_u32 s93, s93, 1650\n"
      "s_add_u32 s86, s100, 0x2a0000\n"
      "s_cmp_ge_u32 s86, 0x3f0000\n"
      "s_cselect_b32 s87, 0x3f0000, 0\n"
      "s_sub_u32 s86, s86, s87\n"
      "s_add_u32 s90, s38, s86\n"
      "s_addc_u32 s91, s39, 0\n"
      "global_load_dwordx4 a[224:227], v61, s[90:91] offset:0 nt\n"
      "global_load_dwordx4 a[228:231], v61, s[90:91] offset:128 nt\n"
      "v_mfma_f32_16x16x32_bf16 v[40:43], a[184:187], v[56:59], v[40:43]\n"
      "v_mfma_f32_16x16x32_bf16 v[44:47], a[188:191], v[56:59], v[44:47]\n"
      "s_waitcnt vmcnt(8)\n"
      "global_load_dwordx4 a[232:235], v61, s[90:91] offset:256 nt\n"
      "global_load_dwordx4 a[236:239], v61, s[90:91] offset:384 nt\n"
      "v_mfma_f32_16x16x32_bf16 v[48:51], a[192:195], v[0:3], 0\n"
      "v_mfma_f32_16x16x32_bf16 v[48:51], a[196:199], v[4:7], v[48:51]\n"
      "s_waitcnt vmcnt(8)\n"
      "global_load_dwordx4 a[240:243], v61, s[90:91] offset:2048 nt\n"
      "global_load_dwordx4 a[244:247], v61, s[90:91] offset:2176 nt\n"
      "v_mfma_f32_16x16x32_bf16 v[48:51], a[200:203], v[8:11], v[48:51]\n"
      "v_mfma_f32_16x16x32_bf16 v[48:51], a[204:207], v[12:15], v[48:51]\n"
      "s_waitcnt vmcnt(8)\n"
      "global_load_dwordx4 a[248:251], v61, s[90:91] offset:2304 nt\n"
      "global_load_dwordx4 a[252:255], v61, s[90:91] offset:2432 nt\n"
      "v_mfma_f32_16x16x32_bf16 v[48:51], a[208:211], v[16:19], v[48:51]\n"
      "v_mfma_f32_16x16x32_bf16 v[48:51], a[212:215], v[20:23], v[48:51]\n"
      "s_waitcnt vmcnt(8)\n"
      "TG_46_%=:\n"
      "s_memrealtime s[86:87]\n"
      "s_waitcnt lgkmcnt(0)\n"
      "s_lshl_b32 s86, s86, 4\n"
      "s_sub_u32 s86, s86, s93\n"
      "s_cmp_ge_i32 s86, 0\n"
      "s_cbranch_scc1 TGO_47_%=\n"
      "s_sleep 1\n"
      "s_branch TG_46_%=\n"
      "TGO_47_%=:\n"
      "s_add_u32 s93, s93, 1650\n"
      "s_add_u32 s86, s100, 0x310000\n"
      "s_cmp_ge_u32 s86, 0x3f0000\n"
      "s_cselect_b32 s87, 0x3f0000, 0\n"
      "s_sub_u32 s86, s86, s87\n"
      "s_add_u32 s90, s36, s86\n"
      "s_addc_u32 s91, s37, 0\n"
      "global_load_dwordx4 v[64:67], v60, s[90:91] offset:-4096 nt\n"
      "global_load_dwordx4 v[68:71], v60, s[90:91] offset:-3840 nt\n"
      "v_mfma_f32_16x16x32_bf16 v[48:51], a[216:219], v[24:27], v[48:51]\n"
      "v_mfma_f32_16x16x32_bf16 v[48:51], a[220:223], v[28:31], v[48:51]\n"
      "s_nop 7\n"
      "v_cndmask_b32_e64 v53, v48, v49, s[96:97]\n"
      "v_cndmask_b32_e64 v54, v50, v51, s[96:97]\n"
      "v_cndmask_b32_e64 v52, v53, v54, s[98:99]\n"
      "s_nop 1\n"
      "v_add_f32_dpp v52, v52, v52 quad_perm:[1,0,3,2] row_mask:0xf bank_mask:0xf\n"
      "s_nop 1\n"
      "v_add_f32_dpp v52, v52, v52 quad_perm:[2,3,0,1] row_mask:0xf bank_mask:0xf\n"
      "v_cmp_lt_f32_e32 vcc, s60, v52\n"
      "s_cbranch_vccz NORS_48_%=\n"
      "s_swappc_b64 s[66:67], s[68:69]\n"
      "NORS_48_%=:\n"
      "v_subrev_f32_e32 v52, s60, v52\n"
      "v_mul_f32_e32 v52, 0x3db8aa3b, v52\n"
      "v_exp_f32_e32 v52, v52\n"
      "s_nop 0\n"
      "v_cvt_pk_bf16_f32 v53, v52, v52\n"
      "v_add_f32_e32 v62, v62, v52\n"
      "v_and_b32_e32 v53, v53, v63\n"
      "s_nop 0\n"
      "v_add_f32_dpp v62, v52, v62 row_half_mirror row_mask:0xf bank_mask:0xf\n"
      "v_mov_b32_dpp v56, v52 row_half_mirror row_mask:0xf bank_mask:0xf\n"
      "v_cvt_pk_bf16_f32 v56, v56, v56\n"
      "v_and_b32_e32 v56, v56, v63\n"
      "v_cndmask_b32_e64 v55, v53, v56, s[50:51]\n"
      "v_cndmask_b32_e64 v59, v56, v53, s[50:51]\n"
      "v_cndmask_b32_e64 v52, 0, v55, s[52:53]\n"
      "v_cndmask_b32_e64 v53, 0, v55, s[54:55]\n"
      "v_cndmask_b32_e64 v54, 0, v55, s[56:57]\n"
      "v_cndmask_b32_e64 v55, 0, v55, s[58:59]\n"
      "v_cndmask_b32_e64 v56, 0, v59, s[52:53]\n"
      "v_cndmask_b32_e64 v57, 0, v59, s[54:55]\n"
      "v_cndmask_b32_e64 v58, 0, v59, s[56:57]\n"
      "v_cndmask_b32_e64 v59, 0, v59, s[58:59]\n"
      "s_waitcnt vmcnt(8)\n"
      "global_load_dwordx4 v[72:75], v60, s[90:91] offset:-2048 nt\n"
      "global_load_dwordx4 v[76:79], v60, s[90:91] offset:-1792 nt\n"
      "v_mfma_f32_16x16x32_bf16 v[32:35], a[224:227], v[52:55], v[32:35]\n"
      "v_mfma_f32_16x16x32_bf16 v[36:39], a[228:231], v[52:55], v[36:39]\n"
      "s_waitcnt vmcnt(8)\n"
      "global_load_dwordx4 v[80:83], v60, s[90:91] offset:0 nt\n"
      "global_load_dwordx4 v[84:87], v60, s[90:91] offset:256 nt\n"
      "v_mfma_f32_16x16x32_bf16 v[40:43], a[232:235], v[52:55], v[40:43]\n"
      "v_mfma_f32_16x16x32_bf16 v[44:47], a[236:239], v[52:55], v[44:47]\n"
      "s_waitcnt vmcnt(8)\n"
      "global_load_dwordx4 v[88:91], v60, s[90:91] offset:2048 nt\n"
      "global_load_dwordx4 v[92:95], v60, s[90:91] offset:2304 nt\n"
      "v_mfma_f32_16x16x32_bf16 v[32:35], a[240:243], v[56:59], v[32:35]\n"
      "v_mfma_f32_16x16x32_bf16 v[36:39], a[244:247], v[56:59], v[36:39]\n"
      "s_waitcnt vmcnt(8)\n"
      "TG_49_%=:\n"
      "s_memrealtime s[86:87]\n"
      "s_waitcnt lgkmcnt(0)\n"
      "s_lshl_b32 s86, s86, 4\n"
      "s_sub_u32 s86, s86, s93\n"
      "s_cmp_ge_i32 s86, 0\n"
      "s_cbranch_scc1 TGO_50_%=\n"
      "s_sleep 1\n"
      "s_branch TG_49_%=\n"
      "TGO_50_%=:\n"
      "s_add_u32 s93, s93, 1650\n"
      "s_add_u32 s86, s100, 0x310000\n"
      "s_cmp_ge_u32 s86, 0x3f0000\n"
      "s_cselect_b32 s87, 0x3f0000, 0\n"
      "s_sub_u32 s86, s86, s87\n"
      "s_add_u32 s90, s38, s86\n"
      "s_addc_u32 s91, s39, 0\n"
      "global_load_dwordx4 v[96:99], v61, s[90:91] offset:0 nt\n"
      "global_load_dwordx4 v[100:103], v61, s[90:91] offset:128 nt\n"
      "v_mfma_f32_16x16x32_bf16 v[40:43], a[248:251], v[56:59], v[40:43]\n"
      "v_mfma_f32_16x16x32_bf16 v[44:47], a[252:255], v[56:59], v[44:47]\n"
      "s_waitcnt vmcnt(8)\n"
      "global_load_dwordx4 v[104:107], v61, s[90:91] offset:256 nt\n"
      "global_load_dwordx4 v[108:111], v61, s[90:91] offset:384 nt\n"
      "v_mfma_f32_16x16x32_bf16 v[48:51], v[64:67], v[0:3], 0\n"
      "v_mfma_f32_16x16x32_bf16 v[48:51], v[68:71], v[4:7], v[48:51]\n"
      "s_waitcnt vmcnt(8)\n"
      "global_load_dwordx4 v[112:115], v61, s[90:91] offset:2048 nt\n"
      "global_load_dwordx4 v[116:119], v61, s[90:91] offset:2176 nt\n"
      "v_mfma_f32_16x16x32_bf16 v[48:51], v[72:75], v[8:11], v[48:51]\n"
      "v_mfma_f32_16x16x32_bf16 v[48:51], v[76:79], v[12:15], v[48:51]\n"
      "s_waitcnt vmcnt(8)\n"
      "global_load_dwordx4 v[120:123], v61, s[90:91] offset:2304 nt\n"
      "global_load_dwordx4 v[124:127], v61, s[90:91] offset:2432 nt\n"
      "v_mfma_f32_16x16x32_bf16 v[48:51], v[80:83], v[16:19], v[48:51]\n"
      "v_mfma_f32_16x16x32_bf16 v[48:51], v[84:87], v[20:23], v[48:51]\n"
      "s_waitcnt vmcnt(8)\n"
      "TG_51_%=:\n"
      "s_memrealtime s[86:87]\n"
      "s_waitcnt lgkmcnt(0)\n"
      "s_lshl_b32 s86, s86, 4\n"
      "s_sub_u32 s86, s86, s93\n"
      "s_cmp_ge_i32 s86, 0\n"
      "s_cbranch_scc1 TGO_52_%=\n"
      "s_sleep 1\n"
      "s_branch TG_51_%=\n"
      "TGO_52_%=:\n"
      "s_add_u32 s93, s93, 1650\n"
      "s_add_u32 s86, s100, 0x380000\n"
      "s_cmp_ge_u32 s86, 0x3f0000\n"
      "s_cselect_b32 s87, 0x3f0000, 0\n"
      "s_sub_u32 s86, s86, s87\n"
      "s_add_u32 s90, s36, s86\n"
      "s_addc_u32 s91, s37, 0\n"
      "global_load_dwordx4 v[128:131], v60, s[90:91] offset:-4096 \n"
      "global_load_dwordx4 v[132:135], v60, s[90:91] offset:-3840 \n"
      "v_mfma_f32_16x16x32_bf16 v[48:51], v[88:91], v[24:27], v[48:51]\n"
      "v_mfma_f32_16x16x32_bf16 v[48:51], v[92:95], v[28:31], v[48:51]\n"
      "s_nop 7\n"
      "v_cndmask_b32_e64 v53, v48, v49, s[96:97]\n"
      "v_cndmask_b32_e64 v54, v50, v51, s[96:97]\n"
      "v_cndmask_b32_e64 v52, v53, v54, s[98:99]\n"
      "s_nop 1\n"
      "v_add_f32_dpp v52, v52, v52 quad_perm:[1,0,3,2] row_mask:0xf bank_mask:0xf\n"
      "s_nop 1\n"
      "v_add_f32_dpp v52, v52, v52 quad_perm:[2,3,0,1] row_mask:0xf bank_mask:0xf\n"
      "v_cmp_lt_f32_e32 vcc, s60, v52\n"
      "s_cbranch_vccz NORS_53_%=\n"
      "s_swappc_b64 s[66:67], s[68:69]\n"
      "NORS_53_%=:\n"
      "v_subrev_f32_e32 v52, s60, v52\n"
      "v_mul_f32_e32 v52, 0x3db8aa3b, v52\n"
      "v_exp_f32_e32 v52, v52\n"
      "s_nop 0\n"
      "v_cvt_pk_bf16_f32 v53, v52, v52\n"
      "v_add_f32_e32 v62, v62, v52\n"
      "v_and_b32_e32 v53, v53, v63\n"
      "s_nop 0\n"
      "v_add_f32_dpp v62, v52, v62 row_half_mirror row_mask:0xf bank_mask:0xf\n"
      "v_mov_b32_dpp v56, v52 row_half_mirror row_mask:0xf bank_mask:0xf\n"
      "v_cvt_pk_bf16_f32 v56, v56, v56\n"
      "v_and_b32_e32 v56, v56, v63\n"
      "v_cndmask_b32_e64 v55, v53, v56, s[50:51]\n"
      "v_cndmask_b32_e64 v59, v56, v53, s[50:51]\n"
      "v_cndmask_b32_e64 v52, 0, v55, s[52:53]\n"
      "v_cndmask_b32_e64 v53, 0, v55, s[54:55]\n"
      "v_cndmask_b32_e64 v54, 0, v55, s[56:57]\n"
      "v_cndmask_b32_e64 v55, 0, v55, s[58:59]\n"
      "v_cndmask_b32_e64 v56, 0, v59, s[52:53]\n"
      "v_cndmask_b32_e64 v57, 0, v59, s[54:55]\n"
      "v_cndmask_b32_e64 v58, 0, v59, s[56:57]\n"
      "v_cndmask_b32_e64 v59, 0, v59, s[58:59]\n"
      "s_waitcnt vmcnt(8)\n"
      "global_load_dwordx4 v[136:139], v60, s[90:91] offset:-2048 \n"
      "global_load_dwordx4 v[140:143], v60, s[90:91] offset:-1792 \n"
      "v_mfma_f32_16x16x32_bf16 v[32:35], v[96:99], v[52:55], v[32:35]\n"
      "v_mfma_f32_16x16x32_bf16 v[36:39], v[100:103], v[52:55], v[36:39]\n"
      "s_waitcnt vmcnt(8)\n"
      "global_load_dwordx4 v[144:147], v60, s[90:91] offset:0 \n"
      "global_load_dwordx4 v[148:151], v60, s[90:91] offset:256 \n"
      "v_mfma_f32_16x16x32_bf16 v[40:43], v[104:107], v[52:55], v[40:43]\n"
      "v_mfma_f32_16x16x32_bf16 v[44:47], v[108:111], v[52:55], v[44:47]\n"
      "s_waitcnt vmcnt(8)\n"
      "global_load_dwordx4 v[152:155], v60, s[90:91] offset:2048 \n"
      "global_load_dwordx4 v[156:159], v60, s[90:91] offset:2304 \n"
      "v_mfma_f32_16x16x32_bf16 v[32:35], v[112:115], v[56:59], v[32:35]\n"
      "v_mfma_f32_16x16x32_bf16 v[36:39], v[116:119], v[56:59], v[36:39]\n"
      "s_waitcnt vmcnt(8)\n"
      "TG_54_%=:\n"
      "s_memrealtime s[86:87]\n"
      "s_waitcnt lgkmcnt(0)\n"
      "s_lshl_b32 s86, s86, 4\n"
      "s_sub_u32 s86, s86, s93\n"
      "s_cmp_ge_i32 s86, 0\n"
      "s_cbranch_scc1 TGO_55_%=\n"
      "s_sleep 1\n"
      "s_branch TG_54_%=\n"
      "TGO_55_%=:\n"
      "s_add_u32 s93, s93, 1650\n"
      "s_add_u32 s86, s100, 0x380000\n"
      "s_cmp_ge_u32 s86, 0x3f0000\n"
      "s_cselect_b32 s87, 0x3f0000, 0\n"
      "s_sub_u32 s86, s86, s87\n"
      "s_add_u32 s90, s38, s86\n"
      "s_addc_u32 s91, s39, 0\n"
      "global_load_dwordx4 v[160:163], v61, s[90:91] offset:0 \n"
      "global_load_dwordx4 v[164:167], v61, s[90:91] offset:128 \n"
      "v_mfma_f32_16x16x32_bf16 v[40:43], v[120:123], v[56:59], v[40:43]\n"
      "v_mfma_f32_16x16x32_bf16 v[44:47], v[124:127], v[56:59], v[44:47]\n"
      "s_waitcnt vmcnt(9)\n"
      "s_waitcnt vmcnt(8)\n"
      "global_load_dwordx4 v[168:171], v61, s[90:91] offset:256 \n"
      "global_load_dwordx4 v[172:175], v61, s[90:91] offset:384 \n"
      "v_mfma_f32_16x16x32_bf16 v[48:51], v[128:131], v[0:3], 0\n"
      "v_mfma_f32_16x16x32_bf16 v[48:51], v[132:135], v[4:7], v[48:51]\n"
      "s_waitcnt vmcnt(8)\n"
      "global_load_dwordx4 v[176:179], v61, s[90:91] offset:2048 \n"
      "global_load_dwordx4 v[180:183], v61, s[90:91] offset:2176 \n"
      "v_mfma_f32_16x16x32_bf16 v[48:51], v[136:139], v[8:11], v[48:51]\n"
      "v_mfma_f32_16x16x32_bf16 v[48:51], v[140:143], v[12:15], v[48:51]\n"
      "s_waitcnt vmcnt(8)\n"
      "global_load_dwordx4 v[184:187], v61, s[90:91] offset:2304 \n"
      "global_load_dwordx4 v[188:191], v61, s[90:91] offset:2432 \n"
      "v_mfma_f32_16x16x32_bf16 v[48:51], v[144:147], v[16:19], v[48:51]\n"
      "v_mfma_f32_16x16x32_bf16 v[48:51], v[148:151], v[20:23], v[48:51]\n"
      "s_waitcnt vmcnt(8)\n"
      "v_mfma_f32_16x16x32_bf16 v[48:51], v[152:155], v[24:27], v[48:51]\n"
      "v_mfma_f32_16x16x32_bf16 v[48:51], v[156:159], v[28:31], v[48:51]\n"
      "s_nop 7\n"
      "v_cndmask_b32_e64 v53, v48, v49, s[96:97]\n"
      "v_cndmask_b32_e64 v54, v50, v51, s[96:97]\n"
      "v_cndmask_b32_e64 v52, v53, v54, s[98:99]\n"
      "s_nop 1\n"
      "v_add_f32_dpp v52, v52, v52 quad_perm:[1,0,3,2] row_mask:0xf bank_mask:0xf\n"
      "s_nop 1\n"
      "v_add_f32_dpp v52, v52, v52 quad_perm:[2,3,0,1] row_mask:0xf bank_mask:0xf\n"
      "v_cmp_lt_f32_e32 vcc, s60, v52\n"
      "s_cbranch_vccz NORS_56_%=\n"
      "s_swappc_b64 s[66:67], s[68:69]\n"
      "NORS_56_%=:\n"
      "v_subrev_f32_e32 v52, s60, v52\n"
      "v_mul_f32_e32 v52, 0x3db8aa3b, v52\n"
      "v_exp_f32_e32 v52, v52\n"
      "s_nop 0\n"
      "v_cvt_pk_bf16_f32 v53, v52, v52\n"
      "v_add_f32_e32 v62, v62, v52\n"
      "v_and_b32_e32 v53, v53, v63\n"
      "s_nop 0\n"
      "v_add_f32_dpp v62, v52, v62 row_half_mirror row_mask:0xf bank_mask:0xf\n"
      "v_mov_b32_dpp v56, v52 row_half_mirror row_mask:0xf bank_mask:0xf\n"
      "v_cvt_pk_bf16_f32 v56, v56, v56\n"
      "v_and_b32_e32 v56, v56, v63\n"
      "v_cndmask_b32_e64 v55, v53, v56, s[50:51]\n"
      "v_cndmask_b32_e64 v59, v56, v53, s[50:51]\n"
      "v_cndmask_b32_e64 v52, 0, v55, s[52:53]\n"
      "v_cndmask_b32_e64 v53, 0, v55, s[54:55]\n"
      "v_cndmask_b32_e64 v54, 0, v55, s[56:57]\n"
      "v_cndmask_b32_e64 v55, 0, v55, s[58:59]\n"
      "v_cndmask_b32_e64 v56, 0, v59, s[52:53]\n"
      "v_cndmask_b32_e64 v57, 0, v59, s[54:55]\n"
      "v_cndmask_b32_e64 v58, 0, v59, s[56:57]\n"
      "v_cndmask_b32_e64 v59, 0, v59, s[58:59]\n"
      "s_waitcnt vmcnt(6)\n"
      "v_mfma_f32_16x16x32_bf16 v[32:35], v[160:163], v[52:55], v[32:35]\n"
      "v_mfma_f32_16x16x32_bf16 v[36:39], v[164:167], v[52:55], v[36:39]\n"
      "s_waitcnt vmcnt(4)\n"
      "v_mfma_f32_16x16x32_bf16 v[40:43], v[168:171], v[52:55], v[40:43]\n"
      "v_mfma_f32_16x16x32_bf16 v[44:47], v[172:175], v[52:55], v[44:47]\n"
      "s_waitcnt vmcnt(2)\n"
      "v_mfma_f32_16x16x32_bf16 v[32:35], v[176:179], v[56:59], v[32:35]\n"
      "v_mfma_f32_16x16x32_bf16 v[36:39], v[180:183], v[56:59], v[36:39]\n"
      "s_waitcnt vmcnt(0)\n"
      "v_mfma_f32_16x16x32_bf16 v[40:43], v[184:187], v[56:59], v[40:43]\n"
      "v_mfma_f32_16x16x32_bf16 v[44:47], v[188:191], v[56:59], v[44:47]\n"
      "s_add_u32 s74, s44, 0x0\n"
      "s_addc_u32 s75, s45, 0\n"
      "s_add_u32 s76, s46, 0x0\n"
      "s_addc_u32 s77, s47, 0\n"
      "s_mov_b32 s72, 0x24000\n"
      "s_mov_b32 s73, 0x260a0\n"
      "s_nop 1\n"
      "s_swappc_b64 s[66:67], s[70:71]\n"
      "v_mbcnt_lo_u32_b32 v53, -1, 0\n"
      "v_mbcnt_hi_u32_b32 v53, -1, v53\n"
      "v_lshrrev_b32_e32 v52, 4, v53\n"
      "v_lshlrev_b32_e32 v52, 6, v52\n"
      "v_and_b32_e32 v53, 3, v53\n"
      "v_lshl_add_u32 v52, v53, 4, v52\n"
      "v_add_u32_e32 v52, 0x26a40, v52\n"
      "s_mov_b32 vcc_lo, 0xf000f\n"
      "s_mov_b32 vcc_hi, 0xf000f\n"
      "v_mov_b32_e32 v53, 0x26120\n"
      "v_cndmask_b32_e32 v53, v53, v52, vcc\n"
      "ds_read_b128 v[0:3], v53 offset:0\n"
      "ds_read_b128 v[4:7], v53 offset:256\n"
      "s_mov_b32 vcc_lo, 0xf000f0\n"
      "s_mov_b32 vcc_hi, 0xf000f0\n"
      "v_mov_b32_e32 v53, 0x26120\n"
      "v_cndmask_b32_e32 v53, v53, v52, vcc\n"
      "ds_read_b128 v[8:11], v53 offset:0\n"
      "ds_read_b128 v[12:15], v53 offset:256\n"
      "s_mov_b32 vcc_lo, 0xf000f00\n"
      "s_mov_b32 vcc_hi, 0xf000f00\n"
      "v_mov_b32_e32 v53, 0x26120\n"
      "v_cndmask_b32_e32 v53, v53, v52, vcc\n"
      "ds_read_b128 v[16:19], v53 offset:0\n"
      "ds_read_b128 v[20:23], v53 offset:256\n"
      "s_mov_b32 vcc_lo, 0xf000f000\n"
      "s_mov_b32 vcc_hi, 0xf000f000\n"
      "v_mov_b32_e32 v53, 0x26120\n"
      "v_cndmask_b32_e32 v53, v53, v52, vcc\n"
      "ds_read_b128 v[24:27], v53 offset:0\n"
      "ds_read_b128 v[28:31], v53 offset:256\n"
      "s_waitcnt lgkmcnt(0)\n"
      "v_mov_b32_e32 v32, 0\n"
      "v_mov_b32_e32 v33, 0\n"
      "v_mov_b32_e32 v34, 0\n"
      "v_mov_b32_e32 v35, 0\n"
      "v_mov_b32_e32 v36, 0\n"
      "v_mov_b32_e32 v37, 0\n"
      "v_mov_b32_e32 v38, 0\n"
      "v_mov_b32_e32 v39, 0\n"
      "v_mov_b32_e32 v40, 0\n"
      "v_mov_b32_e32 v41, 0\n"
      "v_mov_b32_e32 v42, 0\n"
      "v_mov_b32_e32 v43, 0\n"
      "v_mov_b32_e32 v44, 0\n"
      "v_mov_b32_e32 v45, 0\n"
      "v_mov_b32_e32 v46, 0\n"
      "v_mov_b32_e32 v47, 0\n"
      "v_mov_b32_e32 v62, 0\n"
      "s_mov_b32 s60, 0xff800000\n"
      "s_mov_b32 s61, 1\n"
      "s_mov_b32 s63, 0x4070000\n"
      "s_mov_b32 s94, 1700\n"
      "s_add_u32 s93, s93, 3000\n"
      "TG_57_%=:\n"
      "s_memrealtime s[86:87]\n"
      "s_waitcnt lgkmcnt(0)\n"
      "s_lshl_b32 s86, s86, 4\n"
      "s_sub_u32 s86, s86, s93\n"
      "s_cmp_ge_i32 s86, 0\n"
      "s_cbranch_scc1 TGO_58_%=\n"
      "s_sleep 1\n"
      "s_branch TG_57_%=\n"
      "TGO_58_%=:\n"
      "s_add_u32 s93, s93, s94\n"
      "s_add_u32 s86, s100, 0x4000000\n"
      "s_cmp_ge_u32 s86, 0x43f0000\n"
      "s_cselect_b32 s87, 0x3f0000, 0\n"
      "s_sub_u32 s86, s86, s87\n"
      "s_add_u32 s90, s36, s86\n"
      "s_addc_u32 s91, s37, 0\n"
      "global_load_dwordx4 v[128:131], v60, s[90:91] offset:-4096 \n"
      "global_load_dwordx4 v[132:135], v60, s[90:91] offset:-3840 \n"
      "global_load_dwordx4 v[136:139], v60, s[90:91] offset:-2048 \n"
      "global_load_dwordx4 v[140:143], v60, s[90:91] offset:-1792 \n"
      "global_load_dwordx4 v[144:147], v60, s[90:91] offset:0 \n"
      "global_load_dwordx4 v[148:151], v60, s[90:91] offset:256 \n"
      "global_load_dwordx4 v[152:155], v60, s[90:91] offset:2048 \n"
      "global_load_dwordx4 v[156:159], v60, s[90:91] offset:2304 \n"
      "TG_59_%=:\n"
      "s_memrealtime s[86:87]\n"
      "s_waitcnt lgkmcnt(0)\n"
      "s_lshl_b32 s86, s86, 4\n"
      "s_sub_u32 s86, s86, s93\n"
      "s_cmp_ge_i32 s86, 0\n"
      "s_cbranch_scc1 TGO_60_%=\n"
      "s_sleep 1\n"
      "s_branch TG_59_%=\n"
      "TGO_60_%=:\n"
      "s_add_u32 s93, s93, s94\n"
      "s_add_u32 s86, s100, 0x4000000\n"
      "s_cmp_ge_u32 s86, 0x43f0000\n"
      "s_cselect_b32 s87, 0x3f0000, 0\n"
      "s_sub_u32 s86, s86, s87\n"
      "s_add_u32 s90, s38, s86\n"
      "s_addc_u32 s91, s39, 0\n"
      "global_load_dwordx4 v[160:163], v61, s[90:91] offset:0 \n"
      "global_load_dwordx4 v[164:167], v61, s[90:91] offset:128 \n"
      "s_waitcnt vmcnt(8)\n"
      "global_load_dwordx4 v[168:171], v61, s[90:91] offset:256 \n"
      "global_load_dwordx4 v[172:175], v61, s[90:91] offset:384 \n"
      "v_mfma_f32_16x16x32_bf16 v[48:51], v[128:131], v[0:3], 0\n"
      "v_mfma_f32_16x16x32_bf16 v[48:51], v[132:135], v[4:7], v[48:51]\n"
      "s_waitcnt vmcnt(8)\n"
      "global_load_dwordx4 v[176:179], v61, s[90:91] offset:2048 \n"
      "global_load_dwordx4 v[180:183], v61, s[90:91] offset:2176 \n"
      "v_mfma_f32_16x16x32_bf16 v[48:51], v[136:139], v[8:11], v[48:51]\n"
      "v_mfma_f32_16x16x32_bf16 v[48:51], v[140:143], v[12:15], v[48:51]\n"
      "s_waitcnt vmcnt(8)\n"
      "global_load_dwordx4 v[184:187], v61, s[90:91] offset:2304 \n"
      "global_load_dwordx4 v[188:191], v61, s[90:91] offset:2432 \n"
      "v_mfma_f32_16x16x32_bf16 v[48:51], v[144:147], v[16:19], v[48:51]\n"
      "v_mfma_f32_16x16x32_bf16 v[48:51], v[148:151], v[20:23], v[48:51]\n"
      "s_waitcnt vmcnt(8)\n"
      "TG_61_%=:\n"
      "s_memrealtime s[86:87]\n"
      "s_waitcnt lgkmcnt(0)\n"
      "s_lshl_b32 s86, s86, 4\n"
      "s_sub_u32 s86, s86, s93\n"
      "s_cmp_ge_i32 s86, 0\n"
      "s_cbranch_scc1 TGO_62_%=\n"
      "s_sleep 1\n"
      "s_branch TG_61_%=\n"
      "TGO_62_%=:\n"
      "s_add_u32 s93, s93, s94\n"
      "s_add_u32 s86, s100, 0x4070000\n"
      "s_cmp_ge_u32 s86, 0x43f0000\n"
      "s_cselect_b32 s87, 0x3f0000, 0\n"
      "s_sub_u32 s86, s86, s87\n"
      "s_add_u32 s90, s36, s86\n"
      "s_addc_u32 s91, s37, 0\n"
      "global_load_dwordx4 v[128:131], v60, s[90:91] offset:-4096 nt\n"
      "global_load_dwordx4 v[132:135], v60, s[90:91] offset:-3840 nt\n"
      "v_mfma_f32_16x16x32_bf16 v[48:51], v[152:155], v[24:27], v[48:51]\n"
      "v_mfma_f32_16x16x32_bf16 v[48:51], v[156:159], v[28:31], v[48:51]\n"
      "s_nop 7\n"
      "v_cndmask_b32_e64 v53, v48, v49, s[96:97]\n"
      "v_cndmask_b32_e64 v54, v50, v51, s[96:97]\n"
      "v_cndmask_b32_e64 v52, v53, v54, s[98:99]\n"
      "s_nop 1\n"
      "v_add_f32_dpp v52, v52, v52 quad_perm:[1,0,3,2] row_mask:0xf bank_mask:0xf\n"
      "s_nop 1\n"
      "v_add_f32_dpp v52, v52, v52 quad_perm:[2,3,0,1] row_mask:0xf bank_mask:0xf\n"
      "v_cmp_lt_f32_e32 vcc, s60, v52\n"
      "s_cbranch_vccz NORS_63_%=\n"
      "s_swappc_b64 s[66:67], s[68:69]\n"
      "NORS_63_%=:\n"
      "v_subrev_f32_e32 v52, s60, v52\n"
      "v_mul_f32_e32 v52, 0x3db8aa3b, v52\n"
      "v_exp_f32_e32 v52, v52\n"
      "s_nop 0\n"
      "v_cvt_pk_bf16_f32 v53, v52, v52\n"
      "v_add_f32_e32 v62, v62, v52\n"
      "v_and_b32_e32 v53, v53, v63\n"
      "s_nop 0\n"
      "v_add_f32_dpp v62, v52, v62 row_half_mirror row_mask:0xf bank_mask:0xf\n"
      "v_mov_b32_dpp v56, v52 row_half_mirror row_mask:0xf bank_mask:0xf\n"
      "v_cvt_pk_bf16_f32 v56, v56, v56\n"
      "v_and_b32_e32 v56, v56, v63\n"
      "v_cndmask_b32_e64 v55, v53, v56, s[50:51]\n"
      "v_cndmask_b32_e64 v59, v56, v53, s[50:51]\n"
      "v_cndmask_b32_e64 v52, 0, v55, s[52:53]\n"
      "v_cndmask_b32_e64 v53, 0, v55, s[54:55]\n"
      "v_cndmask_b32_e64 v54, 0, v55, s[56:57]\n"
      "v_cndmask_b32_e64 v55, 0, v55, s[58:59]\n"
      "v_cndmask_b32_e64 v56, 0, v59, s[52:53]\n"
      "v_cndmask_b32_e64 v57, 0, v59, s[54:55]\n"
      "v_cndmask_b32_e64 v58, 0, v59, s[56:57]\n"
      "v_cndmask_b32_e64 v59, 0, v59, s[58:59]\n"
      "s_waitcnt vmcnt(8)\n"
      "global_load_dwordx4 v[136:139], v60, s[90:91] offset:-2048 nt\n"
      "global_load_dwordx4 v[140:143], v60, s[90:91] offset:-1792 nt\n"
      "v_mfma_f32_16x16x32_bf16 v[32:35], v[160:163], v[52:55], v[32:35]\n"
      "v_mfma_f32_16x16x32_bf16 v[36:39], v[164:167], v[52:55], v[36:39]\n"
      "s_waitcnt vmcnt(8)\n"
      "global_load_dwordx4 v[144:147], v60, s[90:91] offset:0 nt\n"
      "global_load_dwordx4 v[148:151], v60, s[90:91] offset:256 nt\n"
      "v_mfma_f32_16x16x32_bf16 v[40:43], v[168:171], v[52:55], v[40:43]\n"
      "v_mfma_f32_16x16x32_bf16 v[44:47], v[172:175], v[52:55], v[44:47]\n"
      "s_waitcnt vmcnt(8)\n"
      "global_load_dwordx4 v[152:155], v60, s[90:91] offset:2048 nt\n"
      "global_load_dwordx4 v[156:159], v60, s[90:91] offset:2304 nt\n"
      "v_mfma_f32_16x16x32_bf16 v[32:35], v[176:179], v[56:59], v[32:35]\n"
      "v_mfma_f32_16x16x32_bf16 v[36:39], v[180:183], v[56:59], v[36:39]\n"
      "s_waitcnt vmcnt(8)\n"
      "TG_64_%=:\n"
      "s_memrealtime s[86:87]\n"
      "s_waitcnt lgkmcnt(0)\n"
      "s_lshl_b32 s86, s86, 4\n"
      "s_sub_u32 s86, s86, s93\n"
      "s_cmp_ge_i32 s86, 0\n"
      "s_cbranch_scc1 TGO_65_%=\n"
      "s_sleep 1\n"
      "s_branch TG_64_%=\n"
      "TGO_65_%=:\n"
      "s_add_u32 s93, s93, s94\n"
      "s_add_u32 s86, s100, 0x4070000\n"
      "s_cmp_ge_u32 s86, 0x43f0000\n"
      "s_cselect_b32 s87, 0x3f0000, 0\n"
      "s_sub_u32 s86, s86, s87\n"
      "s_add_u32 s90, s38, s86\n"
      "s_addc_u32 s91, s39, 0\n"
      "global_load_dwordx4 v[160:163], v61, s[90:91] offset:0 nt\n"
      "global_load_dwordx4 v[164:167], v61, s[90:91] offset:128 nt\n"
      "v_mfma_f32_16x16x32_bf16 v[40:43], v[184:187], v[56:59], v[40:43]\n"
      "v_mfma_f32_16x16x32_bf16 v[44:47], v[188:191], v[56:59], v[44:47]\n"
      "s_nop 7\n"
      "LOOP_66_%=:\n"
      "s_cmp_lt_u32 s61, 3\n"
      "s_nop 0\n"
      "s_cbranch_scc1 CONT_69_%=\n"
      "v_mbcnt_lo_u32_b32 v52, -1, 0\n"
      "v_mbcnt_hi_u32_b32 v52, -1, v52\n"
      "v_lshlrev_b32_e32 v52, 4, v52\n"
      "v_add_u32_e32 v52, 0x27240, v52\n"
      "v_mov_b32_e32 v53, 0x260f0\n"
      "ds_read_b128 v[48:51], v52\n"
      "ds_read_b32 v53, v53\n"
      "s_waitcnt lgkmcnt(0)\n"
      "v_cmp_ne_u32_e64 s[80:81], s62, v49\n"
      "v_cmp_ne_u32_e64 s[82:83], s62, v51\n"
      "v_cmp_eq_u32_e32 vcc, 1, v53\n"
      "s_nop 4\n"
      "s_cbranch_vccnz SWITCH_67_%=\n"
      "s_or_b64 s[80:81], s[80:81], s[82:83]\n"
      "s_cmp_eq_u64 s[80:81], 0\n"
      "s_nop 0\n"
      "s_cbranch_scc0 QN_70_%=\n"
      "v_mov_b32_e32 v49, v50\n"
      "v_lshrrev_b32_e32 v52, 1, v52\n"
      "v_add_u32_e32 v52, 0x12f20, v52\n"
      "ds_write_b64 v52, v[48:49]\n"
      "v_mov_b32_e32 v52, 0x260f0\n"
      "v_mov_b32_e32 v53, 1\n"
      "ds_write_b32 v52, v53\n"
      "s_waitcnt lgkmcnt(0)\n"
      "s_nop 2\n"
      "s_branch SWITCH_67_%=\n"
      "s_nop 0\n"
      "QN_70_%=:\n"
      "s_cmp_ge_u32 s61, 4\n"
      "s_nop 0\n"
      "s_cbranch_scc1 BLOCK_68_%=\n"
      "s_nop 0\n"
      "CONT_69_%=:\n"
      "s_waitcnt vmcnt(8)\n"
      "global_load_dwordx4 v[168:171], v61, s[90:91] offset:256 nt\n"
      "global_load_dwordx4 v[172:175], v61, s[90:91] offset:384 nt\n"
      "v_mfma_f32_16x16x32_bf16 v[48:51], v[128:131], v[0:3], 0\n"
      "v_mfma_f32_16x16x32_bf16 v[48:51], v[132:135], v[4:7], v[48:51]\n"
      "s_waitcnt vmcnt(8)\n"
      "global_load_dwordx4 v[176:179], v61, s[90:91] offset:2048 nt\n"
      "global_load_dwordx4 v[180:183], v61, s[90:91] offset:2176 nt\n"
      "v_mfma_f32_16x16x32_bf16 v[48:51], v[136:139], v[8:11], v[48:51]\n"
      "v_mfma_f32_16x16x32_bf16 v[48:51], v[140:143], v[12:15], v[48:51]\n"
      "s_waitcnt vmcnt(8)\n"
      "global_load_dwordx4 v[184:187], v61, s[90:91] offset:2304 nt\n"
      "global_load_dwordx4 v[188:191], v61, s[90:91] offset:2432 nt\n"
      "v_mfma_f32_16x16x32_bf16 v[48:51], v[144:147], v[16:19], v[48:51]\n"
      "v_mfma_f32_16x16x32_bf16 v[48:51], v[148:151], v[20:23], v[48:51]\n"
      "s_waitcnt vmcnt(8)\n"
      "TG_71_%=:\n"
      "s_memrealtime s[86:87]\n"
      "s_waitcnt lgkmcnt(0)\n"
      "s_lshl_b32 s86, s86, 4\n"
      "s_sub_u32 s86, s86, s93\n"
      "s_cmp_ge_i32 s86, 0\n"
      "s_cbranch_scc1 TGO_72_%=\n"
      "s_sleep 1\n"
      "s_branch TG_71_%=\n"
      "TGO_72_%=:\n"
      "s_add_u32 s93, s93, s94\n"
      "s_add_u32 s63, s63, 0x70000\n"
      "s_add_u32 s86, s100, s63\n"
      "s_cmp_ge_u32 s86, 0x43f0000\n"
      "s_cselect_b32 s87, 0x3f0000, 0\n"
      "s_sub_u32 s86, s86, s87\n"
      "s_add_u32 s90, s36, s86\n"
      "s_addc_u32 s91, s37, 0\n"
      "global_load_dwordx4 v[128:131], v60, s[90:91] offset:-4096 nt\n"
      "global_load_dwordx4 v[132:135], v60, s[90:91] offset:-3840 nt\n"
      "v_mfma_f32_16x16x32_bf16 v[48:51], v[152:155], v[24:27], v[48:51]\n"
      "v_mfma_f32_16x16x32_bf16 v[48:51], v[156:159], v[28:31], v[48:51]\n"
      "s_nop 7\n"
      "v_cndmask_b32_e64 v53, v48, v49, s[96:97]\n"
      "v_cndmask_b32_e64 v54, v50, v51, s[96:97]\n"
      "v_cndmask_b32_e64 v52, v53, v54, s[98:99]\n"
      "s_nop 1\n"
      "v_add_f32_dpp v52, v52, v52 quad_perm:[1,0,3,2] row_mask:0xf bank_mask:0xf\n"
      "s_nop 1\n"
      "v_add_f32_dpp v52, v52, v52 quad_perm:[2,3,0,1] row_mask:0xf bank_mask:0xf\n"
      "v_cmp_lt_f32_e32 vcc, s60, v52\n"
      "s_cbranch_vccz NORS_73_%=\n"
      "s_swappc_b64 s[66:67], s[68:69]\n"
      "NORS_73_%=:\n"
      "v_subrev_f32_e32 v52, s60, v52\n"
      "v_mul_f32_e32 v52, 0x3db8aa3b, v52\n"
      "v_exp_f32_e32 v52, v52\n"
      "s_nop 0\n"
      "v_cvt_pk_bf16_f32 v53, v52, v52\n"
      "v_add_f32_e32 v62, v62, v52\n"
      "v_and_b32_e32 v53, v53, v63\n"
      "s_nop 0\n"
      "v_add_f32_dpp v62, v52, v62 row_half_mirror row_mask:0xf bank_mask:0xf\n"
      "v_mov_b32_dpp v56, v52 row_half_mirror row_mask:0xf bank_mask:0xf\n"
      "v_cvt_pk_bf16_f32 v56, v56, v56\n"
      "v_and_b32_e32 v56, v56, v63\n"
      "v_cndmask_b32_e64 v55, v53, v56, s[50:51]\n"
      "v_cndmask_b32_e64 v59, v56, v53, s[50:51]\n"
      "v_cndmask_b32_e64 v52, 0, v55, s[52:53]\n"
      "v_cndmask_b32_e64 v53, 0, v55, s[54:55]\n"
      "v_cndmask_b32_e64 v54, 0, v55, s[56:57]\n"
      "v_cndmask_b32_e64 v55, 0, v55, s[58:59]\n"
      "v_cndmask_b32_e64 v56, 0, v59, s[52:53]\n"
      "v_cndmask_b32_e64 v57, 0, v59, s[54:55]\n"
      "v_cndmask_b32_e64 v58, 0, v59, s[56:57]\n"
      "v_cndmask_b32_e64 v59, 0, v59, s[58:59]\n"
      "s_add_u32 s64, s42, 0x20000\n"
      "s_addc_u32 s65, s43, 0\n"
      "s_mov_b32 m0, 0x27240\n"
      "s_nop 0\n"
      "global_load_lds_dwordx4 v61, s[64:65] sc1\n"
      "s_waitcnt vmcnt(8)\n"
      "global_load_dwordx4 v[136:139], v60, s[90:91] offset:-2048 nt\n"
      "global_load_dwordx4 v[140:143], v60, s[90:91] offset:-1792 nt\n"
      "v_mfma_f32_16x16x32_bf16 v[32:35], v[160:163], v[52:55], v[32:35]\n"
      "v_mfma_f32_16x16x32_bf16 v[36:39], v[164:167], v[52:55], v[36:39]\n"
      "s_waitcnt vmcnt(8)\n"
      "global_load_dwordx4 v[144:147], v60, s[90:91] offset:0 nt\n"
      "global_load_dwordx4 v[148:151], v60, s[90:91] offset:256 nt\n"
      "v_mfma_f32_16x16x32_bf16 v[40:43], v[168:171], v[52:55], v[40:43]\n"
      "v_mfma_f32_16x16x32_bf16 v[44:47], v[172:175], v[52:55], v[44:47]\n"
      "s_waitcnt vmcnt(8)\n"
      "global_load_dwordx4 v[152:155], v60, s[90:91] offset:2048 nt\n"
      "global_load_dwordx4 v[156:159], v60, s[90:91] offset:2304 nt\n"
      "v_mfma_f32_16x16x32_bf16 v[32:35], v[176:179], v[56:59], v[32:35]\n"
      "v_mfma_f32_16x16x32_bf16 v[36:39], v[180:183], v[56:59], v[36:39]\n"
      "s_waitcnt vmcnt(8)\n"
      "TG_74_%=:\n"
      "s_memrealtime s[86:87]\n"
      "s_waitcnt lgkmcnt(0)\n"
      "s_lshl_b32 s86, s86, 4\n"
      "s_sub_u32 s86, s86, s93\n"
      "s_cmp_ge_i32 s86, 0\n"
      "s_cbranch_scc1 TGO_75_%=\n"
      "s_sleep 1\n"
      "s_branch TG_74_%=\n"
      "TGO_75_%=:\n"
      "s_add_u32 s93, s93, s94\n"
      "s_add_u32 s86, s100, s63\n"
      "s_cmp_ge_u32 s86, 0x43f0000\n"
      "s_cselect_b32 s87, 0x3f0000, 0\n"
      "s_sub_u32 s86, s86, s87\n"
      "s_add_u32 s90, s38, s86\n"
      "s_addc_u32 s91, s39, 0\n"
      "global_load_dwordx4 v[160:163], v61, s[90:91] offset:0 nt\n"
      "global_load_dwordx4 v[164:167], v61, s[90:91] offset:128 nt\n"
      "v_mfma_f32_16x16x32_bf16 v[40:43], v[184:187], v[56:59], v[40:43]\n"
      "v_mfma_f32_16x16x32_bf16 v[44:47], v[188:191], v[56:59], v[44:47]\n"
      "s_add_u32 s61, s61, 1\n"
      "s_nop 6\n"
      "s_branch LOOP_66_%=\n"
      "s_nop 0\n"
      "BLOCK_68_%=:\n"
      "s_mov_b32 s79, 0\n"
      "s_nop 0\n"
      "QW_76_%=:\n"
      "v_mbcnt_lo_u32_b32 v52, -1, 0\n"
      "v_mbcnt_hi_u32_b32 v52, -1, v52\n"
      "v_lshlrev_b32_e32 v52, 4, v52\n"
      "v_add_u32_e32 v52, 0x27240, v52\n"
      "v_mov_b32_e32 v53, 0x260f0\n"
      "ds_read_b128 v[48:51], v52\n"
      "ds_read_b32 v53, v53\n"
      "s_waitcnt lgkmcnt(0)\n"
      "v_cmp_ne_u32_e64 s[80:81], s62, v49\n"
      "v_cmp_ne_u32_e64 s[82:83], s62, v51\n"
      "v_cmp_eq_u32_e32 vcc, 1, v53\n"
      "s_nop 4\n"
      "s_cbranch_vccnz QD_77_%=\n"
      "s_or_b64 s[80:81], s[80:81], s[82:83]\n"
      "s_cmp_eq_u64 s[80:81], 0\n"
      "s_nop 0\n"
      "s_cbranch_scc0 QN_78_%=\n"
      "v_mov_b32_e32 v49, v50\n"
      "v_lshrrev_b32_e32 v52, 1, v52\n"
      "v_add_u32_e32 v52, 0x12f20, v52\n"
      "ds_write_b64 v52, v[48:49]\n"
      "v_mov_b32_e32 v52, 0x260f0\n"
      "v_mov_b32_e32 v53, 1\n"
      "ds_write_b32 v52, v53\n"
      "s_waitcnt lgkmcnt(0)\n"
      "s_nop 2\n"
      "s_branch QD_77_%=\n"
      "s_nop 0\n"
      "QN_78_%=:\n"
      "s_add_u32 s64, s42, 0x20000\n"
      "s_addc_u32 s65, s43, 0\n"
      "s_mov_b32 m0, 0x27240\n"
      "s_nop 0\n"
      "global_load_lds_dwordx4 v61, s[64:65] sc1\n"
      "s_sleep 2\n"
      "s_add_u32 s79, s79, 1\n"
      "s_cmp_lt_u32 s79, 0x100000\n"
      "s_waitcnt vmcnt(40)\n"
      "s_nop 0\n"
      "s_cbranch_scc1 QW_76_%=\n"
      "s_nop 0\n"
      "QD_77_%=:\n"
      "SWITCH_67_%=:\n"
      "s_cmp_eq_u32 s61, 3\n"
      "s_nop 0\n"
      "s_cbranch_scc1 VAR3_79_%=\n"
      "s_nop 0\n"
      "s_branch VAR4_80_%=\n"
      "s_nop 0\n"
      "VAR3_79_%=:\n"
      "v_mbcnt_lo_u32_b32 v52, -1, 0\n"
      "v_mbcnt_hi_u32_b32 v52, -1, v52\n"
      "v_lshlrev_b32_e32 v52, 4, v52\n"
      "s_lshl_b32 s80, s48, 12\n"
      "s_add_u32 s80, s80, 0x20000\n"
      "v_add_u32_e32 v52, s80, v52\n"
      "ds_write_b128 v52, v[32:35] offset:0\n"
      "ds_write_b128 v52, v[36:39] offset:1024\n"
      "ds_write_b128 v52, v[40:43] offset:2048\n"
      "ds_write_b128 v52, v[44:47] offset:3072\n"
      "v_mbcnt_lo_u32_b32 v52, -1, 0\n"
      "v_mbcnt_hi_u32_b32 v52, -1, v52\n"
      "v_lshlrev_b32_e32 v52, 2, v52\n"
      "s_lshl_b32 s80, s48, 8\n"
      "s_add_u32 s80, s80, 0x26240\n"
      "v_add_u32_e32 v52, s80, v52\n"
      "ds_write_b32 v52, v62\n"
      "s_mov_b32 s78, s60\n"
      "s_waitcnt lgkmcnt(0)\n"
      "v_mbcnt_lo_u32_b32 v53, -1, 0\n"
      "v_mbcnt_hi_u32_b32 v53, -1, v53\n"
      "v_lshrrev_b32_e32 v52, 4, v53\n"
      "v_lshlrev_b32_e32 v52, 6, v52\n"
      "v_and_b32_e32 v53, 3, v53\n"
      "v_lshl_add_u32 v52, v53, 4, v52\n"
      "v_add_u32_e32 v52, 0x26840, v52\n"
      "s_mov_b32 vcc_lo, 0xf000f\n"
      "s_mov_b32 vcc_hi, 0xf000f\n"
      "v_mov_b32_e32 v53, 0x26120\n"
      "v_cndmask_b32_e32 v53, v53, v52, vcc\n"
      "ds_read_b128 v[0:3], v53 offset:0\n"
      "ds_read_b128 v[4:7], v53 offset:256\n"
      "s_mov_b32 vcc_lo, 0xf000f0\n"
      "s_mov_b32 vcc_hi, 0xf000f0\n"
      "v_mov_b32_e32 v53, 0x26120\n"
      "v_cndmask_b32_e32 v53, v53, v52, vcc\n"
      "ds_read_b128 v[8:11], v53 offset:0\n"
      "ds_read_b128 v[12:15], v53 offset:256\n"
      "s_mov_b32 vcc_lo, 0xf000f00\n"
      "s_mov_b32 vcc_hi, 0xf000f00\n"
      "v_mov_b32_e32 v53, 0x26120\n"
      "v_cndmask_b32_e32 v53, v53, v52, vcc\n"
      "ds_read_b128 v[16:19], v53 offset:0\n"
      "ds_read_b128 v[20:23], v53 offset:256\n"
      "s_mov_b32 vcc_lo, 0xf000f000\n"
      "s_mov_b32 vcc_hi, 0xf000f000\n"
      "v_mov_b32_e32 v53, 0x26120\n"
      "v_cndmask_b32_e32 v53, v53, v52, vcc\n"
      "ds_read_b128 v[24:27], v53 offset:0\n"
      "ds_read_b128 v[28:31], v53 offset:256\n"
      "s_waitcnt lgkmcnt(0)\n"
      "v_mov_b32_e32 v32, 0\n"
      "v_mov_b32_e32 v33, 0\n"
      "v_mov_b32_e32 v34, 0\n"
      "v_mov_b32_e32 v35, 0\n"
      "v_mov_b32_e32 v36, 0\n"
      "v_mov_b32_e32 v37, 0\n"
      "v_mov_b32_e32 v38, 0\n"
      "v_mov_b32_e32 v39, 0\n"
      "v_mov_b32_e32 v40, 0\n"
      "v_mov_b32_e32 v41, 0\n"
      "v_mov_b32_e32 v42, 0\n"
      "v_mov_b32_e32 v43, 0\n"
      "v_mov_b32_e32 v44, 0\n"
      "v_mov_b32_e32 v45, 0\n"
      "v_mov_b32_e32 v46, 0\n"
      "v_mov_b32_e32 v47, 0\n"
      "v_mov_b32_e32 v62, 0\n"
      "s_mov_b32 s60, 0xff800000\n"
      "global_load_dwordx4 v[168:171], v61, s[90:91] offset:256 nt\n"
      "global_load_dwordx4 v[172:175], v61, s[90:91] offset:384 nt\n"
      "v_mfma_f32_16x16x32_bf16 v[48:51], v[192:195], v[0:3], 0\n"
      "v_mfma_f32_16x16x32_bf16 v[48:51], v[196:199], v[4:7], v[48:51]\n"
      "global_load_dwordx4 v[176:179], v61, s[90:91] offset:2048 nt\n"
      "global_load_dwordx4 v[180:183], v61, s[90:91] offset:2176 nt\n"
      "v_mfma_f32_16x16x32_bf16 v[48:51], v[200:203], v[8:11], v[48:51]\n"
      "v_mfma_f32_16x16x32_bf16 v[48:51], v[204:207], v[12:15], v[48:51]\n"
      "global_load_dwordx4 v[184:187], v61, s[90:91] offset:2304 nt\n"
      "global_load_dwordx4 v[188:191], v61, s[90:91] offset:2432 nt\n"
      "v_mfma_f32_16x16x32_bf16 v[48:51], v[208:211], v[16:19], v[48:51]\n"
      "v_mfma_f32_16x16x32_bf16 v[48:51], v[212:215], v[20:23], v[48:51]\n"
      "v_mfma_f32_16x16x32_bf16 v[48:51], v[216:219], v[24:27], v[48:51]\n"
      "v_mfma_f32_16x16x32_bf16 v[48:51], v[220:223], v[28:31], v[48:51]\n"
      "s_add_u32 s86, s100, 0x41c0000\n"
      "s_cmp_ge_u32 s86, 0x43f0000\n"
      "s_cselect_b32 s87, 0x3f0000, 0\n"
      "s_sub_u32 s86, s86, s87\n"
      "s_add_u32 s90, s36, s86\n"
      "s_addc_u32 s91, s37, 0\n"
      "global_load_dwordx4 v[192:195], v60, s[90:91] offset:-4096 nt\n"
      "global_load_dwordx4 v[196:199], v60, s[90:91] offset:-3840 nt\n"
      "global_load_dwordx4 v[200:203], v60, s[90:91] offset:-2048 nt\n"
      "global_load_dwordx4 v[204:207], v60, s[90:91] offset:-1792 nt\n"
      "global_load_dwordx4 v[208:211], v60, s[90:91] offset:0 nt\n"
      "global_load_dwordx4 v[212:215], v60, s[90:91] offset:256 nt\n"
      "global_load_dwordx4 v[216:219], v60, s[90:91] offset:2048 nt\n"
      "global_load_dwordx4 v[220:223], v60, s[90:91] offset:2304 nt\n"
      "v_cndmask_b32_e64 v53, v48, v49, s[96:97]\n"
      "v_cndmask_b32_e64 v54, v50, v51, s[96:97]\n"
      "v_cndmask_b32_e64 v52, v53, v54, s[98:99]\n"
      "s_nop 1\n"
      "v_add_f32_dpp v52, v52, v52 quad_perm:[1,0,3,2] row_mask:0xf bank_mask:0xf\n"
      "s_nop 1\n"
      "v_add_f32_dpp v52, v52, v52 quad_perm:[2,3,0,1] row_mask:0xf bank_mask:0xf\n"
      "v_cmp_lt_f32_e32 vcc, s60, v52\n"
      "s_cbranch_vccz NORS_81_%=\n"
      "s_swappc_b64 s[66:67], s[68:69]\n"
      "NORS_81_%=:\n"
      "v_subrev_f32_e32 v52, s60, v52\n"
      "v_mul_f32_e32 v52, 0x3db8aa3b, v52\n"
      "v_exp_f32_e32 v52, v52\n"
      "s_nop 0\n"
      "v_cvt_pk_bf16_f32 v53, v52, v52\n"
      "v_add_f32_e32 v62, v62, v52\n"
      "v_and_b32_e32 v53, v53, v63\n"
      "s_nop 0\n"
      "v_add_f32_dpp v62, v52, v62 row_half_mirror row_mask:0xf bank_mask:0xf\n"
      "v_mov_b32_dpp v56, v52 row_half_mirror row_mask:0xf bank_mask:0xf\n"
      "v_cvt_pk_bf16_f32 v56, v56, v56\n"
      "v_and_b32_e32 v56, v56, v63\n"
      "v_cndmask_b32_e64 v55, v53, v56, s[50:51]\n"
      "v_cndmask_b32_e64 v59, v56, v53, s[50:51]\n"
      "v_cndmask_b32_e64 v52, 0, v55, s[52:53]\n"
      "v_cndmask_b32_e64 v53, 0, v55, s[54:55]\n"
      "v_cndmask_b32_e64 v54, 0, v55, s[56:57]\n"
      "v_cndmask_b32_e64 v55, 0, v55, s[58:59]\n"
      "v_cndmask_b32_e64 v56, 0, v59, s[52:53]\n"
      "v_cndmask_b32_e64 v57, 0, v59, s[54:55]\n"
      "v_cndmask_b32_e64 v58, 0, v59, s[56:57]\n"
      "v_cndmask_b32_e64 v59, 0, v59, s[58:59]\n"
      "v_mfma_f32_16x16x32_bf16 v[32:35], v[224:227], v[52:55], v[32:35]\n"
      "v_mfma_f32_16x16x32_bf16 v[36:39], v[228:231], v[52:55], v[36:39]\n"
      "v_mfma_f32_16x16x32_bf16 v[40:43], v[232:235], v[52:55], v[40:43]\n"
      "v_mfma_f32_16x16x32_bf16 v[44:47], v[236:239], v[52:55], v[44:47]\n"
      "v_mfma_f32_16x16x32_bf16 v[32:35], v[240:243], v[56:59], v[32:35]\n"
      "v_mfma_f32_16x16x32_bf16 v[36:39], v[244:247], v[56:59], v[36:39]\n"
      "v_mfma_f32_16x16x32_bf16 v[40:43], v[248:251], v[56:59], v[40:43]\n"
      "v_mfma_f32_16x16x32_bf16 v[44:47], v[252:255], v[56:59], v[44:47]\n"
      "s_add_u32 s86, s100, 0x41c0000\n"
      "s_cmp_ge_u32 s86, 0x43f0000\n"
      "s_cselect_b32 s87, 0x3f0000, 0\n"
      "s_sub_u32 s86, s86, s87\n"
      "s_add_u32 s90, s38, s86\n"
      "s_addc_u32 s91, s39, 0\n"
      "global_load_dwordx4 v[224:227], v61, s[90:91] offset:0 nt\n"
      "global_load_dwordx4 v[228:231], v61, s[90:91] offset:128 nt\n"
      "v_mfma_f32_16x16x32_bf16 v[48:51], a[0:3], v[0:3], 0\n"
      "v_mfma_f32_16x16x32_bf16 v[48:51], a[4:7], v[4:7], v[48:51]\n"
      "v_mfma_f32_16x16x32_bf16 v[48:51], a[8:11], v[8:11], v[48:51]\n"
      "v_mfma_f32_16x16x32_bf16 v[48:51], a[12:15], v[12:15], v[48:51]\n"
      "v_mfma_f32_16x16x32_bf16 v[48:51], a[16:19], v[16:19], v[48:51]\n"
      "v_mfma_f32_16x16x32_bf16 v[48:51], a[20:23], v[20:23], v[48:51]\n"
      "v_mfma_f32_16x16x32_bf16 v[48:51], a[24:27], v[24:27], v[48:51]\n"
      "v_mfma_f32_16x16x32_bf16 v[48:51], a[28:31], v[28:31], v[48:51]\n"
      "s_nop 7\n"
      "v_cndmask_b32_e64 v53, v48, v49, s[96:97]\n"
      "v_cndmask_b32_e64 v54, v50, v51, s[96:97]\n"
      "v_cndmask_b32_e64 v52, v53, v54, s[98:99]\n"
      "s_nop 1\n"
      "v_add_f32_dpp v52, v52, v52 quad_perm:[1,0,3,2] row_mask:0xf bank_mask:0xf\n"
      "s_nop 1\n"
      "v_add_f32_dpp v52, v52, v52 quad_perm:[2,3,0,1] row_mask:0xf bank_mask:0xf\n"
      "v_cmp_lt_f32_e32 vcc, s60, v52\n"
      "s_cbranch_vccz NORS_82_%=\n"
      "s_swappc_b64 s[66:67], s[68:69]\n"
      "NORS_82_%=:\n"
      "v_subrev_f32_e32 v52, s60, v52\n"
      "v_mul_f32_e32 v52, 0x3db8aa3b, v52\n"
      "v_exp_f32_e32 v52, v52\n"
      "s_nop 0\n"
      "v_cvt_pk_bf16_f32 v53, v52, v52\n"
      "v_add_f32_e32 v62, v62, v52\n"
      "v_and_b32_e32 v53, v53, v63\n"
      "s_nop 0\n"
      "v_add_f32_dpp v62, v52, v62 row_half_mirror row_mask:0xf bank_mask:0xf\n"
      "v_mov_b32_dpp v56, v52 row_half_mirror row_mask:0xf bank_mask:0xf\n"
      "v_cvt_pk_bf16_f32 v56, v56, v56\n"
      "v_and_b32_e32 v56, v56, v63\n"
      "v_cndmask_b32_e64 v55, v53, v56, s[50:51]\n"
      "v_cndmask_b32_e64 v59, v56, v53, s[50:51]\n"
      "v_cndmask_b32_e64 v52, 0, v55, s[52:53]\n"
      "v_cndmask_b32_e64 v53, 0, v55, s[54:55]\n"
      "v_cndmask_b32_e64 v54, 0, v55, s[56:57]\n"
      "v_cndmask_b32_e64 v55, 0, v55, s[58:59]\n"
      "v_cndmask_b32_e64 v56, 0, v59, s[52:53]\n"
      "v_cndmask_b32_e64 v57, 0, v59, s[54:55]\n"
      "v_cndmask_b32_e64 v58, 0, v59, s[56:57]\n"
      "v_cndmask_b32_e64 v59, 0, v59, s[58:59]\n"
      "v_mfma_f32_16x16x32_bf16 v[32:35], a[32:35], v[52:55], v[32:35]\n"
      "v_mfma_f32_16x16x32_bf16 v[36:39], a[36:39], v[52:55], v[36:39]\n"
      "v_mfma_f32_16x16x32_bf16 v[40:43], a[40:43], v[52:55], v[40:43]\n"
      "v_mfma_f32_16x16x32_bf16 v[44:47], a[44:47], v[52:55], v[44:47]\n"
      "v_mfma_f32_16x16x32_bf16 v[32:35], a[48:51], v[56:59], v[32:35]\n"
      "v_mfma_f32_16x16x32_bf16 v[36:39], a[52:55], v[56:59], v[36:39]\n"
      "v_mfma_f32_16x16x32_bf16 v[40:43], a[56:59], v[56:59], v[40:43]\n"
      "v_mfma_f32_16x16x32_bf16 v[44:47], a[60:63], v[56:59], v[44:47]\n"
      "v_mfma_f32_16x16x32_bf16 v[48:51], a[64:67], v[0:3], 0\n"
      "v_mfma_f32_16x16x32_bf16 v[48:51], a[68:71], v[4:7], v[48:51]\n"
      "v_mfma_f32_16x16x32_bf16 v[48:51], a[72:75], v[8:11], v[48:51]\n"
      "v_mfma_f32_16x16x32_bf16 v[48:51], a[76:79], v[12:15], v[48:51]\n"
      "v_mfma_f32_16x16x32_bf16 v[48:51], a[80:83], v[16:19], v[48:51]\n"
      "v_mfma_f32_16x16x32_bf16 v[48:51], a[84:87], v[20:23], v[48:51]\n"
      "v_mfma_f32_16x16x32_bf16 v[48:51], a[88:91], v[24:27], v[48:51]\n"
      "v_mfma_f32_16x16x32_bf16 v[48:51], a[92:95], v[28:31], v[48:51]\n"
      "s_nop 7\n"
      "v_cndmask_b32_e64 v53, v48, v49, s[96:97]\n"
      "v_cndmask_b32_e64 v54, v50, v51, s[96:97]\n"
      "v_cndmask_b32_e64 v52, v53, v54, s[98:99]\n"
      "s_nop 1\n"
      "v_add_f32_dpp v52, v52, v52 quad_perm:[1,0,3,2] row_mask:0xf bank_mask:0xf\n"
      "s_nop 1\n"
      "v_add_f32_dpp v52, v52, v52 quad_perm:[2,3,0,1] row_mask:0xf bank_mask:0xf\n"
      "v_cmp_lt_f32_e32 vcc, s60, v52\n"
      "s_cbranch_vccz NORS_83_%=\n"
      "s_swappc_b64 s[66:67], s[68:69]\n"
      "NORS_83_%=:\n"
      "v_subrev_f32_e32 v52, s60, v52\n"
      "v_mul_f32_e32 v52, 0x3db8aa3b, v52\n"
      "v_exp_f32_e32 v52, v52\n"
      "s_nop 0\n"
      "v_cvt_pk_bf16_f32 v53, v52, v52\n"
      "v_add_f32_e32 v62, v62, v52\n"
      "v_and_b32_e32 v53, v53, v63\n"
      "s_nop 0\n"
      "v_add_f32_dpp v62, v52, v62 row_half_mirror row_mask:0xf bank_mask:0xf\n"
      "v_mov_b32_dpp v56, v52 row_half_mirror row_mask:0xf bank_mask:0xf\n"
      "v_cvt_pk_bf16_f32 v56, v56, v56\n"
      "v_and_b32_e32 v56, v56, v63\n"
      "v_cndmask_b32_e64 v55, v53, v56, s[50:51]\n"
      "v_cndmask_b32_e64 v59, v56, v53, s[50:51]\n"
      "v_cndmask_b32_e64 v52, 0, v55, s[52:53]\n"
      "v_cndmask_b32_e64 v53, 0, v55, s[54:55]\n"
      "v_cndmask_b32_e64 v54, 0, v55, s[56:57]\n"
      "v_cndmask_b32_e64 v55, 0, v55, s[58:59]\n"
      "v_cndmask_b32_e64 v56, 0, v59, s[52:53]\n"
      "v_cndmask_b32_e64 v57, 0, v59, s[54:55]\n"
      "v_cndmask_b32_e64 v58, 0, v59, s[56:57]\n"
      "v_cndmask_b32_e64 v59, 0, v59, s[58:59]\n"
      "v_mfma_f32_16x16x32_bf16 v[32:35], a[96:99], v[52:55], v[32:35]\n"
      "v_mfma_f32_16x16x32_bf16 v[36:39], a[100:103], v[52:55], v[36:39]\n"
      "v_mfma_f32_16x16x32_bf16 v[40:43], a[104:107], v[52:55], v[40:43]\n"
      "v_mfma_f32_16x16x32_bf16 v[44:47], a[108:111], v[52:55], v[44:47]\n"
      "v_mfma_f32_16x16x32_bf16 v[32:35], a[112:115], v[56:59], v[32:35]\n"
      "v_mfma_f32_16x16x32_bf16 v[36:39], a[116:119], v[56:59], v[36:39]\n"
      "v_mfma_f32_16x16x32_bf16 v[40:43], a[120:123], v[56:59], v[40:43]\n"
      "v_mfma_f32_16x16x32_bf16 v[44:47], a[124:127], v[56:59], v[44:47]\n"
      "v_mfma_f32_16x16x32_bf16 v[48:51], a[128:131], v[0:3], 0\n"
      "v_mfma_f32_16x16x32_bf16 v[48:51], a[132:135], v[4:7], v[48:51]\n"
      "v_mfma_f32_16x16x32_bf16 v[48:51], a[136:139], v[8:11], v[48:51]\n"
      "v_mfma_f32_16x16x32_bf16 v[48:51], a[140:143], v[12:15], v[48:51]\n"
      "v_mfma_f32_16x16x32_bf16 v[48:51], a[144:147], v[16:19], v[48:51]\n"
      "v_mfma_f32_16x16x32_bf16 v[48:51], a[148:151], v[20:23], v[48:51]\n"
      "v_mfma_f32_16x16x32_bf16 v[48:51], a[152:155], v[24:27], v[48:51]\n"
      "v_mfma_f32_16x16x32_bf16 v[48:51], a[156:159], v[28:31], v[48:51]\n"
      "s_nop 7\n"
      "v_cndmask_b32_e64 v53, v48, v49, s[96:97]\n"
      "v_cndmask_b32_e64 v54, v50, v51, s[96:97]\n"
      "v_cndmask_b32_e64 v52, v53, v54, s[98:99]\n"
      "s_nop 1\n"
      "v_add_f32_dpp v52, v52, v52 quad_perm:[1,0,3,2] row_mask:0xf bank_mask:0xf\n"
      "s_nop 1\n"
      "v_add_f32_dpp v52, v52, v52 quad_perm:[2,3,0,1] row_mask:0xf bank_mask:0xf\n"
      "v_cmp_lt_f32_e32 vcc, s60, v52\n"
      "s_cbranch_vccz NORS_84_%=\n"
      "s_swappc_b64 s[66:67], s[68:69]\n"
      "NORS_84_%=:\n"
      "v_subrev_f32_e32 v52, s60, v52\n"
      "v_mul_f32_e32 v52, 0x3db8aa3b, v52\n"
      "v_exp_f32_e32 v52, v52\n"
      "s_nop 0\n"
      "v_cvt_pk_bf16_f32 v53, v52, v52\n"
      "v_add_f32_e32 v62, v62, v52\n"
      "v_and_b32_e32 v53, v53, v63\n"
      "s_nop 0\n"
      "v_add_f32_dpp v62, v52, v62 row_half_mirror row_mask:0xf bank_mask:0xf\n"
      "v_mov_b32_dpp v56, v52 row_half_mirror row_mask:0xf bank_mask:0xf\n"
      "v_cvt_pk_bf16_f32 v56, v56, v56\n"
      "v_and_b32_e32 v56, v56, v63\n"
      "v_cndmask_b32_e64 v55, v53, v56, s[50:51]\n"
      "v_cndmask_b32_e64 v59, v56, v53, s[50:51]\n"
      "v_cndmask_b32_e64 v52, 0, v55, s[52:53]\n"
      "v_cndmask_b32_e64 v53, 0, v55, s[54:55]\n"
      "v_cndmask_b32_e64 v54, 0, v55, s[56:57]\n"
      "v_cndmask_b32_e64 v55, 0, v55, s[58:59]\n"
      "v_cndmask_b32_e64 v56, 0, v59, s[52:53]\n"
      "v_cndmask_b32_e64 v57, 0, v59, s[54:55]\n"
      "v_cndmask_b32_e64 v58, 0, v59, s[56:57]\n"
      "v_cndmask_b32_e64 v59, 0, v59, s[58:59]\n"
      "v_mfma_f32_16x16x32_bf16 v[32:35], a[160:163], v[52:55], v[32:35]\n"
      "v_mfma_f32_16x16x32_bf16 v[36:39], a[164:167], v[52:55], v[36:39]\n"
      "v_mfma_f32_16x16x32_bf16 v[40:43], a[168:171], v[52:55], v[40:43]\n"
      "v_mfma_f32_16x16x32_bf16 v[44:47], a[172:175], v[52:55], v[44:47]\n"
      "v_mfma_f32_16x16x32_bf16 v[32:35], a[176:179], v[56:59], v[32:35]\n"
      "v_mfma_f32_16x16x32_bf16 v[36:39], a[180:183], v[56:59], v[36:39]\n"
      "v_mfma_f32_16x16x32_bf16 v[40:43], a[184:187], v[56:59], v[40:43]\n"
      "v_mfma_f32_16x16x32_bf16 v[44:47], a[188:191], v[56:59], v[44:47]\n"
      "v_mfma_f32_16x16x32_bf16 v[48:51], a[192:195], v[0:3], 0\n"
      "v_mfma_f32_16x16x32_bf16 v[48:51], a[196:199], v[4:7], v[48:51]\n"
      "v_mfma_f32_16x16x32_bf16 v[48:51], a[200:203], v[8:11], v[48:51]\n"
      "v_mfma_f32_16x16x32_bf16 v[48:51], a[204:207], v[12:15], v[48:51]\n"
      "v_mfma_f32_16x16x32_bf16 v[48:51], a[208:211], v[16:19], v[48:51]\n"
      "v_mfma_f32_16x16x32_bf16 v[48:51], a[212:215], v[20:23], v[48:51]\n"
      "v_mfma_f32_16x16x32_bf16 v[48:51], a[216:219], v[24:27], v[48:51]\n"
      "v_mfma_f32_16x16x32_bf16 v[48:51], a[220:223], v[28:31], v[48:51]\n"
      "s_nop 7\n"
      "v_cndmask_b32_e64 v53, v48, v49, s[96:97]\n"
      "v_cndmask_b32_e64 v54, v50, v51, s[96:97]\n"
      "v_cndmask_b32_e64 v52, v53, v54, s[98:99]\n"
      "s_nop 1\n"
      "v_add_f32_dpp v52, v52, v52 quad_perm:[1,0,3,2] row_mask:0xf bank_mask:0xf\n"
      "s_nop 1\n"
      "v_add_f32_dpp v52, v52, v52 quad_perm:[2,3,0,1] row_mask:0xf bank_mask:0xf\n"
      "v_cmp_lt_f32_e32 vcc, s60, v52\n"
      "s_cbranch_vccz NORS_85_%=\n"
      "s_swappc_b64 s[66:67], s[68:69]\n"
      "NORS_85_%=:\n"
      "v_subrev_f32_e32 v52, s60, v52\n"
      "v_mul_f32_e32 v52, 0x3db8aa3b, v52\n"
      "v_exp_f32_e32 v52, v52\n"
      "s_nop 0\n"
      "v_cvt_pk_bf16_f32 v53, v52, v52\n"
      "v_add_f32_e32 v62, v62, v52\n"
      "v_and_b32_e32 v53, v53, v63\n"
      "s_nop 0\n"
      "v_add_f32_dpp v62, v52, v62 row_half_mirror row_mask:0xf bank_mask:0xf\n"
      "v_mov_b32_dpp v56, v52 row_half_mirror row_mask:0xf bank_mask:0xf\n"
      "v_cvt_pk_bf16_f32 v56, v56, v56\n"
      "v_and_b32_e32 v56, v56, v63\n"
      "v_cndmask_b32_e64 v55, v53, v56, s[50:51]\n"
      "v_cndmask_b32_e64 v59, v56, v53, s[50:51]\n"
      "v_cndmask_b32_e64 v52, 0, v55, s[52:53]\n"
      "v_cndmask_b32_e64 v53, 0, v55, s[54:55]\n"
      "v_cndmask_b32_e64 v54, 0, v55, s[56:57]\n"
      "v_cndmask_b32_e64 v55, 0, v55, s[58:59]\n"
      "v_cndmask_b32_e64 v56, 0, v59, s[52:53]\n"
      "v_cndmask_b32_e64 v57, 0, v59, s[54:55]\n"
      "v_cndmask_b32_e64 v58, 0, v59, s[56:57]\n"
      "v_cndmask_b32_e64 v59, 0, v59, s[58:59]\n"
      "v_mfma_f32_16x16x32_bf16 v[32:35], a[224:227], v[52:55], v[32:35]\n"
      "v_mfma_f32_16x16x32_bf16 v[36:39], a[228:231], v[52:55], v[36:39]\n"
      "v_mfma_f32_16x16x32_bf16 v[40:43], a[232:235], v[52:55], v[40:43]\n"
      "v_mfma_f32_16x16x32_bf16 v[44:47], a[236:239], v[52:55], v[44:47]\n"
      "v_mfma_f32_16x16x32_bf16 v[32:35], a[240:243], v[56:59], v[32:35]\n"
      "v_mfma_f32_16x16x32_bf16 v[36:39], a[244:247], v[56:59], v[36:39]\n"
      "v_mfma_f32_16x16x32_bf16 v[40:43], a[248:251], v[56:59], v[40:43]\n"
      "v_mfma_f32_16x16x32_bf16 v[44:47], a[252:255], v[56:59], v[44:47]\n"
      "s_nop 7\n"
      "v_mbcnt_lo_u32_b32 v52, -1, 0\n"
      "v_mbcnt_hi_u32_b32 v52, -1, v52\n"
      "v_lshlrev_b32_e32 v52, 4, v52\n"
      "s_lshl_b32 s80, s48, 12\n"
      "s_add_u32 s80, s80, 0x20000\n"
      "v_add_u32_e32 v52, s80, v52\n"
      "ds_read_b128 v[0:3], v52 offset:0\n"
      "ds_read_b128 v[4:7], v52 offset:1024\n"
      "ds_read_b128 v[8:11], v52 offset:2048\n"
      "ds_read_b128 v[12:15], v52 offset:3072\n"
      "v_mbcnt_lo_u32_b32 v53, -1, 0\n"
      "v_mbcnt_hi_u32_b32 v53, -1, v53\n"
      "v_lshlrev_b32_e32 v53, 2, v53\n"
      "s_lshl_b32 s80, s48, 8\n"
      "s_add_u32 s80, s80, 0x26240\n"
      "v_add_u32_e32 v53, s80, v53\n"
      "ds_read_b32 v16, v53\n"
      "s_waitcnt lgkmcnt(0)\n"
      "ds_write_b128 v52, v[32:35] offset:0\n"
      "ds_write_b128 v52, v[36:39] offset:1024\n"
      "ds_write_b128 v52, v[40:43] offset:2048\n"
      "ds_write_b128 v52, v[44:47] offset:3072\n"
      "ds_write_b32 v53, v62\n"
      "s_mov_b32 s80, s78\n"
      "s_mov_b32 s78, s60\n"
      "s_mov_b32 s60, s80\n"
      "s_waitcnt lgkmcnt(0)\n"
      "v_mov_b32_e32 v32, v0\n"
      "v_mov_b32_e32 v33, v1\n"
      "v_mov_b32_e32 v34, v2\n"
      "v_mov_b32_e32 v35, v3\n"
      "v_mov_b32_e32 v36, v4\n"
      "v_mov_b32_e32 v37, v5\n"
      "v_mov_b32_e32 v38, v6\n"
      "v_mov_b32_e32 v39, v7\n"
      "v_mov_b32_e32 v40, v8\n"
      "v_mov_b32_e32 v41, v9\n"
      "v_mov_b32_e32 v42, v10\n"
      "v_mov_b32_e32 v43, v11\n"
      "v_mov_b32_e32 v44, v12\n"
      "v_mov_b32_e32 v45, v13\n"
      "v_mov_b32_e32 v46, v14\n"
      "v_mov_b32_e32 v47, v15\n"
      "v_mov_b32_e32 v62, v16\n"
      "v_mbcnt_lo_u32_b32 v53, -1, 0\n"
      "v_mbcnt_hi_u32_b32 v53, -1, v53\n"
      "v_lshrrev_b32_e32 v52, 4, v53\n"
      "v_lshlrev_b32_e32 v52, 6, v52\n"
      "v_and_b32_e32 v53, 3, v53\n"
      "v_lshl_add_u32 v52, v53, 4, v52\n"
      "v_add_u32_e32 v52, 0x26a40, v52\n"
      "s_mov_b32 vcc_lo, 0xf000f\n"
      "s_mov_b32 vcc_hi, 0xf000f\n"
      "v_mov_b32_e32 v53, 0x26120\n"
      "v_cndmask_b32_e32 v53, v53, v52, vcc\n"
      "ds_read_b128 v[0:3], v53 offset:0\n"
      "ds_read_b128 v[4:7], v53 offset:256\n"
      "s_mov_b32 vcc_lo, 0xf000f0\n"
      "s_mov_b32 vcc_hi, 0xf000f0\n"
      "v_mov_b32_e32 v53, 0x26120\n"
      "v_cndmask_b32_e32 v53, v53, v52, vcc\n"
      "ds_read_b128 v[8:11], v53 offset:0\n"
      "ds_read_b128 v[12:15], v53 offset:256\n"
      "s_mov_b32 vcc_lo, 0xf000f00\n"
      "s_mov_b32 vcc_hi, 0xf000f00\n"
      "v_mov_b32_e32 v53, 0x26120\n"
      "v_cndmask_b32_e32 v53, v53, v52, vcc\n"
      "ds_read_b128 v[16:19], v53 offset:0\n"
      "ds_read_b128 v[20:23], v53 offset:256\n"
      "s_mov_b32 vcc_lo, 0xf000f000\n"
      "s_mov_b32 vcc_hi, 0xf000f000\n"
      "v_mov_b32_e32 v53, 0x26120\n"
      "v_cndmask_b32_e32 v53, v53, v52, vcc\n"
      "ds_read_b128 v[24:27], v53 offset:0\n"
      "ds_read_b128 v[28:31], v53 offset:256\n"
      "s_waitcnt lgkmcnt(0)\n"
      "s_waitcnt vmcnt(24)\n"
      "v_mfma_f32_16x16x32_bf16 v[48:51], v[128:131], v[0:3], 0\n"
      "v_mfma_f32_16x16x32_bf16 v[48:51], v[132:135], v[4:7], v[48:51]\n"
      "s_waitcnt vmcnt(22)\n"
      "v_mfma_f32_16x16x32_bf16 v[48:51], v[136:139], v[8:11], v[48:51]\n"
      "v_mfma_f32_16x16x32_bf16 v[48:51], v[140:143], v[12:15], v[48:51]\n"
      "s_waitcnt vmcnt(20)\n"
      "v_mfma_f32_16x16x32_bf16 v[48:51], v[144:147], v[16:19], v[48:51]\n"
      "v_mfma_f32_16x16x32_bf16 v[48:51], v[148:151], v[20:23], v[48:51]\n"
      "s_waitcnt vmcnt(18)\n"
      "v_mfma_f32_16x16x32_bf16 v[48:51], v[152:155], v[24:27], v[48:51]\n"
      "v_mfma_f32_16x16x32_bf16 v[48:51], v[156:159], v[28:31], v[48:51]\n"
      "s_nop 7\n"
      "v_cndmask_b32_e64 v53, v48, v49, s[96:97]\n"
      "v_cndmask_b32_e64 v54, v50, v51, s[96:97]\n"
      "v_cndmask_b32_e64 v52, v53, v54, s[98:99]\n"
      "s_nop 1\n"
      "v_add_f32_dpp v52, v52, v52 quad_perm:[1,0,3,2] row_mask:0xf bank_mask:0xf\n"
      "s_nop 1\n"
      "v_add_f32_dpp v52, v52, v52 quad_perm:[2,3,0,1] row_mask:0xf bank_mask:0xf\n"
      "v_cmp_lt_f32_e32 vcc, s60, v52\n"
      "s_cbranch_vccz NORS_86_%=\n"
      "s_swappc_b64 s[66:67], s[68:69]\n"
      "NORS_86_%=:\n"
      "v_subrev_f32_e32 v52, s60, v52\n"
      "v_mul_f32_e32 v52, 0x3db8aa3b, v52\n"
      "v_exp_f32_e32 v52, v52\n"
      "s_nop 0\n"
      "v_cvt_pk_bf16_f32 v53, v52, v52\n"
      "v_add_f32_e32 v62, v62, v52\n"
      "v_and_b32_e32 v53, v53, v63\n"
      "s_nop 0\n"
      "v_add_f32_dpp v62, v52, v62 row_half_mirror row_mask:0xf bank_mask:0xf\n"
      "v_mov_b32_dpp v56, v52 row_half_mirror row_mask:0xf bank_mask:0xf\n"
      "v_cvt_pk_bf16_f32 v56, v56, v56\n"
      "v_and_b32_e32 v56, v56, v63\n"
      "v_cndmask_b32_e64 v55, v53, v56, s[50:51]\n"
      "v_cndmask_b32_e64 v59, v56, v53, s[50:51]\n"
      "v_cndmask_b32_e64 v52, 0, v55, s[52:53]\n"
      "v_cndmask_b32_e64 v53, 0, v55, s[54:55]\n"
      "v_cndmask_b32_e64 v54, 0, v55, s[56:57]\n"
      "v_cndmask_b32_e64 v55, 0, v55, s[58:59]\n"
      "v_cndmask_b32_e64 v56, 0, v59, s[52:53]\n"
      "v_cndmask_b32_e64 v57, 0, v59, s[54:55]\n"
      "v_cndmask_b32_e64 v58, 0, v59, s[56:57]\n"
      "v_cndmask_b32_e64 v59, 0, v59, s[58:59]\n"
      "s_waitcnt vmcnt(16)\n"
      "v_mfma_f32_16x16x32_bf16 v[32:35], v[160:163], v[52:55], v[32:35]\n"
      "v_mfma_f32_16x16x32_bf16 v[36:39], v[164:167], v[52:55], v[36:39]\n"
      "s_waitcnt vmcnt(14)\n"
      "global_load_dwordx4 v[232:235], v61, s[90:91] offset:256 nt\n"
      "global_load_dwordx4 v[236:239], v61, s[90:91] offset:384 nt\n"
      "v_mfma_f32_16x16x32_bf16 v[40:43], v[168:171], v[52:55], v[40:43]\n"
      "v_mfma_f32_16x16x32_bf16 v[44:47], v[172:175], v[52:55], v[44:47]\n"
      "s_waitcnt vmcnt(14)\n"
      "global_load_dwordx4 v[240:243], v61, s[90:91] offset:2048 nt\n"
      "global_load_dwordx4 v[244:247], v61, s[90:91] offset:2176 nt\n"
      "v_mfma_f32_16x16x32_bf16 v[32:35], v[176:179], v[56:59], v[32:35]\n"
      "v_mfma_f32_16x16x32_bf16 v[36:39], v[180:183], v[56:59], v[36:39]\n"
      "s_waitcnt vmcnt(14)\n"
      "global_load_dwordx4 v[248:251], v61, s[90:91] offset:2304 nt\n"
      "global_load_dwordx4 v[252:255], v61, s[90:91] offset:2432 nt\n"
      "v_mfma_f32_16x16x32_bf16 v[40:43], v[184:187], v[56:59], v[40:43]\n"
      "v_mfma_f32_16x16x32_bf16 v[44:47], v[188:191], v[56:59], v[44:47]\n"
      "s_waitcnt vmcnt(14)\n"
      "s_add_u32 s86, s100, 0x4230000\n"
      "s_cmp_ge_u32 s86, 0x43f0000\n"
      "s_cselect_b32 s87, 0x3f0000, 0\n"
      "s_sub_u32 s86, s86, s87\n"
      "s_add_u32 s90, s36, s86\n"
      "s_addc_u32 s91, s37, 0\n"
      "global_load_dwordx4 a[0:3], v60, s[90:91] offset:-4096 nt\n"
      "global_load_dwordx4 a[4:7], v60, s[90:91] offset:-3840 nt\n"
      "v_mfma_f32_16x16x32_bf16 v[48:51], v[192:195], v[0:3], 0\n"
      "v_mfma_f32_16x16x32_bf16 v[48:51], v[196:199], v[4:7], v[48:51]\n"
      "s_waitcnt vmcnt(14)\n"
      "global_load_dwordx4 a[8:11], v60, s[90:91] offset:-2048 nt\n"
      "global_load_dwordx4 a[12:15], v60, s[90:91] offset:-1792 nt\n"
      "v_mfma_f32_16x16x32_bf16 v[48:51], v[200:203], v[8:11], v[48:51]\n"
      "v_mfma_f32_16x16x32_bf16 v[48:51], v[204:207], v[12:15], v[48:51]\n"
      "s_waitcnt vmcnt(14)\n"
      "global_load_dwordx4 a[16:19], v60, s[90:91] offset:0 nt\n"
      "global_load_dwordx4 a[20:23], v60, s[90:91] offset:256 nt\n"
      "v_mfma_f32_16x16x32_bf16 v[48:51], v[208:211], v[16:19], v[48:51]\n"
      "v_mfma_f32_16x16x32_bf16 v[48:51], v[212:215], v[20:23], v[48:51]\n"
      "s_waitcnt vmcnt(14)\n"
      "global_load_dwordx4 a[24:27], v60, s[90:91] offset:2048 nt\n"
      "global_load_dwordx4 a[28:31], v60, s[90:91] offset:2304 nt\n"
      "v_mfma_f32_16x16x32_bf16 v[48:51], v[216:219], v[24:27], v[48:51]\n"
      "v_mfma_f32_16x16x32_bf16 v[48:51], v[220:223], v[28:31], v[48:51]\n"
      "s_nop 7\n"
      "v_cndmask_b32_e64 v53, v48, v49, s[96:97]\n"
      "v_cndmask_b32_e64 v54, v50, v51, s[96:97]\n"
      "v_cndmask_b32_e64 v52, v53, v54, s[98:99]\n"
      "s_nop 1\n"
      "v_add_f32_dpp v52, v52, v52 quad_perm:[1,0,3,2] row_mask:0xf bank_mask:0xf\n"
      "s_nop 1\n"
      "v_add_f32_dpp v52, v52, v52 quad_perm:[2,3,0,1] row_mask:0xf bank_mask:0xf\n"
      "v_cmp_lt_f32_e32 vcc, s60, v52\n"
      "s_cbranch_vccz NORS_87_%=\n"
      "s_swappc_b64 s[66:67], s[68:69]\n"
      "NORS_87_%=:\n"
      "v_subrev_f32_e32 v52, s60, v52\n"
      "v_mul_f32_e32 v52, 0x3db8aa3b, v52\n"
      "v_exp_f32_e32 v52, v52\n"
      "s_nop 0\n"
      "v_cvt_pk_bf16_f32 v53, v52, v52\n"
      "v_add_f32_e32 v62, v62, v52\n"
      "v_and_b32_e32 v53, v53, v63\n"
      "s_nop 0\n"
      "v_add_f32_dpp v62, v52, v62 row_half_mirror row_mask:0xf bank_mask:0xf\n"
      "v_mov_b32_dpp v56, v52 row_half_mirror row_mask:0xf bank_mask:0xf\n"
      "v_cvt_pk_bf16_f32 v56, v56, v56\n"
      "v_and_b32_e32 v56, v56, v63\n"
      "v_cndmask_b32_e64 v55, v53, v56, s[50:51]\n"
      "v_cndmask_b32_e64 v59, v56, v53, s[50:51]\n"
      "v_cndmask_b32_e64 v52, 0, v55, s[52:53]\n"
      "v_cndmask_b32_e64 v53, 0, v55, s[54:55]\n"
      "v_cndmask_b32_e64 v54, 0, v55, s[56:57]\n"
      "v_cndmask_b32_e64 v55, 0, v55, s[58:59]\n"
      "v_cndmask_b32_e64 v56, 0, v59, s[52:53]\n"
      "v_cndmask_b32_e64 v57, 0, v59, s[54:55]\n"
      "v_cndmask_b32_e64 v58, 0, v59, s[56:57]\n"
      "v_cndmask_b32_e64 v59, 0, v59, s[58:59]\n"
      "s_waitcnt vmcnt(14)\n"
      "s_add_u32 s86, s100, 0x4230000\n"
      "s_cmp_ge_u32 s86, 0x43f0000\n"
      "s_cselect_b32 s87, 0x3f0000, 0\n"
      "s_sub_u32 s86, s86, s87\n"
      "s_add_u32 s90, s38, s86\n"
      "s_addc_u32 s91, s39, 0\n"
      "global_load_dwordx4 a[32:35], v61, s[90:91] offset:0 nt\n"
      "global_load_dwordx4 a[36:39], v61, s[90:91] offset:128 nt\n"
      "v_mfma_f32_16x16x32_bf16 v[32:35], v[224:227], v[52:55], v[32:35]\n"
      "v_mfma_f32_16x16x32_bf16 v[36:39], v[228:231], v[52:55], v[36:39]\n"
      "s_waitcnt vmcnt(14)\n"
      "global_load_dwordx4 a[40:43], v61, s[90:91] offset:256 nt\n"
      "global_load_dwordx4 a[44:47], v61, s[90:91] offset:384 nt\n"
      "v_mfma_f32_16x16x32_bf16 v[40:43], v[232:235], v[52:55], v[40:43]\n"
      "v_mfma_f32_16x16x32_bf16 v[44:47], v[236:239], v[52:55], v[44:47]\n"
      "s_waitcnt vmcnt(14)\n"
      "global_load_dwordx4 a[48:51], v61, s[90:91] offset:2048 nt\n"
      "global_load_dwordx4 a[52:55], v61, s[90:91] offset:2176 nt\n"
      "v_mfma_f32_16x16x32_bf16 v[32:35], v[240:243], v[56:59], v[32:35]\n"
      "v_mfma_f32_16x16x32_bf16 v[36:39], v[244:247], v[56:59], v[36:39]\n"
      "s_waitcnt vmcnt(14)\n"
      "global_load_dwordx4 a[56:59], v61, s[90:91] offset:2304 nt\n"
      "global_load_dwordx4 a[60:63], v61, s[90:91] offset:2432 nt\n"
      "v_mfma_f32_16x16x32_bf16 v[40:43], v[248:251], v[56:59], v[40:43]\n"
      "v_mfma_f32_16x16x32_bf16 v[44:47], v[252:255], v[56:59], v[44:47]\n"
      "s_waitcnt vmcnt(14)\n"
      "s_add_u32 s86, s100, 0x42a0000\n"
      "s_cmp_ge_u32 s86, 0x43f0000\n"
      "s_cselect_b32 s87, 0x3f0000, 0\n"
      "s_sub_u32 s86, s86, s87\n"
      "s_add_u32 s90, s36, s86\n"
      "s_addc_u32 s91, s37, 0\n"
      "global_load_dwordx4 a[64:67], v60, s[90:91] offset:-4096 nt\n"
      "global_load_dwordx4 a[68:71], v60, s[90:91] offset:-3840 nt\n"
      "v_mfma_f32_16x16x32_bf16 v[48:51], a[0:3], v[0:3], 0\n"
      "v_mfma_f32_16x16x32_bf16 v[48:51], a[4:7], v[4:7], v[48:51]\n"
      "s_waitcnt vmcnt(14)\n"
      "global_load_dwordx4 a[72:75], v60, s[90:91] offset:-2048 nt\n"
      "global_load_dwordx4 a[76:79], v60, s[90:91] offset:-1792 nt\n"
      "v_mfma_f32_16x16x32_bf16 v[48:51], a[8:11], v[8:11], v[48:51]\n"
      "v_mfma_f32_16x16x32_bf16 v[48:51], a[12:15], v[12:15], v[48:51]\n"
      "s_waitcnt vmcnt(14)\n"
      "global_load_dwordx4 a[80:83], v60, s[90:91] offset:0 nt\n"
      "global_load_dwordx4 a[84:87], v60, s[90:91] offset:256 nt\n"
      "v_mfma_f32_16x16x32_bf16 v[48:51], a[16:19], v[16:19], v[48:51]\n"
      "v_mfma_f32_16x16x32_bf16 v[48:51], a[20:23], v[20:23], v[48:51]\n"
      "s_waitcnt vmcnt(14)\n"
      "global_load_dwordx4 a[88:91], v60, s[90:91] offset:2048 nt\n"
      "global_load_dwordx4 a[92:95], v60, s[90:91] offset:2304 nt\n"
      "v_mfma_f32_16x16x32_bf16 v[48:51], a[24:27], v[24:27], v[48:51]\n"
      "v_mfma_f32_16x16x32_bf16 v[48:51], a[28:31], v[28:31], v[48:51]\n"
      "s_nop 7\n"
      "v_cndmask_b32_e64 v53, v48, v49, s[96:97]\n"
      "v_cndmask_b32_e64 v54, v50, v51, s[96:97]\n"
      "v_cndmask_b32_e64 v52, v53, v54, s[98:99]\n"
      "s_nop 1\n"
      "v_add_f32_dpp v52, v52, v52 quad_perm:[1,0,3,2] row_mask:0xf bank_mask:0xf\n"
      "s_nop 1\n"
      "v_add_f32_dpp v52, v52, v52 quad_perm:[2,3,0,1] row_mask:0xf bank_mask:0xf\n"
      "v_cmp_lt_f32_e32 vcc, s60, v52\n"
      "s_cbranch_vccz NORS_88_%=\n"
      "s_swappc_b64 s[66:67], s[68:69]\n"
      "NORS_88_%=:\n"
      "v_subrev_f32_e32 v52, s60, v52\n"
      "v_mul_f32_e32 v52, 0x3db8aa3b, v52\n"
      "v_exp_f32_e32 v52, v52\n"
      "s_nop 0\n"
      "v_cvt_pk_bf16_f32 v53, v52, v52\n"
      "v_add_f32_e32 v62, v62, v52\n"
      "v_and_b32_e32 v53, v53, v63\n"
      "s_nop 0\n"
      "v_add_f32_dpp v62, v52, v62 row_half_mirror row_mask:0xf bank_mask:0xf\n"
      "v_mov_b32_dpp v56, v52 row_half_mirror row_mask:0xf bank_mask:0xf\n"
      "v_cvt_pk_bf16_f32 v56, v56, v56\n"
      "v_and_b32_e32 v56, v56, v63\n"
      "v_cndmask_b32_e64 v55, v53, v56, s[50:51]\n"
      "v_cndmask_b32_e64 v59, v56, v53, s[50:51]\n"
      "v_cndmask_b32_e64 v52, 0, v55, s[52:53]\n"
      "v_cndmask_b32_e64 v53, 0, v55, s[54:55]\n"
      "v_cndmask_b32_e64 v54, 0, v55, s[56:57]\n"
      "v_cndmask_b32_e64 v55, 0, v55, s[58:59]\n"
      "v_cndmask_b32_e64 v56, 0, v59, s[52:53]\n"
      "v_cndmask_b32_e64 v57, 0, v59, s[54:55]\n"
      "v_cndmask_b32_e64 v58, 0, v59, s[56:57]\n"
      "v_cndmask_b32_e64 v59, 0, v59, s[58:59]\n"
      "s_waitcnt vmcnt(14)\n"
      "s_add_u32 s86, s100, 0x42a0000\n"
      "s_cmp_ge_u32 s86, 0x43f0000\n"
      "s_cselect_b32 s87, 0x3f0000, 0\n"
      "s_sub_u32 s86, s86, s87\n"
      "s_add_u32 s90, s38, s86\n"
      "s_addc_u32 s91, s39, 0\n"
      "global_load_dwordx4 a[96:99], v61, s[90:91] offset:0 nt\n"
      "global_load_dwordx4 a[100:103], v61, s[90:91] offset:128 nt\n"
      "v_mfma_f32_16x16x32_bf16 v[32:35], a[32:35], v[52:55], v[32:35]\n"
      "v_mfma_f32_16x16x32_bf16 v[36:39], a[36:39], v[52:55], v[36:39]\n"
      "s_waitcnt vmcnt(14)\n"
      "global_load_dwordx4 a[104:107], v61, s[90:91] offset:256 nt\n"
      "global_load_dwordx4 a[108:111], v61, s[90:91] offset:384 nt\n"
      "v_mfma_f32_16x16x32_bf16 v[40:43], a[40:43], v[52:55], v[40:43]\n"
      "v_mfma_f32_16x16x32_bf16 v[44:47], a[44:47], v[52:55], v[44:47]\n"
      "s_waitcnt vmcnt(14)\n"
      "global_load_dwordx4 a[112:115], v61, s[90:91] offset:2048 nt\n"
      "global_load_dwordx4 a[116:119], v61, s[90:91] offset:2176 nt\n"
      "v_mfma_f32_16x16x32_bf16 v[32:35], a[48:51], v[56:59], v[32:35]\n"
      "v_mfma_f32_16x16x32_bf16 v[36:39], a[52:55], v[56:59], v[36:39]\n"
      "s_waitcnt vmcnt(14)\n"
      "global_load_dwordx4 a[120:123], v61, s[90:91] offset:2304 nt\n"
      "global_load_dwordx4 a[124:127], v61, s[90:91] offset:2432 nt\n"
      "v_mfma_f32_16x16x32_bf16 v[40:43], a[56:59], v[56:59], v[40:43]\n"
      "v_mfma_f32_16x16x32_bf16 v[44:47], a[60:63], v[56:59], v[44:47]\n"
      "s_waitcnt vmcnt(14)\n"
      "s_add_u32 s86, s100, 0x4310000\n"
      "s_cmp_ge_u32 s86, 0x43f0000\n"
      "s_cselect_b32 s87, 0x3f0000, 0\n"
      "s_sub_u32 s86, s86, s87\n"
      "s_add_u32 s90, s36, s86\n"
      "s_addc_u32 s91, s37, 0\n"
      "global_load_dwordx4 a[128:131], v60, s[90:91] offset:-4096 nt\n"
      "global_load_dwordx4 a[132:135], v60, s[90:91] offset:-3840 nt\n"
      "v_mfma_f32_16x16x32_bf16 v[48:51], a[64:67], v[0:3], 0\n"
      "v_mfma_f32_16x16x32_bf16 v[48:51], a[68:71], v[4:7], v[48:51]\n"
      "s_waitcnt vmcnt(14)\n"
      "global_load_dwordx4 a[136:139], v60, s[90:91] offset:-2048 nt\n"
      "global_load_dwordx4 a[140:143], v60, s[90:91] offset:-1792 nt\n"
      "v_mfma_f32_16x16x32_bf16 v[48:51], a[72:75], v[8:11], v[48:51]\n"
      "v_mfma_f32_16x16x32_bf16 v[48:51], a[76:79], v[12:15], v[48:51]\n"
      "s_waitcnt vmcnt(14)\n"
      "global_load_dwordx4 a[144:147], v60, s[90:91] offset:0 nt\n"
      "global_load_dwordx4 a[148:151], v60, s[90:91] offset:256 nt\n"
      "v_mfma_f32_16x16x32_bf16 v[48:51], a[80:83], v[16:19], v[48:51]\n"
      "v_mfma_f32_16x16x32_bf16 v[48:51], a[84:87], v[20:23], v[48:51]\n"
      "s_waitcnt vmcnt(14)\n"
      "global_load_dwordx4 a[152:155], v60, s[90:91] offset:2048 nt\n"
      "global_load_dwordx4 a[156:159], v60, s[90:91] offset:2304 nt\n"
      "v_mfma_f32_16x16x32_bf16 v[48:51], a[88:91], v[24:27], v[48:51]\n"
      "v_mfma_f32_16x16x32_bf16 v[48:51], a[92:95], v[28:31], v[48:51]\n"
      "s_nop 7\n"
      "v_cndmask_b32_e64 v53, v48, v49, s[96:97]\n"
      "v_cndmask_b32_e64 v54, v50, v51, s[96:97]\n"
      "v_cndmask_b32_e64 v52, v53, v54, s[98:99]\n"
      "s_nop 1\n"
      "v_add_f32_dpp v52, v52, v52 quad_perm:[1,0,3,2] row_mask:0xf bank_mask:0xf\n"
      "s_nop 1\n"
      "v_add_f32_dpp v52, v52, v52 quad_perm:[2,3,0,1] row_mask:0xf bank_mask:0xf\n"
      "v_cmp_lt_f32_e32 vcc, s60, v52\n"
      "s_cbranch_vccz NORS_89_%=\n"
      "s_swappc_b64 s[66:67], s[68:69]\n"
      "NORS_89_%=:\n"
      "v_subrev_f32_e32 v52, s60, v52\n"
      "v_mul_f32_e32 v52, 0x3db8aa3b, v52\n"
      "v_exp_f32_e32 v52, v52\n"
      "s_nop 0\n"
      "v_cvt_pk_bf16_f32 v53, v52, v52\n"
      "v_add_f32_e32 v62, v62, v52\n"
      "v_and_b32_e32 v53, v53, v63\n"
      "s_nop 0\n"
      "v_add_f32_dpp v62, v52, v62 row_half_mirror row_mask:0xf bank_mask:0xf\n"
      "v_mov_b32_dpp v56, v52 row_half_mirror row_mask:0xf bank_mask:0xf\n"
      "v_cvt_pk_bf16_f32 v56, v56, v56\n"
      "v_and_b32_e32 v56, v56, v63\n"
      "v_cndmask_b32_e64 v55, v53, v56, s[50:51]\n"
      "v_cndmask_b32_e64 v59, v56, v53, s[50:51]\n"
      "v_cndmask_b32_e64 v52, 0, v55, s[52:53]\n"
      "v_cndmask_b32_e64 v53, 0, v55, s[54:55]\n"
      "v_cndmask_b32_e64 v54, 0, v55, s[56:57]\n"
      "v_cndmask_b32_e64 v55, 0, v55, s[58:59]\n"
      "v_cndmask_b32_e64 v56, 0, v59, s[52:53]\n"
      "v_cndmask_b32_e64 v57, 0, v59, s[54:55]\n"
      "v_cndmask_b32_e64 v58, 0, v59, s[56:57]\n"
      "v_cndmask_b32_e64 v59, 0, v59, s[58:59]\n"
      "s_waitcnt vmcnt(14)\n"
      "s_add_u32 s86, s100, 0x4310000\n"
      "s_cmp_ge_u32 s86, 0x43f0000\n"
      "s_cselect_b32 s87, 0x3f0000, 0\n"
      "s_sub_u32 s86, s86, s87\n"
      "s_add_u32 s90, s38, s86\n"
      "s_addc_u32 s91, s39, 0\n"
      "global_load_dwordx4 a[160:163], v61, s[90:91] offset:0 nt\n"
      "global_load_dwordx4 a[164:167], v61, s[90:91] offset:128 nt\n"
      "v_mfma_f32_16x16x32_bf16 v[32:35], a[96:99], v[52:55], v[32:35]\n"
      "v_mfma_f32_16x16x32_bf16 v[36:39], a[100:103], v[52:55], v[36:39]\n"
      "s_waitcnt vmcnt(14)\n"
      "global_load_dwordx4 a[168:171], v61, s[90:91] offset:256 nt\n"
      "global_load_dwordx4 a[172:175], v61, s[90:91] offset:384 nt\n"
      "v_mfma_f32_16x16x32_bf16 v[40:43], a[104:107], v[52:55], v[40:43]\n"
      "v_mfma_f32_16x16x32_bf16 v[44:47], a[108:111], v[52:55], v[44:47]\n"
      "s_waitcnt vmcnt(14)\n"
      "global_load_dwordx4 a[176:179], v61, s[90:91] offset:2048 nt\n"
      "global_load_dwordx4 a[180:183], v61, s[90:91] offset:2176 nt\n"
      "v_mfma_f32_16x16x32_bf16 v[32:35], a[112:115], v[56:59], v[32:35]\n"
      "v_mfma_f32_16x16x32_bf16 v[36:39], a[116:119], v[56:59], v[36:39]\n"
      "s_waitcnt vmcnt(14)\n"
      "global_load_dwordx4 a[184:187], v61, s[90:91] offset:2304 nt\n"
      "global_load_dwordx4 a[188:191], v61, s[90:91] offset:2432 nt\n"
      "v_mfma_f32_16x16x32_bf16 v[40:43], a[120:123], v[56:59], v[40:43]\n"
      "v_mfma_f32_16x16x32_bf16 v[44:47], a[124:127], v[56:59], v[44:47]\n"
      "s_waitcnt vmcnt(14)\n"
      "s_add_u32 s86, s100, 0x4380000\n"
      "s_cmp_ge_u32 s86, 0x43f0000\n"
      "s_cselect_b32 s87, 0x3f0000, 0\n"
      "s_sub_u32 s86, s86, s87\n"
      "s_add_u32 s90, s36, s86\n"
      "s_addc_u32 s91, s37, 0\n"
      "global_load_dwordx4 a[192:195], v60, s[90:91] offset:-4096 nt\n"
      "global_load_dwordx4 a[196:199], v60, s[90:91] offset:-3840 nt\n"
      "v_mfma_f32_16x16x32_bf16 v[48:51], a[128:131], v[0:3], 0\n"
      "v_mfma_f32_16x16x32_bf16 v[48:51], a[132:135], v[4:7], v[48:51]\n"
      "s_waitcnt vmcnt(14)\n"
      "global_load_dwordx4 a[200:203], v60, s[90:91] offset:-2048 nt\n"
      "global_load_dwordx4 a[204:207], v60, s[90:91] offset:-1792 nt\n"
      "v_mfma_f32_16x16x32_bf16 v[48:51], a[136:139], v[8:11], v[48:51]\n"
      "v_mfma_f32_16x16x32_bf16 v[48:51], a[140:143], v[12:15], v[48:51]\n"
      "s_waitcnt vmcnt(14)\n"
      "global_load_dwordx4 a[208:211], v60, s[90:91] offset:0 nt\n"
      "global_load_dwordx4 a[212:215], v60, s[90:91] offset:256 nt\n"
      "v_mfma_f32_16x16x32_bf16 v[48:51], a[144:147], v[16:19], v[48:51]\n"
      "v_mfma_f32_16x16x32_bf16 v[48:51], a[148:151], v[20:23], v[48:51]\n"
      "s_waitcnt vmcnt(14)\n"
      "global_load_dwordx4 a[216:219], v60, s[90:91] offset:2048 nt\n"
      "global_load_dwordx4 a[220:223], v60, s[90:91] offset:2304 nt\n"
      "v_mfma_f32_16x16x32_bf16 v[48:51], a[152:155], v[24:27], v[48:51]\n"
      "v_mfma_f32_16x16x32_bf16 v[48:51], a[156:159], v[28:31], v[48:51]\n"
      "s_nop 7\n"
      "v_cndmask_b32_e64 v53, v48, v49, s[96:97]\n"
      "v_cndmask_b32_e64 v54, v50, v51, s[96:97]\n"
      "v_cndmask_b32_e64 v52, v53, v54, s[98:99]\n"
      "s_nop 1\n"
      "v_add_f32_dpp v52, v52, v52 quad_perm:[1,0,3,2] row_mask:0xf bank_mask:0xf\n"
      "s_nop 1\n"
      "v_add_f32_dpp v52, v52, v52 quad_perm:[2,3,0,1] row_mask:0xf bank_mask:0xf\n"
      "v_cmp_lt_f32_e32 vcc, s60, v52\n"
      "s_cbranch_vccz NORS_90_%=\n"
      "s_swappc_b64 s[66:67], s[68:69]\n"
      "NORS_90_%=:\n"
      "v_subrev_f32_e32 v52, s60, v52\n"
      "v_mul_f32_e32 v52, 0x3db8aa3b, v52\n"
      "v_exp_f32_e32 v52, v52\n"
      "s_nop 0\n"
      "v_cvt_pk_bf16_f32 v53, v52, v52\n"
      "v_add_f32_e32 v62, v62, v52\n"
      "v_and_b32_e32 v53, v53, v63\n"
      "s_nop 0\n"
      "v_add_f32_dpp v62, v52, v62 row_half_mirror row_mask:0xf bank_mask:0xf\n"
      "v_mov_b32_dpp v56, v52 row_half_mirror row_mask:0xf bank_mask:0xf\n"
      "v_cvt_pk_bf16_f32 v56, v56, v56\n"
      "v_and_b32_e32 v56, v56, v63\n"
      "v_cndmask_b32_e64 v55, v53, v56, s[50:51]\n"
      "v_cndmask_b32_e64 v59, v56, v53, s[50:51]\n"
      "v_cndmask_b32_e64 v52, 0, v55, s[52:53]\n"
      "v_cndmask_b32_e64 v53, 0, v55, s[54:55]\n"
      "v_cndmask_b32_e64 v54, 0, v55, s[56:57]\n"
      "v_cndmask_b32_e64 v55, 0, v55, s[58:59]\n"
      "v_cndmask_b32_e64 v56, 0, v59, s[52:53]\n"
      "v_cndmask_b32_e64 v57, 0, v59, s[54:55]\n"
      "v_cndmask_b32_e64 v58, 0, v59, s[56:57]\n"
      "v_cndmask_b32_e64 v59, 0, v59, s[58:59]\n"
      "s_waitcnt vmcnt(14)\n"
      "s_add_u32 s86, s100, 0x4380000\n"
      "s_cmp_ge_u32 s86, 0x43f0000\n"
      "s_cselect_b32 s87, 0x3f0000, 0\n"
      "s_sub_u32 s86, s86, s87\n"
      "s_add_u32 s90, s38, s86\n"
      "s_addc_u32 s91, s39, 0\n"
      "global_load_dwordx4 a[224:227], v61, s[90:91] offset:0 nt\n"
      "global_load_dwordx4 a[228:231], v61, s[90:91] offset:128 nt\n"
      "v_mfma_f32_16x16x32_bf16 v[32:35], a[160:163], v[52:55], v[32:35]\n"
      "v_mfma_f32_16x16x32_bf16 v[36:39], a[164:167], v[52:55], v[36:39]\n"
      "s_waitcnt vmcnt(14)\n"
      "global_load_dwordx4 a[232:235], v61, s[90:91] offset:256 nt\n"
      "global_load_dwordx4 a[236:239], v61, s[90:91] offset:384 nt\n"
      "v_mfma_f32_16x16x32_bf16 v[40:43], a[168:171], v[52:55], v[40:43]\n"
      "v_mfma_f32_16x16x32_bf16 v[44:47], a[172:175], v[52:55], v[44:47]\n"
      "s_waitcnt vmcnt(14)\n"
      "global_load_dwordx4 a[240:243], v61, s[90:91] offset:2048 nt\n"
      "global_load_dwordx4 a[244:247], v61, s[90:91] offset:2176 nt\n"
      "v_mfma_f32_16x16x32_bf16 v[32:35], a[176:179], v[56:59], v[32:35]\n"
      "v_mfma_f32_16x16x32_bf16 v[36:39], a[180:183], v[56:59], v[36:39]\n"
      "s_waitcnt vmcnt(14)\n"
      "global_load_dwordx4 a[248:251], v61, s[90:91] offset:2304 nt\n"
      "global_load_dwordx4 a[252:255], v61, s[90:91] offset:2432 nt\n"
      "v_mfma_f32_16x16x32_bf16 v[40:43], a[184:187], v[56:59], v[40:43]\n"
      "v_mfma_f32_16x16x32_bf16 v[44:47], a[188:191], v[56:59], v[44:47]\n"
      "s_waitcnt vmcnt(14)\n"
      "v_mfma_f32_16x16x32_bf16 v[48:51], a[192:195], v[0:3], 0\n"
      "v_mfma_f32_16x16x32_bf16 v[48:51], a[196:199], v[4:7], v[48:51]\n"
      "s_waitcnt vmcnt(12)\n"
      "v_mfma_f32_16x16x32_bf16 v[48:51], a[200:203], v[8:11], v[48:51]\n"
      "v_mfma_f32_16x16x32_bf16 v[48:51], a[204:207], v[12:15], v[48:51]\n"
      "s_waitcnt vmcnt(10)\n"
      "v_mfma_f32_16x16x32_bf16 v[48:51], a[208:211], v[16:19], v[48:51]\n"
      "v_mfma_f32_16x16x32_bf16 v[48:51], a[212:215], v[20:23], v[48:51]\n"
      "s_waitcnt vmcnt(8)\n"
      "v_mfma_f32_16x16x32_bf16 v[48:51], a[216:219], v[24:27], v[48:51]\n"
      "v_mfma_f32_16x16x32_bf16 v[48:51], a[220:223], v[28:31], v[48:51]\n"
      "s_nop 7\n"
      "v_cndmask_b32_e64 v53, v48, v49, s[96:97]\n"
      "v_cndmask_b32_e64 v54, v50, v51, s[96:97]\n"
      "v_cndmask_b32_e64 v52, v53, v54, s[98:99]\n"
      "s_nop 1\n"
      "v_add_f32_dpp v52, v52, v52 quad_perm:[1,0,3,2] row_mask:0xf bank_mask:0xf\n"
      "s_nop 1\n"
      "v_add_f32_dpp v52, v52, v52 quad_perm:[2,3,0,1] row_mask:0xf bank_mask:0xf\n"
      "v_cmp_lt_f32_e32 vcc, s60, v52\n"
      "s_cbranch_vccz NORS_91_%=\n"
      "s_swappc_b64 s[66:67], s[68:69]\n"
      "NORS_91_%=:\n"
      "v_subrev_f32_e32 v52, s60, v52\n"
      "v_mul_f32_e32 v52, 0x3db8aa3b, v52\n"
      "v_exp_f32_e32 v52, v52\n"
      "s_nop 0\n"
      "v_cvt_pk_bf16_f32 v53, v52, v52\n"
      "v_add_f32_e32 v62, v62, v52\n"
      "v_and_b32_e32 v53, v53, v63\n"
      "s_nop 0\n"
      "v_add_f32_dpp v62, v52, v62 row_half_mirror row_mask:0xf bank_mask:0xf\n"
      "v_mov_b32_dpp v56, v52 row_half_mirror row_mask:0xf bank_mask:0xf\n"
      "v_cvt_pk_bf16_f32 v56, v56, v56\n"
      "v_and_b32_e32 v56, v56, v63\n"
      "v_cndmask_b32_e64 v55, v53, v56, s[50:51]\n"
      "v_cndmask_b32_e64 v59, v56, v53, s[50:51]\n"
      "v_cndmask_b32_e64 v52, 0, v55, s[52:53]\n"
      "v_cndmask_b32_e64 v53, 0, v55, s[54:55]\n"
      "v_cndmask_b32_e64 v54, 0, v55, s[56:57]\n"
      "v_cndmask_b32_e64 v55, 0, v55, s[58:59]\n"
      "v_cndmask_b32_e64 v56, 0, v59, s[52:53]\n"
      "v_cndmask_b32_e64 v57, 0, v59, s[54:55]\n"
      "v_cndmask_b32_e64 v58, 0, v59, s[56:57]\n"
      "v_cndmask_b32_e64 v59, 0, v59, s[58:59]\n"
      "s_waitcnt vmcnt(6)\n"
      "v_mfma_f32_16x16x32_bf16 v[32:35], a[224:227], v[52:55], v[32:35]\n"
      "v_mfma_f32_16x16x32_bf16 v[36:39], a[228:231], v[52:55], v[36:39]\n"
      "s_waitcnt vmcnt(4)\n"
      "v_mfma_f32_16x16x32_bf16 v[40:43], a[232:235], v[52:55], v[40:43]\n"
      "v_mfma_f32_16x16x32_bf16 v[44:47], a[236:239], v[52:55], v[44:47]\n"
      "s_waitcnt vmcnt(2)\n"
      "v_mfma_f32_16x16x32_bf16 v[32:35], a[240:243], v[56:59], v[32:35]\n"
      "v_mfma_f32_16x16x32_bf16 v[36:39], a[244:247], v[56:59], v[36:39]\n"
      "s_waitcnt vmcnt(0)\n"
      "v_mfma_f32_16x16x32_bf16 v[40:43], a[248:251], v[56:59], v[40:43]\n"
      "v_mfma_f32_16x16x32_bf16 v[44:47], a[252:255], v[56:59], v[44:47]\n"
      "s_add_u32 s74, s44, 0x80000\n"
      "s_addc_u32 s75, s45, 0\n"
      "s_add_u32 s76, s46, 0x1000\n"
      "s_addc_u32 s77, s47, 0\n"
      "s_mov_b32 s72, 0x25050\n"
      "s_mov_b32 s73, 0x260a8\n"
      "s_nop 1\n"
      "s_swappc_b64 s[66:67], s[70:71]\n"
      "v_mbcnt_lo_u32_b32 v52, -1, 0\n"
      "v_mbcnt_hi_u32_b32 v52, -1, v52\n"
      "v_lshlrev_b32_e32 v52, 4, v52\n"
      "s_lshl_b32 s80, s48, 12\n"
      "s_add_u32 s80, s80, 0x20000\n"
      "v_add_u32_e32 v52, s80, v52\n"
      "ds_read_b128 v[32:35], v52 offset:0\n"
      "ds_read_b128 v[36:39], v52 offset:1024\n"
      "ds_read_b128 v[40:43], v52 offset:2048\n"
      "ds_read_b128 v[44:47], v52 offset:3072\n"
      "v_mbcnt_lo_u32_b32 v53, -1, 0\n"
      "v_mbcnt_hi_u32_b32 v53, -1, v53\n"
      "v_lshlrev_b32_e32 v53, 2, v53\n"
      "s_lshl_b32 s80, s48, 8\n"
      "s_add_u32 s80, s80, 0x26240\n"
      "v_add_u32_e32 v53, s80, v53\n"
      "ds_read_b32 v62, v53\n"
      "s_mov_b32 s60, s78\n"
      "s_waitcnt lgkmcnt(0)\n"
      "v_mbcnt_lo_u32_b32 v53, -1, 0\n"
      "v_mbcnt_hi_u32_b32 v53, -1, v53\n"
      "v_lshrrev_b32_e32 v52, 4, v53\n"
      "v_lshlrev_b32_e32 v52, 6, v52\n"
      "v_and_b32_e32 v53, 3, v53\n"
      "v_lshl_add_u32 v52, v53, 4, v52\n"
      "v_add_u32_e32 v52, 0x26840, v52\n"
      "s_mov_b32 vcc_lo, 0xf000f\n"
      "s_mov_b32 vcc_hi, 0xf000f\n"
      "v_mov_b32_e32 v53, 0x26120\n"
      "v_cndmask_b32_e32 v53, v53, v52, vcc\n"
      "ds_read_b128 v[0:3], v53 offset:0\n"
      "ds_read_b128 v[4:7], v53 offset:256\n"
      "s_mov_b32 vcc_lo, 0xf000f0\n"
      "s_mov_b32 vcc_hi, 0xf000f0\n"
      "v_mov_b32_e32 v53, 0x26120\n"
      "v_cndmask_b32_e32 v53, v53, v52, vcc\n"
      "ds_read_b128 v[8:11], v53 offset:0\n"
      "ds_read_b128 v[12:15], v53 offset:256\n"
      "s_mov_b32 vcc_lo, 0xf000f00\n"
      "s_mov_b32 vcc_hi, 0xf000f00\n"
      "v_mov_b32_e32 v53, 0x26120\n"
      "v_cndmask_b32_e32 v53, v53, v52, vcc\n"
      "ds_read_b128 v[16:19], v53 offset:0\n"
      "ds_read_b128 v[20:23], v53 offset:256\n"
      "s_mov_b32 vcc_lo, 0xf000f000\n"
      "s_mov_b32 vcc_hi, 0xf000f000\n"
      "v_mov_b32_e32 v53, 0x26120\n"
      "v_cndmask_b32_e32 v53, v53, v52, vcc\n"
      "ds_read_b128 v[24:27], v53 offset:0\n"
      "ds_read_b128 v[28:31], v53 offset:256\n"
      "s_waitcnt lgkmcnt(0)\n"
      "v_mfma_f32_16x16x32_bf16 v[48:51], v[64:67], v[0:3], 0\n"
      "v_mfma_f32_16x16x32_bf16 v[48:51], v[68:71], v[4:7], v[48:51]\n"
      "v_mfma_f32_16x16x32_bf16 v[48:51], v[72:75], v[8:11], v[48:51]\n"
      "v_mfma_f32_16x16x32_bf16 v[48:51], v[76:79], v[12:15], v[48:51]\n"
      "v_mfma_f32_16x16x32_bf16 v[48:51], v[80:83], v[16:19], v[48:51]\n"
      "v_mfma_f32_16x16x32_bf16 v[48:51], v[84:87], v[20:23], v[48:51]\n"
      "v_mfma_f32_16x16x32_bf16 v[48:51], v[88:91], v[24:27], v[48:51]\n"
      "v_mfma_f32_16x16x32_bf16 v[48:51], v[92:95], v[28:31], v[48:51]\n"
      "s_nop 7\n"
      "v_cndmask_b32_e64 v53, v48, v49, s[96:97]\n"
      "v_cndmask_b32_e64 v54, v50, v51, s[96:97]\n"
      "v_cndmask_b32_e64 v52, v53, v54, s[98:99]\n"
      "s_nop 1\n"
      "v_add_f32_dpp v52, v52, v52 quad_perm:[1,0,3,2] row_mask:0xf bank_mask:0xf\n"
      "s_nop 1\n"
      "v_add_f32_dpp v52, v52, v52 quad_perm:[2,3,0,1] row_mask:0xf bank_mask:0xf\n"
      "v_cmp_lt_f32_e32 vcc, s60, v52\n"
      "s_cbranch_vccz NORS_92_%=\n"
      "s_swappc_b64 s[66:67], s[68:69]\n"
      "NORS_92_%=:\n"
      "v_subrev_f32_e32 v52, s60, v52\n"
      "v_mul_f32_e32 v52, 0x3db8aa3b, v52\n"
      "v_exp_f32_e32 v52, v52\n"
      "s_nop 0\n"
      "v_cvt_pk_bf16_f32 v53, v52, v52\n"
      "v_add_f32_e32 v62, v62, v52\n"
      "v_and_b32_e32 v53, v53, v63\n"
      "s_nop 0\n"
      "v_add_f32_dpp v62, v52, v62 row_half_mirror row_mask:0xf bank_mask:0xf\n"
      "v_mov_b32_dpp v56, v52 row_half_mirror row_mask:0xf bank_mask:0xf\n"
      "v_cvt_pk_bf16_f32 v56, v56, v56\n"
      "v_and_b32_e32 v56, v56, v63\n"
      "v_cndmask_b32_e64 v55, v53, v56, s[50:51]\n"
      "v_cndmask_b32_e64 v59, v56, v53, s[50:51]\n"
      "v_cndmask_b32_e64 v52, 0, v55, s[52:53]\n"
      "v_cndmask_b32_e64 v53, 0, v55, s[54:55]\n"
      "v_cndmask_b32_e64 v54, 0, v55, s[56:57]\n"
      "v_cndmask_b32_e64 v55, 0, v55, s[58:59]\n"
      "v_cndmask_b32_e64 v56, 0, v59, s[52:53]\n"
      "v_cndmask_b32_e64 v57, 0, v59, s[54:55]\n"
      "v_cndmask_b32_e64 v58, 0, v59, s[56:57]\n"
      "v_cndmask_b32_e64 v59, 0, v59, s[58:59]\n"
      "v_mfma_f32_16x16x32_bf16 v[32:35], v[96:99], v[52:55], v[32:35]\n"
      "v_mfma_f32_16x16x32_bf16 v[36:39], v[100:103], v[52:55], v[36:39]\n"
      "v_mfma_f32_16x16x32_bf16 v[40:43], v[104:107], v[52:55], v[40:43]\n"
      "v_mfma_f32_16x16x32_bf16 v[44:47], v[108:111], v[52:55], v[44:47]\n"
      "v_mfma_f32_16x16x32_bf16 v[32:35], v[112:115], v[56:59], v[32:35]\n"
      "v_mfma_f32_16x16x32_bf16 v[36:39], v[116:119], v[56:59], v[36:39]\n"
      "v_mfma_f32_16x16x32_bf16 v[40:43], v[120:123], v[56:59], v[40:43]\n"
      "v_mfma_f32_16x16x32_bf16 v[44:47], v[124:127], v[56:59], v[44:47]\n"
      "s_nop 7\n"
      "v_mbcnt_lo_u32_b32 v52, -1, 0\n"
      "v_mbcnt_hi_u32_b32 v52, -1, v52\n"
      "v_lshlrev_b32_e32 v52, 4, v52\n"
      "v_add_u32_e32 v52, s49, v52\n"
      "ds_read_b128 v[64:67], v52 offset:0\n"
      "ds_read_b128 v[68:71], v52 offset:1024\n"
      "ds_read_b128 v[72:75], v52 offset:2048\n"
      "ds_read_b128 v[76:79], v52 offset:3072\n"
      "ds_read_b128 v[80:83], v52 offset:4096\n"
      "ds_read_b128 v[84:87], v52 offset:5120\n"
      "ds_read_b128 v[88:91], v52 offset:6144\n"
      "ds_read_b128 v[92:95], v52 offset:7168\n"
      "ds_read_b128 v[96:99], v52 offset:8192\n"
      "ds_read_b128 v[100:103], v52 offset:9216\n"
      "ds_read_b128 v[104:107], v52 offset:10240\n"
      "ds_read_b128 v[108:111], v52 offset:11264\n"
      "ds_read_b128 v[112:115], v52 offset:12288\n"
      "ds_read_b128 v[116:119], v52 offset:13312\n"
      "ds_read_b128 v[120:123], v52 offset:14336\n"
      "ds_read_b128 v[124:127], v52 offset:15360\n"
      "s_waitcnt lgkmcnt(0)\n"
      "s_nop 0\n"
      "v_mbcnt_lo_u32_b32 v48, -1, 0\n"
      "v_mbcnt_hi_u32_b32 v48, -1, v48\n"
      "v_lshlrev_b32_e32 v48, 4, v48\n"
      "v_add_u32_e32 v48, s49, v48\n"
      "ds_write_b128 v48, v[128:131] offset:0\n"
      "ds_write_b128 v48, v[132:135] offset:1024\n"
      "ds_write_b128 v48, v[136:139] offset:2048\n"
      "ds_write_b128 v48, v[140:143] offset:3072\n"
      "ds_write_b128 v48, v[144:147] offset:4096\n"
      "ds_write_b128 v48, v[148:151] offset:5120\n"
      "ds_write_b128 v48, v[152:155] offset:6144\n"
      "ds_write_b128 v48, v[156:159] offset:7168\n"
      "ds_write_b128 v48, v[160:163] offset:8192\n"
      "ds_write_b128 v48, v[164:167] offset:9216\n"
      "ds_write_b128 v48, v[168:171] offset:10240\n"
      "ds_write_b128 v48, v[172:175] offset:11264\n"
      "ds_write_b128 v48, v[176:179] offset:12288\n"
      "ds_write_b128 v48, v[180:183] offset:13312\n"
      "ds_write_b128 v48, v[184:187] offset:14336\n"
      "ds_write_b128 v48, v[188:191] offset:15360\n"
      "s_waitcnt lgkmcnt(0)\n"
      "s_add_u32 s86, s100, 0x380000\n"
      "s_cmp_ge_u32 s86, 0x3f0000\n"
      "s_cselect_b32 s87, 0x3f0000, 0\n"
      "s_sub_u32 s86, s86, s87\n"
      "s_add_u32 s90, s36, s86\n"
      "s_addc_u32 s91, s37, 0\n"
      "global_load_dwordx4 v[128:131], v60, s[90:91] offset:-4096 nt\n"
      "global_load_dwordx4 v[132:135], v60, s[90:91] offset:-3840 nt\n"
      "global_load_dwordx4 v[136:139], v60, s[90:91] offset:-2048 nt\n"
      "global_load_dwordx4 v[140:143], v60, s[90:91] offset:-1792 nt\n"
      "global_load_dwordx4 v[144:147], v60, s[90:91] offset:0 nt\n"
      "global_load_dwordx4 v[148:151], v60, s[90:91] offset:256 nt\n"
      "global_load_dwordx4 v[152:155], v60, s[90:91] offset:2048 nt\n"
      "global_load_dwordx4 v[156:159], v60, s[90:91] offset:2304 nt\n"
      "s_add_u32 s86, s100, 0x380000\n"
      "s_cmp_ge_u32 s86, 0x3f0000\n"
      "s_cselect_b32 s87, 0x3f0000, 0\n"
      "s_sub_u32 s86, s86, s87\n"
      "s_add_u32 s90, s38, s86\n"
      "s_addc_u32 s91, s39, 0\n"
      "global_load_dwordx4 v[160:163], v61, s[90:91] offset:0 nt\n"
      "global_load_dwordx4 v[164:167], v61, s[90:91] offset:128 nt\n"
      "global_load_dwordx4 v[168:171], v61, s[90:91] offset:256 nt\n"
      "global_load_dwordx4 v[172:175], v61, s[90:91] offset:384 nt\n"
      "global_load_dwordx4 v[176:179], v61, s[90:91] offset:2048 nt\n"
      "global_load_dwordx4 v[180:183], v61, s[90:91] offset:2176 nt\n"
      "global_load_dwordx4 v[184:187], v61, s[90:91] offset:2304 nt\n"
      "global_load_dwordx4 v[188:191], v61, s[90:91] offset:2432 nt\n"
      "v_mfma_f32_16x16x32_bf16 v[48:51], v[64:67], v[0:3], 0\n"
      "v_mfma_f32_16x16x32_bf16 v[48:51], v[68:71], v[4:7], v[48:51]\n"
      "v_mfma_f32_16x16x32_bf16 v[48:51], v[72:75], v[8:11], v[48:51]\n"
      "v_mfma_f32_16x16x32_bf16 v[48:51], v[76:79], v[12:15], v[48:51]\n"
      "v_mfma_f32_16x16x32_bf16 v[48:51], v[80:83], v[16:19], v[48:51]\n"
      "v_mfma_f32_16x16x32_bf16 v[48:51], v[84:87], v[20:23], v[48:51]\n"
      "v_mfma_f32_16x16x32_bf16 v[48:51], v[88:91], v[24:27], v[48:51]\n"
      "v_mfma_f32_16x16x32_bf16 v[48:51], v[92:95], v[28:31], v[48:51]\n"
      "s_nop 7\n"
      "v_cndmask_b32_e64 v53, v48, v49, s[96:97]\n"
      "v_cndmask_b32_e64 v54, v50, v51, s[96:97]\n"
      "v_cndmask_b32_e64 v52, v53, v54, s[98:99]\n"
      "s_nop 1\n"
      "v_add_f32_dpp v52, v52, v52 quad_perm:[1,0,3,2] row_mask:0xf bank_mask:0xf\n"
      "s_nop 1\n"
      "v_add_f32_dpp v52, v52, v52 quad_perm:[2,3,0,1] row_mask:0xf bank_mask:0xf\n"
      "v_cmp_lt_f32_e32 vcc, s60, v52\n"
      "s_cbranch_vccz NORS_93_%=\n"
      "s_swappc_b64 s[66:67], s[68:69]\n"
      "NORS_93_%=:\n"
      "v_subrev_f32_e32 v52, s60, v52\n"
      "v_mul_f32_e32 v52, 0x3db8aa3b, v52\n"
      "v_exp_f32_e32 v52, v52\n"
      "s_nop 0\n"
      "v_cvt_pk_bf16_f32 v53, v52, v52\n"
      "v_add_f32_e32 v62, v62, v52\n"
      "v_and_b32_e32 v53, v53, v63\n"
      "s_nop 0\n"
      "v_add_f32_dpp v62, v52, v62 row_half_mirror row_mask:0xf bank_mask:0xf\n"
      "v_mov_b32_dpp v56, v52 row_half_mirror row_mask:0xf bank_mask:0xf\n"
      "v_cvt_pk_bf16_f32 v56, v56, v56\n"
      "v_and_b32_e32 v56, v56, v63\n"
      "v_cndmask_b32_e64 v55, v53, v56, s[50:51]\n"
      "v_cndmask_b32_e64 v59, v56, v53, s[50:51]\n"
      "v_cndmask_b32_e64 v52, 0, v55, s[52:53]\n"
      "v_cndmask_b32_e64 v53, 0, v55, s[54:55]\n"
      "v_cndmask_b32_e64 v54, 0, v55, s[56:57]\n"
      "v_cndmask_b32_e64 v55, 0, v55, s[58:59]\n"
      "v_cndmask_b32_e64 v56, 0, v59, s[52:53]\n"
      "v_cndmask_b32_e64 v57, 0, v59, s[54:55]\n"
      "v_cndmask_b32_e64 v58, 0, v59, s[56:57]\n"
      "v_cndmask_b32_e64 v59, 0, v59, s[58:59]\n"
      "v_mfma_f32_16x16x32_bf16 v[32:35], v[96:99], v[52:55], v[32:35]\n"
      "v_mfma_f32_16x16x32_bf16 v[36:39], v[100:103], v[52:55], v[36:39]\n"
      "v_mfma_f32_16x16x32_bf16 v[40:43], v[104:107], v[52:55], v[40:43]\n"
      "v_mfma_f32_16x16x32_bf16 v[44:47], v[108:111], v[52:55], v[44:47]\n"
      "v_mfma_f32_16x16x32_bf16 v[32:35], v[112:115], v[56:59], v[32:35]\n"
      "v_mfma_f32_16x16x32_bf16 v[36:39], v[116:119], v[56:59], v[36:39]\n"
      "v_mfma_f32_16x16x32_bf16 v[40:43], v[120:123], v[56:59], v[40:43]\n"
      "v_mfma_f32_16x16x32_bf16 v[44:47], v[124:127], v[56:59], v[44:47]\n"
      "s_nop 7\n"
      "v_mbcnt_lo_u32_b32 v52, -1, 0\n"
      "v_mbcnt_hi_u32_b32 v52, -1, v52\n"
      "v_lshlrev_b32_e32 v52, 4, v52\n"
      "v_add_u32_e32 v52, s49, v52\n"
      "ds_read_b128 v[64:67], v52 offset:16384\n"
      "ds_read_b128 v[68:71], v52 offset:17408\n"
      "ds_read_b128 v[72:75], v52 offset:18432\n"
      "ds_read_b128 v[76:79], v52 offset:19456\n"
      "ds_read_b128 v[80:83], v52 offset:20480\n"
      "ds_read_b128 v[84:87], v52 offset:21504\n"
      "ds_read_b128 v[88:91], v52 offset:22528\n"
      "ds_read_b128 v[92:95], v52 offset:23552\n"
      "ds_read_b128 v[96:99], v52 offset:24576\n"
      "ds_read_b128 v[100:103], v52 offset:25600\n"
      "ds_read_b128 v[104:107], v52 offset:26624\n"
      "ds_read_b128 v[108:111], v52 offset:27648\n"
      "ds_read_b128 v[112:115], v52 offset:28672\n"
      "ds_read_b128 v[116:119], v52 offset:29696\n"
      "ds_read_b128 v[120:123], v52 offset:30720\n"
      "ds_read_b128 v[124:127], v52 offset:31744\n"
      "s_waitcnt lgkmcnt(0)\n"
      "s_nop 0\n"
      "v_mbcnt_lo_u32_b32 v48, -1, 0\n"
      "v_mbcnt_hi_u32_b32 v48, -1, v48\n"
      "v_lshlrev_b32_e32 v48, 4, v48\n"
      "v_add_u32_e32 v48, s49, v48\n"
      "ds_write_b128 v48, v[192:195] offset:16384\n"
      "ds_write_b128 v48, v[196:199] offset:17408\n"
      "ds_write_b128 v48, v[200:203] offset:18432\n"
      "ds_write_b128 v48, v[204:207] offset:19456\n"
      "ds_write_b128 v48, v[208:211] offset:20480\n"
      "ds_write_b128 v48, v[212:215] offset:21504\n"
      "ds_write_b128 v48, v[216:219] offset:22528\n"
      "ds_write_b128 v48, v[220:223] offset:23552\n"
      "ds_write_b128 v48, v[224:227] offset:24576\n"
      "ds_write_b128 v48, v[228:231] offset:25600\n"
      "ds_write_b128 v48, v[232:235] offset:26624\n"
      "ds_write_b128 v48, v[236:239] offset:27648\n"
      "ds_write_b128 v48, v[240:243] offset:28672\n"
      "ds_write_b128 v48, v[244:247] offset:29696\n"
      "ds_write_b128 v48, v[248:251] offset:30720\n"
      "ds_write_b128 v48, v[252:255] offset:31744\n"
      "s_waitcnt lgkmcnt(0)\n"
      "v_mfma_f32_16x16x32_bf16 v[48:51], v[64:67], v[0:3], 0\n"
      "v_mfma_f32_16x16x32_bf16 v[48:51], v[68:71], v[4:7], v[48:51]\n"
      "v_mfma_f32_16x16x32_bf16 v[48:51], v[72:75], v[8:11], v[48:51]\n"
      "v_mfma_f32_16x16x32_bf16 v[48:51], v[76:79], v[12:15], v[48:51]\n"
      "v_mfma_f32_16x16x32_bf16 v[48:51], v[80:83], v[16:19], v[48:51]\n"
      "v_mfma_f32_16x16x32_bf16 v[48:51], v[84:87], v[20:23], v[48:51]\n"
      "v_mfma_f32_16x16x32_bf16 v[48:51], v[88:91], v[24:27], v[48:51]\n"
      "v_mfma_f32_16x16x32_bf16 v[48:51], v[92:95], v[28:31], v[48:51]\n"
      "s_nop 7\n"
      "v_cndmask_b32_e64 v53, v48, v49, s[96:97]\n"
      "v_cndmask_b32_e64 v54, v50, v51, s[96:97]\n"
      "v_cndmask_b32_e64 v52, v53, v54, s[98:99]\n"
      "s_nop 1\n"
      "v_add_f32_dpp v52, v52, v52 quad_perm:[1,0,3,2] row_mask:0xf bank_mask:0xf\n"
      "s_nop 1\n"
      "v_add_f32_dpp v52, v52, v52 quad_perm:[2,3,0,1] row_mask:0xf bank_mask:0xf\n"
      "v_cmp_lt_f32_e32 vcc, s60, v52\n"
      "s_cbranch_vccz NORS_94_%=\n"
      "s_swappc_b64 s[66:67], s[68:69]\n"
      "NORS_94_%=:\n"
      "v_subrev_f32_e32 v52, s60, v52\n"
      "v_mul_f32_e32 v52, 0x3db8aa3b, v52\n"
      "v_exp_f32_e32 v52, v52\n"
      "s_nop 0\n"
      "v_cvt_pk_bf16_f32 v53, v52, v52\n"
      "v_add_f32_e32 v62, v62, v52\n"
      "v_and_b32_e32 v53, v53, v63\n"
      "s_nop 0\n"
      "v_add_f32_dpp v62, v52, v62 row_half_mirror row_mask:0xf bank_mask:0xf\n"
      "v_mov_b32_dpp v56, v52 row_half_mirror row_mask:0xf bank_mask:0xf\n"
      "v_cvt_pk_bf16_f32 v56, v56, v56\n"
      "v_and_b32_e32 v56, v56, v63\n"
      "v_cndmask_b32_e64 v55, v53, v56, s[50:51]\n"
      "v_cndmask_b32_e64 v59, v56, v53, s[50:51]\n"
      "v_cndmask_b32_e64 v52, 0, v55, s[52:53]\n"
      "v_cndmask_b32_e64 v53, 0, v55, s[54:55]\n"
      "v_cndmask_b32_e64 v54, 0, v55, s[56:57]\n"
      "v_cndmask_b32_e64 v55, 0, v55, s[58:59]\n"
      "v_cndmask_b32_e64 v56, 0, v59, s[52:53]\n"
      "v_cndmask_b32_e64 v57, 0, v59, s[54:55]\n"
      "v_cndmask_b32_e64 v58, 0, v59, s[56:57]\n"
      "v_cndmask_b32_e64 v59, 0, v59, s[58:59]\n"
      "v_mfma_f32_16x16x32_bf16 v[32:35], v[96:99], v[52:55], v[32:35]\n"
      "v_mfma_f32_16x16x32_bf16 v[36:39], v[100:103], v[52:55], v[36:39]\n"
      "v_mfma_f32_16x16x32_bf16 v[40:43], v[104:107], v[52:55], v[40:43]\n"
      "v_mfma_f32_16x16x32_bf16 v[44:47], v[108:111], v[52:55], v[44:47]\n"
      "v_mfma_f32_16x16x32_bf16 v[32:35], v[112:115], v[56:59], v[32:35]\n"
      "v_mfma_f32_16x16x32_bf16 v[36:39], v[116:119], v[56:59], v[36:39]\n"
      "v_mfma_f32_16x16x32_bf16 v[40:43], v[120:123], v[56:59], v[40:43]\n"
      "v_mfma_f32_16x16x32_bf16 v[44:47], v[124:127], v[56:59], v[44:47]\n"
      "s_waitcnt vmcnt(14)\n"
      "s_add_u32 s86, s100, 0x40e0000\n"
      "s_cmp_ge_u32 s86, 0x43f0000\n"
      "s_cselect_b32 s87, 0x3f0000, 0\n"
      "s_sub_u32 s86, s86, s87\n"
      "s_add_u32 s90, s36, s86\n"
      "s_addc_u32 s91, s37, 0\n"
      "global_load_dwordx4 v[192:195], v60, s[90:91] offset:-4096 nt\n"
      "global_load_dwordx4 v[196:199], v60, s[90:91] offset:-3840 nt\n"
      "v_mfma_f32_16x16x32_bf16 v[48:51], v[128:131], v[0:3], 0\n"
      "v_mfma_f32_16x16x32_bf16 v[48:51], v[132:135], v[4:7], v[48:51]\n"
      "s_waitcnt vmcnt(14)\n"
      "global_load_dwordx4 v[200:203], v60, s[90:91] offset:-2048 nt\n"
      "global_load_dwordx4 v[204:207], v60, s[90:91] offset:-1792 nt\n"
      "v_mfma_f32_16x16x32_bf16 v[48:51], v[136:139], v[8:11], v[48:51]\n"
      "v_mfma_f32_16x16x32_bf16 v[48:51], v[140:143], v[12:15], v[48:51]\n"
      "s_waitcnt vmcnt(14)\n"
      "global_load_dwordx4 v[208:211], v60, s[90:91] offset:0 nt\n"
      "global_load_dwordx4 v[212:215], v60, s[90:91] offset:256 nt\n"
      "v_mfma_f32_16x16x32_bf16 v[48:51], v[144:147], v[16:19], v[48:51]\n"
      "v_mfma_f32_16x16x32_bf16 v[48:51], v[148:151], v[20:23], v[48:51]\n"
      "s_waitcnt vmcnt(14)\n"
      "global_load_dwordx4 v[216:219], v60, s[90:91] offset:2048 nt\n"
      "global_load_dwordx4 v[220:223], v60, s[90:91] offset:2304 nt\n"
      "v_mfma_f32_16x16x32_bf16 v[48:51], v[152:155], v[24:27], v[48:51]\n"
      "v_mfma_f32_16x16x32_bf16 v[48:51], v[156:159], v[28:31], v[48:51]\n"
      "s_nop 7\n"
      "v_cndmask_b32_e64 v53, v48, v49, s[96:97]\n"
      "v_cndmask_b32_e64 v54, v50, v51, s[96:97]\n"
      "v_cndmask_b32_e64 v52, v53, v54, s[98:99]\n"
      "s_nop 1\n"
      "v_add_f32_dpp v52, v52, v52 quad_perm:[1,0,3,2] row_mask:0xf bank_mask:0xf\n"
      "s_nop 1\n"
      "v_add_f32_dpp v52, v52, v52 quad_perm:[2,3,0,1] row_mask:0xf bank_mask:0xf\n"
      "v_cmp_lt_f32_e32 vcc, s60, v52\n"
      "s_cbranch_vccz NORS_95_%=\n"
      "s_swappc_b64 s[66:67], s[68:69]\n"
      "NORS_95_%=:\n"
      "v_subrev_f32_e32 v52, s60, v52\n"
      "v_mul_f32_e32 v52, 0x3db8aa3b, v52\n"
      "v_exp_f32_e32 v52, v52\n"
      "s_nop 0\n"
      "v_cvt_pk_bf16_f32 v53, v52, v52\n"
      "v_add_f32_e32 v62, v62, v52\n"
      "v_and_b32_e32 v53, v53, v63\n"
      "s_nop 0\n"
      "v_add_f32_dpp v62, v52, v62 row_half_mirror row_mask:0xf bank_mask:0xf\n"
      "v_mov_b32_dpp v56, v52 row_half_mirror row_mask:0xf bank_mask:0xf\n"
      "v_cvt_pk_bf16_f32 v56, v56, v56\n"
      "v_and_b32_e32 v56, v56, v63\n"
      "v_cndmask_b32_e64 v55, v53, v56, s[50:51]\n"
      "v_cndmask_b32_e64 v59, v56, v53, s[50:51]\n"
      "v_cndmask_b32_e64 v52, 0, v55, s[52:53]\n"
      "v_cndmask_b32_e64 v53, 0, v55, s[54:55]\n"
      "v_cndmask_b32_e64 v54, 0, v55, s[56:57]\n"
      "v_cndmask_b32_e64 v55, 0, v55, s[58:59]\n"
      "v_cndmask_b32_e64 v56, 0, v59, s[52:53]\n"
      "v_cndmask_b32_e64 v57, 0, v59, s[54:55]\n"
      "v_cndmask_b32_e64 v58, 0, v59, s[56:57]\n"
      "v_cndmask_b32_e64 v59, 0, v59, s[58:59]\n"
      "s_waitcnt vmcnt(14)\n"
      "s_add_u32 s86, s100, 0x4070000\n"
      "s_cmp_ge_u32 s86, 0x43f0000\n"
      "s_cselect_b32 s87, 0x3f0000, 0\n"
      "s_sub_u32 s86, s86, s87\n"
      "s_add_u32 s90, s36, s86\n"
      "s_addc_u32 s91, s37, 0\n"
      "global_load_dwordx4 v[224:227], v60, s[90:91] offset:-4096 nt\n"
      "global_load_dwordx4 v[228:231], v60, s[90:91] offset:-3840 nt\n"
      "v_mfma_f32_16x16x32_bf16 v[32:35], v[160:163], v[52:55], v[32:35]\n"
      "v_mfma_f32_16x16x32_bf16 v[36:39], v[164:167], v[52:55], v[36:39]\n"
      "s_waitcnt vmcnt(14)\n"
      "global_load_dwordx4 v[232:235], v60, s[90:91] offset:-2048 nt\n"
      "global_load_dwordx4 v[236:239], v60, s[90:91] offset:-1792 nt\n"
      "v_mfma_f32_16x16x32_bf16 v[40:43], v[168:171], v[52:55], v[40:43]\n"
      "v_mfma_f32_16x16x32_bf16 v[44:47], v[172:175], v[52:55], v[44:47]\n"
      "s_waitcnt vmcnt(14)\n"
      "global_load_dwordx4 v[240:243], v60, s[90:91] offset:0 nt\n"
      "global_load_dwordx4 v[244:247], v60, s[90:91] offset:256 nt\n"
      "v_mfma_f32_16x16x32_bf16 v[32:35], v[176:179], v[56:59], v[32:35]\n"
      "v_mfma_f32_16x16x32_bf16 v[36:39], v[180:183], v[56:59], v[36:39]\n"
      "s_waitcnt vmcnt(14)\n"
      "global_load_dwordx4 v[248:251], v60, s[90:91] offset:2048 nt\n"
      "global_load_dwordx4 v[252:255], v60, s[90:91] offset:2304 nt\n"
      "v_mfma_f32_16x16x32_bf16 v[40:43], v[184:187], v[56:59], v[40:43]\n"
      "v_mfma_f32_16x16x32_bf16 v[44:47], v[188:191], v[56:59], v[44:47]\n"
      "s_add_u32 s74, s44, 0x40000\n"
      "s_addc_u32 s75, s45, 0\n"
      "s_add_u32 s76, s46, 0x800\n"
      "s_addc_u32 s77, s47, 0\n"
      "s_mov_b32 s72, 0x24000\n"
      "s_mov_b32 s73, 0x260a4\n"
      "s_nop 1\n"
      "s_swappc_b64 s[66:67], s[70:71]\n"
      "s_waitcnt vmcnt(14)\n"
      "s_add_u32 s86, s100, 0x4000000\n"
      "s_cmp_ge_u32 s86, 0x43f0000\n"
      "s_cselect_b32 s87, 0x3f0000, 0\n"
      "s_sub_u32 s86, s86, s87\n"
      "s_add_u32 s90, s36, s86\n"
      "s_addc_u32 s91, s37, 0\n"
      "global_load_dwordx4 v[64:67], v60, s[90:91] offset:-4096 nt\n"
      "global_load_dwordx4 v[68:71], v60, s[90:91] offset:-3840 nt\n"
      "s_waitcnt vmcnt(14)\n"
      "global_load_dwordx4 v[72:75], v60, s[90:91] offset:-2048 nt\n"
      "global_load_dwordx4 v[76:79], v60, s[90:91] offset:-1792 nt\n"
      "s_waitcnt vmcnt(14)\n"
      "global_load_dwordx4 v[80:83], v60, s[90:91] offset:0 nt\n"
      "global_load_dwordx4 v[84:87], v60, s[90:91] offset:256 nt\n"
      "s_waitcnt vmcnt(14)\n"
      "global_load_dwordx4 v[88:91], v60, s[90:91] offset:2048 nt\n"
      "global_load_dwordx4 v[92:95], v60, s[90:91] offset:2304 nt\n"
      "s_waitcnt vmcnt(14)\n"
      "s_add_u32 s86, s100, 0x40e0000\n"
      "s_cmp_ge_u32 s86, 0x43f0000\n"
      "s_cselect_b32 s87, 0x3f0000, 0\n"
      "s_sub_u32 s86, s86, s87\n"
      "s_add_u32 s90, s38, s86\n"
      "s_addc_u32 s91, s39, 0\n"
      "global_load_dwordx4 v[96:99], v61, s[90:91] offset:0 nt\n"
      "global_load_dwordx4 v[100:103], v61, s[90:91] offset:128 nt\n"
      "s_waitcnt vmcnt(14)\n"
      "global_load_dwordx4 v[104:107], v61, s[90:91] offset:256 nt\n"
      "global_load_dwordx4 v[108:111], v61, s[90:91] offset:384 nt\n"
      "s_waitcnt vmcnt(14)\n"
      "global_load_dwordx4 v[112:115], v61, s[90:91] offset:2048 nt\n"
      "global_load_dwordx4 v[116:119], v61, s[90:91] offset:2176 nt\n"
      "s_waitcnt vmcnt(14)\n"
      "global_load_dwordx4 v[120:123], v61, s[90:91] offset:2304 nt\n"
      "global_load_dwordx4 v[124:127], v61, s[90:91] offset:2432 nt\n"
      "s_add_u32 s64, s42, 0x60000\n"
      "s_addc_u32 s65, s43, 0\n"
      "s_mov_b32 m0, 0x27a40\n"
      "s_nop 0\n"
      "global_load_lds_dwordx4 v61, s[64:65] sc1\n"
      "s_mov_b32 s79, 0\n"
      "s_nop 0\n"
      "QW_96_%=:\n"
      "v_mbcnt_lo_u32_b32 v52, -1, 0\n"
      "v_mbcnt_hi_u32_b32 v52, -1, v52\n"
      "v_lshlrev_b32_e32 v52, 4, v52\n"
      "v_add_u32_e32 v52, 0x27a40, v52\n"
      "v_mov_b32_e32 v53, 0x26110\n"
      "ds_read_b128 v[48:51], v52\n"
      "ds_read_b32 v53, v53\n"
      "s_waitcnt lgkmcnt(0)\n"
      "v_cmp_ne_u32_e64 s[80:81], s62, v49\n"
      "v_cmp_ne_u32_e64 s[82:83], s62, v51\n"
      "v_cmp_eq_u32_e32 vcc, 1, v53\n"
      "s_nop 4\n"
      "s_cbranch_vccnz QD_97_%=\n"
      "s_or_b64 s[80:81], s[80:81], s[82:83]\n"
      "s_cmp_eq_u64 s[80:81], 0\n"
      "s_nop 0\n"
      "s_cbranch_scc0 QN_98_%=\n"
      "v_mov_b32_e32 v49, v50\n"
      "v_lshrrev_b32_e32 v52, 1, v52\n"
      "v_add_u32_e32 v52, 0x12f20, v52\n"
      "ds_write_b64 v52, v[48:49]\n"
      "v_mov_b32_e32 v52, 0x26110\n"
      "v_mov_b32_e32 v53, 1\n"
      "ds_write_b32 v52, v53\n"
      "s_waitcnt lgkmcnt(0)\n"
      "s_nop 2\n"
      "s_branch QD_97_%=\n"
      "s_nop 0\n"
      "QN_98_%=:\n"
      "s_add_u32 s64, s42, 0x60000\n"
      "s_addc_u32 s65, s43, 0\n"
      "s_mov_b32 m0, 0x27a40\n"
      "s_nop 0\n"
      "global_load_lds_dwordx4 v61, s[64:65] sc1\n"
      "s_sleep 2\n"
      "s_add_u32 s79, s79, 1\n"
      "s_cmp_lt_u32 s79, 0x100000\n"
      "s_waitcnt vmcnt(40)\n"
      "s_nop 0\n"
      "s_cbranch_scc1 QW_96_%=\n"
      "s_nop 0\n"
      "QD_97_%=:\n"
      "v_mbcnt_lo_u32_b32 v53, -1, 0\n"
      "v_mbcnt_hi_u32_b32 v53, -1, v53\n"
      "v_lshrrev_b32_e32 v52, 4, v53\n"
      "v_lshlrev_b32_e32 v52, 6, v52\n"
      "v_and_b32_e32 v53, 3, v53\n"
      "v_lshl_add_u32 v52, v53, 4, v52\n"
      "v_add_u32_e32 v52, 0x26c40, v52\n"
      "s_mov_b32 vcc_lo, 0xf000f\n"
      "s_mov_b32 vcc_hi, 0xf000f\n"
      "v_mov_b32_e32 v53, 0x26120\n"
      "v_cndmask_b32_e32 v53, v53, v52, vcc\n"
      "ds_read_b128 v[0:3], v53 offset:0\n"
      "ds_read_b128 v[4:7], v53 offset:256\n"
      "s_mov_b32 vcc_lo, 0xf000f0\n"
      "s_mov_b32 vcc_hi, 0xf000f0\n"
      "v_mov_b32_e32 v53, 0x26120\n"
      "v_cndmask_b32_e32 v53, v53, v52, vcc\n"
      "ds_read_b128 v[8:11], v53 offset:0\n"
      "ds_read_b128 v[12:15], v53 offset:256\n"
      "s_mov_b32 vcc_lo, 0xf000f00\n"
      "s_mov_b32 vcc_hi, 0xf000f00\n"
      "v_mov_b32_e32 v53, 0x26120\n"
      "v_cndmask_b32_e32 v53, v53, v52, vcc\n"
      "ds_read_b128 v[16:19], v53 offset:0\n"
      "ds_read_b128 v[20:23], v53 offset:256\n"
      "s_mov_b32 vcc_lo, 0xf000f000\n"
      "s_mov_b32 vcc_hi, 0xf000f000\n"
      "v_mov_b32_e32 v53, 0x26120\n"
      "v_cndmask_b32_e32 v53, v53, v52, vcc\n"
      "ds_read_b128 v[24:27], v53 offset:0\n"
      "ds_read_b128 v[28:31], v53 offset:256\n"
      "s_waitcnt lgkmcnt(0)\n"
      "v_mbcnt_lo_u32_b32 v59, -1, 0\n"
      "v_mbcnt_hi_u32_b32 v59, -1, v59\n"
      "v_lshlrev_b32_e32 v59, 4, v59\n"
      "v_add_u32_e32 v59, s49, v59\n"
      "v_mov_b32_e32 v58, 0xff800000\n"
      "v_mfma_f32_16x16x32_bf16 v[48:51], a[0:3], v[0:3], 0\n"
      "v_mfma_f32_16x16x32_bf16 v[48:51], a[4:7], v[4:7], v[48:51]\n"
      "v_mfma_f32_16x16x32_bf16 v[48:51], a[8:11], v[8:11], v[48:51]\n"
      "v_mfma_f32_16x16x32_bf16 v[48:51], a[12:15], v[12:15], v[48:51]\n"
      "v_mfma_f32_16x16x32_bf16 v[48:51], a[16:19], v[16:19], v[48:51]\n"
      "v_mfma_f32_16x16x32_bf16 v[48:51], a[20:23], v[20:23], v[48:51]\n"
      "v_mfma_f32_16x16x32_bf16 v[48:51], a[24:27], v[24:27], v[48:51]\n"
      "v_mfma_f32_16x16x32_bf16 v[48:51], a[28:31], v[28:31], v[48:51]\n"
      "s_waitcnt vmcnt(15)\n"
      "s_add_u32 s86, s100, 0x4070000\n"
      "s_cmp_ge_u32 s86, 0x43f0000\n"
      "s_cselect_b32 s87, 0x3f0000, 0\n"
      "s_sub_u32 s86, s86, s87\n"
      "s_add_u32 s90, s38, s86\n"
      "s_addc_u32 s91, s39, 0\n"
      "global_load_dwordx4 v[128:131], v61, s[90:91] offset:0 nt\n"
      "v_mfma_f32_16x16x32_bf16 v[52:55], a[64:67], v[0:3], 0\n"
      "v_mfma_f32_16x16x32_bf16 v[52:55], a[68:71], v[4:7], v[52:55]\n"
      "v_mfma_f32_16x16x32_bf16 v[52:55], a[72:75], v[8:11], v[52:55]\n"
      "v_mfma_f32_16x16x32_bf16 v[52:55], a[76:79], v[12:15], v[52:55]\n"
      "v_mfma_f32_16x16x32_bf16 v[52:55], a[80:83], v[16:19], v[52:55]\n"
      "v_mfma_f32_16x16x32_bf16 v[52:55], a[84:87], v[20:23], v[52:55]\n"
      "v_mfma_f32_16x16x32_bf16 v[52:55], a[88:91], v[24:27], v[52:55]\n"
      "v_mfma_f32_16x16x32_bf16 v[52:55], a[92:95], v[28:31], v[52:55]\n"
      "v_cndmask_b32_e64 v56, v48, v49, s[96:97]\n"
      "v_cndmask_b32_e64 v57, v50, v51, s[96:97]\n"
      "v_cndmask_b32_e64 v32, v56, v57, s[98:99]\n"
      "s_nop 1\n"
      "v_add_f32_dpp v32, v32, v32 quad_perm:[1,0,3,2] row_mask:0xf bank_mask:0xf\n"
      "s_nop 1\n"
      "v_add_f32_dpp v32, v32, v32 quad_perm:[2,3,0,1] row_mask:0xf bank_mask:0xf\n"
      "v_max_f32_e32 v58, v58, v32\n"
      "ds_read_b128 a[64:67], v59 offset:0\n"
      "ds_read_b128 a[68:71], v59 offset:1024\n"
      "ds_read_b128 a[72:75], v59 offset:2048\n"
      "ds_read_b128 a[76:79], v59 offset:3072\n"
      "ds_read_b128 a[80:83], v59 offset:4096\n"
      "ds_read_b128 a[84:87], v59 offset:5120\n"
      "ds_read_b128 a[88:91], v59 offset:6144\n"
      "ds_read_b128 a[92:95], v59 offset:7168\n"
      "s_waitcnt vmcnt(15)\n"
      "global_load_dwordx4 v[132:135], v61, s[90:91] offset:128 nt\n"
      "v_mfma_f32_16x16x32_bf16 v[48:51], a[128:131], v[0:3], 0\n"
      "v_mfma_f32_16x16x32_bf16 v[48:51], a[132:135], v[4:7], v[48:51]\n"
      "v_mfma_f32_16x16x32_bf16 v[48:51], a[136:139], v[8:11], v[48:51]\n"
      "v_mfma_f32_16x16x32_bf16 v[48:51], a[140:143], v[12:15], v[48:51]\n"
      "v_mfma_f32_16x16x32_bf16 v[48:51], a[144:147], v[16:19], v[48:51]\n"
      "v_mfma_f32_16x16x32_bf16 v[48:51], a[148:151], v[20:23], v[48:51]\n"
      "v_mfma_f32_16x16x32_bf16 v[48:51], a[152:155], v[24:27], v[48:51]\n"
      "v_mfma_f32_16x16x32_bf16 v[48:51], a[156:159], v[28:31], v[48:51]\n"
      "v_cndmask_b32_e64 v56, v52, v53, s[96:97]\n"
      "v_cndmask_b32_e64 v57, v54, v55, s[96:97]\n"
      "v_cndmask_b32_e64 v33, v56, v57, s[98:99]\n"
      "s_nop 1\n"
      "v_add_f32_dpp v33, v33, v33 quad_perm:[1,0,3,2] row_mask:0xf bank_mask:0xf\n"
      "s_nop 1\n"
      "v_add_f32_dpp v33, v33, v33 quad_perm:[2,3,0,1] row_mask:0xf bank_mask:0xf\n"
      "v_max_f32_e32 v58, v58, v33\n"
      "ds_read_b128 a[128:131], v59 offset:16384\n"
      "ds_read_b128 a[132:135], v59 offset:17408\n"
      "ds_read_b128 a[136:139], v59 offset:18432\n"
      "ds_read_b128 a[140:143], v59 offset:19456\n"
      "ds_read_b128 a[144:147], v59 offset:20480\n"
      "ds_read_b128 a[148:151], v59 offset:21504\n"
      "ds_read_b128 a[152:155], v59 offset:22528\n"
      "ds_read_b128 a[156:159], v59 offset:23552\n"
      "s_waitcnt vmcnt(15)\n"
      "global_load_dwordx4 v[136:139], v61, s[90:91] offset:256 nt\n"
      "v_mfma_f32_16x16x32_bf16 v[52:55], a[192:195], v[0:3], 0\n"
      "v_mfma_f32_16x16x32_bf16 v[52:55], a[196:199], v[4:7], v[52:55]\n"
      "v_mfma_f32_16x16x32_bf16 v[52:55], a[200:203], v[8:11], v[52:55]\n"
      "v_mfma_f32_16x16x32_bf16 v[52:55], a[204:207], v[12:15], v[52:55]\n"
      "v_mfma_f32_16x16x32_bf16 v[52:55], a[208:211], v[16:19], v[52:55]\n"
      "v_mfma_f32_16x16x32_bf16 v[52:55], a[212:215], v[20:23], v[52:55]\n"
      "v_mfma_f32_16x16x32_bf16 v[52:55], a[216:219], v[24:27], v[52:55]\n"
      "v_mfma_f32_16x16x32_bf16 v[52:55], a[220:223], v[28:31], v[52:55]\n"
      "v_cndmask_b32_e64 v56, v48, v49, s[96:97]\n"
      "v_cndmask_b32_e64 v57, v50, v51, s[96:97]\n"
      "v_cndmask_b32_e64 v34, v56, v57, s[98:99]\n"
      "s_nop 1\n"
      "v_add_f32_dpp v34, v34, v34 quad_perm:[1,0,3,2] row_mask:0xf bank_mask:0xf\n"
      "s_nop 1\n"
      "v_add_f32_dpp v34, v34, v34 quad_perm:[2,3,0,1] row_mask:0xf bank_mask:0xf\n"
      "v_max_f32_e32 v58, v58, v34\n"
      "ds_read_b128 a[192:195], v59 offset:8192\n"
      "ds_read_b128 a[196:199], v59 offset:9216\n"
      "ds_read_b128 a[200:203], v59 offset:10240\n"
      "ds_read_b128 a[204:207], v59 offset:11264\n"
      "ds_read_b128 a[208:211], v59 offset:12288\n"
      "ds_read_b128 a[212:215], v59 offset:13312\n"
      "ds_read_b128 a[216:219], v59 offset:14336\n"
      "ds_read_b128 a[220:223], v59 offset:15360\n"
      "s_waitcnt vmcnt(15)\n"
      "global_load_dwordx4 v[140:143], v61, s[90:91] offset:384 nt\n"
      "v_mfma_f32_16x16x32_bf16 v[48:51], v[192:195], v[0:3], 0\n"
      "v_mfma_f32_16x16x32_bf16 v[48:51], v[196:199], v[4:7], v[48:51]\n"
      "v_mfma_f32_16x16x32_bf16 v[48:51], v[200:203], v[8:11], v[48:51]\n"
      "v_mfma_f32_16x16x32_bf16 v[48:51], v[204:207], v[12:15], v[48:51]\n"
      "v_mfma_f32_16x16x32_bf16 v[48:51], v[208:211], v[16:19], v[48:51]\n"
      "v_mfma_f32_16x16x32_bf16 v[48:51], v[212:215], v[20:23], v[48:51]\n"
      "v_mfma_f32_16x16x32_bf16 v[48:51], v[216:219], v[24:27], v[48:51]\n"
      "v_mfma_f32_16x16x32_bf16 v[48:51], v[220:223], v[28:31], v[48:51]\n"
      "v_cndmask_b32_e64 v56, v52, v53, s[96:97]\n"
      "v_cndmask_b32_e64 v57, v54, v55, s[96:97]\n"
      "v_cndmask_b32_e64 v35, v56, v57, s[98:99]\n"
      "s_nop 1\n"
      "v_add_f32_dpp v35, v35, v35 quad_perm:[1,0,3,2] row_mask:0xf bank_mask:0xf\n"
      "s_nop 1\n"
      "v_add_f32_dpp v35, v35, v35 quad_perm:[2,3,0,1] row_mask:0xf bank_mask:0xf\n"
      "v_max_f32_e32 v58, v58, v35\n"
      "ds_read_b128 v[192:195], v59 offset:24576\n"
      "ds_read_b128 v[196:199], v59 offset:25600\n"
      "ds_read_b128 v[200:203], v59 offset:26624\n"
      "ds_read_b128 v[204:207], v59 offset:27648\n"
      "ds_read_b128 v[208:211], v59 offset:28672\n"
      "ds_read_b128 v[212:215], v59 offset:29696\n"
      "ds_read_b128 v[216:219], v59 offset:30720\n"
      "ds_read_b128 v[220:223], v59 offset:31744\n"
      "s_waitcnt vmcnt(15)\n"
      "global_load_dwordx4 v[144:147], v61, s[90:91] offset:2048 nt\n"
      "v_mfma_f32_16x16x32_bf16 v[52:55], v[224:227], v[0:3], 0\n"
      "v_mfma_f32_16x16x32_bf16 v[52:55], v[228:231], v[4:7], v[52:55]\n"
      "v_mfma_f32_16x16x32_bf16 v[52:55], v[232:235], v[8:11], v[52:55]\n"
      "v_mfma_f32_16x16x32_bf16 v[52:55], v[236:239], v[12:15], v[52:55]\n"
      "v_mfma_f32_16x16x32_bf16 v[52:55], v[240:243], v[16:19], v[52:55]\n"
      "v_mfma_f32_16x16x32_bf16 v[52:55], v[244:247], v[20:23], v[52:55]\n"
      "v_mfma_f32_16x16x32_bf16 v[52:55], v[248:251], v[24:27], v[52:55]\n"
      "v_mfma_f32_16x16x32_bf16 v[52:55], v[252:255], v[28:31], v[52:55]\n"
      "v_cndmask_b32_e64 v56, v48, v49, s[96:97]\n"
      "v_cndmask_b32_e64 v57, v50, v51, s[96:97]\n"
      "v_cndmask_b32_e64 v36, v56, v57, s[98:99]\n"
      "s_nop 1\n"
      "v_add_f32_dpp v36, v36, v36 quad_perm:[1,0,3,2] row_mask:0xf bank_mask:0xf\n"
      "s_nop 1\n"
      "v_add_f32_dpp v36, v36, v36 quad_perm:[2,3,0,1] row_mask:0xf bank_mask:0xf\n"
      "v_max_f32_e32 v58, v58, v36\n"
      "s_waitcnt vmcnt(15)\n"
      "global_load_dwordx4 v[148:151], v61, s[90:91] offset:2176 nt\n"
      "s_waitcnt vmcnt(14)\n"
      "v_mfma_f32_16x16x32_bf16 v[48:51], v[64:67], v[0:3], 0\n"
      "v_mfma_f32_16x16x32_bf16 v[48:51], v[68:71], v[4:7], v[48:51]\n"
      "v_mfma_f32_16x16x32_bf16 v[48:51], v[72:75], v[8:11], v[48:51]\n"
      "v_mfma_f32_16x16x32_bf16 v[48:51], v[76:79], v[12:15], v[48:51]\n"
      "v_mfma_f32_16x16x32_bf16 v[48:51], v[80:83], v[16:19], v[48:51]\n"
      "v_mfma_f32_16x16x32_bf16 v[48:51], v[84:87], v[20:23], v[48:51]\n"
      "v_mfma_f32_16x16x32_bf16 v[48:51], v[88:91], v[24:27], v[48:51]\n"
      "v_mfma_f32_16x16x32_bf16 v[48:51], v[92:95], v[28:31], v[48:51]\n"
      "v_cndmask_b32_e64 v56, v52, v53, s[96:97]\n"
      "v_cndmask_b32_e64 v57, v54, v55, s[96:97]\n"
      "v_cndmask_b32_e64 v37, v56, v57, s[98:99]\n"
      "s_nop 1\n"
      "v_add_f32_dpp v37, v37, v37 quad_perm:[1,0,3,2] row_mask:0xf bank_mask:0xf\n"
      "s_nop 1\n"
      "v_add_f32_dpp v37, v37, v37 quad_perm:[2,3,0,1] row_mask:0xf bank_mask:0xf\n"
      "v_max_f32_e32 v58, v58, v37\n"
      "global_load_dwordx4 v[152:155], v61, s[90:91] offset:2304 nt\n"
      "global_load_dwordx4 v[156:159], v61, s[90:91] offset:2432 nt\n"
      "s_waitcnt vmcnt(15)\n"
      "s_add_u32 s86, s100, 0x4000000\n"
      "s_cmp_ge_u32 s86, 0x43f0000\n"
      "s_cselect_b32 s87, 0x3f0000, 0\n"
      "s_sub_u32 s86, s86, s87\n"
      "s_add_u32 s90, s38, s86\n"
      "s_addc_u32 s91, s39, 0\n"
      "global_load_dwordx4 v[160:163], v61, s[90:91] offset:0 nt\n"
      "s_waitcnt lgkmcnt(15)\n"
      "v_mfma_f32_16x16x32_bf16 v[52:55], a[64:67], v[0:3], 0\n"
      "v_mfma_f32_16x16x32_bf16 v[52:55], a[68:71], v[4:7], v[52:55]\n"
      "v_mfma_f32_16x16x32_bf16 v[52:55], a[72:75], v[8:11], v[52:55]\n"
      "v_mfma_f32_16x16x32_bf16 v[52:55], a[76:79], v[12:15], v[52:55]\n"
      "v_mfma_f32_16x16x32_bf16 v[52:55], a[80:83], v[16:19], v[52:55]\n"
      "v_mfma_f32_16x16x32_bf16 v[52:55], a[84:87], v[20:23], v[52:55]\n"
      "v_mfma_f32_16x16x32_bf16 v[52:55], a[88:91], v[24:27], v[52:55]\n"
      "v_mfma_f32_16x16x32_bf16 v[52:55], a[92:95], v[28:31], v[52:55]\n"
      "v_cndmask_b32_e64 v56, v48, v49, s[96:97]\n"
      "v_cndmask_b32_e64 v57, v50, v51, s[96:97]\n"
      "v_cndmask_b32_e64 v38, v56, v57, s[98:99]\n"
      "s_nop 1\n"
      "v_add_f32_dpp v38, v38, v38 quad_perm:[1,0,3,2] row_mask:0xf bank_mask:0xf\n"
      "s_nop 1\n"
      "v_add_f32_dpp v38, v38, v38 quad_perm:[2,3,0,1] row_mask:0xf bank_mask:0xf\n"
      "v_max_f32_e32 v58, v58, v38\n"
      "s_waitcnt vmcnt(15)\n"
      "global_load_dwordx4 v[164:167], v61, s[90:91] offset:128 nt\n"
      "v_mfma_f32_16x16x32_bf16 v[48:51], a[128:131], v[0:3], 0\n"
      "v_mfma_f32_16x16x32_bf16 v[48:51], a[132:135], v[4:7], v[48:51]\n"
      "v_mfma_f32_16x16x32_bf16 v[48:51], a[136:139], v[8:11], v[48:51]\n"
      "v_mfma_f32_16x16x32_bf16 v[48:51], a[140:143], v[12:15], v[48:51]\n"
      "v_mfma_f32_16x16x32_bf16 v[48:51], a[144:147], v[16:19], v[48:51]\n"
      "v_mfma_f32_16x16x32_bf16 v[48:51], a[148:151], v[20:23], v[48:51]\n"
      "v_mfma_f32_16x16x32_bf16 v[48:51], a[152:155], v[24:27], v[48:51]\n"
      "v_mfma_f32_16x16x32_bf16 v[48:51], a[156:159], v[28:31], v[48:51]\n"
      "v_cndmask_b32_e64 v56, v52, v53, s[96:97]\n"
      "v_cndmask_b32_e64 v57, v54, v55, s[96:97]\n"
      "v_cndmask_b32_e64 v39, v56, v57, s[98:99]\n"
      "s_nop 1\n"
      "v_add_f32_dpp v39, v39, v39 quad_perm:[1,0,3,2] row_mask:0xf bank_mask:0xf\n"
      "s_nop 1\n"
      "v_add_f32_dpp v39, v39, v39 quad_perm:[2,3,0,1] row_mask:0xf bank_mask:0xf\n"
      "v_max_f32_e32 v58, v58, v39\n"
      "s_waitcnt vmcnt(15)\n"
      "global_load_dwordx4 v[168:171], v61, s[90:91] offset:256 nt\n"
      "v_cndmask_b32_e64 v56, v48, v49, s[96:97]\n"
      "v_cndmask_b32_e64 v57, v50, v51, s[96:97]\n"
      "v_cndmask_b32_e64 v40, v56, v57, s[98:99]\n"
      "s_nop 1\n"
      "v_add_f32_dpp v40, v40, v40 quad_perm:[1,0,3,2] row_mask:0xf bank_mask:0xf\n"
      "s_nop 1\n"
      "v_add_f32_dpp v40, v40, v40 quad_perm:[2,3,0,1] row_mask:0xf bank_mask:0xf\n"
      "v_max_f32_e32 v58, v58, v40\n"
      "s_nop 1\n"
      "v_max_f32_dpp v58, v58, v58 row_ror:4 row_mask:0xf bank_mask:0xf\n"
      "s_nop 1\n"
      "v_max_f32_dpp v58, v58, v58 row_ror:8 row_mask:0xf bank_mask:0xf\n"
      "s_nop 0\n"
      "v_readlane_b32 s80, v58, 0\n"
      "v_readlane_b32 s81, v58, 16\n"
      "v_readlane_b32 s82, v58, 32\n"
      "v_readlane_b32 s83, v58, 48\n"
      "v_mov_b32_e32 v58, s80\n"
      "v_max_f32_e32 v58, s81, v58\n"
      "v_max_f32_e32 v58, s82, v58\n"
      "v_max_f32_e32 v58, s83, v58\n"
      "v_cndmask_b32_e64 v48, 0, v63, s[52:53]\n"
      "v_cndmask_b32_e64 v49, 0, v63, s[54:55]\n"
      "v_cndmask_b32_e64 v50, 0, v63, s[56:57]\n"
      "v_cndmask_b32_e64 v51, 0, v63, s[58:59]\n"
      "v_readfirstlane_b32 s60, v58\n"
      "v_mul_f32_e32 v52, s34, v58\n"
      "v_mov_b32_e32 v62, 0\n"
      "v_fma_f32 v35, v35, s34, -v52\n"
      "v_exp_f32_e32 v35, v35\n"
      "s_nop 0\n"
      "v_cvt_pk_bf16_f32 v53, v35, v35\n"
      "v_add_f32_e32 v62, v62, v35\n"
      "s_nop 0\n"
      "v_mov_b32_dpp v54, v53 row_half_mirror row_mask:0xf bank_mask:0xf\n"
      "v_add_f32_dpp v62, v35, v62 row_half_mirror row_mask:0xf bank_mask:0xf\n"
      "v_cndmask_b32_e64 v55, v53, v54, s[50:51]\n"
      "v_cndmask_b32_e64 v56, v54, v53, s[50:51]\n"
      "v_and_b32_e32 v16, v55, v48\n"
      "v_and_b32_e32 v17, v55, v49\n"
      "v_and_b32_e32 v18, v55, v50\n"
      "v_and_b32_e32 v19, v55, v51\n"
      "v_and_b32_e32 v20, v56, v48\n"
      "v_and_b32_e32 v21, v56, v49\n"
      "v_and_b32_e32 v22, v56, v50\n"
      "v_and_b32_e32 v23, v56, v51\n"
      "v_mfma_f32_16x16x32_bf16 v[0:3], a[224:227], v[16:19], 0\n"
      "v_fma_f32 v34, v34, s34, -v52\n"
      "v_exp_f32_e32 v34, v34\n"
      "v_mfma_f32_16x16x32_bf16 v[4:7], a[228:231], v[16:19], 0\n"
      "s_nop 0\n"
      "v_cvt_pk_bf16_f32 v53, v34, v34\n"
      "v_mfma_f32_16x16x32_bf16 v[8:11], a[232:235], v[16:19], 0\n"
      "v_add_f32_e32 v62, v62, v34\n"
      "v_mov_b32_dpp v54, v53 row_half_mirror row_mask:0xf bank_mask:0xf\n"
      "v_mfma_f32_16x16x32_bf16 v[12:15], a[236:239], v[16:19], 0\n"
      "v_add_f32_dpp v62, v34, v62 row_half_mirror row_mask:0xf bank_mask:0xf\n"
      "v_cndmask_b32_e64 v55, v53, v54, s[50:51]\n"
      "v_mfma_f32_16x16x32_bf16 v[0:3], a[240:243], v[20:23], v[0:3]\n"
      "v_cndmask_b32_e64 v56, v54, v53, s[50:51]\n"
      "v_and_b32_e32 v24, v55, v48\n"
      "v_mfma_f32_16x16x32_bf16 v[4:7], a[244:247], v[20:23], v[4:7]\n"
      "v_and_b32_e32 v25, v55, v49\n"
      "v_and_b32_e32 v26, v55, v50\n"
      "v_mfma_f32_16x16x32_bf16 v[8:11], a[248:251], v[20:23], v[8:11]\n"
      "v_and_b32_e32 v27, v55, v51\n"
      "v_and_b32_e32 v28, v56, v48\n"
      "v_mfma_f32_16x16x32_bf16 v[12:15], a[252:255], v[20:23], v[12:15]\n"
      "v_and_b32_e32 v29, v56, v49\n"
      "v_and_b32_e32 v30, v56, v50\n"
      "v_and_b32_e32 v31, v56, v51\n"
      "s_waitcnt vmcnt(15)\n"
      "global_load_dwordx4 v[172:175], v61, s[90:91] offset:384 nt\n"
      "v_mfma_f32_16x16x32_bf16 v[0:3], a[160:163], v[24:27], v[0:3]\n"
      "v_fma_f32 v33, v33, s34, -v52\n"
      "v_exp_f32_e32 v33, v33\n"
      "v_mfma_f32_16x16x32_bf16 v[4:7], a[164:167], v[24:27], v[4:7]\n"
      "s_nop 0\n"
      "v_cvt_pk_bf16_f32 v53, v33, v33\n"
      "v_mfma_f32_16x16x32_bf16 v[8:11], a[168:171], v[24:27], v[8:11]\n"
      "v_add_f32_e32 v62, v62, v33\n"
      "v_mov_b32_dpp v54, v53 row_half_mirror row_mask:0xf bank_mask:0xf\n"
      "v_mfma_f32_16x16x32_bf16 v[12:15], a[172:175], v[24:27], v[12:15]\n"
      "v_add_f32_dpp v62, v33, v62 row_half_mirror row_mask:0xf bank_mask:0xf\n"
      "v_cndmask_b32_e64 v55, v53, v54, s[50:51]\n"
      "v_mfma_f32_16x16x32_bf16 v[0:3], a[176:179], v[28:31], v[0:3]\n"
      "v_cndmask_b32_e64 v56, v54, v53, s[50:51]\n"
      "v_and_b32_e32 v16, v55, v48\n"
      "v_mfma_f32_16x16x32_bf16 v[4:7], a[180:183], v[28:31], v[4:7]\n"
      "v_and_b32_e32 v17, v55, v49\n"
      "v_and_b32_e32 v18, v55, v50\n"
      "v_mfma_f32_16x16x32_bf16 v[8:11], a[184:187], v[28:31], v[8:11]\n"
      "v_and_b32_e32 v19, v55, v51\n"
      "v_and_b32_e32 v20, v56, v48\n"
      "v_mfma_f32_16x16x32_bf16 v[12:15], a[188:191], v[28:31], v[12:15]\n"
      "v_and_b32_e32 v21, v56, v49\n"
      "v_and_b32_e32 v22, v56, v50\n"
      "v_and_b32_e32 v23, v56, v51\n"
      "s_waitcnt vmcnt(15)\n"
      "global_load_dwordx4 v[176:179], v61, s[90:91] offset:2048 nt\n"
      "v_mfma_f32_16x16x32_bf16 v[0:3], a[96:99], v[16:19], v[0:3]\n"
      "v_fma_f32 v32, v32, s34, -v52\n"
      "v_exp_f32_e32 v32, v32\n"
      "v_mfma_f32_16x16x32_bf16 v[4:7], a[100:103], v[16:19], v[4:7]\n"
      "s_nop 0\n"
      "v_cvt_pk_bf16_f32 v53, v32, v32\n"
      "v_mfma_f32_16x16x32_bf16 v[8:11], a[104:107], v[16:19], v[8:11]\n"
      "v_add_f32_e32 v62, v62, v32\n"
      "v_mov_b32_dpp v54, v53 row_half_mirror row_mask:0xf bank_mask:0xf\n"
      "v_mfma_f32_16x16x32_bf16 v[12:15], a[108:111], v[16:19], v[12:15]\n"
      "v_add_f32_dpp v62, v32, v62 row_half_mirror row_mask:0xf bank_mask:0xf\n"
      "v_cndmask_b32_e64 v55, v53, v54, s[50:51]\n"
      "v_mfma_f32_16x16x32_bf16 v[0:3], a[112:115], v[20:23], v[0:3]\n"
      "v_cndmask_b32_e64 v56, v54, v53, s[50:51]\n"
      "v_and_b32_e32 v24, v55, v48\n"
      "v_mfma_f32_16x16x32_bf16 v[4:7], a[116:119], v[20:23], v[4:7]\n"
      "v_and_b32_e32 v25, v55, v49\n"
      "v_and_b32_e32 v26, v55, v50\n"
      "v_mfma_f32_16x16x32_bf16 v[8:11], a[120:123], v[20:23], v[8:11]\n"
      "v_and_b32_e32 v27, v55, v51\n"
      "v_and_b32_e32 v28, v56, v48\n"
      "v_mfma_f32_16x16x32_bf16 v[12:15], a[124:127], v[20:23], v[12:15]\n"
      "v_and_b32_e32 v29, v56, v49\n"
      "v_and_b32_e32 v30, v56, v50\n"
      "v_and_b32_e32 v31, v56, v51\n"
      "s_waitcnt vmcnt(15)\n"
      "global_load_dwordx4 v[180:183], v61, s[90:91] offset:2176 nt\n"
      "v_mfma_f32_16x16x32_bf16 v[0:3], a[32:35], v[24:27], v[0:3]\n"
      "v_fma_f32 v40, v40, s34, -v52\n"
      "v_exp_f32_e32 v40, v40\n"
      "v_mfma_f32_16x16x32_bf16 v[4:7], a[36:39], v[24:27], v[4:7]\n"
      "s_nop 0\n"
      "v_cvt_pk_bf16_f32 v53, v40, v40\n"
      "v_mfma_f32_16x16x32_bf16 v[8:11], a[40:43], v[24:27], v[8:11]\n"
      "v_add_f32_e32 v62, v62, v40\n"
      "v_mov_b32_dpp v54, v53 row_half_mirror row_mask:0xf bank_mask:0xf\n"
      "v_mfma_f32_16x16x32_bf16 v[12:15], a[44:47], v[24:27], v[12:15]\n"
      "v_add_f32_dpp v62, v40, v62 row_half_mirror row_mask:0xf bank_mask:0xf\n"
      "v_cndmask_b32_e64 v55, v53, v54, s[50:51]\n"
      "v_mfma_f32_16x16x32_bf16 v[0:3], a[48:51], v[28:31], v[0:3]\n"
      "v_cndmask_b32_e64 v56, v54, v53, s[50:51]\n"
      "v_and_b32_e32 v16, v55, v48\n"
      "v_mfma_f32_16x16x32_bf16 v[4:7], a[52:55], v[28:31], v[4:7]\n"
      "v_and_b32_e32 v17, v55, v49\n"
      "v_and_b32_e32 v18, v55, v50\n"
      "v_mfma_f32_16x16x32_bf16 v[8:11], a[56:59], v[28:31], v[8:11]\n"
      "v_and_b32_e32 v19, v55, v51\n"
      "v_and_b32_e32 v20, v56, v48\n"
      "v_mfma_f32_16x16x32_bf16 v[12:15], a[60:63], v[28:31], v[12:15]\n"
      "v_and_b32_e32 v21, v56, v49\n"
      "v_and_b32_e32 v22, v56, v50\n"
      "v_and_b32_e32 v23, v56, v51\n"
      "s_waitcnt vmcnt(15)\n"
      "global_load_dwordx4 v[184:187], v61, s[90:91] offset:2304 nt\n"
      "s_waitcnt lgkmcnt(0)\n"
      "v_mfma_f32_16x16x32_bf16 v[0:3], v[192:195], v[16:19], v[0:3]\n"
      "v_fma_f32 v39, v39, s34, -v52\n"
      "v_exp_f32_e32 v39, v39\n"
      "v_mfma_f32_16x16x32_bf16 v[4:7], v[196:199], v[16:19], v[4:7]\n"
      "s_nop 0\n"
      "v_cvt_pk_bf16_f32 v53, v39, v39\n"
      "v_mfma_f32_16x16x32_bf16 v[8:11], v[200:203], v[16:19], v[8:11]\n"
      "v_add_f32_e32 v62, v62, v39\n"
      "v_mov_b32_dpp v54, v53 row_half_mirror row_mask:0xf bank_mask:0xf\n"
      "v_mfma_f32_16x16x32_bf16 v[12:15], v[204:207], v[16:19], v[12:15]\n"
      "v_add_f32_dpp v62, v39, v62 row_half_mirror row_mask:0xf bank_mask:0xf\n"
      "v_cndmask_b32_e64 v55, v53, v54, s[50:51]\n"
      "v_mfma_f32_16x16x32_bf16 v[0:3], v[208:211], v[20:23], v[0:3]\n"
      "v_cndmask_b32_e64 v56, v54, v53, s[50:51]\n"
      "v_and_b32_e32 v24, v55, v48\n"
      "v_mfma_f32_16x16x32_bf16 v[4:7], v[212:215], v[20:23], v[4:7]\n"
      "v_and_b32_e32 v25, v55, v49\n"
      "v_and_b32_e32 v26, v55, v50\n"
      "v_mfma_f32_16x16x32_bf16 v[8:11], v[216:219], v[20:23], v[8:11]\n"
      "v_and_b32_e32 v27, v55, v51\n"
      "v_and_b32_e32 v28, v56, v48\n"
      "v_mfma_f32_16x16x32_bf16 v[12:15], v[220:223], v[20:23], v[12:15]\n"
      "v_and_b32_e32 v29, v56, v49\n"
      "v_and_b32_e32 v30, v56, v50\n"
      "v_and_b32_e32 v31, v56, v51\n"
      "s_waitcnt vmcnt(15)\n"
      "global_load_dwordx4 v[188:191], v61, s[90:91] offset:2432 nt\n"
      "v_mfma_f32_16x16x32_bf16 v[0:3], a[192:195], v[24:27], v[0:3]\n"
      "v_fma_f32 v36, v36, s34, -v52\n"
      "v_exp_f32_e32 v36, v36\n"
      "v_mfma_f32_16x16x32_bf16 v[4:7], a[196:199], v[24:27], v[4:7]\n"
      "s_nop 0\n"
      "v_cvt_pk_bf16_f32 v53, v36, v36\n"
      "v_mfma_f32_16x16x32_bf16 v[8:11], a[200:203], v[24:27], v[8:11]\n"
      "v_add_f32_e32 v62, v62, v36\n"
      "v_mov_b32_dpp v54, v53 row_half_mirror row_mask:0xf bank_mask:0xf\n"
      "v_mfma_f32_16x16x32_bf16 v[12:15], a[204:207], v[24:27], v[12:15]\n"
      "v_add_f32_dpp v62, v36, v62 row_half_mirror row_mask:0xf bank_mask:0xf\n"
      "v_cndmask_b32_e64 v55, v53, v54, s[50:51]\n"
      "v_mfma_f32_16x16x32_bf16 v[0:3], a[208:211], v[28:31], v[0:3]\n"
      "v_cndmask_b32_e64 v56, v54, v53, s[50:51]\n"
      "v_and_b32_e32 v16, v55, v48\n"
      "v_mfma_f32_16x16x32_bf16 v[4:7], a[212:215], v[28:31], v[4:7]\n"
      "v_and_b32_e32 v17, v55, v49\n"
      "v_and_b32_e32 v18, v55, v50\n"
      "v_mfma_f32_16x16x32_bf16 v[8:11], a[216:219], v[28:31], v[8:11]\n"
      "v_and_b32_e32 v19, v55, v51\n"
      "v_and_b32_e32 v20, v56, v48\n"
      "v_mfma_f32_16x16x32_bf16 v[12:15], a[220:223], v[28:31], v[12:15]\n"
      "v_and_b32_e32 v21, v56, v49\n"
      "v_and_b32_e32 v22, v56, v50\n"
      "v_and_b32_e32 v23, v56, v51\n"
      "v_mfma_f32_16x16x32_bf16 v[0:3], v[96:99], v[16:19], v[0:3]\n"
      "v_fma_f32 v37, v37, s34, -v52\n"
      "v_exp_f32_e32 v37, v37\n"
      "v_mfma_f32_16x16x32_bf16 v[4:7], v[100:103], v[16:19], v[4:7]\n"
      "s_nop 0\n"
      "v_cvt_pk_bf16_f32 v53, v37, v37\n"
      "v_mfma_f32_16x16x32_bf16 v[8:11], v[104:107], v[16:19], v[8:11]\n"
      "v_add_f32_e32 v62, v62, v37\n"
      "v_mov_b32_dpp v54, v53 row_half_mirror row_mask:0xf bank_mask:0xf\n"
      "v_mfma_f32_16x16x32_bf16 v[12:15], v[108:111], v[16:19], v[12:15]\n"
      "v_add_f32_dpp v62, v37, v62 row_half_mirror row_mask:0xf bank_mask:0xf\n"
      "v_cndmask_b32_e64 v55, v53, v54, s[50:51]\n"
      "v_mfma_f32_16x16x32_bf16 v[0:3], v[112:115], v[20:23], v[0:3]\n"
      "v_cndmask_b32_e64 v56, v54, v53, s[50:51]\n"
      "v_and_b32_e32 v24, v55, v48\n"
      "v_mfma_f32_16x16x32_bf16 v[4:7], v[116:119], v[20:23], v[4:7]\n"
      "v_and_b32_e32 v25, v55, v49\n"
      "v_and_b32_e32 v26, v55, v50\n"
      "v_mfma_f32_16x16x32_bf16 v[8:11], v[120:123], v[20:23], v[8:11]\n"
      "v_and_b32_e32 v27, v55, v51\n"
      "v_and_b32_e32 v28, v56, v48\n"
      "v_mfma_f32_16x16x32_bf16 v[12:15], v[124:127], v[20:23], v[12:15]\n"
      "v_and_b32_e32 v29, v56, v49\n"
      "v_and_b32_e32 v30, v56, v50\n"
      "v_and_b32_e32 v31, v56, v51\n"
      "s_waitcnt vmcnt(8)\n"
      "v_mfma_f32_16x16x32_bf16 v[0:3], v[128:131], v[24:27], v[0:3]\n"
      "v_fma_f32 v38, v38, s34, -v52\n"
      "v_exp_f32_e32 v38, v38\n"
      "v_mfma_f32_16x16x32_bf16 v[4:7], v[132:135], v[24:27], v[4:7]\n"
      "s_nop 0\n"
      "v_cvt_pk_bf16_f32 v53, v38, v38\n"
      "v_mfma_f32_16x16x32_bf16 v[8:11], v[136:139], v[24:27], v[8:11]\n"
      "v_add_f32_e32 v62, v62, v38\n"
      "v_mov_b32_dpp v54, v53 row_half_mirror row_mask:0xf bank_mask:0xf\n"
      "v_mfma_f32_16x16x32_bf16 v[12:15], v[140:143], v[24:27], v[12:15]\n"
      "v_add_f32_dpp v62, v38, v62 row_half_mirror row_mask:0xf bank_mask:0xf\n"
      "v_cndmask_b32_e64 v55, v53, v54, s[50:51]\n"
      "v_mfma_f32_16x16x32_bf16 v[0:3], v[144:147], v[28:31], v[0:3]\n"
      "v_cndmask_b32_e64 v56, v54, v53, s[50:51]\n"
      "v_and_b32_e32 v16, v55, v48\n"
      "v_mfma_f32_16x16x32_bf16 v[4:7], v[148:151], v[28:31], v[4:7]\n"
      "v_and_b32_e32 v17, v55, v49\n"
      "v_and_b32_e32 v18, v55, v50\n"
      "v_mfma_f32_16x16x32_bf16 v[8:11], v[152:155], v[28:31], v[8:11]\n"
      "v_and_b32_e32 v19, v55, v51\n"
      "v_and_b32_e32 v20, v56, v48\n"
      "v_mfma_f32_16x16x32_bf16 v[12:15], v[156:159], v[28:31], v[12:15]\n"
      "v_and_b32_e32 v21, v56, v49\n"
      "v_and_b32_e32 v22, v56, v50\n"
      "v_and_b32_e32 v23, v56, v51\n"
      "s_waitcnt vmcnt(0)\n"
      "v_mfma_f32_16x16x32_bf16 v[0:3], v[160:163], v[16:19], v[0:3]\n"
      "v_mfma_f32_16x16x32_bf16 v[4:7], v[164:167], v[16:19], v[4:7]\n"
      "v_mfma_f32_16x16x32_bf16 v[8:11], v[168:171], v[16:19], v[8:11]\n"
      "v_mfma_f32_16x16x32_bf16 v[12:15], v[172:175], v[16:19], v[12:15]\n"
      "v_mfma_f32_16x16x32_bf16 v[32:35], v[176:179], v[20:23], v[0:3]\n"
      "v_mfma_f32_16x16x32_bf16 v[36:39], v[180:183], v[20:23], v[4:7]\n"
      "v_mfma_f32_16x16x32_bf16 v[40:43], v[184:187], v[20:23], v[8:11]\n"
      "v_mfma_f32_16x16x32_bf16 v[44:47], v[188:191], v[20:23], v[12:15]\n"
      "s_add_u32 s74, s44, 0xc0000\n"
      "s_addc_u32 s75, s45, 0\n"
      "s_add_u32 s76, s46, 0x1800\n"
      "s_addc_u32 s77, s47, 0\n"
      "s_mov_b32 s72, 0x25050\n"
      "s_mov_b32 s73, 0x260ac\n"
      "s_nop 1\n"
      "s_swappc_b64 s[66:67], s[70:71]\n"
      "s_branch END_25_%=\n"
      "s_nop 0\n"
      "VAR4_80_%=:\n"
      "v_mbcnt_lo_u32_b32 v52, -1, 0\n"
      "v_mbcnt_hi_u32_b32 v52, -1, v52\n"
      "v_lshlrev_b32_e32 v52, 4, v52\n"
      "s_lshl_b32 s80, s48, 12\n"
      "s_add_u32 s80, s80, 0x20000\n"
      "v_add_u32_e32 v52, s80, v52\n"
      "ds_write_b128 v52, v[32:35] offset:0\n"
      "ds_write_b128 v52, v[36:39] offset:1024\n"
      "ds_write_b128 v52, v[40:43] offset:2048\n"
      "ds_write_b128 v52, v[44:47] offset:3072\n"
      "v_mbcnt_lo_u32_b32 v52, -1, 0\n"
      "v_mbcnt_hi_u32_b32 v52, -1, v52\n"
      "v_lshlrev_b32_e32 v52, 2, v52\n"
      "s_lshl_b32 s80, s48, 8\n"
      "s_add_u32 s80, s80, 0x26240\n"
      "v_add_u32_e32 v52, s80, v52\n"
      "ds_write_b32 v52, v62\n"
      "s_mov_b32 s78, s60\n"
      "s_waitcnt lgkmcnt(0)\n"
      "v_mbcnt_lo_u32_b32 v53, -1, 0\n"
      "v_mbcnt_hi_u32_b32 v53, -1, v53\n"
      "v_lshrrev_b32_e32 v52, 4, v53\n"
      "v_lshlrev_b32_e32 v52, 6, v52\n"
      "v_and_b32_e32 v53, 3, v53\n"
      "v_lshl_add_u32 v52, v53, 4, v52\n"
      "v_add_u32_e32 v52, 0x26840, v52\n"
      "s_mov_b32 vcc_lo, 0xf000f\n"
      "s_mov_b32 vcc_hi, 0xf000f\n"
      "v_mov_b32_e32 v53, 0x26120\n"
      "v_cndmask_b32_e32 v53, v53, v52, vcc\n"
      "ds_read_b128 v[0:3], v53 offset:0\n"
      "ds_read_b128 v[4:7], v53 offset:256\n"
      "s_mov_b32 vcc_lo, 0xf000f0\n"
      "s_mov_b32 vcc_hi, 0xf000f0\n"
      "v_mov_b32_e32 v53, 0x26120\n"
      "v_cndmask_b32_e32 v53, v53, v52, vcc\n"
      "ds_read_b128 v[8:11], v53 offset:0\n"
      "ds_read_b128 v[12:15], v53 offset:256\n"
      "s_mov_b32 vcc_lo, 0xf000f00\n"
      "s_mov_b32 vcc_hi, 0xf000f00\n"
      "v_mov_b32_e32 v53, 0x26120\n"
      "v_cndmask_b32_e32 v53, v53, v52, vcc\n"
      "ds_read_b128 v[16:19], v53 offset:0\n"
      "ds_read_b128 v[20:23], v53 offset:256\n"
      "s_mov_b32 vcc_lo, 0xf000f000\n"
      "s_mov_b32 vcc_hi, 0xf000f000\n"
      "v_mov_b32_e32 v53, 0x26120\n"
      "v_cndmask_b32_e32 v53, v53, v52, vcc\n"
      "ds_read_b128 v[24:27], v53 offset:0\n"
      "ds_read_b128 v[28:31], v53 offset:256\n"
      "s_waitcnt lgkmcnt(0)\n"
      "v_mov_b32_e32 v32, 0\n"
      "v_mov_b32_e32 v33, 0\n"
      "v_mov_b32_e32 v34, 0\n"
      "v_mov_b32_e32 v35, 0\n"
      "v_mov_b32_e32 v36, 0\n"
      "v_mov_b32_e32 v37, 0\n"
      "v_mov_b32_e32 v38, 0\n"
      "v_mov_b32_e32 v39, 0\n"
      "v_mov_b32_e32 v40, 0\n"
      "v_mov_b32_e32 v41, 0\n"
      "v_mov_b32_e32 v42, 0\n"
      "v_mov_b32_e32 v43, 0\n"
      "v_mov_b32_e32 v44, 0\n"
      "v_mov_b32_e32 v45, 0\n"
      "v_mov_b32_e32 v46, 0\n"
      "v_mov_b32_e32 v47, 0\n"
      "v_mov_b32_e32 v62, 0\n"
      "s_mov_b32 s60, 0xff800000\n"
      "global_load_dwordx4 v[168:171], v61, s[90:91] offset:256 nt\n"
      "global_load_dwordx4 v[172:175], v61, s[90:91] offset:384 nt\n"
      "v_mfma_f32_16x16x32_bf16 v[48:51], v[192:195], v[0:3], 0\n"
      "v_mfma_f32_16x16x32_bf16 v[48:51], v[196:199], v[4:7], v[48:51]\n"
      "global_load_dwordx4 v[176:179], v61, s[90:91] offset:2048 nt\n"
      "global_load_dwordx4 v[180:183], v61, s[90:91] offset:2176 nt\n"
      "v_mfma_f32_16x16x32_bf16 v[48:51], v[200:203], v[8:11], v[48:51]\n"
      "v_mfma_f32_16x16x32_bf16 v[48:51], v[204:207], v[12:15], v[48:51]\n"
      "global_load_dwordx4 v[184:187], v61, s[90:91] offset:2304 nt\n"
      "global_load_dwordx4 v[188:191], v61, s[90:91] offset:2432 nt\n"
      "v_mfma_f32_16x16x32_bf16 v[48:51], v[208:211], v[16:19], v[48:51]\n"
      "v_mfma_f32_16x16x32_bf16 v[48:51], v[212:215], v[20:23], v[48:51]\n"
      "v_mfma_f32_16x16x32_bf16 v[48:51], v[216:219], v[24:27], v[48:51]\n"
      "v_mfma_f32_16x16x32_bf16 v[48:51], v[220:223], v[28:31], v[48:51]\n"
      "s_add_u32 s86, s100, 0x4230000\n"
      "s_cmp_ge_u32 s86, 0x43f0000\n"
      "s_cselect_b32 s87, 0x3f0000, 0\n"
      "s_sub_u32 s86, s86, s87\n"
      "s_add_u32 s90, s36, s86\n"
      "s_addc_u32 s91, s37, 0\n"
      "global_load_dwordx4 v[192:195], v60, s[90:91] offset:-4096 nt\n"
      "global_load_dwordx4 v[196:199], v60, s[90:91] offset:-3840 nt\n"
      "global_load_dwordx4 v[200:203], v60, s[90:91] offset:-2048 nt\n"
      "global_load_dwordx4 v[204:207], v60, s[90:91] offset:-1792 nt\n"
      "global_load_dwordx4 v[208:211], v60, s[90:91] offset:0 nt\n"
      "global_load_dwordx4 v[212:215], v60, s[90:91] offset:256 nt\n"
      "global_load_dwordx4 v[216:219], v60, s[90:91] offset:2048 nt\n"
      "global_load_dwordx4 v[220:223], v60, s[90:91] offset:2304 nt\n"
      "v_cndmask_b32_e64 v53, v48, v49, s[96:97]\n"
      "v_cndmask_b32_e64 v54, v50, v51, s[96:97]\n"
      "v_cndmask_b32_e64 v52, v53, v54, s[98:99]\n"
      "s_nop 1\n"
      "v_add_f32_dpp v52, v52, v52 quad_perm:[1,0,3,2] row_mask:0xf bank_mask:0xf\n"
      "s_nop 1\n"
      "v_add_f32_dpp v52, v52, v52 quad_perm:[2,3,0,1] row_mask:0xf bank_mask:0xf\n"
      "v_cmp_lt_f32_e32 vcc, s60, v52\n"
      "s_cbranch_vccz NORS_99_%=\n"
      "s_swappc_b64 s[66:67], s[68:69]\n"
      "NORS_99_%=:\n"
      "v_subrev_f32_e32 v52, s60, v52\n"
      "v_mul_f32_e32 v52, 0x3db8aa3b, v52\n"
      "v_exp_f32_e32 v52, v52\n"
      "s_nop 0\n"
      "v_cvt_pk_bf16_f32 v53, v52, v52\n"
      "v_add_f32_e32 v62, v62, v52\n"
      "v_and_b32_e32 v53, v53, v63\n"
      "s_nop 0\n"
      "v_add_f32_dpp v62, v52, v62 row_half_mirror row_mask:0xf bank_mask:0xf\n"
      "v_mov_b32_dpp v56, v52 row_half_mirror row_mask:0xf bank_mask:0xf\n"
      "v_cvt_pk_bf16_f32 v56, v56, v56\n"
      "v_and_b32_e32 v56, v56, v63\n"
      "v_cndmask_b32_e64 v55, v53, v56, s[50:51]\n"
      "v_cndmask_b32_e64 v59, v56, v53, s[50:51]\n"
      "v_cndmask_b32_e64 v52, 0, v55, s[52:53]\n"
      "v_cndmask_b32_e64 v53, 0, v55, s[54:55]\n"
      "v_cndmask_b32_e64 v54, 0, v55, s[56:57]\n"
      "v_cndmask_b32_e64 v55, 0, v55, s[58:59]\n"
      "v_cndmask_b32_e64 v56, 0, v59, s[52:53]\n"
      "v_cndmask_b32_e64 v57, 0, v59, s[54:55]\n"
      "v_cndmask_b32_e64 v58, 0, v59, s[56:57]\n"
      "v_cndmask_b32_e64 v59, 0, v59, s[58:59]\n"
      "v_mfma_f32_16x16x32_bf16 v[32:35], v[224:227], v[52:55], v[32:35]\n"
      "v_mfma_f32_16x16x32_bf16 v[36:39], v[228:231], v[52:55], v[36:39]\n"
      "v_mfma_f32_16x16x32_bf16 v[40:43], v[232:235], v[52:55], v[40:43]\n"
      "v_mfma_f32_16x16x32_bf16 v[44:47], v[236:239], v[52:55], v[44:47]\n"
      "v_mfma_f32_16x16x32_bf16 v[32:35], v[240:243], v[56:59], v[32:35]\n"
      "v_mfma_f32_16x16x32_bf16 v[36:39], v[244:247], v[56:59], v[36:39]\n"
      "v_mfma_f32_16x16x32_bf16 v[40:43], v[248:251], v[56:59], v[40:43]\n"
      "v_mfma_f32_16x16x32_bf16 v[44:47], v[252:255], v[56:59], v[44:47]\n"
      "s_add_u32 s86, s100, 0x4230000\n"
      "s_cmp_ge_u32 s86, 0x43f0000\n"
      "s_cselect_b32 s87, 0x3f0000, 0\n"
      "s_sub_u32 s86, s86, s87\n"
      "s_add_u32 s90, s38, s86\n"
      "s_addc_u32 s91, s39, 0\n"
      "global_load_dwordx4 v[224:227], v61, s[90:91] offset:0 nt\n"
      "global_load_dwordx4 v[228:231], v61, s[90:91] offset:128 nt\n"
      "v_mfma_f32_16x16x32_bf16 v[48:51], a[0:3], v[0:3], 0\n"
      "v_mfma_f32_16x16x32_bf16 v[48:51], a[4:7], v[4:7], v[48:51]\n"
      "v_mfma_f32_16x16x32_bf16 v[48:51], a[8:11], v[8:11], v[48:51]\n"
      "v_mfma_f32_16x16x32_bf16 v[48:51], a[12:15], v[12:15], v[48:51]\n"
      "v_mfma_f32_16x16x32_bf16 v[48:51], a[16:19], v[16:19], v[48:51]\n"
      "v_mfma_f32_16x16x32_bf16 v[48:51], a[20:23], v[20:23], v[48:51]\n"
      "v_mfma_f32_16x16x32_bf16 v[48:51], a[24:27], v[24:27], v[48:51]\n"
      "v_mfma_f32_16x16x32_bf16 v[48:51], a[28:31], v[28:31], v[48:51]\n"
      "s_nop 7\n"
      "v_cndmask_b32_e64 v53, v48, v49, s[96:97]\n"
      "v_cndmask_b32_e64 v54, v50, v51, s[96:97]\n"
      "v_cndmask_b32_e64 v52, v53, v54, s[98:99]\n"
      "s_nop 1\n"
      "v_add_f32_dpp v52, v52, v52 quad_perm:[1,0,3,2] row_mask:0xf bank_mask:0xf\n"
      "s_nop 1\n"
      "v_add_f32_dpp v52, v52, v52 quad_perm:[2,3,0,1] row_mask:0xf bank_mask:0xf\n"
      "v_cmp_lt_f32_e32 vcc, s60, v52\n"
      "s_cbranch_vccz NORS_100_%=\n"
      "s_swappc_b64 s[66:67], s[68:69]\n"
      "NORS_100_%=:\n"
      "v_subrev_f32_e32 v52, s60, v52\n"
      "v_mul_f32_e32 v52, 0x3db8aa3b, v52\n"
      "v_exp_f32_e32 v52, v52\n"
      "s_nop 0\n"
      "v_cvt_pk_bf16_f32 v53, v52, v52\n"
      "v_add_f32_e32 v62, v62, v52\n"
      "v_and_b32_e32 v53, v53, v63\n"
      "s_nop 0\n"
      "v_add_f32_dpp v62, v52, v62 row_half_mirror row_mask:0xf bank_mask:0xf\n"
      "v_mov_b32_dpp v56, v52 row_half_mirror row_mask:0xf bank_mask:0xf\n"
      "v_cvt_pk_bf16_f32 v56, v56, v56\n"
      "v_and_b32_e32 v56, v56, v63\n"
      "v_cndmask_b32_e64 v55, v53, v56, s[50:51]\n"
      "v_cndmask_b32_e64 v59, v56, v53, s[50:51]\n"
      "v_cndmask_b32_e64 v52, 0, v55, s[52:53]\n"
      "v_cndmask_b32_e64 v53, 0, v55, s[54:55]\n"
      "v_cndmask_b32_e64 v54, 0, v55, s[56:57]\n"
      "v_cndmask_b32_e64 v55, 0, v55, s[58:59]\n"
      "v_cndmask_b32_e64 v56, 0, v59, s[52:53]\n"
      "v_cndmask_b32_e64 v57, 0, v59, s[54:55]\n"
      "v_cndmask_b32_e64 v58, 0, v59, s[56:57]\n"
      "v_cndmask_b32_e64 v59, 0, v59, s[58:59]\n"
      "v_mfma_f32_16x16x32_bf16 v[32:35], a[32:35], v[52:55], v[32:35]\n"
      "v_mfma_f32_16x16x32_bf16 v[36:39], a[36:39], v[52:55], v[36:39]\n"
      "v_mfma_f32_16x16x32_bf16 v[40:43], a[40:43], v[52:55], v[40:43]\n"
      "v_mfma_f32_16x16x32_bf16 v[44:47], a[44:47], v[52:55], v[44:47]\n"
      "v_mfma_f32_16x16x32_bf16 v[32:35], a[48:51], v[56:59], v[32:35]\n"
      "v_mfma_f32_16x16x32_bf16 v[36:39], a[52:55], v[56:59], v[36:39]\n"
      "v_mfma_f32_16x16x32_bf16 v[40:43], a[56:59], v[56:59], v[40:43]\n"
      "v_mfma_f32_16x16x32_bf16 v[44:47], a[60:63], v[56:59], v[44:47]\n"
      "v_mfma_f32_16x16x32_bf16 v[48:51], a[64:67], v[0:3], 0\n"
      "v_mfma_f32_16x16x32_bf16 v[48:51], a[68:71], v[4:7], v[48:51]\n"
      "v_mfma_f32_16x16x32_bf16 v[48:51], a[72:75], v[8:11], v[48:51]\n"
      "v_mfma_f32_16x16x32_bf16 v[48:51], a[76:79], v[12:15], v[48:51]\n"
      "v_mfma_f32_16x16x32_bf16 v[48:51], a[80:83], v[16:19], v[48:51]\n"
      "v_mfma_f32_16x16x32_bf16 v[48:51], a[84:87], v[20:23], v[48:51]\n"
      "v_mfma_f32_16x16x32_bf16 v[48:51], a[88:91], v[24:27], v[48:51]\n"
      "v_mfma_f32_16x16x32_bf16 v[48:51], a[92:95], v[28:31], v[48:51]\n"
      "s_nop 7\n"
      "v_cndmask_b32_e64 v53, v48, v49, s[96:97]\n"
      "v_cndmask_b32_e64 v54, v50, v51, s[96:97]\n"
      "v_cndmask_b32_e64 v52, v53, v54, s[98:99]\n"
      "s_nop 1\n"
      "v_add_f32_dpp v52, v52, v52 quad_perm:[1,0,3,2] row_mask:0xf bank_mask:0xf\n"
      "s_nop 1\n"
      "v_add_f32_dpp v52, v52, v52 quad_perm:[2,3,0,1] row_mask:0xf bank_mask:0xf\n"
      "v_cmp_lt_f32_e32 vcc, s60, v52\n"
      "s_cbranch_vccz NORS_101_%=\n"
      "s_swappc_b64 s[66:67], s[68:69]\n"
      "NORS_101_%=:\n"
      "v_subrev_f32_e32 v52, s60, v52\n"
      "v_mul_f32_e32 v52, 0x3db8aa3b, v52\n"
      "v_exp_f32_e32 v52, v52\n"
      "s_nop 0\n"
      "v_cvt_pk_bf16_f32 v53, v52, v52\n"
      "v_add_f32_e32 v62, v62, v52\n"
      "v_and_b32_e32 v53, v53, v63\n"
      "s_nop 0\n"
      "v_add_f32_dpp v62, v52, v62 row_half_mirror row_mask:0xf bank_mask:0xf\n"
      "v_mov_b32_dpp v56, v52 row_half_mirror row_mask:0xf bank_mask:0xf\n"
      "v_cvt_pk_bf16_f32 v56, v56, v56\n"
      "v_and_b32_e32 v56, v56, v63\n"
      "v_cndmask_b32_e64 v55, v53, v56, s[50:51]\n"
      "v_cndmask_b32_e64 v59, v56, v53, s[50:51]\n"
      "v_cndmask_b32_e64 v52, 0, v55, s[52:53]\n"
      "v_cndmask_b32_e64 v53, 0, v55, s[54:55]\n"
      "v_cndmask_b32_e64 v54, 0, v55, s[56:57]\n"
      "v_cndmask_b32_e64 v55, 0, v55, s[58:59]\n"
      "v_cndmask_b32_e64 v56, 0, v59, s[52:53]\n"
      "v_cndmask_b32_e64 v57, 0, v59, s[54:55]\n"
      "v_cndmask_b32_e64 v58, 0, v59, s[56:57]\n"
      "v_cndmask_b32_e64 v59, 0, v59, s[58:59]\n"
      "v_mfma_f32_16x16x32_bf16 v[32:35], a[96:99], v[52:55], v[32:35]\n"
      "v_mfma_f32_16x16x32_bf16 v[36:39], a[100:103], v[52:55], v[36:39]\n"
      "v_mfma_f32_16x16x32_bf16 v[40:43], a[104:107], v[52:55], v[40:43]\n"
      "v_mfma_f32_16x16x32_bf16 v[44:47], a[108:111], v[52:55], v[44:47]\n"
      "v_mfma_f32_16x16x32_bf16 v[32:35], a[112:115], v[56:59], v[32:35]\n"
      "v_mfma_f32_16x16x32_bf16 v[36:39], a[116:119], v[56:59], v[36:39]\n"
      "v_mfma_f32_16x16x32_bf16 v[40:43], a[120:123], v[56:59], v[40:43]\n"
      "v_mfma_f32_16x16x32_bf16 v[44:47], a[124:127], v[56:59], v[44:47]\n"
      "v_mfma_f32_16x16x32_bf16 v[48:51], a[128:131], v[0:3], 0\n"
      "v_mfma_f32_16x16x32_bf16 v[48:51], a[132:135], v[4:7], v[48:51]\n"
      "v_mfma_f32_16x16x32_bf16 v[48:51], a[136:139], v[8:11], v[48:51]\n"
      "v_mfma_f32_16x16x32_bf16 v[48:51], a[140:143], v[12:15], v[48:51]\n"
      "v_mfma_f32_16x16x32_bf16 v[48:51], a[144:147], v[16:19], v[48:51]\n"
      "v_mfma_f32_16x16x32_bf16 v[48:51], a[148:151], v[20:23], v[48:51]\n"
      "v_mfma_f32_16x16x32_bf16 v[48:51], a[152:155], v[24:27], v[48:51]\n"
      "v_mfma_f32_16x16x32_bf16 v[48:51], a[156:159], v[28:31], v[48:51]\n"
      "s_nop 7\n"
      "v_cndmask_b32_e64 v53, v48, v49, s[96:97]\n"
      "v_cndmask_b32_e64 v54, v50, v51, s[96:97]\n"
      "v_cndmask_b32_e64 v52, v53, v54, s[98:99]\n"
      "s_nop 1\n"
      "v_add_f32_dpp v52, v52, v52 quad_perm:[1,0,3,2] row_mask:0xf bank_mask:0xf\n"
      "s_nop 1\n"
      "v_add_f32_dpp v52, v52, v52 quad_perm:[2,3,0,1] row_mask:0xf bank_mask:0xf\n"
      "v_cmp_lt_f32_e32 vcc, s60, v52\n"
      "s_cbranch_vccz NORS_102_%=\n"
      "s_swappc_b64 s[66:67], s[68:69]\n"
      "NORS_102_%=:\n"
      "v_subrev_f32_e32 v52, s60, v52\n"
      "v_mul_f32_e32 v52, 0x3db8aa3b, v52\n"
      "v_exp_f32_e32 v52, v52\n"
      "s_nop 0\n"
      "v_cvt_pk_bf16_f32 v53, v52, v52\n"
      "v_add_f32_e32 v62, v62, v52\n"
      "v_and_b32_e32 v53, v53, v63\n"
      "s_nop 0\n"
      "v_add_f32_dpp v62, v52, v62 row_half_mirror row_mask:0xf bank_mask:0xf\n"
      "v_mov_b32_dpp v56, v52 row_half_mirror row_mask:0xf bank_mask:0xf\n"
      "v_cvt_pk_bf16_f32 v56, v56, v56\n"
      "v_and_b32_e32 v56, v56, v63\n"
      "v_cndmask_b32_e64 v55, v53, v56, s[50:51]\n"
      "v_cndmask_b32_e64 v59, v56, v53, s[50:51]\n"
      "v_cndmask_b32_e64 v52, 0, v55, s[52:53]\n"
      "v_cndmask_b32_e64 v53, 0, v55, s[54:55]\n"
      "v_cndmask_b32_e64 v54, 0, v55, s[56:57]\n"
      "v_cndmask_b32_e64 v55, 0, v55, s[58:59]\n"
      "v_cndmask_b32_e64 v56, 0, v59, s[52:53]\n"
      "v_cndmask_b32_e64 v57, 0, v59, s[54:55]\n"
      "v_cndmask_b32_e64 v58, 0, v59, s[56:57]\n"
      "v_cndmask_b32_e64 v59, 0, v59, s[58:59]\n"
      "v_mfma_f32_16x16x32_bf16 v[32:35], a[160:163], v[52:55], v[32:35]\n"
      "v_mfma_f32_16x16x32_bf16 v[36:39], a[164:167], v[52:55], v[36:39]\n"
      "v_mfma_f32_16x16x32_bf16 v[40:43], a[168:171], v[52:55], v[40:43]\n"
      "v_mfma_f32_16x16x32_bf16 v[44:47], a[172:175], v[52:55], v[44:47]\n"
      "v_mfma_f32_16x16x32_bf16 v[32:35], a[176:179], v[56:59], v[32:35]\n"
      "v_mfma_f32_16x16x32_bf16 v[36:39], a[180:183], v[56:59], v[36:39]\n"
      "v_mfma_f32_16x16x32_bf16 v[40:43], a[184:187], v[56:59], v[40:43]\n"
      "v_mfma_f32_16x16x32_bf16 v[44:47], a[188:191], v[56:59], v[44:47]\n"
      "s_nop 7\n"
      "v_mbcnt_lo_u32_b32 v52, -1, 0\n"
      "v_mbcnt_hi_u32_b32 v52, -1, v52\n"
      "v_lshlrev_b32_e32 v52, 4, v52\n"
      "s_lshl_b32 s80, s48, 12\n"
      "s_add_u32 s80, s80, 0x20000\n"
      "v_add_u32_e32 v52, s80, v52\n"
      "ds_read_b128 v[0:3], v52 offset:0\n"
      "ds_read_b128 v[4:7], v52 offset:1024\n"
      "ds_read_b128 v[8:11], v52 offset:2048\n"
      "ds_read_b128 v[12:15], v52 offset:3072\n"
      "v_mbcnt_lo_u32_b32 v53, -1, 0\n"
      "v_mbcnt_hi_u32_b32 v53, -1, v53\n"
      "v_lshlrev_b32_e32 v53, 2, v53\n"
      "s_lshl_b32 s80, s48, 8\n"
      "s_add_u32 s80, s80, 0x26240\n"
      "v_add_u32_e32 v53, s80, v53\n"
      "ds_read_b32 v16, v53\n"
      "s_waitcnt lgkmcnt(0)\n"
      "ds_write_b128 v52, v[32:35] offset:0\n"
      "ds_write_b128 v52, v[36:39] offset:1024\n"
      "ds_write_b128 v52, v[40:43] offset:2048\n"
      "ds_write_b128 v52, v[44:47] offset:3072\n"
      "ds_write_b32 v53, v62\n"
      "s_mov_b32 s80, s78\n"
      "s_mov_b32 s78, s60\n"
      "s_mov_b32 s60, s80\n"
      "s_waitcnt lgkmcnt(0)\n"
      "v_mov_b32_e32 v32, v0\n"
      "v_mov_b32_e32 v33, v1\n"
      "v_mov_b32_e32 v34, v2\n"
      "v_mov_b32_e32 v35, v3\n"
      "v_mov_b32_e32 v36, v4\n"
      "v_mov_b32_e32 v37, v5\n"
      "v_mov_b32_e32 v38, v6\n"
      "v_mov_b32_e32 v39, v7\n"
      "v_mov_b32_e32 v40, v8\n"
      "v_mov_b32_e32 v41, v9\n"
      "v_mov_b32_e32 v42, v10\n"
      "v_mov_b32_e32 v43, v11\n"
      "v_mov_b32_e32 v44, v12\n"
      "v_mov_b32_e32 v45, v13\n"
      "v_mov_b32_e32 v46, v14\n"
      "v_mov_b32_e32 v47, v15\n"
      "v_mov_b32_e32 v62, v16\n"
      "v_mbcnt_lo_u32_b32 v53, -1, 0\n"
      "v_mbcnt_hi_u32_b32 v53, -1, v53\n"
      "v_lshrrev_b32_e32 v52, 4, v53\n"
      "v_lshlrev_b32_e32 v52, 6, v52\n"
      "v_and_b32_e32 v53, 3, v53\n"
      "v_lshl_add_u32 v52, v53, 4, v52\n"
      "v_add_u32_e32 v52, 0x26a40, v52\n"
      "s_mov_b32 vcc_lo, 0xf000f\n"
      "s_mov_b32 vcc_hi, 0xf000f\n"
      "v_mov_b32_e32 v53, 0x26120\n"
      "v_cndmask_b32_e32 v53, v53, v52, vcc\n"
      "ds_read_b128 v[0:3], v53 offset:0\n"
      "ds_read_b128 v[4:7], v53 offset:256\n"
      "s_mov_b32 vcc_lo, 0xf000f0\n"
      "s_mov_b32 vcc_hi, 0xf000f0\n"
      "v_mov_b32_e32 v53, 0x26120\n"
      "v_cndmask_b32_e32 v53, v53, v52, vcc\n"
      "ds_read_b128 v[8:11], v53 offset:0\n"
      "ds_read_b128 v[12:15], v53 offset:256\n"
      "s_mov_b32 vcc_lo, 0xf000f00\n"
      "s_mov_b32 vcc_hi, 0xf000f00\n"
      "v_mov_b32_e32 v53, 0x26120\n"
      "v_cndmask_b32_e32 v53, v53, v52, vcc\n"
      "ds_read_b128 v[16:19], v53 offset:0\n"
      "ds_read_b128 v[20:23], v53 offset:256\n"
      "s_mov_b32 vcc_lo, 0xf000f000\n"
      "s_mov_b32 vcc_hi, 0xf000f000\n"
      "v_mov_b32_e32 v53, 0x26120\n"
      "v_cndmask_b32_e32 v53, v53, v52, vcc\n"
      "ds_read_b128 v[24:27], v53 offset:0\n"
      "ds_read_b128 v[28:31], v53 offset:256\n"
      "s_waitcnt lgkmcnt(0)\n"
      "s_waitcnt vmcnt(24)\n"
      "v_mfma_f32_16x16x32_bf16 v[48:51], v[128:131], v[0:3], 0\n"
      "v_mfma_f32_16x16x32_bf16 v[48:51], v[132:135], v[4:7], v[48:51]\n"
      "s_waitcnt vmcnt(22)\n"
      "v_mfma_f32_16x16x32_bf16 v[48:51], v[136:139], v[8:11], v[48:51]\n"
      "v_mfma_f32_16x16x32_bf16 v[48:51], v[140:143], v[12:15], v[48:51]\n"
      "s_waitcnt vmcnt(20)\n"
      "v_mfma_f32_16x16x32_bf16 v[48:51], v[144:147], v[16:19], v[48:51]\n"
      "v_mfma_f32_16x16x32_bf16 v[48:51], v[148:151], v[20:23], v[48:51]\n"
      "s_waitcnt vmcnt(18)\n"
      "v_mfma_f32_16x16x32_bf16 v[48:51], v[152:155], v[24:27], v[48:51]\n"
      "v_mfma_f32_16x16x32_bf16 v[48:51], v[156:159], v[28:31], v[48:51]\n"
      "s_nop 7\n"
      "v_cndmask_b32_e64 v53, v48, v49, s[96:97]\n"
      "v_cndmask_b32_e64 v54, v50, v51, s[96:97]\n"
      "v_cndmask_b32_e64 v52, v53, v54, s[98:99]\n"
      "s_nop 1\n"
      "v_add_f32_dpp v52, v52, v52 quad_perm:[1,0,3,2] row_mask:0xf bank_mask:0xf\n"
      "s_nop 1\n"
      "v_add_f32_dpp v52, v52, v52 quad_perm:[2,3,0,1] row_mask:0xf bank_mask:0xf\n"
      "v_cmp_lt_f32_e32 vcc, s60, v52\n"
      "s_cbranch_vccz NORS_103_%=\n"
      "s_swappc_b64 s[66:67], s[68:69]\n"
      "NORS_103_%=:\n"
      "v_subrev_f32_e32 v52, s60, v52\n"
      "v_mul_f32_e32 v52, 0x3db8aa3b, v52\n"
      "v_exp_f32_e32 v52, v52\n"
      "s_nop 0\n"
      "v_cvt_pk_bf16_f32 v53, v52, v52\n"
      "v_add_f32_e32 v62, v62, v52\n"
      "v_and_b32_e32 v53, v53, v63\n"
      "s_nop 0\n"
      "v_add_f32_dpp v62, v52, v62 row_half_mirror row_mask:0xf bank_mask:0xf\n"
      "v_mov_b32_dpp v56, v52 row_half_mirror row_mask:0xf bank_mask:0xf\n"
      "v_cvt_pk_bf16_f32 v56, v56, v56\n"
      "v_and_b32_e32 v56, v56, v63\n"
      "v_cndmask_b32_e64 v55, v53, v56, s[50:51]\n"
      "v_cndmask_b32_e64 v59, v56, v53, s[50:51]\n"
      "v_cndmask_b32_e64 v52, 0, v55, s[52:53]\n"
      "v_cndmask_b32_e64 v53, 0, v55, s[54:55]\n"
      "v_cndmask_b32_e64 v54, 0, v55, s[56:57]\n"
      "v_cndmask_b32_e64 v55, 0, v55, s[58:59]\n"
      "v_cndmask_b32_e64 v56, 0, v59, s[52:53]\n"
      "v_cndmask_b32_e64 v57, 0, v59, s[54:55]\n"
      "v_cndmask_b32_e64 v58, 0, v59, s[56:57]\n"
      "v_cndmask_b32_e64 v59, 0, v59, s[58:59]\n"
      "s_waitcnt vmcnt(16)\n"
      "v_mfma_f32_16x16x32_bf16 v[32:35], v[160:163], v[52:55], v[32:35]\n"
      "v_mfma_f32_16x16x32_bf16 v[36:39], v[164:167], v[52:55], v[36:39]\n"
      "s_waitcnt vmcnt(14)\n"
      "global_load_dwordx4 v[232:235], v61, s[90:91] offset:256 nt\n"
      "global_load_dwordx4 v[236:239], v61, s[90:91] offset:384 nt\n"
      "v_mfma_f32_16x16x32_bf16 v[40:43], v[168:171], v[52:55], v[40:43]\n"
      "v_mfma_f32_16x16x32_bf16 v[44:47], v[172:175], v[52:55], v[44:47]\n"
      "s_waitcnt vmcnt(14)\n"
      "global_load_dwordx4 v[240:243], v61, s[90:91] offset:2048 nt\n"
      "global_load_dwordx4 v[244:247], v61, s[90:91] offset:2176 nt\n"
      "v_mfma_f32_16x16x32_bf16 v[32:35], v[176:179], v[56:59], v[32:35]\n"
      "v_mfma_f32_16x16x32_bf16 v[36:39], v[180:183], v[56:59], v[36:39]\n"
      "s_waitcnt vmcnt(14)\n"
      "global_load_dwordx4 v[248:251], v61, s[90:91] offset:2304 nt\n"
      "global_load_dwordx4 v[252:255], v61, s[90:91] offset:2432 nt\n"
      "v_mfma_f32_16x16x32_bf16 v[40:43], v[184:187], v[56:59], v[40:43]\n"
      "v_mfma_f32_16x16x32_bf16 v[44:47], v[188:191], v[56:59], v[44:47]\n"
      "s_waitcnt vmcnt(14)\n"
      "s_add_u32 s86, s100, 0x42a0000\n"
      "s_cmp_ge_u32 s86, 0x43f0000\n"
      "s_cselect_b32 s87, 0x3f0000, 0\n"
      "s_sub_u32 s86, s86, s87\n"
      "s_add_u32 s90, s36, s86\n"
      "s_addc_u32 s91, s37, 0\n"
      "global_load_dwordx4 a[0:3], v60, s[90:91] offset:-4096 nt\n"
      "global_load_dwordx4 a[4:7], v60, s[90:91] offset:-3840 nt\n"
      "v_mfma_f32_16x16x32_bf16 v[48:51], v[192:195], v[0:3], 0\n"
      "v_mfma_f32_16x16x32_bf16 v[48:51], v[196:199], v[4:7], v[48:51]\n"
      "s_waitcnt vmcnt(14)\n"
      "global_load_dwordx4 a[8:11], v60, s[90:91] offset:-2048 nt\n"
      "global_load_dwordx4 a[12:15], v60, s[90:91] offset:-1792 nt\n"
      "v_mfma_f32_16x16x32_bf16 v[48:51], v[200:203], v[8:11], v[48:51]\n"
      "v_mfma_f32_16x16x32_bf16 v[48:51], v[204:207], v[12:15], v[48:51]\n"
      "s_waitcnt vmcnt(14)\n"
      "global_load_dwordx4 a[16:19], v60, s[90:91] offset:0 nt\n"
      "global_load_dwordx4 a[20:23], v60, s[90:91] offset:256 nt\n"
      "v_mfma_f32_16x16x32_bf16 v[48:51], v[208:211], v[16:19], v[48:51]\n"
      "v_mfma_f32_16x16x32_bf16 v[48:51], v[212:215], v[20:23], v[48:51]\n"
      "s_waitcnt vmcnt(14)\n"
      "global_load_dwordx4 a[24:27], v60, s[90:91] offset:2048 nt\n"
      "global_load_dwordx4 a[28:31], v60, s[90:91] offset:2304 nt\n"
      "v_mfma_f32_16x16x32_bf16 v[48:51], v[216:219], v[24:27], v[48:51]\n"
      "v_mfma_f32_16x16x32_bf16 v[48:51], v[220:223], v[28:31], v[48:51]\n"
      "s_nop 7\n"
      "v_cndmask_b32_e64 v53, v48, v49, s[96:97]\n"
      "v_cndmask_b32_e64 v54, v50, v51, s[96:97]\n"
      "v_cndmask_b32_e64 v52, v53, v54, s[98:99]\n"
      "s_nop 1\n"
      "v_add_f32_dpp v52, v52, v52 quad_perm:[1,0,3,2] row_mask:0xf bank_mask:0xf\n"
      "s_nop 1\n"
      "v_add_f32_dpp v52, v52, v52 quad_perm:[2,3,0,1] row_mask:0xf bank_mask:0xf\n"
      "v_cmp_lt_f32_e32 vcc, s60, v52\n"
      "s_cbranch_vccz NORS_104_%=\n"
      "s_swappc_b64 s[66:67], s[68:69]\n"
      "NORS_104_%=:\n"
      "v_subrev_f32_e32 v52, s60, v52\n"
      "v_mul_f32_e32 v52, 0x3db8aa3b, v52\n"
      "v_exp_f32_e32 v52, v52\n"
      "s_nop 0\n"
      "v_cvt_pk_bf16_f32 v53, v52, v52\n"
      "v_add_f32_e32 v62, v62, v52\n"
      "v_and_b32_e32 v53, v53, v63\n"
      "s_nop 0\n"
      "v_add_f32_dpp v62, v52, v62 row_half_mirror row_mask:0xf bank_mask:0xf\n"
      "v_mov_b32_dpp v56, v52 row_half_mirror row_mask:0xf bank_mask:0xf\n"
      "v_cvt_pk_bf16_f32 v56, v56, v56\n"
      "v_and_b32_e32 v56, v56, v63\n"
      "v_cndmask_b32_e64 v55, v53, v56, s[50:51]\n"
      "v_cndmask_b32_e64 v59, v56, v53, s[50:51]\n"
      "v_cndmask_b32_e64 v52, 0, v55, s[52:53]\n"
      "v_cndmask_b32_e64 v53, 0, v55, s[54:55]\n"
      "v_cndmask_b32_e64 v54, 0, v55, s[56:57]\n"
      "v_cndmask_b32_e64 v55, 0, v55, s[58:59]\n"
      "v_cndmask_b32_e64 v56, 0, v59, s[52:53]\n"
      "v_cndmask_b32_e64 v57, 0, v59, s[54:55]\n"
      "v_cndmask_b32_e64 v58, 0, v59, s[56:57]\n"
      "v_cndmask_b32_e64 v59, 0, v59, s[58:59]\n"
      "s_waitcnt vmcnt(14)\n"
      "s_add_u32 s86, s100, 0x42a0000\n"
      "s_cmp_ge_u32 s86, 0x43f0000\n"
      "s_cselect_b32 s87, 0x3f0000, 0\n"
      "s_sub_u32 s86, s86, s87\n"
      "s_add_u32 s90, s38, s86\n"
      "s_addc_u32 s91, s39, 0\n"
      "global_load_dwordx4 a[32:35], v61, s[90:91] offset:0 nt\n"
      "global_load_dwordx4 a[36:39], v61, s[90:91] offset:128 nt\n"
      "v_mfma_f32_16x16x32_bf16 v[32:35], v[224:227], v[52:55], v[32:35]\n"
      "v_mfma_f32_16x16x32_bf16 v[36:39], v[228:231], v[52:55], v[36:39]\n"
      "s_waitcnt vmcnt(14)\n"
      "global_load_dwordx4 a[40:43], v61, s[90:91] offset:256 nt\n"
      "global_load_dwordx4 a[44:47], v61, s[90:91] offset:384 nt\n"
      "v_mfma_f32_16x16x32_bf16 v[40:43], v[232:235], v[52:55], v[40:43]\n"
      "v_mfma_f32_16x16x32_bf16 v[44:47], v[236:239], v[52:55], v[44:47]\n"
      "s_waitcnt vmcnt(14)\n"
      "global_load_dwordx4 a[48:51], v61, s[90:91] offset:2048 nt\n"
      "global_load_dwordx4 a[52:55], v61, s[90:91] offset:2176 nt\n"
      "v_mfma_f32_16x16x32_bf16 v[32:35], v[240:243], v[56:59], v[32:35]\n"
      "v_mfma_f32_16x16x32_bf16 v[36:39], v[244:247], v[56:59], v[36:39]\n"
      "s_waitcnt vmcnt(14)\n"
      "global_load_dwordx4 a[56:59], v61, s[90:91] offset:2304 nt\n"
      "global_load_dwordx4 a[60:63], v61, s[90:91] offset:2432 nt\n"
      "v_mfma_f32_16x16x32_bf16 v[40:43], v[248:251], v[56:59], v[40:43]\n"
      "v_mfma_f32_16x16x32_bf16 v[44:47], v[252:255], v[56:59], v[44:47]\n"
      "s_waitcnt vmcnt(14)\n"
      "s_add_u32 s86, s100, 0x4310000\n"
      "s_cmp_ge_u32 s86, 0x43f0000\n"
      "s_cselect_b32 s87, 0x3f0000, 0\n"
      "s_sub_u32 s86, s86, s87\n"
      "s_add_u32 s90, s36, s86\n"
      "s_addc_u32 s91, s37, 0\n"
      "global_load_dwordx4 a[64:67], v60, s[90:91] offset:-4096 nt\n"
      "global_load_dwordx4 a[68:71], v60, s[90:91] offset:-3840 nt\n"
      "v_mfma_f32_16x16x32_bf16 v[48:51], a[0:3], v[0:3], 0\n"
      "v_mfma_f32_16x16x32_bf16 v[48:51], a[4:7], v[4:7], v[48:51]\n"
      "s_waitcnt vmcnt(14)\n"
      "global_load_dwordx4 a[72:75], v60, s[90:91] offset:-2048 nt\n"
      "global_load_dwordx4 a[76:79], v60, s[90:91] offset:-1792 nt\n"
      "v_mfma_f32_16x16x32_bf16 v[48:51], a[8:11], v[8:11], v[48:51]\n"
      "v_mfma_f32_16x16x32_bf16 v[48:51], a[12:15], v[12:15], v[48:51]\n"
      "s_waitcnt vmcnt(14)\n"
      "global_load_dwordx4 a[80:83], v60, s[90:91] offset:0 nt\n"
      "global_load_dwordx4 a[84:87], v60, s[90:91] offset:256 nt\n"
      "v_mfma_f32_16x16x32_bf16 v[48:51], a[16:19], v[16:19], v[48:51]\n"
      "v_mfma_f32_16x16x32_bf16 v[48:51], a[20:23], v[20:23], v[48:51]\n"
      "s_waitcnt vmcnt(14)\n"
      "global_load_dwordx4 a[88:91], v60, s[90:91] offset:2048 nt\n"
      "global_load_dwordx4 a[92:95], v60, s[90:91] offset:2304 nt\n"
      "v_mfma_f32_16x16x32_bf16 v[48:51], a[24:27], v[24:27], v[48:51]\n"
      "v_mfma_f32_16x16x32_bf16 v[48:51], a[28:31], v[28:31], v[48:51]\n"
      "s_nop 7\n"
      "v_cndmask_b32_e64 v53, v48, v49, s[96:97]\n"
      "v_cndmask_b32_e64 v54, v50, v51, s[96:97]\n"
      "v_cndmask_b32_e64 v52, v53, v54, s[98:99]\n"
      "s_nop 1\n"
      "v_add_f32_dpp v52, v52, v52 quad_perm:[1,0,3,2] row_mask:0xf bank_mask:0xf\n"
      "s_nop 1\n"
      "v_add_f32_dpp v52, v52, v52 quad_perm:[2,3,0,1] row_mask:0xf bank_mask:0xf\n"
      "v_cmp_lt_f32_e32 vcc, s60, v52\n"
      "s_cbranch_vccz NORS_105_%=\n"
      "s_swappc_b64 s[66:67], s[68:69]\n"
      "NORS_105_%=:\n"
      "v_subrev_f32_e32 v52, s60, v52\n"
      "v_mul_f32_e32 v52, 0x3db8aa3b, v52\n"
      "v_exp_f32_e32 v52, v52\n"
      "s_nop 0\n"
      "v_cvt_pk_bf16_f32 v53, v52, v52\n"
      "v_add_f32_e32 v62, v62, v52\n"
      "v_and_b32_e32 v53, v53, v63\n"
      "s_nop 0\n"
      "v_add_f32_dpp v62, v52, v62 row_half_mirror row_mask:0xf bank_mask:0xf\n"
      "v_mov_b32_dpp v56, v52 row_half_mirror row_mask:0xf bank_mask:0xf\n"
      "v_cvt_pk_bf16_f32 v56, v56, v56\n"
      "v_and_b32_e32 v56, v56, v63\n"
      "v_cndmask_b32_e64 v55, v53, v56, s[50:51]\n"
      "v_cndmask_b32_e64 v59, v56, v53, s[50:51]\n"
      "v_cndmask_b32_e64 v52, 0, v55, s[52:53]\n"
      "v_cndmask_b32_e64 v53, 0, v55, s[54:55]\n"
      "v_cndmask_b32_e64 v54, 0, v55, s[56:57]\n"
      "v_cndmask_b32_e64 v55, 0, v55, s[58:59]\n"
      "v_cndmask_b32_e64 v56, 0, v59, s[52:53]\n"
      "v_cndmask_b32_e64 v57, 0, v59, s[54:55]\n"
      "v_cndmask_b32_e64 v58, 0, v59, s[56:57]\n"
      "v_cndmask_b32_e64 v59, 0, v59, s[58:59]\n"
      "s_waitcnt vmcnt(14)\n"
      "s_add_u32 s86, s100, 0x4310000\n"
      "s_cmp_ge_u32 s86, 0x43f0000\n"
      "s_cselect_b32 s87, 0x3f0000, 0\n"
      "s_sub_u32 s86, s86, s87\n"
      "s_add_u32 s90, s38, s86\n"
      "s_addc_u32 s91, s39, 0\n"
      "global_load_dwordx4 a[96:99], v61, s[90:91] offset:0 nt\n"
      "global_load_dwordx4 a[100:103], v61, s[90:91] offset:128 nt\n"
      "v_mfma_f32_16x16x32_bf16 v[32:35], a[32:35], v[52:55], v[32:35]\n"
      "v_mfma_f32_16x16x32_bf16 v[36:39], a[36:39], v[52:55], v[36:39]\n"
      "s_waitcnt vmcnt(14)\n"
      "global_load_dwordx4 a[104:107], v61, s[90:91] offset:256 nt\n"
      "global_load_dwordx4 a[108:111], v61, s[90:91] offset:384 nt\n"
      "v_mfma_f32_16x16x32_bf16 v[40:43], a[40:43], v[52:55], v[40:43]\n"
      "v_mfma_f32_16x16x32_bf16 v[44:47], a[44:47], v[52:55], v[44:47]\n"
      "s_waitcnt vmcnt(14)\n"
      "global_load_dwordx4 a[112:115], v61, s[90:91] offset:2048 nt\n"
      "global_load_dwordx4 a[116:119], v61, s[90:91] offset:2176 nt\n"
      "v_mfma_f32_16x16x32_bf16 v[32:35], a[48:51], v[56:59], v[32:35]\n"
      "v_mfma_f32_16x16x32_bf16 v[36:39], a[52:55], v[56:59], v[36:39]\n"
      "s_waitcnt vmcnt(14)\n"
      "global_load_dwordx4 a[120:123], v61, s[90:91] offset:2304 nt\n"
      "global_load_dwordx4 a[124:127], v61, s[90:91] offset:2432 nt\n"
      "v_mfma_f32_16x16x32_bf16 v[40:43], a[56:59], v[56:59], v[40:43]\n"
      "v_mfma_f32_16x16x32_bf16 v[44:47], a[60:63], v[56:59], v[44:47]\n"
      "s_waitcnt vmcnt(14)\n"
      "s_add_u32 s86, s100, 0x4380000\n"
      "s_cmp_ge_u32 s86, 0x43f0000\n"
      "s_cselect_b32 s87, 0x3f0000, 0\n"
      "s_sub_u32 s86, s86, s87\n"
      "s_add_u32 s90, s36, s86\n"
      "s_addc_u32 s91, s37, 0\n"
      "global_load_dwordx4 a[128:131], v60, s[90:91] offset:-4096 nt\n"
      "global_load_dwordx4 a[132:135], v60, s[90:91] offset:-3840 nt\n"
      "v_mfma_f32_16x16x32_bf16 v[48:51], a[64:67], v[0:3], 0\n"
      "v_mfma_f32_16x16x32_bf16 v[48:51], a[68:71], v[4:7], v[48:51]\n"
      "s_waitcnt vmcnt(14)\n"
      "global_load_dwordx4 a[136:139], v60, s[90:91] offset:-2048 nt\n"
      "global_load_dwordx4 a[140:143], v60, s[90:91] offset:-1792 nt\n"
      "v_mfma_f32_16x16x32_bf16 v[48:51], a[72:75], v[8:11], v[48:51]\n"
      "v_mfma_f32_16x16x32_bf16 v[48:51], a[76:79], v[12:15], v[48:51]\n"
      "s_waitcnt vmcnt(14)\n"
      "global_load_dwordx4 a[144:147], v60, s[90:91] offset:0 nt\n"
      "global_load_dwordx4 a[148:151], v60, s[90:91] offset:256 nt\n"
      "v_mfma_f32_16x16x32_bf16 v[48:51], a[80:83], v[16:19], v[48:51]\n"
      "v_mfma_f32_16x16x32_bf16 v[48:51], a[84:87], v[20:23], v[48:51]\n"
      "s_waitcnt vmcnt(14)\n"
      "global_load_dwordx4 a[152:155], v60, s[90:91] offset:2048 nt\n"
      "global_load_dwordx4 a[156:159], v60, s[90:91] offset:2304 nt\n"
      "v_mfma_f32_16x16x32_bf16 v[48:51], a[88:91], v[24:27], v[48:51]\n"
      "v_mfma_f32_16x16x32_bf16 v[48:51], a[92:95], v[28:31], v[48:51]\n"
      "s_nop 7\n"
      "v_cndmask_b32_e64 v53, v48, v49, s[96:97]\n"
      "v_cndmask_b32_e64 v54, v50, v51, s[96:97]\n"
      "v_cndmask_b32_e64 v52, v53, v54, s[98:99]\n"
      "s_nop 1\n"
      "v_add_f32_dpp v52, v52, v52 quad_perm:[1,0,3,2] row_mask:0xf bank_mask:0xf\n"
      "s_nop 1\n"
      "v_add_f32_dpp v52, v52, v52 quad_perm:[2,3,0,1] row_mask:0xf bank_mask:0xf\n"
      "v_cmp_lt_f32_e32 vcc, s60, v52\n"
      "s_cbranch_vccz NORS_106_%=\n"
      "s_swappc_b64 s[66:67], s[68:69]\n"
      "NORS_106_%=:\n"
      "v_subrev_f32_e32 v52, s60, v52\n"
      "v_mul_f32_e32 v52, 0x3db8aa3b, v52\n"
      "v_exp_f32_e32 v52, v52\n"
      "s_nop 0\n"
      "v_cvt_pk_bf16_f32 v53, v52, v52\n"
      "v_add_f32_e32 v62, v62, v52\n"
      "v_and_b32_e32 v53, v53, v63\n"
      "s_nop 0\n"
      "v_add_f32_dpp v62, v52, v62 row_half_mirror row_mask:0xf bank_mask:0xf\n"
      "v_mov_b32_dpp v56, v52 row_half_mirror row_mask:0xf bank_mask:0xf\n"
      "v_cvt_pk_bf16_f32 v56, v56, v56\n"
      "v_and_b32_e32 v56, v56, v63\n"
      "v_cndmask_b32_e64 v55, v53, v56, s[50:51]\n"
      "v_cndmask_b32_e64 v59, v56, v53, s[50:51]\n"
      "v_cndmask_b32_e64 v52, 0, v55, s[52:53]\n"
      "v_cndmask_b32_e64 v53, 0, v55, s[54:55]\n"
      "v_cndmask_b32_e64 v54, 0, v55, s[56:57]\n"
      "v_cndmask_b32_e64 v55, 0, v55, s[58:59]\n"
      "v_cndmask_b32_e64 v56, 0, v59, s[52:53]\n"
      "v_cndmask_b32_e64 v57, 0, v59, s[54:55]\n"
      "v_cndmask_b32_e64 v58, 0, v59, s[56:57]\n"
      "v_cndmask_b32_e64 v59, 0, v59, s[58:59]\n"
      "s_waitcnt vmcnt(14)\n"
      "s_add_u32 s86, s100, 0x4380000\n"
      "s_cmp_ge_u32 s86, 0x43f0000\n"
      "s_cselect_b32 s87, 0x3f0000, 0\n"
      "s_sub_u32 s86, s86, s87\n"
      "s_add_u32 s90, s38, s86\n"
      "s_addc_u32 s91, s39, 0\n"
      "global_load_dwordx4 a[160:163], v61, s[90:91] offset:0 nt\n"
      "global_load_dwordx4 a[164:167], v61, s[90:91] offset:128 nt\n"
      "v_mfma_f32_16x16x32_bf16 v[32:35], a[96:99], v[52:55], v[32:35]\n"
      "v_mfma_f32_16x16x32_bf16 v[36:39], a[100:103], v[52:55], v[36:39]\n"
      "s_waitcnt vmcnt(14)\n"
      "global_load_dwordx4 a[168:171], v61, s[90:91] offset:256 nt\n"
      "global_load_dwordx4 a[172:175], v61, s[90:91] offset:384 nt\n"
      "v_mfma_f32_16x16x32_bf16 v[40:43], a[104:107], v[52:55], v[40:43]\n"
      "v_mfma_f32_16x16x32_bf16 v[44:47], a[108:111], v[52:55], v[44:47]\n"
      "s_waitcnt vmcnt(14)\n"
      "global_load_dwordx4 a[176:179], v61, s[90:91] offset:2048 nt\n"
      "global_load_dwordx4 a[180:183], v61, s[90:91] offset:2176 nt\n"
      "v_mfma_f32_16x16x32_bf16 v[32:35], a[112:115], v[56:59], v[32:35]\n"
      "v_mfma_f32_16x16x32_bf16 v[36:39], a[116:119], v[56:59], v[36:39]\n"
      "s_waitcnt vmcnt(14)\n"
      "global_load_dwordx4 a[184:187], v61, s[90:91] offset:2304 nt\n"
      "global_load_dwordx4 a[188:191], v61, s[90:91] offset:2432 nt\n"
      "v_mfma_f32_16x16x32_bf16 v[40:43], a[120:123], v[56:59], v[40:43]\n"
      "v_mfma_f32_16x16x32_bf16 v[44:47], a[124:127], v[56:59], v[44:47]\n"
      "s_waitcnt vmcnt(14)\n"
      "v_mfma_f32_16x16x32_bf16 v[48:51], a[128:131], v[0:3], 0\n"
      "v_mfma_f32_16x16x32_bf16 v[48:51], a[132:135], v[4:7], v[48:51]\n"
      "s_waitcnt vmcnt(12)\n"
      "v_mfma_f32_16x16x32_bf16 v[48:51], a[136:139], v[8:11], v[48:51]\n"
      "v_mfma_f32_16x16x32_bf16 v[48:51], a[140:143], v[12:15], v[48:51]\n"
      "s_waitcnt vmcnt(10)\n"
      "v_mfma_f32_16x16x32_bf16 v[48:51], a[144:147], v[16:19], v[48:51]\n"
      "v_mfma_f32_16x16x32_bf16 v[48:51], a[148:151], v[20:23], v[48:51]\n"
      "s_waitcnt vmcnt(8)\n"
      "v_mfma_f32_16x16x32_bf16 v[48:51], a[152:155], v[24:27], v[48:51]\n"
      "v_mfma_f32_16x16x32_bf16 v[48:51], a[156:159], v[28:31], v[48:51]\n"
      "s_nop 7\n"
      "v_cndmask_b32_e64 v53, v48, v49, s[96:97]\n"
      "v_cndmask_b32_e64 v54, v50, v51, s[96:97]\n"
      "v_cndmask_b32_e64 v52, v53, v54, s[98:99]\n"
      "s_nop 1\n"
      "v_add_f32_dpp v52, v52, v52 quad_perm:[1,0,3,2] row_mask:0xf bank_mask:0xf\n"
      "s_nop 1\n"
      "v_add_f32_dpp v52, v52, v52 quad_perm:[2,3,0,1] row_mask:0xf bank_mask:0xf\n"
      "v_cmp_lt_f32_e32 vcc, s60, v52\n"
      "s_cbranch_vccz NORS_107_%=\n"
      "s_swappc_b64 s[66:67], s[68:69]\n"
      "NORS_107_%=:\n"
      "v_subrev_f32_e32 v52, s60, v52\n"
      "v_mul_f32_e32 v52, 0x3db8aa3b, v52\n"
      "v_exp_f32_e32 v52, v52\n"
      "s_nop 0\n"
      "v_cvt_pk_bf16_f32 v53, v52, v52\n"
      "v_add_f32_e32 v62, v62, v52\n"
      "v_and_b32_e32 v53, v53, v63\n"
      "s_nop 0\n"
      "v_add_f32_dpp v62, v52, v62 row_half_mirror row_mask:0xf bank_mask:0xf\n"
      "v_mov_b32_dpp v56, v52 row_half_mirror row_mask:0xf bank_mask:0xf\n"
      "v_cvt_pk_bf16_f32 v56, v56, v56\n"
      "v_and_b32_e32 v56, v56, v63\n"
      "v_cndmask_b32_e64 v55, v53, v56, s[50:51]\n"
      "v_cndmask_b32_e64 v59, v56, v53, s[50:51]\n"
      "v_cndmask_b32_e64 v52, 0, v55, s[52:53]\n"
      "v_cndmask_b32_e64 v53, 0, v55, s[54:55]\n"
      "v_cndmask_b32_e64 v54, 0, v55, s[56:57]\n"
      "v_cndmask_b32_e64 v55, 0, v55, s[58:59]\n"
      "v_cndmask_b32_e64 v56, 0, v59, s[52:53]\n"
      "v_cndmask_b32_e64 v57, 0, v59, s[54:55]\n"
      "v_cndmask_b32_e64 v58, 0, v59, s[56:57]\n"
      "v_cndmask_b32_e64 v59, 0, v59, s[58:59]\n"
      "s_waitcnt vmcnt(6)\n"
      "v_mfma_f32_16x16x32_bf16 v[32:35], a[160:163], v[52:55], v[32:35]\n"
      "v_mfma_f32_16x16x32_bf16 v[36:39], a[164:167], v[52:55], v[36:39]\n"
      "s_waitcnt vmcnt(4)\n"
      "v_mfma_f32_16x16x32_bf16 v[40:43], a[168:171], v[52:55], v[40:43]\n"
      "v_mfma_f32_16x16x32_bf16 v[44:47], a[172:175], v[52:55], v[44:47]\n"
      "s_waitcnt vmcnt(2)\n"
      "v_mfma_f32_16x16x32_bf16 v[32:35], a[176:179], v[56:59], v[32:35]\n"
      "v_mfma_f32_16x16x32_bf16 v[36:39], a[180:183], v[56:59], v[36:39]\n"
      "s_waitcnt vmcnt(0)\n"
      "v_mfma_f32_16x16x32_bf16 v[40:43], a[184:187], v[56:59], v[40:43]\n"
      "v_mfma_f32_16x16x32_bf16 v[44:47], a[188:191], v[56:59], v[44:47]\n"
      "s_add_u32 s74, s44, 0x80000\n"
      "s_addc_u32 s75, s45, 0\n"
      "s_add_u32 s76, s46, 0x1000\n"
      "s_addc_u32 s77, s47, 0\n"
      "s_mov_b32 s72, 0x25050\n"
      "s_mov_b32 s73, 0x260a8\n"
      "s_nop 1\n"
      "s_swappc_b64 s[66:67], s[70:71]\n"
      "v_mbcnt_lo_u32_b32 v52, -1, 0\n"
      "v_mbcnt_hi_u32_b32 v52, -1, v52\n"
      "v_lshlrev_b32_e32 v52, 4, v52\n"
      "s_lshl_b32 s80, s48, 12\n"
      "s_add_u32 s80, s80, 0x20000\n"
      "v_add_u32_e32 v52, s80, v52\n"
      "ds_read_b128 v[32:35], v52 offset:0\n"
      "ds_read_b128 v[36:39], v52 offset:1024\n"
      "ds_read_b128 v[40:43], v52 offset:2048\n"
      "ds_read_b128 v[44:47], v52 offset:3072\n"
      "v_mbcnt_lo_u32_b32 v53, -1, 0\n"
      "v_mbcnt_hi_u32_b32 v53, -1, v53\n"
      "v_lshlrev_b32_e32 v53, 2, v53\n"
      "s_lshl_b32 s80, s48, 8\n"
      "s_add_u32 s80, s80, 0x26240\n"
      "v_add_u32_e32 v53, s80, v53\n"
      "ds_read_b32 v62, v53\n"
      "s_mov_b32 s60, s78\n"
      "s_waitcnt lgkmcnt(0)\n"
      "v_mbcnt_lo_u32_b32 v53, -1, 0\n"
      "v_mbcnt_hi_u32_b32 v53, -1, v53\n"
      "v_lshrrev_b32_e32 v52, 4, v53\n"
      "v_lshlrev_b32_e32 v52, 6, v52\n"
      "v_and_b32_e32 v53, 3, v53\n"
      "v_lshl_add_u32 v52, v53, 4, v52\n"
      "v_add_u32_e32 v52, 0x26840, v52\n"
      "s_mov_b32 vcc_lo, 0xf000f\n"
      "s_mov_b32 vcc_hi, 0xf000f\n"
      "v_mov_b32_e32 v53, 0x26120\n"
      "v_cndmask_b32_e32 v53, v53, v52, vcc\n"
      "ds_read_b128 v[0:3], v53 offset:0\n"
      "ds_read_b128 v[4:7], v53 offset:256\n"
      "s_mov_b32 vcc_lo, 0xf000f0\n"
      "s_mov_b32 vcc_hi, 0xf000f0\n"
      "v_mov_b32_e32 v53, 0x26120\n"
      "v_cndmask_b32_e32 v53, v53, v52, vcc\n"
      "ds_read_b128 v[8:11], v53 offset:0\n"
      "ds_read_b128 v[12:15], v53 offset:256\n"
      "s_mov_b32 vcc_lo, 0xf000f00\n"
      "s_mov_b32 vcc_hi, 0xf000f00\n"
      "v_mov_b32_e32 v53, 0x26120\n"
      "v_cndmask_b32_e32 v53, v53, v52, vcc\n"
      "ds_read_b128 v[16:19], v53 offset:0\n"
      "ds_read_b128 v[20:23], v53 offset:256\n"
      "s_mov_b32 vcc_lo, 0xf000f000\n"
      "s_mov_b32 vcc_hi, 0xf000f000\n"
      "v_mov_b32_e32 v53, 0x26120\n"
      "v_cndmask_b32_e32 v53, v53, v52, vcc\n"
      "ds_read_b128 v[24:27], v53 offset:0\n"
      "ds_read_b128 v[28:31], v53 offset:256\n"
      "s_waitcnt lgkmcnt(0)\n"
      "v_mfma_f32_16x16x32_bf16 v[48:51], a[192:195], v[0:3], 0\n"
      "v_mfma_f32_16x16x32_bf16 v[48:51], a[196:199], v[4:7], v[48:51]\n"
      "v_mfma_f32_16x16x32_bf16 v[48:51], a[200:203], v[8:11], v[48:51]\n"
      "v_mfma_f32_16x16x32_bf16 v[48:51], a[204:207], v[12:15], v[48:51]\n"
      "v_mfma_f32_16x16x32_bf16 v[48:51], a[208:211], v[16:19], v[48:51]\n"
      "v_mfma_f32_16x16x32_bf16 v[48:51], a[212:215], v[20:23], v[48:51]\n"
      "v_mfma_f32_16x16x32_bf16 v[48:51], a[216:219], v[24:27], v[48:51]\n"
      "v_mfma_f32_16x16x32_bf16 v[48:51], a[220:223], v[28:31], v[48:51]\n"
      "s_nop 7\n"
      "v_cndmask_b32_e64 v53, v48, v49, s[96:97]\n"
      "v_cndmask_b32_e64 v54, v50, v51, s[96:97]\n"
      "v_cndmask_b32_e64 v52, v53, v54, s[98:99]\n"
      "s_nop 1\n"
      "v_add_f32_dpp v52, v52, v52 quad_perm:[1,0,3,2] row_mask:0xf bank_mask:0xf\n"
      "s_nop 1\n"
      "v_add_f32_dpp v52, v52, v52 quad_perm:[2,3,0,1] row_mask:0xf bank_mask:0xf\n"
      "v_cmp_lt_f32_e32 vcc, s60, v52\n"
      "s_cbranch_vccz NORS_108_%=\n"
      "s_swappc_b64 s[66:67], s[68:69]\n"
      "NORS_108_%=:\n"
      "v_subrev_f32_e32 v52, s60, v52\n"
      "v_mul_f32_e32 v52, 0x3db8aa3b, v52\n"
      "v_exp_f32_e32 v52, v52\n"
      "s_nop 0\n"
      "v_cvt_pk_bf16_f32 v53, v52, v52\n"
      "v_add_f32_e32 v62, v62, v52\n"
      "v_and_b32_e32 v53, v53, v63\n"
      "s_nop 0\n"
      "v_add_f32_dpp v62, v52, v62 row_half_mirror row_mask:0xf bank_mask:0xf\n"
      "v_mov_b32_dpp v56, v52 row_half_mirror row_mask:0xf bank_mask:0xf\n"
      "v_cvt_pk_bf16_f32 v56, v56, v56\n"
      "v_and_b32_e32 v56, v56, v63\n"
      "v_cndmask_b32_e64 v55, v53, v56, s[50:51]\n"
      "v_cndmask_b32_e64 v59, v56, v53, s[50:51]\n"
      "v_cndmask_b32_e64 v52, 0, v55, s[52:53]\n"
      "v_cndmask_b32_e64 v53, 0, v55, s[54:55]\n"
      "v_cndmask_b32_e64 v54, 0, v55, s[56:57]\n"
      "v_cndmask_b32_e64 v55, 0, v55, s[58:59]\n"
      "v_cndmask_b32_e64 v56, 0, v59, s[52:53]\n"
      "v_cndmask_b32_e64 v57, 0, v59, s[54:55]\n"
      "v_cndmask_b32_e64 v58, 0, v59, s[56:57]\n"
      "v_cndmask_b32_e64 v59, 0, v59, s[58:59]\n"
      "v_mfma_f32_16x16x32_bf16 v[32:35], a[224:227], v[52:55], v[32:35]\n"
      "v_mfma_f32_16x16x32_bf16 v[36:39], a[228:231], v[52:55], v[36:39]\n"
      "v_mfma_f32_16x16x32_bf16 v[40:43], a[232:235], v[52:55], v[40:43]\n"
      "v_mfma_f32_16x16x32_bf16 v[44:47], a[236:239], v[52:55], v[44:47]\n"
      "v_mfma_f32_16x16x32_bf16 v[32:35], a[240:243], v[56:59], v[32:35]\n"
      "v_mfma_f32_16x16x32_bf16 v[36:39], a[244:247], v[56:59], v[36:39]\n"
      "v_mfma_f32_16x16x32_bf16 v[40:43], a[248:251], v[56:59], v[40:43]\n"
      "v_mfma_f32_16x16x32_bf16 v[44:47], a[252:255], v[56:59], v[44:47]\n"
      "s_nop 7\n"
      "v_mbcnt_lo_u32_b32 v52, -1, 0\n"
      "v_mbcnt_hi_u32_b32 v52, -1, v52\n"
      "v_lshlrev_b32_e32 v52, 4, v52\n"
      "v_add_u32_e32 v52, s49, v52\n"
      "ds_read_b128 a[192:195], v52 offset:0\n"
      "ds_read_b128 a[196:199], v52 offset:1024\n"
      "ds_read_b128 a[200:203], v52 offset:2048\n"
      "ds_read_b128 a[204:207], v52 offset:3072\n"
      "ds_read_b128 a[208:211], v52 offset:4096\n"
      "ds_read_b128 a[212:215], v52 offset:5120\n"
      "ds_read_b128 a[216:219], v52 offset:6144\n"
      "ds_read_b128 a[220:223], v52 offset:7168\n"
      "ds_read_b128 a[224:227], v52 offset:8192\n"
      "ds_read_b128 a[228:231], v52 offset:9216\n"
      "ds_read_b128 a[232:235], v52 offset:10240\n"
      "ds_read_b128 a[236:239], v52 offset:11264\n"
      "ds_read_b128 a[240:243], v52 offset:12288\n"
      "ds_read_b128 a[244:247], v52 offset:13312\n"
      "ds_read_b128 a[248:251], v52 offset:14336\n"
      "ds_read_b128 a[252:255], v52 offset:15360\n"
      "v_mfma_f32_16x16x32_bf16 v[48:51], v[64:67], v[0:3], 0\n"
      "v_mfma_f32_16x16x32_bf16 v[48:51], v[68:71], v[4:7], v[48:51]\n"
      "v_mfma_f32_16x16x32_bf16 v[48:51], v[72:75], v[8:11], v[48:51]\n"
      "v_mfma_f32_16x16x32_bf16 v[48:51], v[76:79], v[12:15], v[48:51]\n"
      "v_mfma_f32_16x16x32_bf16 v[48:51], v[80:83], v[16:19], v[48:51]\n"
      "v_mfma_f32_16x16x32_bf16 v[48:51], v[84:87], v[20:23], v[48:51]\n"
      "v_mfma_f32_16x16x32_bf16 v[48:51], v[88:91], v[24:27], v[48:51]\n"
      "v_mfma_f32_16x16x32_bf16 v[48:51], v[92:95], v[28:31], v[48:51]\n"
      "s_nop 7\n"
      "v_cndmask_b32_e64 v53, v48, v49, s[96:97]\n"
      "v_cndmask_b32_e64 v54, v50, v51, s[96:97]\n"
      "v_cndmask_b32_e64 v52, v53, v54, s[98:99]\n"
      "s_nop 1\n"
      "v_add_f32_dpp v52, v52, v52 quad_perm:[1,0,3,2] row_mask:0xf bank_mask:0xf\n"
      "s_nop 1\n"
      "v_add_f32_dpp v52, v52, v52 quad_perm:[2,3,0,1] row_mask:0xf bank_mask:0xf\n"
      "v_cmp_lt_f32_e32 vcc, s60, v52\n"
      "s_cbranch_vccz NORS_109_%=\n"
      "s_swappc_b64 s[66:67], s[68:69]\n"
      "NORS_109_%=:\n"
      "v_subrev_f32_e32 v52, s60, v52\n"
      "v_mul_f32_e32 v52, 0x3db8aa3b, v52\n"
      "v_exp_f32_e32 v52, v52\n"
      "s_nop 0\n"
      "v_cvt_pk_bf16_f32 v53, v52, v52\n"
      "v_add_f32_e32 v62, v62, v52\n"
      "v_and_b32_e32 v53, v53, v63\n"
      "s_nop 0\n"
      "v_add_f32_dpp v62, v52, v62 row_half_mirror row_mask:0xf bank_mask:0xf\n"
      "v_mov_b32_dpp v56, v52 row_half_mirror row_mask:0xf bank_mask:0xf\n"
      "v_cvt_pk_bf16_f32 v56, v56, v56\n"
      "v_and_b32_e32 v56, v56, v63\n"
      "v_cndmask_b32_e64 v55, v53, v56, s[50:51]\n"
      "v_cndmask_b32_e64 v59, v56, v53, s[50:51]\n"
      "v_cndmask_b32_e64 v52, 0, v55, s[52:53]\n"
      "v_cndmask_b32_e64 v53, 0, v55, s[54:55]\n"
      "v_cndmask_b32_e64 v54, 0, v55, s[56:57]\n"
      "v_cndmask_b32_e64 v55, 0, v55, s[58:59]\n"
      "v_cndmask_b32_e64 v56, 0, v59, s[52:53]\n"
      "v_cndmask_b32_e64 v57, 0, v59, s[54:55]\n"
      "v_cndmask_b32_e64 v58, 0, v59, s[56:57]\n"
      "v_cndmask_b32_e64 v59, 0, v59, s[58:59]\n"
      "v_mfma_f32_16x16x32_bf16 v[32:35], v[96:99], v[52:55], v[32:35]\n"
      "v_mfma_f32_16x16x32_bf16 v[36:39], v[100:103], v[52:55], v[36:39]\n"
      "v_mfma_f32_16x16x32_bf16 v[40:43], v[104:107], v[52:55], v[40:43]\n"
      "v_mfma_f32_16x16x32_bf16 v[44:47], v[108:111], v[52:55], v[44:47]\n"
      "v_mfma_f32_16x16x32_bf16 v[32:35], v[112:115], v[56:59], v[32:35]\n"
      "v_mfma_f32_16x16x32_bf16 v[36:39], v[116:119], v[56:59], v[36:39]\n"
      "v_mfma_f32_16x16x32_bf16 v[40:43], v[120:123], v[56:59], v[40:43]\n"
      "v_mfma_f32_16x16x32_bf16 v[44:47], v[124:127], v[56:59], v[44:47]\n"
      "s_nop 7\n"
      "v_mbcnt_lo_u32_b32 v52, -1, 0\n"
      "v_mbcnt_hi_u32_b32 v52, -1, v52\n"
      "v_lshlrev_b32_e32 v52, 4, v52\n"
      "v_add_u32_e32 v52, s49, v52\n"
      "ds_read_b128 v[64:67], v52 offset:16384\n"
      "ds_read_b128 v[68:71], v52 offset:17408\n"
      "ds_read_b128 v[72:75], v52 offset:18432\n"
      "ds_read_b128 v[76:79], v52 offset:19456\n"
      "ds_read_b128 v[80:83], v52 offset:20480\n"
      "ds_read_b128 v[84:87], v52 offset:21504\n"
      "ds_read_b128 v[88:91], v52 offset:22528\n"
      "ds_read_b128 v[92:95], v52 offset:23552\n"
      "ds_read_b128 v[96:99], v52 offset:24576\n"
      "ds_read_b128 v[100:103], v52 offset:25600\n"
      "ds_read_b128 v[104:107], v52 offset:26624\n"
      "ds_read_b128 v[108:111], v52 offset:27648\n"
      "ds_read_b128 v[112:115], v52 offset:28672\n"
      "ds_read_b128 v[116:119], v52 offset:29696\n"
      "ds_read_b128 v[120:123], v52 offset:30720\n"
      "ds_read_b128 v[124:127], v52 offset:31744\n"
      "s_waitcnt lgkmcnt(15)\n"
      "v_mfma_f32_16x16x32_bf16 v[48:51], a[192:195], v[0:3], 0\n"
      "v_mfma_f32_16x16x32_bf16 v[48:51], a[196:199], v[4:7], v[48:51]\n"
      "v_mfma_f32_16x16x32_bf16 v[48:51], a[200:203], v[8:11], v[48:51]\n"
      "v_mfma_f32_16x16x32_bf16 v[48:51], a[204:207], v[12:15], v[48:51]\n"
      "v_mfma_f32_16x16x32_bf16 v[48:51], a[208:211], v[16:19], v[48:51]\n"
      "v_mfma_f32_16x16x32_bf16 v[48:51], a[212:215], v[20:23], v[48:51]\n"
      "v_mfma_f32_16x16x32_bf16 v[48:51], a[216:219], v[24:27], v[48:51]\n"
      "v_mfma_f32_16x16x32_bf16 v[48:51], a[220:223], v[28:31], v[48:51]\n"
      "s_add_u32 s86, s100, 0x380000\n"
      "s_cmp_ge_u32 s86, 0x3f0000\n"
      "s_cselect_b32 s87, 0x3f0000, 0\n"
      "s_sub_u32 s86, s86, s87\n"
      "s_add_u32 s90, s36, s86\n"
      "s_addc_u32 s91, s37, 0\n"
      "global_load_dwordx4 a[192:195], v60, s[90:91] offset:-4096 nt\n"
      "global_load_dwordx4 a[196:199], v60, s[90:91] offset:-3840 nt\n"
      "global_load_dwordx4 a[200:203], v60, s[90:91] offset:-2048 nt\n"
      "global_load_dwordx4 a[204:207], v60, s[90:91] offset:-1792 nt\n"
      "global_load_dwordx4 a[208:211], v60, s[90:91] offset:0 nt\n"
      "global_load_dwordx4 a[212:215], v60, s[90:91] offset:256 nt\n"
      "global_load_dwordx4 a[216:219], v60, s[90:91] offset:2048 nt\n"
      "global_load_dwordx4 a[220:223], v60, s[90:91] offset:2304 nt\n"
      "v_cndmask_b32_e64 v53, v48, v49, s[96:97]\n"
      "v_cndmask_b32_e64 v54, v50, v51, s[96:97]\n"
      "v_cndmask_b32_e64 v52, v53, v54, s[98:99]\n"
      "s_nop 1\n"
      "v_add_f32_dpp v52, v52, v52 quad_perm:[1,0,3,2] row_mask:0xf bank_mask:0xf\n"
      "s_nop 1\n"
      "v_add_f32_dpp v52, v52, v52 quad_perm:[2,3,0,1] row_mask:0xf bank_mask:0xf\n"
      "v_cmp_lt_f32_e32 vcc, s60, v52\n"
      "s_cbranch_vccz NORS_110_%=\n"
      "s_swappc_b64 s[66:67], s[68:69]\n"
      "NORS_110_%=:\n"
      "v_subrev_f32_e32 v52, s60, v52\n"
      "v_mul_f32_e32 v52, 0x3db8aa3b, v52\n"
      "v_exp_f32_e32 v52, v52\n"
      "s_nop 0\n"
      "v_cvt_pk_bf16_f32 v53, v52, v52\n"
      "v_add_f32_e32 v62, v62, v52\n"
      "v_and_b32_e32 v53, v53, v63\n"
      "s_nop 0\n"
      "v_add_f32_dpp v62, v52, v62 row_half_mirror row_mask:0xf bank_mask:0xf\n"
      "v_mov_b32_dpp v56, v52 row_half_mirror row_mask:0xf bank_mask:0xf\n"
      "v_cvt_pk_bf16_f32 v56, v56, v56\n"
      "v_and_b32_e32 v56, v56, v63\n"
      "v_cndmask_b32_e64 v55, v53, v56, s[50:51]\n"
      "v_cndmask_b32_e64 v59, v56, v53, s[50:51]\n"
      "v_cndmask_b32_e64 v52, 0, v55, s[52:53]\n"
      "v_cndmask_b32_e64 v53, 0, v55, s[54:55]\n"
      "v_cndmask_b32_e64 v54, 0, v55, s[56:57]\n"
      "v_cndmask_b32_e64 v55, 0, v55, s[58:59]\n"
      "v_cndmask_b32_e64 v56, 0, v59, s[52:53]\n"
      "v_cndmask_b32_e64 v57, 0, v59, s[54:55]\n"
      "v_cndmask_b32_e64 v58, 0, v59, s[56:57]\n"
      "v_cndmask_b32_e64 v59, 0, v59, s[58:59]\n"
      "v_mfma_f32_16x16x32_bf16 v[32:35], a[224:227], v[52:55], v[32:35]\n"
      "v_mfma_f32_16x16x32_bf16 v[36:39], a[228:231], v[52:55], v[36:39]\n"
      "v_mfma_f32_16x16x32_bf16 v[40:43], a[232:235], v[52:55], v[40:43]\n"
      "v_mfma_f32_16x16x32_bf16 v[44:47], a[236:239], v[52:55], v[44:47]\n"
      "v_mfma_f32_16x16x32_bf16 v[32:35], a[240:243], v[56:59], v[32:35]\n"
      "v_mfma_f32_16x16x32_bf16 v[36:39], a[244:247], v[56:59], v[36:39]\n"
      "v_mfma_f32_16x16x32_bf16 v[40:43], a[248:251], v[56:59], v[40:43]\n"
      "v_mfma_f32_16x16x32_bf16 v[44:47], a[252:255], v[56:59], v[44:47]\n"
      "s_add_u32 s86, s100, 0x380000\n"
      "s_cmp_ge_u32 s86, 0x3f0000\n"
      "s_cselect_b32 s87, 0x3f0000, 0\n"
      "s_sub_u32 s86, s86, s87\n"
      "s_add_u32 s90, s38, s86\n"
      "s_addc_u32 s91, s39, 0\n"
      "global_load_dwordx4 a[224:227], v61, s[90:91] offset:0 nt\n"
      "global_load_dwordx4 a[228:231], v61, s[90:91] offset:128 nt\n"
      "global_load_dwordx4 a[232:235], v61, s[90:91] offset:256 nt\n"
      "global_load_dwordx4 a[236:239], v61, s[90:91] offset:384 nt\n"
      "global_load_dwordx4 a[240:243], v61, s[90:91] offset:2048 nt\n"
      "global_load_dwordx4 a[244:247], v61, s[90:91] offset:2176 nt\n"
      "global_load_dwordx4 a[248:251], v61, s[90:91] offset:2304 nt\n"
      "global_load_dwordx4 a[252:255], v61, s[90:91] offset:2432 nt\n"
      "s_waitcnt lgkmcnt(0)\n"
      "s_nop 0\n"
      "v_mbcnt_lo_u32_b32 v48, -1, 0\n"
      "v_mbcnt_hi_u32_b32 v48, -1, v48\n"
      "v_lshlrev_b32_e32 v48, 4, v48\n"
      "v_add_u32_e32 v48, s49, v48\n"
      "ds_write_b128 v48, v[128:131] offset:0\n"
      "ds_write_b128 v48, v[132:135] offset:1024\n"
      "ds_write_b128 v48, v[136:139] offset:2048\n"
      "ds_write_b128 v48, v[140:143] offset:3072\n"
      "ds_write_b128 v48, v[144:147] offset:4096\n"
      "ds_write_b128 v48, v[148:151] offset:5120\n"
      "ds_write_b128 v48, v[152:155] offset:6144\n"
      "ds_write_b128 v48, v[156:159] offset:7168\n"
      "ds_write_b128 v48, v[160:163] offset:8192\n"
      "ds_write_b128 v48, v[164:167] offset:9216\n"
      "ds_write_b128 v48, v[168:171] offset:10240\n"
      "ds_write_b128 v48, v[172:175] offset:11264\n"
      "ds_write_b128 v48, v[176:179] offset:12288\n"
      "ds_write_b128 v48, v[180:183] offset:13312\n"
      "ds_write_b128 v48, v[184:187] offset:14336\n"
      "ds_write_b128 v48, v[188:191] offset:15360\n"
      "s_waitcnt lgkmcnt(0)\n"
      "v_mfma_f32_16x16x32_bf16 v[48:51], v[64:67], v[0:3], 0\n"
      "v_mfma_f32_16x16x32_bf16 v[48:51], v[68:71], v[4:7], v[48:51]\n"
      "v_mfma_f32_16x16x32_bf16 v[48:51], v[72:75], v[8:11], v[48:51]\n"
      "v_mfma_f32_16x16x32_bf16 v[48:51], v[76:79], v[12:15], v[48:51]\n"
      "v_mfma_f32_16x16x32_bf16 v[48:51], v[80:83], v[16:19], v[48:51]\n"
      "v_mfma_f32_16x16x32_bf16 v[48:51], v[84:87], v[20:23], v[48:51]\n"
      "v_mfma_f32_16x16x32_bf16 v[48:51], v[88:91], v[24:27], v[48:51]\n"
      "v_mfma_f32_16x16x32_bf16 v[48:51], v[92:95], v[28:31], v[48:51]\n"
      "s_nop 7\n"
      "v_cndmask_b32_e64 v53, v48, v49, s[96:97]\n"
      "v_cndmask_b32_e64 v54, v50, v51, s[96:97]\n"
      "v_cndmask_b32_e64 v52, v53, v54, s[98:99]\n"
      "s_nop 1\n"
      "v_add_f32_dpp v52, v52, v52 quad_perm:[1,0,3,2] row_mask:0xf bank_mask:0xf\n"
      "s_nop 1\n"
      "v_add_f32_dpp v52, v52, v52 quad_perm:[2,3,0,1] row_mask:0xf bank_mask:0xf\n"
      "v_cmp_lt_f32_e32 vcc, s60, v52\n"
      "s_cbranch_vccz NORS_111_%=\n"
      "s_swappc_b64 s[66:67], s[68:69]\n"
      "NORS_111_%=:\n"
      "v_subrev_f32_e32 v52, s60, v52\n"
      "v_mul_f32_e32 v52, 0x3db8aa3b, v52\n"
      "v_exp_f32_e32 v52, v52\n"
      "s_nop 0\n"
      "v_cvt_pk_bf16_f32 v53, v52, v52\n"
      "v_add_f32_e32 v62, v62, v52\n"
      "v_and_b32_e32 v53, v53, v63\n"
      "s_nop 0\n"
      "v_add_f32_dpp v62, v52, v62 row_half_mirror row_mask:0xf bank_mask:0xf\n"
      "v_mov_b32_dpp v56, v52 row_half_mirror row_mask:0xf bank_mask:0xf\n"
      "v_cvt_pk_bf16_f32 v56, v56, v56\n"
      "v_and_b32_e32 v56, v56, v63\n"
      "v_cndmask_b32_e64 v55, v53, v56, s[50:51]\n"
      "v_cndmask_b32_e64 v59, v56, v53, s[50:51]\n"
      "v_cndmask_b32_e64 v52, 0, v55, s[52:53]\n"
      "v_cndmask_b32_e64 v53, 0, v55, s[54:55]\n"
      "v_cndmask_b32_e64 v54, 0, v55, s[56:57]\n"
      "v_cndmask_b32_e64 v55, 0, v55, s[58:59]\n"
      "v_cndmask_b32_e64 v56, 0, v59, s[52:53]\n"
      "v_cndmask_b32_e64 v57, 0, v59, s[54:55]\n"
      "v_cndmask_b32_e64 v58, 0, v59, s[56:57]\n"
      "v_cndmask_b32_e64 v59, 0, v59, s[58:59]\n"
      "v_mfma_f32_16x16x32_bf16 v[32:35], v[96:99], v[52:55], v[32:35]\n"
      "v_mfma_f32_16x16x32_bf16 v[36:39], v[100:103], v[52:55], v[36:39]\n"
      "v_mfma_f32_16x16x32_bf16 v[40:43], v[104:107], v[52:55], v[40:43]\n"
      "v_mfma_f32_16x16x32_bf16 v[44:47], v[108:111], v[52:55], v[44:47]\n"
      "v_mfma_f32_16x16x32_bf16 v[32:35], v[112:115], v[56:59], v[32:35]\n"
      "v_mfma_f32_16x16x32_bf16 v[36:39], v[116:119], v[56:59], v[36:39]\n"
      "v_mfma_f32_16x16x32_bf16 v[40:43], v[120:123], v[56:59], v[40:43]\n"
      "v_mfma_f32_16x16x32_bf16 v[44:47], v[124:127], v[56:59], v[44:47]\n"
      "s_nop 7\n"
      "v_mbcnt_lo_u32_b32 v48, -1, 0\n"
      "v_mbcnt_hi_u32_b32 v48, -1, v48\n"
      "v_lshlrev_b32_e32 v48, 4, v48\n"
      "v_add_u32_e32 v48, s49, v48\n"
      "ds_write_b128 v48, v[192:195] offset:16384\n"
      "ds_write_b128 v48, v[196:199] offset:17408\n"
      "ds_write_b128 v48, v[200:203] offset:18432\n"
      "ds_write_b128 v48, v[204:207] offset:19456\n"
      "ds_write_b128 v48, v[208:211] offset:20480\n"
      "ds_write_b128 v48, v[212:215] offset:21504\n"
      "ds_write_b128 v48, v[216:219] offset:22528\n"
      "ds_write_b128 v48, v[220:223] offset:23552\n"
      "s_waitcnt vmcnt(15)\n"
      "s_add_u32 s86, s100, 0x4150000\n"
      "s_cmp_ge_u32 s86, 0x43f0000\n"
      "s_cselect_b32 s87, 0x3f0000, 0\n"
      "s_sub_u32 s86, s86, s87\n"
      "s_add_u32 s90, s36, s86\n"
      "s_addc_u32 s91, s37, 0\n"
      "global_load_dwordx4 v[128:131], v60, s[90:91] offset:-4096 nt\n"
      "ds_write_b128 v48, v[224:227] offset:24576\n"
      "ds_write_b128 v48, v[228:231] offset:25600\n"
      "ds_write_b128 v48, v[232:235] offset:26624\n"
      "ds_write_b128 v48, v[236:239] offset:27648\n"
      "ds_write_b128 v48, v[240:243] offset:28672\n"
      "ds_write_b128 v48, v[244:247] offset:29696\n"
      "ds_write_b128 v48, v[248:251] offset:30720\n"
      "ds_write_b128 v48, v[252:255] offset:31744\n"
      "s_waitcnt lgkmcnt(0)\n"
      "s_waitcnt vmcnt(15)\n"
      "global_load_dwordx4 v[132:135], v60, s[90:91] offset:-3840 nt\n"
      "v_mfma_f32_16x16x32_bf16 v[48:51], a[192:195], v[0:3], 0\n"
      "v_mfma_f32_16x16x32_bf16 v[48:51], a[196:199], v[4:7], v[48:51]\n"
      "s_waitcnt vmcnt(14)\n"
      "global_load_dwordx4 v[136:139], v60, s[90:91] offset:-2048 nt\n"
      "global_load_dwordx4 v[140:143], v60, s[90:91] offset:-1792 nt\n"
      "v_mfma_f32_16x16x32_bf16 v[48:51], a[200:203], v[8:11], v[48:51]\n"
      "v_mfma_f32_16x16x32_bf16 v[48:51], a[204:207], v[12:15], v[48:51]\n"
      "s_waitcnt vmcnt(14)\n"
      "global_load_dwordx4 v[144:147], v60, s[90:91] offset:0 nt\n"
      "global_load_dwordx4 v[148:151], v60, s[90:91] offset:256 nt\n"
      "v_mfma_f32_16x16x32_bf16 v[48:51], a[208:211], v[16:19], v[48:51]\n"
      "v_mfma_f32_16x16x32_bf16 v[48:51], a[212:215], v[20:23], v[48:51]\n"
      "s_waitcnt vmcnt(14)\n"
      "global_load_dwordx4 v[152:155], v60, s[90:91] offset:2048 nt\n"
      "global_load_dwordx4 v[156:159], v60, s[90:91] offset:2304 nt\n"
      "v_mfma_f32_16x16x32_bf16 v[48:51], a[216:219], v[24:27], v[48:51]\n"
      "v_mfma_f32_16x16x32_bf16 v[48:51], a[220:223], v[28:31], v[48:51]\n"
      "s_nop 7\n"
      "v_cndmask_b32_e64 v53, v48, v49, s[96:97]\n"
      "v_cndmask_b32_e64 v54, v50, v51, s[96:97]\n"
      "v_cndmask_b32_e64 v52, v53, v54, s[98:99]\n"
      "s_nop 1\n"
      "v_add_f32_dpp v52, v52, v52 quad_perm:[1,0,3,2] row_mask:0xf bank_mask:0xf\n"
      "s_nop 1\n"
      "v_add_f32_dpp v52, v52, v52 quad_perm:[2,3,0,1] row_mask:0xf bank_mask:0xf\n"
      "v_cmp_lt_f32_e32 vcc, s60, v52\n"
      "s_cbranch_vccz NORS_112_%=\n"
      "s_swappc_b64 s[66:67], s[68:69]\n"
      "NORS_112_%=:\n"
      "v_subrev_f32_e32 v52, s60, v52\n"
      "v_mul_f32_e32 v52, 0x3db8aa3b, v52\n"
      "v_exp_f32_e32 v52, v52\n"
      "s_nop 0\n"
      "v_cvt_pk_bf16_f32 v53, v52, v52\n"
      "v_add_f32_e32 v62, v62, v52\n"
      "v_and_b32_e32 v53, v53, v63\n"
      "s_nop 0\n"
      "v_add_f32_dpp v62, v52, v62 row_half_mirror row_mask:0xf bank_mask:0xf\n"
      "v_mov_b32_dpp v56, v52 row_half_mirror row_mask:0xf bank_mask:0xf\n"
      "v_cvt_pk_bf16_f32 v56, v56, v56\n"
      "v_and_b32_e32 v56, v56, v63\n"
      "v_cndmask_b32_e64 v55, v53, v56, s[50:51]\n"
      "v_cndmask_b32_e64 v59, v56, v53, s[50:51]\n"
      "v_cndmask_b32_e64 v52, 0, v55, s[52:53]\n"
      "v_cndmask_b32_e64 v53, 0, v55, s[54:55]\n"
      "v_cndmask_b32_e64 v54, 0, v55, s[56:57]\n"
      "v_cndmask_b32_e64 v55, 0, v55, s[58:59]\n"
      "v_cndmask_b32_e64 v56, 0, v59, s[52:53]\n"
      "v_cndmask_b32_e64 v57, 0, v59, s[54:55]\n"
      "v_cndmask_b32_e64 v58, 0, v59, s[56:57]\n"
      "v_cndmask_b32_e64 v59, 0, v59, s[58:59]\n"
      "s_waitcnt vmcnt(14)\n"
      "s_add_u32 s86, s100, 0x40e0000\n"
      "s_cmp_ge_u32 s86, 0x43f0000\n"
      "s_cselect_b32 s87, 0x3f0000, 0\n"
      "s_sub_u32 s86, s86, s87\n"
      "s_add_u32 s90, s36, s86\n"
      "s_addc_u32 s91, s37, 0\n"
      "global_load_dwordx4 v[160:163], v60, s[90:91] offset:-4096 nt\n"
      "global_load_dwordx4 v[164:167], v60, s[90:91] offset:-3840 nt\n"
      "v_mfma_f32_16x16x32_bf16 v[32:35], a[224:227], v[52:55], v[32:35]\n"
      "v_mfma_f32_16x16x32_bf16 v[36:39], a[228:231], v[52:55], v[36:39]\n"
      "s_waitcnt vmcnt(14)\n"
      "global_load_dwordx4 v[168:171], v60, s[90:91] offset:-2048 nt\n"
      "global_load_dwordx4 v[172:175], v60, s[90:91] offset:-1792 nt\n"
      "v_mfma_f32_16x16x32_bf16 v[40:43], a[232:235], v[52:55], v[40:43]\n"
      "v_mfma_f32_16x16x32_bf16 v[44:47], a[236:239], v[52:55], v[44:47]\n"
      "s_waitcnt vmcnt(14)\n"
      "global_load_dwordx4 v[176:179], v60, s[90:91] offset:0 nt\n"
      "global_load_dwordx4 v[180:183], v60, s[90:91] offset:256 nt\n"
      "v_mfma_f32_16x16x32_bf16 v[32:35], a[240:243], v[56:59], v[32:35]\n"
      "v_mfma_f32_16x16x32_bf16 v[36:39], a[244:247], v[56:59], v[36:39]\n"
      "s_waitcnt vmcnt(14)\n"
      "global_load_dwordx4 v[184:187], v60, s[90:91] offset:2048 nt\n"
      "global_load_dwordx4 v[188:191], v60, s[90:91] offset:2304 nt\n"
      "v_mfma_f32_16x16x32_bf16 v[40:43], a[248:251], v[56:59], v[40:43]\n"
      "v_mfma_f32_16x16x32_bf16 v[44:47], a[252:255], v[56:59], v[44:47]\n"
      "s_add_u32 s74, s44, 0x40000\n"
      "s_addc_u32 s75, s45, 0\n"
      "s_add_u32 s76, s46, 0x800\n"
      "s_addc_u32 s77, s47, 0\n"
      "s_mov_b32 s72, 0x24000\n"
      "s_mov_b32 s73, 0x260a4\n"
      "s_nop 1\n"
      "s_swappc_b64 s[66:67], s[70:71]\n"
      "s_waitcnt vmcnt(14)\n"
      "s_add_u32 s86, s100, 0x4070000\n"
      "s_cmp_ge_u32 s86, 0x43f0000\n"
      "s_cselect_b32 s87, 0x3f0000, 0\n"
      "s_sub_u32 s86, s86, s87\n"
      "s_add_u32 s90, s36, s86\n"
      "s_addc_u32 s91, s37, 0\n"
      "global_load_dwordx4 v[64:67], v60, s[90:91] offset:-4096 nt\n"
      "global_load_dwordx4 v[68:71], v60, s[90:91] offset:-3840 nt\n"
      "s_waitcnt vmcnt(14)\n"
      "global_load_dwordx4 v[72:75], v60, s[90:91] offset:-2048 nt\n"
      "global_load_dwordx4 v[76:79], v60, s[90:91] offset:-1792 nt\n"
      "s_waitcnt vmcnt(14)\n"
      "global_load_dwordx4 v[80:83], v60, s[90:91] offset:0 nt\n"
      "global_load_dwordx4 v[84:87], v60, s[90:91] offset:256 nt\n"
      "s_waitcnt vmcnt(14)\n"
      "global_load_dwordx4 v[88:91], v60, s[90:91] offset:2048 nt\n"
      "global_load_dwordx4 v[92:95], v60, s[90:91] offset:2304 nt\n"
      "s_waitcnt vmcnt(14)\n"
      "s_add_u32 s86, s100, 0x4000000\n"
      "s_cmp_ge_u32 s86, 0x43f0000\n"
      "s_cselect_b32 s87, 0x3f0000, 0\n"
      "s_sub_u32 s86, s86, s87\n"
      "s_add_u32 s90, s36, s86\n"
      "s_addc_u32 s91, s37, 0\n"
      "global_load_dwordx4 v[96:99], v60, s[90:91] offset:-4096 nt\n"
      "global_load_dwordx4 v[100:103], v60, s[90:91] offset:-3840 nt\n"
      "s_waitcnt vmcnt(14)\n"
      "global_load_dwordx4 v[104:107], v60, s[90:91] offset:-2048 nt\n"
      "global_load_dwordx4 v[108:111], v60, s[90:91] offset:-1792 nt\n"
      "s_waitcnt vmcnt(14)\n"
      "global_load_dwordx4 v[112:115], v60, s[90:91] offset:0 nt\n"
      "global_load_dwordx4 v[116:119], v60, s[90:91] offset:256 nt\n"
      "s_waitcnt vmcnt(14)\n"
      "global_load_dwordx4 v[120:123], v60, s[90:91] offset:2048 nt\n"
      "global_load_dwordx4 v[124:127], v60, s[90:91] offset:2304 nt\n"
      "s_waitcnt vmcnt(14)\n"
      "s_add_u32 s86, s100, 0x4150000\n"
      "s_cmp_ge_u32 s86, 0x43f0000\n"
      "s_cselect_b32 s87, 0x3f0000, 0\n"
      "s_sub_u32 s86, s86, s87\n"
      "s_add_u32 s90, s38, s86\n"
      "s_addc_u32 s91, s39, 0\n"
      "global_load_dwordx4 v[192:195], v61, s[90:91] offset:0 nt\n"
      "global_load_dwordx4 v[196:199], v61, s[90:91] offset:128 nt\n"
      "s_waitcnt vmcnt(14)\n"
      "global_load_dwordx4 v[200:203], v61, s[90:91] offset:256 nt\n"
      "global_load_dwordx4 v[204:207], v61, s[90:91] offset:384 nt\n"
      "s_waitcnt vmcnt(14)\n"
      "global_load_dwordx4 v[208:211], v61, s[90:91] offset:2048 nt\n"
      "global_load_dwordx4 v[212:215], v61, s[90:91] offset:2176 nt\n"
      "s_waitcnt vmcnt(14)\n"
      "global_load_dwordx4 v[216:219], v61, s[90:91] offset:2304 nt\n"
      "global_load_dwordx4 v[220:223], v61, s[90:91] offset:2432 nt\n"
      "s_waitcnt vmcnt(14)\n"
      "s_add_u32 s86, s100, 0x40e0000\n"
      "s_cmp_ge_u32 s86, 0x43f0000\n"
      "s_cselect_b32 s87, 0x3f0000, 0\n"
      "s_sub_u32 s86, s86, s87\n"
      "s_add_u32 s90, s38, s86\n"
      "s_addc_u32 s91, s39, 0\n"
      "global_load_dwordx4 v[224:227], v61, s[90:91] offset:0 nt\n"
      "global_load_dwordx4 v[228:231], v61, s[90:91] offset:128 nt\n"
      "s_waitcnt vmcnt(14)\n"
      "global_load_dwordx4 v[232:235], v61, s[90:91] offset:256 nt\n"
      "global_load_dwordx4 v[236:239], v61, s[90:91] offset:384 nt\n"
      "s_waitcnt vmcnt(14)\n"
      "global_load_dwordx4 v[240:243], v61, s[90:91] offset:2048 nt\n"
      "global_load_dwordx4 v[244:247], v61, s[90:91] offset:2176 nt\n"
      "s_waitcnt vmcnt(14)\n"
      "global_load_dwordx4 v[248:251], v61, s[90:91] offset:2304 nt\n"
      "global_load_dwordx4 v[252:255], v61, s[90:91] offset:2432 nt\n"
      "s_add_u32 s64, s42, 0x60000\n"
      "s_addc_u32 s65, s43, 0\n"
      "s_mov_b32 m0, 0x27a40\n"
      "s_nop 0\n"
      "global_load_lds_dwordx4 v61, s[64:65] sc1\n"
      "s_mov_b32 s79, 0\n"
      "s_nop 0\n"
      "QW_113_%=:\n"
      "v_mbcnt_lo_u32_b32 v52, -1, 0\n"
      "v_mbcnt_hi_u32_b32 v52, -1, v52\n"
      "v_lshlrev_b32_e32 v52, 4, v52\n"
      "v_add_u32_e32 v52, 0x27a40, v52\n"
      "v_mov_b32_e32 v53, 0x26110\n"
      "ds_read_b128 v[48:51], v52\n"
      "ds_read_b32 v53, v53\n"
      "s_waitcnt lgkmcnt(0)\n"
      "v_cmp_ne_u32_e64 s[80:81], s62, v49\n"
      "v_cmp_ne_u32_e64 s[82:83], s62, v51\n"
      "v_cmp_eq_u32_e32 vcc, 1, v53\n"
      "s_nop 4\n"
      "s_cbranch_vccnz QD_114_%=\n"
      "s_or_b64 s[80:81], s[80:81], s[82:83]\n"
      "s_cmp_eq_u64 s[80:81], 0\n"
      "s_nop 0\n"
      "s_cbranch_scc0 QN_115_%=\n"
      "v_mov_b32_e32 v49, v50\n"
      "v_lshrrev_b32_e32 v52, 1, v52\n"
      "v_add_u32_e32 v52, 0x12f20, v52\n"
      "ds_write_b64 v52, v[48:49]\n"
      "v_mov_b32_e32 v52, 0x26110\n"
      "v_mov_b32_e32 v53, 1\n"
      "ds_write_b32 v52, v53\n"
      "s_waitcnt lgkmcnt(0)\n"
      "s_nop 2\n"
      "s_branch QD_114_%=\n"
      "s_nop 0\n"
      "QN_115_%=:\n"
      "s_add_u32 s64, s42, 0x60000\n"
      "s_addc_u32 s65, s43, 0\n"
      "s_mov_b32 m0, 0x27a40\n"
      "s_nop 0\n"
      "global_load_lds_dwordx4 v61, s[64:65] sc1\n"
      "s_sleep 2\n"
      "s_add_u32 s79, s79, 1\n"
      "s_cmp_lt_u32 s79, 0x100000\n"
      "s_waitcnt vmcnt(40)\n"
      "s_nop 0\n"
      "s_cbranch_scc1 QW_113_%=\n"
      "s_nop 0\n"
      "QD_114_%=:\n"
      "v_mbcnt_lo_u32_b32 v53, -1, 0\n"
      "v_mbcnt_hi_u32_b32 v53, -1, v53\n"
      "v_lshrrev_b32_e32 v52, 4, v53\n"
      "v_lshlrev_b32_e32 v52, 6, v52\n"
      "v_and_b32_e32 v53, 3, v53\n"
      "v_lshl_add_u32 v52, v53, 4, v52\n"
      "v_add_u32_e32 v52, 0x26c40, v52\n"
      "s_mov_b32 vcc_lo, 0xf000f\n"
      "s_mov_b32 vcc_hi, 0xf000f\n"
      "v_mov_b32_e32 v53, 0x26120\n"
      "v_cndmask_b32_e32 v53, v53, v52, vcc\n"
      "ds_read_b128 v[0:3], v53 offset:0\n"
      "ds_read_b128 v[4:7], v53 offset:256\n"
      "s_mov_b32 vcc_lo, 0xf000f0\n"
      "s_mov_b32 vcc_hi, 0xf000f0\n"
      "v_mov_b32_e32 v53, 0x26120\n"
      "v_cndmask_b32_e32 v53, v53, v52, vcc\n"
      "ds_read_b128 v[8:11], v53 offset:0\n"
      "ds_read_b128 v[12:15], v53 offset:256\n"
      "s_mov_b32 vcc_lo, 0xf000f00\n"
      "s_mov_b32 vcc_hi, 0xf000f00\n"
      "v_mov_b32_e32 v53, 0x26120\n"
      "v_cndmask_b32_e32 v53, v53, v52, vcc\n"
      "ds_read_b128 v[16:19], v53 offset:0\n"
      "ds_read_b128 v[20:23], v53 offset:256\n"
      "s_mov_b32 vcc_lo, 0xf000f000\n"
      "s_mov_b32 vcc_hi, 0xf000f000\n"
      "v_mov_b32_e32 v53, 0x26120\n"
      "v_cndmask_b32_e32 v53, v53, v52, vcc\n"
      "ds_read_b128 v[24:27], v53 offset:0\n"
      "ds_read_b128 v[28:31], v53 offset:256\n"
      "s_waitcnt lgkmcnt(0)\n"
      "v_mbcnt_lo_u32_b32 v59, -1, 0\n"
      "v_mbcnt_hi_u32_b32 v59, -1, v59\n"
      "v_lshlrev_b32_e32 v59, 4, v59\n"
      "v_add_u32_e32 v59, s49, v59\n"
      "v_mov_b32_e32 v58, 0xff800000\n"
      "v_mfma_f32_16x16x32_bf16 v[48:51], a[0:3], v[0:3], 0\n"
      "v_mfma_f32_16x16x32_bf16 v[48:51], a[4:7], v[4:7], v[48:51]\n"
      "v_mfma_f32_16x16x32_bf16 v[48:51], a[8:11], v[8:11], v[48:51]\n"
      "v_mfma_f32_16x16x32_bf16 v[48:51], a[12:15], v[12:15], v[48:51]\n"
      "v_mfma_f32_16x16x32_bf16 v[48:51], a[16:19], v[16:19], v[48:51]\n"
      "v_mfma_f32_16x16x32_bf16 v[48:51], a[20:23], v[20:23], v[48:51]\n"
      "v_mfma_f32_16x16x32_bf16 v[48:51], a[24:27], v[24:27], v[48:51]\n"
      "v_mfma_f32_16x16x32_bf16 v[48:51], a[28:31], v[28:31], v[48:51]\n"
      "s_waitcnt vmcnt(15)\n"
      "s_add_u32 s86, s100, 0x4070000\n"
      "s_cmp_ge_u32 s86, 0x43f0000\n"
      "s_cselect_b32 s87, 0x3f0000, 0\n"
      "s_sub_u32 s86, s86, s87\n"
      "s_add_u32 s90, s38, s86\n"
      "s_addc_u32 s91, s39, 0\n"
      "global_load_dwordx4 a[192:195], v61, s[90:91] offset:0 nt\n"
      "v_mfma_f32_16x16x32_bf16 v[52:55], a[64:67], v[0:3], 0\n"
      "v_mfma_f32_16x16x32_bf16 v[52:55], a[68:71], v[4:7], v[52:55]\n"
      "v_mfma_f32_16x16x32_bf16 v[52:55], a[72:75], v[8:11], v[52:55]\n"
      "v_mfma_f32_16x16x32_bf16 v[52:55], a[76:79], v[12:15], v[52:55]\n"
      "v_mfma_f32_16x16x32_bf16 v[52:55], a[80:83], v[16:19], v[52:55]\n"
      "v_mfma_f32_16x16x32_bf16 v[52:55], a[84:87], v[20:23], v[52:55]\n"
      "v_mfma_f32_16x16x32_bf16 v[52:55], a[88:91], v[24:27], v[52:55]\n"
      "v_mfma_f32_16x16x32_bf16 v[52:55], a[92:95], v[28:31], v[52:55]\n"
      "v_cndmask_b32_e64 v56, v48, v49, s[96:97]\n"
      "v_cndmask_b32_e64 v57, v50, v51, s[96:97]\n"
      "v_cndmask_b32_e64 v32, v56, v57, s[98:99]\n"
      "s_nop 1\n"
      "v_add_f32_dpp v32, v32, v32 quad_perm:[1,0,3,2] row_mask:0xf bank_mask:0xf\n"
      "s_nop 1\n"
      "v_add_f32_dpp v32, v32, v32 quad_perm:[2,3,0,1] row_mask:0xf bank_mask:0xf\n"
      "v_max_f32_e32 v58, v58, v32\n"
      "ds_read_b128 a[64:67], v59 offset:0\n"
      "ds_read_b128 a[68:71], v59 offset:1024\n"
      "ds_read_b128 a[72:75], v59 offset:2048\n"
      "ds_read_b128 a[76:79], v59 offset:3072\n"
      "ds_read_b128 a[80:83], v59 offset:4096\n"
      "ds_read_b128 a[84:87], v59 offset:5120\n"
      "ds_read_b128 a[88:91], v59 offset:6144\n"
      "ds_read_b128 a[92:95], v59 offset:7168\n"
      "s_waitcnt vmcnt(15)\n"
      "global_load_dwordx4 a[196:199], v61, s[90:91] offset:128 nt\n"
      "v_mfma_f32_16x16x32_bf16 v[48:51], a[128:131], v[0:3], 0\n"
      "v_mfma_f32_16x16x32_bf16 v[48:51], a[132:135], v[4:7], v[48:51]\n"
      "v_mfma_f32_16x16x32_bf16 v[48:51], a[136:139], v[8:11], v[48:51]\n"
      "v_mfma_f32_16x16x32_bf16 v[48:51], a[140:143], v[12:15], v[48:51]\n"
      "v_mfma_f32_16x16x32_bf16 v[48:51], a[144:147], v[16:19], v[48:51]\n"
      "v_mfma_f32_16x16x32_bf16 v[48:51], a[148:151], v[20:23], v[48:51]\n"
      "v_mfma_f32_16x16x32_bf16 v[48:51], a[152:155], v[24:27], v[48:51]\n"
      "v_mfma_f32_16x16x32_bf16 v[48:51], a[156:159], v[28:31], v[48:51]\n"
      "v_cndmask_b32_e64 v56, v52, v53, s[96:97]\n"
      "v_cndmask_b32_e64 v57, v54, v55, s[96:97]\n"
      "v_cndmask_b32_e64 v33, v56, v57, s[98:99]\n"
      "s_nop 1\n"
      "v_add_f32_dpp v33, v33, v33 quad_perm:[1,0,3,2] row_mask:0xf bank_mask:0xf\n"
      "s_nop 1\n"
      "v_add_f32_dpp v33, v33, v33 quad_perm:[2,3,0,1] row_mask:0xf bank_mask:0xf\n"
      "v_max_f32_e32 v58, v58, v33\n"
      "ds_read_b128 a[128:131], v59 offset:16384\n"
      "ds_read_b128 a[132:135], v59 offset:17408\n"
      "ds_read_b128 a[136:139], v59 offset:18432\n"
      "ds_read_b128 a[140:143], v59 offset:19456\n"
      "ds_read_b128 a[144:147], v59 offset:20480\n"
      "ds_read_b128 a[148:151], v59 offset:21504\n"
      "ds_read_b128 a[152:155], v59 offset:22528\n"
      "ds_read_b128 a[156:159], v59 offset:23552\n"
      "s_waitcnt vmcnt(15)\n"
      "global_load_dwordx4 a[200:203], v61, s[90:91] offset:256 nt\n"
      "v_mfma_f32_16x16x32_bf16 v[52:55], v[128:131], v[0:3], 0\n"
      "v_mfma_f32_16x16x32_bf16 v[52:55], v[132:135], v[4:7], v[52:55]\n"
      "v_mfma_f32_16x16x32_bf16 v[52:55], v[136:139], v[8:11], v[52:55]\n"
      "v_mfma_f32_16x16x32_bf16 v[52:55], v[140:143], v[12:15], v[52:55]\n"
      "v_mfma_f32_16x16x32_bf16 v[52:55], v[144:147], v[16:19], v[52:55]\n"
      "v_mfma_f32_16x16x32_bf16 v[52:55], v[148:151], v[20:23], v[52:55]\n"
      "v_mfma_f32_16x16x32_bf16 v[52:55], v[152:155], v[24:27], v[52:55]\n"
      "v_mfma_f32_16x16x32_bf16 v[52:55], v[156:159], v[28:31], v[52:55]\n"
      "v_cndmask_b32_e64 v56, v48, v49, s[96:97]\n"
      "v_cndmask_b32_e64 v57, v50, v51, s[96:97]\n"
      "v_cndmask_b32_e64 v34, v56, v57, s[98:99]\n"
      "s_nop 1\n"
      "v_add_f32_dpp v34, v34, v34 quad_perm:[1,0,3,2] row_mask:0xf bank_mask:0xf\n"
      "s_nop 1\n"
      "v_add_f32_dpp v34, v34, v34 quad_perm:[2,3,0,1] row_mask:0xf bank_mask:0xf\n"
      "v_max_f32_e32 v58, v58, v34\n"
      "ds_read_b128 v[128:131], v59 offset:8192\n"
      "ds_read_b128 v[132:135], v59 offset:9216\n"
      "ds_read_b128 v[136:139], v59 offset:10240\n"
      "ds_read_b128 v[140:143], v59 offset:11264\n"
      "ds_read_b128 v[144:147], v59 offset:12288\n"
      "ds_read_b128 v[148:151], v59 offset:13312\n"
      "ds_read_b128 v[152:155], v59 offset:14336\n"
      "ds_read_b128 v[156:159], v59 offset:15360\n"
      "s_waitcnt vmcnt(15)\n"
      "global_load_dwordx4 a[204:207], v61, s[90:91] offset:384 nt\n"
      "v_mfma_f32_16x16x32_bf16 v[48:51], v[160:163], v[0:3], 0\n"
      "v_mfma_f32_16x16x32_bf16 v[48:51], v[164:167], v[4:7], v[48:51]\n"
      "v_mfma_f32_16x16x32_bf16 v[48:51], v[168:171], v[8:11], v[48:51]\n"
      "v_mfma_f32_16x16x32_bf16 v[48:51], v[172:175], v[12:15], v[48:51]\n"
      "v_mfma_f32_16x16x32_bf16 v[48:51], v[176:179], v[16:19], v[48:51]\n"
      "v_mfma_f32_16x16x32_bf16 v[48:51], v[180:183], v[20:23], v[48:51]\n"
      "v_mfma_f32_16x16x32_bf16 v[48:51], v[184:187], v[24:27], v[48:51]\n"
      "v_mfma_f32_16x16x32_bf16 v[48:51], v[188:191], v[28:31], v[48:51]\n"
      "v_cndmask_b32_e64 v56, v52, v53, s[96:97]\n"
      "v_cndmask_b32_e64 v57, v54, v55, s[96:97]\n"
      "v_cndmask_b32_e64 v35, v56, v57, s[98:99]\n"
      "s_nop 1\n"
      "v_add_f32_dpp v35, v35, v35 quad_perm:[1,0,3,2] row_mask:0xf bank_mask:0xf\n"
      "s_nop 1\n"
      "v_add_f32_dpp v35, v35, v35 quad_perm:[2,3,0,1] row_mask:0xf bank_mask:0xf\n"
      "v_max_f32_e32 v58, v58, v35\n"
      "ds_read_b128 v[160:163], v59 offset:24576\n"
      "ds_read_b128 v[164:167], v59 offset:25600\n"
      "ds_read_b128 v[168:171], v59 offset:26624\n"
      "ds_read_b128 v[172:175], v59 offset:27648\n"
      "ds_read_b128 v[176:179], v59 offset:28672\n"
      "ds_read_b128 v[180:183], v59 offset:29696\n"
      "ds_read_b128 v[184:187], v59 offset:30720\n"
      "ds_read_b128 v[188:191], v59 offset:31744\n"
      "s_waitcnt vmcnt(15)\n"
      "global_load_dwordx4 a[208:211], v61, s[90:91] offset:2048 nt\n"
      "v_mfma_f32_16x16x32_bf16 v[52:55], v[64:67], v[0:3], 0\n"
      "v_mfma_f32_16x16x32_bf16 v[52:55], v[68:71], v[4:7], v[52:55]\n"
      "v_mfma_f32_16x16x32_bf16 v[52:55], v[72:75], v[8:11], v[52:55]\n"
      "v_mfma_f32_16x16x32_bf16 v[52:55], v[76:79], v[12:15], v[52:55]\n"
      "v_mfma_f32_16x16x32_bf16 v[52:55], v[80:83], v[16:19], v[52:55]\n"
      "v_mfma_f32_16x16x32_bf16 v[52:55], v[84:87], v[20:23], v[52:55]\n"
      "v_mfma_f32_16x16x32_bf16 v[52:55], v[88:91], v[24:27], v[52:55]\n"
      "v_mfma_f32_16x16x32_bf16 v[52:55], v[92:95], v[28:31], v[52:55]\n"
      "v_cndmask_b32_e64 v56, v48, v49, s[96:97]\n"
      "v_cndmask_b32_e64 v57, v50, v51, s[96:97]\n"
      "v_cndmask_b32_e64 v36, v56, v57, s[98:99]\n"
      "s_nop 1\n"
      "v_add_f32_dpp v36, v36, v36 quad_perm:[1,0,3,2] row_mask:0xf bank_mask:0xf\n"
      "s_nop 1\n"
      "v_add_f32_dpp v36, v36, v36 quad_perm:[2,3,0,1] row_mask:0xf bank_mask:0xf\n"
      "v_max_f32_e32 v58, v58, v36\n"
      "s_waitcnt vmcnt(15)\n"
      "global_load_dwordx4 a[212:215], v61, s[90:91] offset:2176 nt\n"
      "v_mfma_f32_16x16x32_bf16 v[48:51], v[96:99], v[0:3], 0\n"
      "v_mfma_f32_16x16x32_bf16 v[48:51], v[100:103], v[4:7], v[48:51]\n"
      "v_mfma_f32_16x16x32_bf16 v[48:51], v[104:107], v[8:11], v[48:51]\n"
      "v_mfma_f32_16x16x32_bf16 v[48:51], v[108:111], v[12:15], v[48:51]\n"
      "v_mfma_f32_16x16x32_bf16 v[48:51], v[112:115], v[16:19], v[48:51]\n"
      "v_mfma_f32_16x16x32_bf16 v[48:51], v[116:119], v[20:23], v[48:51]\n"
      "v_mfma_f32_16x16x32_bf16 v[48:51], v[120:123], v[24:27], v[48:51]\n"
      "v_mfma_f32_16x16x32_bf16 v[48:51], v[124:127], v[28:31], v[48:51]\n"
      "v_cndmask_b32_e64 v56, v52, v53, s[96:97]\n"
      "v_cndmask_b32_e64 v57, v54, v55, s[96:97]\n"
      "v_cndmask_b32_e64 v37, v56, v57, s[98:99]\n"
      "s_nop 1\n"
      "v_add_f32_dpp v37, v37, v37 quad_perm:[1,0,3,2] row_mask:0xf bank_mask:0xf\n"
      "s_nop 1\n"
      "v_add_f32_dpp v37, v37, v37 quad_perm:[2,3,0,1] row_mask:0xf bank_mask:0xf\n"
      "v_max_f32_e32 v58, v58, v37\n"
      "s_waitcnt vmcnt(15)\n"
      "global_load_dwordx4 a[216:219], v61, s[90:91] offset:2304 nt\n"
      "s_waitcnt lgkmcnt(15)\n"
      "v_mfma_f32_16x16x32_bf16 v[52:55], a[64:67], v[0:3], 0\n"
      "v_mfma_f32_16x16x32_bf16 v[52:55], a[68:71], v[4:7], v[52:55]\n"
      "v_mfma_f32_16x16x32_bf16 v[52:55], a[72:75], v[8:11], v[52:55]\n"
      "v_mfma_f32_16x16x32_bf16 v[52:55], a[76:79], v[12:15], v[52:55]\n"
      "v_mfma_f32_16x16x32_bf16 v[52:55], a[80:83], v[16:19], v[52:55]\n"
      "v_mfma_f32_16x16x32_bf16 v[52:55], a[84:87], v[20:23], v[52:55]\n"
      "v_mfma_f32_16x16x32_bf16 v[52:55], a[88:91], v[24:27], v[52:55]\n"
      "v_mfma_f32_16x16x32_bf16 v[52:55], a[92:95], v[28:31], v[52:55]\n"
      "v_cndmask_b32_e64 v56, v48, v49, s[96:97]\n"
      "v_cndmask_b32_e64 v57, v50, v51, s[96:97]\n"
      "v_cndmask_b32_e64 v38, v56, v57, s[98:99]\n"
      "s_nop 1\n"
      "v_add_f32_dpp v38, v38, v38 quad_perm:[1,0,3,2] row_mask:0xf bank_mask:0xf\n"
      "s_nop 1\n"
      "v_add_f32_dpp v38, v38, v38 quad_perm:[2,3,0,1] row_mask:0xf bank_mask:0xf\n"
      "v_max_f32_e32 v58, v58, v38\n"
      "s_waitcnt vmcnt(15)\n"
      "global_load_dwordx4 a[220:223], v61, s[90:91] offset:2432 nt\n"
      "v_mfma_f32_16x16x32_bf16 v[48:51], a[128:131], v[0:3], 0\n"
      "v_mfma_f32_16x16x32_bf16 v[48:51], a[132:135], v[4:7], v[48:51]\n"
      "v_mfma_f32_16x16x32_bf16 v[48:51], a[136:139], v[8:11], v[48:51]\n"
      "v_mfma_f32_16x16x32_bf16 v[48:51], a[140:143], v[12:15], v[48:51]\n"
      "v_mfma_f32_16x16x32_bf16 v[48:51], a[144:147], v[16:19], v[48:51]\n"
      "v_mfma_f32_16x16x32_bf16 v[48:51], a[148:151], v[20:23], v[48:51]\n"
      "v_mfma_f32_16x16x32_bf16 v[48:51], a[152:155], v[24:27], v[48:51]\n"
      "v_mfma_f32_16x16x32_bf16 v[48:51], a[156:159], v[28:31], v[48:51]\n"
      "v_cndmask_b32_e64 v56, v52, v53, s[96:97]\n"
      "v_cndmask_b32_e64 v57, v54, v55, s[96:97]\n"
      "v_cndmask_b32_e64 v39, v56, v57, s[98:99]\n"
      "s_nop 1\n"
      "v_add_f32_dpp v39, v39, v39 quad_perm:[1,0,3,2] row_mask:0xf bank_mask:0xf\n"
      "s_nop 1\n"
      "v_add_f32_dpp v39, v39, v39 quad_perm:[2,3,0,1] row_mask:0xf bank_mask:0xf\n"
      "v_max_f32_e32 v58, v58, v39\n"
      "s_waitcnt vmcnt(15)\n"
      "s_add_u32 s86, s100, 0x4000000\n"
      "s_cmp_ge_u32 s86, 0x43f0000\n"
      "s_cselect_b32 s87, 0x3f0000, 0\n"
      "s_sub_u32 s86, s86, s87\n"
      "s_add_u32 s90, s38, s86\n"
      "s_addc_u32 s91, s39, 0\n"
      "global_load_dwordx4 a[224:227], v61, s[90:91] offset:0 nt\n"
      "v_cndmask_b32_e64 v56, v48, v49, s[96:97]\n"
      "v_cndmask_b32_e64 v57, v50, v51, s[96:97]\n"
      "v_cndmask_b32_e64 v40, v56, v57, s[98:99]\n"
      "s_nop 1\n"
      "v_add_f32_dpp v40, v40, v40 quad_perm:[1,0,3,2] row_mask:0xf bank_mask:0xf\n"
      "s_nop 1\n"
      "v_add_f32_dpp v40, v40, v40 quad_perm:[2,3,0,1] row_mask:0xf bank_mask:0xf\n"
      "v_max_f32_e32 v58, v58, v40\n"
      "s_nop 1\n"
      "v_max_f32_dpp v58, v58, v58 row_ror:4 row_mask:0xf bank_mask:0xf\n"
      "s_nop 1\n"
      "v_max_f32_dpp v58, v58, v58 row_ror:8 row_mask:0xf bank_mask:0xf\n"
      "s_nop 0\n"
      "v_readlane_b32 s80, v58, 0\n"
      "v_readlane_b32 s81, v58, 16\n"
      "v_readlane_b32 s82, v58, 32\n"
      "v_readlane_b32 s83, v58, 48\n"
      "v_mov_b32_e32 v58, s80\n"
      "v_max_f32_e32 v58, s81, v58\n"
      "v_max_f32_e32 v58, s82, v58\n"
      "v_max_f32_e32 v58, s83, v58\n"
      "v_cndmask_b32_e64 v48, 0, v63, s[52:53]\n"
      "v_cndmask_b32_e64 v49, 0, v63, s[54:55]\n"
      "v_cndmask_b32_e64 v50, 0, v63, s[56:57]\n"
      "v_cndmask_b32_e64 v51, 0, v63, s[58:59]\n"
      "v_readfirstlane_b32 s60, v58\n"
      "v_mul_f32_e32 v52, s34, v58\n"
      "v_mov_b32_e32 v62, 0\n"
      "v_fma_f32 v34, v34, s34, -v52\n"
      "v_exp_f32_e32 v34, v34\n"
      "s_nop 0\n"
      "v_cvt_pk_bf16_f32 v53, v34, v34\n"
      "v_add_f32_e32 v62, v62, v34\n"
      "s_nop 0\n"
      "v_mov_b32_dpp v54, v53 row_half_mirror row_mask:0xf bank_mask:0xf\n"
      "v_add_f32_dpp v62, v34, v62 row_half_mirror row_mask:0xf bank_mask:0xf\n"
      "v_cndmask_b32_e64 v55, v53, v54, s[50:51]\n"
      "v_cndmask_b32_e64 v56, v54, v53, s[50:51]\n"
      "v_and_b32_e32 v16, v55, v48\n"
      "v_and_b32_e32 v17, v55, v49\n"
      "v_and_b32_e32 v18, v55, v50\n"
      "v_and_b32_e32 v19, v55, v51\n"
      "v_and_b32_e32 v20, v56, v48\n"
      "v_and_b32_e32 v21, v56, v49\n"
      "v_and_b32_e32 v22, v56, v50\n"
      "v_and_b32_e32 v23, v56, v51\n"
      "v_mfma_f32_16x16x32_bf16 v[0:3], a[160:163], v[16:19], 0\n"
      "v_fma_f32 v33, v33, s34, -v52\n"
      "v_exp_f32_e32 v33, v33\n"
      "v_mfma_f32_16x16x32_bf16 v[4:7], a[164:167], v[16:19], 0\n"
      "s_nop 0\n"
      "v_cvt_pk_bf16_f32 v53, v33, v33\n"
      "v_mfma_f32_16x16x32_bf16 v[8:11], a[168:171], v[16:19], 0\n"
      "v_add_f32_e32 v62, v62, v33\n"
      "v_mov_b32_dpp v54, v53 row_half_mirror row_mask:0xf bank_mask:0xf\n"
      "v_mfma_f32_16x16x32_bf16 v[12:15], a[172:175], v[16:19], 0\n"
      "v_add_f32_dpp v62, v33, v62 row_half_mirror row_mask:0xf bank_mask:0xf\n"
      "v_cndmask_b32_e64 v55, v53, v54, s[50:51]\n"
      "v_mfma_f32_16x16x32_bf16 v[0:3], a[176:179], v[20:23], v[0:3]\n"
      "v_cndmask_b32_e64 v56, v54, v53, s[50:51]\n"
      "v_and_b32_e32 v24, v55, v48\n"
      "v_mfma_f32_16x16x32_bf16 v[4:7], a[180:183], v[20:23], v[4:7]\n"
      "v_and_b32_e32 v25, v55, v49\n"
      "v_and_b32_e32 v26, v55, v50\n"
      "v_mfma_f32_16x16x32_bf16 v[8:11], a[184:187], v[20:23], v[8:11]\n"
      "v_and_b32_e32 v27, v55, v51\n"
      "v_and_b32_e32 v28, v56, v48\n"
      "v_mfma_f32_16x16x32_bf16 v[12:15], a[188:191], v[20:23], v[12:15]\n"
      "v_and_b32_e32 v29, v56, v49\n"
      "v_and_b32_e32 v30, v56, v50\n"
      "v_and_b32_e32 v31, v56, v51\n"
      "s_waitcnt vmcnt(15)\n"
      "global_load_dwordx4 a[228:231], v61, s[90:91] offset:128 nt\n"
      "v_mfma_f32_16x16x32_bf16 v[0:3], a[96:99], v[24:27], v[0:3]\n"
      "v_fma_f32 v32, v32, s34, -v52\n"
      "v_exp_f32_e32 v32, v32\n"
      "v_mfma_f32_16x16x32_bf16 v[4:7], a[100:103], v[24:27], v[4:7]\n"
      "s_nop 0\n"
      "v_cvt_pk_bf16_f32 v53, v32, v32\n"
      "v_mfma_f32_16x16x32_bf16 v[8:11], a[104:107], v[24:27], v[8:11]\n"
      "v_add_f32_e32 v62, v62, v32\n"
      "v_mov_b32_dpp v54, v53 row_half_mirror row_mask:0xf bank_mask:0xf\n"
      "v_mfma_f32_16x16x32_bf16 v[12:15], a[108:111], v[24:27], v[12:15]\n"
      "v_add_f32_dpp v62, v32, v62 row_half_mirror row_mask:0xf bank_mask:0xf\n"
      "v_cndmask_b32_e64 v55, v53, v54, s[50:51]\n"
      "v_mfma_f32_16x16x32_bf16 v[0:3], a[112:115], v[28:31], v[0:3]\n"
      "v_cndmask_b32_e64 v56, v54, v53, s[50:51]\n"
      "v_and_b32_e32 v16, v55, v48\n"
      "v_mfma_f32_16x16x32_bf16 v[4:7], a[116:119], v[28:31], v[4:7]\n"
      "v_and_b32_e32 v17, v55, v49\n"
      "v_and_b32_e32 v18, v55, v50\n"
      "v_mfma_f32_16x16x32_bf16 v[8:11], a[120:123], v[28:31], v[8:11]\n"
      "v_and_b32_e32 v19, v55, v51\n"
      "v_and_b32_e32 v20, v56, v48\n"
      "v_mfma_f32_16x16x32_bf16 v[12:15], a[124:127], v[28:31], v[12:15]\n"
      "v_and_b32_e32 v21, v56, v49\n"
      "v_and_b32_e32 v22, v56, v50\n"
      "v_and_b32_e32 v23, v56, v51\n"
      "s_waitcnt vmcnt(15)\n"
      "global_load_dwordx4 a[232:235], v61, s[90:91] offset:256 nt\n"
      "v_mfma_f32_16x16x32_bf16 v[0:3], a[32:35], v[16:19], v[0:3]\n"
      "v_fma_f32 v40, v40, s34, -v52\n"
      "v_exp_f32_e32 v40, v40\n"
      "v_mfma_f32_16x16x32_bf16 v[4:7], a[36:39], v[16:19], v[4:7]\n"
      "s_nop 0\n"
      "v_cvt_pk_bf16_f32 v53, v40, v40\n"
      "v_mfma_f32_16x16x32_bf16 v[8:11], a[40:43], v[16:19], v[8:11]\n"
      "v_add_f32_e32 v62, v62, v40\n"
      "v_mov_b32_dpp v54, v53 row_half_mirror row_mask:0xf bank_mask:0xf\n"
      "v_mfma_f32_16x16x32_bf16 v[12:15], a[44:47], v[16:19], v[12:15]\n"
      "v_add_f32_dpp v62, v40, v62 row_half_mirror row_mask:0xf bank_mask:0xf\n"
      "v_cndmask_b32_e64 v55, v53, v54, s[50:51]\n"
      "v_mfma_f32_16x16x32_bf16 v[0:3], a[48:51], v[20:23], v[0:3]\n"
      "v_cndmask_b32_e64 v56, v54, v53, s[50:51]\n"
      "v_and_b32_e32 v24, v55, v48\n"
      "v_mfma_f32_16x16x32_bf16 v[4:7], a[52:55], v[20:23], v[4:7]\n"
      "v_and_b32_e32 v25, v55, v49\n"
      "v_and_b32_e32 v26, v55, v50\n"
      "v_mfma_f32_16x16x32_bf16 v[8:11], a[56:59], v[20:23], v[8:11]\n"
      "v_and_b32_e32 v27, v55, v51\n"
      "v_and_b32_e32 v28, v56, v48\n"
      "v_mfma_f32_16x16x32_bf16 v[12:15], a[60:63], v[20:23], v[12:15]\n"
      "v_and_b32_e32 v29, v56, v49\n"
      "v_and_b32_e32 v30, v56, v50\n"
      "v_and_b32_e32 v31, v56, v51\n"
      "s_waitcnt vmcnt(15)\n"
      "global_load_dwordx4 a[236:239], v61, s[90:91] offset:384 nt\n"
      "s_waitcnt lgkmcnt(0)\n"
      "v_mfma_f32_16x16x32_bf16 v[0:3], v[160:163], v[24:27], v[0:3]\n"
      "v_fma_f32 v39, v39, s34, -v52\n"
      "v_exp_f32_e32 v39, v39\n"
      "v_mfma_f32_16x16x32_bf16 v[4:7], v[164:167], v[24:27], v[4:7]\n"
      "s_nop 0\n"
      "v_cvt_pk_bf16_f32 v53, v39, v39\n"
      "v_mfma_f32_16x16x32_bf16 v[8:11], v[168:171], v[24:27], v[8:11]\n"
      "v_add_f32_e32 v62, v62, v39\n"
      "v_mov_b32_dpp v54, v53 row_half_mirror row_mask:0xf bank_mask:0xf\n"
      "v_mfma_f32_16x16x32_bf16 v[12:15], v[172:175], v[24:27], v[12:15]\n"
      "v_add_f32_dpp v62, v39, v62 row_half_mirror row_mask:0xf bank_mask:0xf\n"
      "v_cndmask_b32_e64 v55, v53, v54, s[50:51]\n"
      "v_mfma_f32_16x16x32_bf16 v[0:3], v[176:179], v[28:31], v[0:3]\n"
      "v_cndmask_b32_e64 v56, v54, v53, s[50:51]\n"
      "v_and_b32_e32 v16, v55, v48\n"
      "v_mfma_f32_16x16x32_bf16 v[4:7], v[180:183], v[28:31], v[4:7]\n"
      "v_and_b32_e32 v17, v55, v49\n"
      "v_and_b32_e32 v18, v55, v50\n"
      "v_mfma_f32_16x16x32_bf16 v[8:11], v[184:187], v[28:31], v[8:11]\n"
      "v_and_b32_e32 v19, v55, v51\n"
      "v_and_b32_e32 v20, v56, v48\n"
      "v_mfma_f32_16x16x32_bf16 v[12:15], v[188:191], v[28:31], v[12:15]\n"
      "v_and_b32_e32 v21, v56, v49\n"
      "v_and_b32_e32 v22, v56, v50\n"
      "v_and_b32_e32 v23, v56, v51\n"
      "s_waitcnt vmcnt(15)\n"
      "global_load_dwordx4 a[240:243], v61, s[90:91] offset:2048 nt\n"
      "v_mfma_f32_16x16x32_bf16 v[0:3], v[128:131], v[16:19], v[0:3]\n"
      "v_fma_f32 v35, v35, s34, -v52\n"
      "v_exp_f32_e32 v35, v35\n"
      "v_mfma_f32_16x16x32_bf16 v[4:7], v[132:135], v[16:19], v[4:7]\n"
      "s_nop 0\n"
      "v_cvt_pk_bf16_f32 v53, v35, v35\n"
      "v_mfma_f32_16x16x32_bf16 v[8:11], v[136:139], v[16:19], v[8:11]\n"
      "v_add_f32_e32 v62, v62, v35\n"
      "v_mov_b32_dpp v54, v53 row_half_mirror row_mask:0xf bank_mask:0xf\n"
      "v_mfma_f32_16x16x32_bf16 v[12:15], v[140:143], v[16:19], v[12:15]\n"
      "v_add_f32_dpp v62, v35, v62 row_half_mirror row_mask:0xf bank_mask:0xf\n"
      "v_cndmask_b32_e64 v55, v53, v54, s[50:51]\n"
      "v_mfma_f32_16x16x32_bf16 v[0:3], v[144:147], v[20:23], v[0:3]\n"
      "v_cndmask_b32_e64 v56, v54, v53, s[50:51]\n"
      "v_and_b32_e32 v24, v55, v48\n"
      "v_mfma_f32_16x16x32_bf16 v[4:7], v[148:151], v[20:23], v[4:7]\n"
      "v_and_b32_e32 v25, v55, v49\n"
      "v_and_b32_e32 v26, v55, v50\n"
      "v_mfma_f32_16x16x32_bf16 v[8:11], v[152:155], v[20:23], v[8:11]\n"
      "v_and_b32_e32 v27, v55, v51\n"
      "v_and_b32_e32 v28, v56, v48\n"
      "v_mfma_f32_16x16x32_bf16 v[12:15], v[156:159], v[20:23], v[12:15]\n"
      "v_and_b32_e32 v29, v56, v49\n"
      "v_and_b32_e32 v30, v56, v50\n"
      "v_and_b32_e32 v31, v56, v51\n"
      "s_waitcnt vmcnt(15)\n"
      "global_load_dwordx4 a[244:247], v61, s[90:91] offset:2176 nt\n"
      "v_mfma_f32_16x16x32_bf16 v[0:3], v[192:195], v[24:27], v[0:3]\n"
      "v_fma_f32 v36, v36, s34, -v52\n"
      "v_exp_f32_e32 v36, v36\n"
      "v_mfma_f32_16x16x32_bf16 v[4:7], v[196:199], v[24:27], v[4:7]\n"
      "s_nop 0\n"
      "v_cvt_pk_bf16_f32 v53, v36, v36\n"
      "v_mfma_f32_16x16x32_bf16 v[8:11], v[200:203], v[24:27], v[8:11]\n"
      "v_add_f32_e32 v62, v62, v36\n"
      "v_mov_b32_dpp v54, v53 row_half_mirror row_mask:0xf bank_mask:0xf\n"
      "v_mfma_f32_16x16x32_bf16 v[12:15], v[204:207], v[24:27], v[12:15]\n"
      "v_add_f32_dpp v62, v36, v62 row_half_mirror row_mask:0xf bank_mask:0xf\n"
      "v_cndmask_b32_e64 v55, v53, v54, s[50:51]\n"
      "v_mfma_f32_16x16x32_bf16 v[0:3], v[208:211], v[28:31], v[0:3]\n"
      "v_cndmask_b32_e64 v56, v54, v53, s[50:51]\n"
      "v_and_b32_e32 v16, v55, v48\n"
      "v_mfma_f32_16x16x32_bf16 v[4:7], v[212:215], v[28:31], v[4:7]\n"
      "v_and_b32_e32 v17, v55, v49\n"
      "v_and_b32_e32 v18, v55, v50\n"
      "v_mfma_f32_16x16x32_bf16 v[8:11], v[216:219], v[28:31], v[8:11]\n"
      "v_and_b32_e32 v19, v55, v51\n"
      "v_and_b32_e32 v20, v56, v48\n"
      "v_mfma_f32_16x16x32_bf16 v[12:15], v[220:223], v[28:31], v[12:15]\n"
      "v_and_b32_e32 v21, v56, v49\n"
      "v_and_b32_e32 v22, v56, v50\n"
      "v_and_b32_e32 v23, v56, v51\n"
      "s_waitcnt vmcnt(15)\n"
      "global_load_dwordx4 a[248:251], v61, s[90:91] offset:2304 nt\n"
      "s_waitcnt vmcnt(15)\n"
      "v_mfma_f32_16x16x32_bf16 v[0:3], v[224:227], v[16:19], v[0:3]\n"
      "v_fma_f32 v37, v37, s34, -v52\n"
      "v_exp_f32_e32 v37, v37\n"
      "v_mfma_f32_16x16x32_bf16 v[4:7], v[228:231], v[16:19], v[4:7]\n"
      "s_nop 0\n"
      "v_cvt_pk_bf16_f32 v53, v37, v37\n"
      "v_mfma_f32_16x16x32_bf16 v[8:11], v[232:235], v[16:19], v[8:11]\n"
      "v_add_f32_e32 v62, v62, v37\n"
      "v_mov_b32_dpp v54, v53 row_half_mirror row_mask:0xf bank_mask:0xf\n"
      "v_mfma_f32_16x16x32_bf16 v[12:15], v[236:239], v[16:19], v[12:15]\n"
      "v_add_f32_dpp v62, v37, v62 row_half_mirror row_mask:0xf bank_mask:0xf\n"
      "v_cndmask_b32_e64 v55, v53, v54, s[50:51]\n"
      "v_mfma_f32_16x16x32_bf16 v[0:3], v[240:243], v[20:23], v[0:3]\n"
      "v_cndmask_b32_e64 v56, v54, v53, s[50:51]\n"
      "v_and_b32_e32 v24, v55, v48\n"
      "v_mfma_f32_16x16x32_bf16 v[4:7], v[244:247], v[20:23], v[4:7]\n"
      "v_and_b32_e32 v25, v55, v49\n"
      "v_and_b32_e32 v26, v55, v50\n"
      "v_mfma_f32_16x16x32_bf16 v[8:11], v[248:251], v[20:23], v[8:11]\n"
      "v_and_b32_e32 v27, v55, v51\n"
      "v_and_b32_e32 v28, v56, v48\n"
      "v_mfma_f32_16x16x32_bf16 v[12:15], v[252:255], v[20:23], v[12:15]\n"
      "v_and_b32_e32 v29, v56, v49\n"
      "v_and_b32_e32 v30, v56, v50\n"
      "v_and_b32_e32 v31, v56, v51\n"
      "global_load_dwordx4 a[252:255], v61, s[90:91] offset:2432 nt\n"
      "s_waitcnt vmcnt(8)\n"
      "v_mfma_f32_16x16x32_bf16 v[0:3], a[192:195], v[24:27], v[0:3]\n"
      "v_fma_f32 v38, v38, s34, -v52\n"
      "v_exp_f32_e32 v38, v38\n"
      "v_mfma_f32_16x16x32_bf16 v[4:7], a[196:199], v[24:27], v[4:7]\n"
      "s_nop 0\n"
      "v_cvt_pk_bf16_f32 v53, v38, v38\n"
      "v_mfma_f32_16x16x32_bf16 v[8:11], a[200:203], v[24:27], v[8:11]\n"
      "v_add_f32_e32 v62, v62, v38\n"
      "v_mov_b32_dpp v54, v53 row_half_mirror row_mask:0xf bank_mask:0xf\n"
      "v_mfma_f32_16x16x32_bf16 v[12:15], a[204:207], v[24:27], v[12:15]\n"
      "v_add_f32_dpp v62, v38, v62 row_half_mirror row_mask:0xf bank_mask:0xf\n"
      "v_cndmask_b32_e64 v55, v53, v54, s[50:51]\n"
      "v_mfma_f32_16x16x32_bf16 v[0:3], a[208:211], v[28:31], v[0:3]\n"
      "v_cndmask_b32_e64 v56, v54, v53, s[50:51]\n"
      "v_and_b32_e32 v16, v55, v48\n"
      "v_mfma_f32_16x16x32_bf16 v[4:7], a[212:215], v[28:31], v[4:7]\n"
      "v_and_b32_e32 v17, v55, v49\n"
      "v_and_b32_e32 v18, v55, v50\n"
      "v_mfma_f32_16x16x32_bf16 v[8:11], a[216:219], v[28:31], v[8:11]\n"
      "v_and_b32_e32 v19, v55, v51\n"
      "v_and_b32_e32 v20, v56, v48\n"
      "v_mfma_f32_16x16x32_bf16 v[12:15], a[220:223], v[28:31], v[12:15]\n"
      "v_and_b32_e32 v21, v56, v49\n"
      "v_and_b32_e32 v22, v56, v50\n"
      "v_and_b32_e32 v23, v56, v51\n"
      "s_waitcnt vmcnt(0)\n"
      "v_mfma_f32_16x16x32_bf16 v[0:3], a[224:227], v[16:19], v[0:3]\n"
      "v_mfma_f32_16x16x32_bf16 v[4:7], a[228:231], v[16:19], v[4:7]\n"
      "v_mfma_f32_16x16x32_bf16 v[8:11], a[232:235], v[16:19], v[8:11]\n"
      "v_mfma_f32_16x16x32_bf16 v[12:15], a[236:239], v[16:19], v[12:15]\n"
      "v_mfma_f32_16x16x32_bf16 v[32:35], a[240:243], v[20:23], v[0:3]\n"
      "v_mfma_f32_16x16x32_bf16 v[36:39], a[244:247], v[20:23], v[4:7]\n"
      "v_mfma_f32_16x16x32_bf16 v[40:43], a[248:251], v[20:23], v[8:11]\n"
      "v_mfma_f32_16x16x32_bf16 v[44:47], a[252:255], v[20:23], v[12:15]\n"
      "s_add_u32 s74, s44, 0xc0000\n"
      "s_addc_u32 s75, s45, 0\n"
      "s_add_u32 s76, s46, 0x1800\n"
      "s_addc_u32 s77, s47, 0\n"
      "s_mov_b32 s72, 0x25050\n"
      "s_mov_b32 s73, 0x260ac\n"
      "s_nop 1\n"
      "s_swappc_b64 s[66:67], s[70:71]\n"
      "s_branch END_25_%=\n"
      "s_nop 0\n"
      "END_25_%=:\n"
      "s_endpgm\n"
      "s_nop 0\n"
      "A_RESC_%=:\n"
      "s_nop 1\n"
      "v_max_f32_dpp v54, v52, v52 row_ror:4 row_mask:0xf bank_mask:0xf\n"
      "s_nop 1\n"
      "v_max_f32_dpp v54, v54, v54 row_ror:8 row_mask:0xf bank_mask:0xf\n"
      "s_nop 1\n"
      "v_readlane_b32 s80, v54, 0\n"
      "v_readlane_b32 s81, v54, 16\n"
      "v_readlane_b32 s82, v54, 32\n"
      "v_readlane_b32 s83, v54, 48\n"
      "v_mov_b32_e32 v53, s80\n"
      "v_max_f32_e32 v53, s81, v53\n"
      "v_max_f32_e32 v53, s82, v53\n"
      "v_max_f32_e32 v53, s83, v53\n"
      "v_sub_f32_e32 v54, s60, v53\n"
      "v_mul_f32_e32 v54, 0x3db8aa3b, v54\n"
      "v_exp_f32_e32 v54, v54\n"
      "v_readfirstlane_b32 s60, v53\n"
      "v_mul_f32_e32 v62, v62, v54\n"
      "v_mul_f32_e32 v32, v32, v54\n"
      "v_mul_f32_e32 v33, v33, v54\n"
      "v_mul_f32_e32 v34, v34, v54\n"
      "v_mul_f32_e32 v35, v35, v54\n"
      "v_mul_f32_e32 v36, v36, v54\n"
      "v_mul_f32_e32 v37, v37, v54\n"
      "v_mul_f32_e32 v38, v38, v54\n"
      "v_mul_f32_e32 v39, v39, v54\n"
      "v_mul_f32_e32 v40, v40, v54\n"
      "v_mul_f32_e32 v41, v41, v54\n"
      "v_mul_f32_e32 v42, v42, v54\n"
      "v_mul_f32_e32 v43, v43, v54\n"
      "v_mul_f32_e32 v44, v44, v54\n"
      "v_mul_f32_e32 v45, v45, v54\n"
      "v_mul_f32_e32 v46, v46, v54\n"
      "v_mul_f32_e32 v47, v47, v54\n"
      "s_nop 4\n"
      "s_setpc_b64 s[66:67]\n"
      "s_nop 0\n"
      "A_FIN_%=:\n"
      "v_mbcnt_lo_u32_b32 v0, -1, 0\n"
      "v_mbcnt_hi_u32_b32 v0, -1, v0\n"
      "v_add_f32_dpp v62, v62, v62 row_ror:8 row_mask:0xf bank_mask:0xf\n"
      "v_permlane32_swap_b32_e32 v32, v40\n"
      "v_permlane32_swap_b32_e32 v33, v41\n"
      "v_permlane32_swap_b32_e32 v34, v42\n"
      "v_permlane32_swap_b32_e32 v35, v43\n"
      "v_permlane32_swap_b32_e32 v36, v44\n"
      "v_permlane32_swap_b32_e32 v37, v45\n"
      "v_permlane32_swap_b32_e32 v38, v46\n"
      "v_permlane32_swap_b32_e32 v39, v47\n"
      "v_add_f32_dpp v32, v40, v32 row_ror:8 row_mask:0xf bank_mask:0xf\n"
      "v_add_f32_dpp v33, v41, v33 row_ror:8 row_mask:0xf bank_mask:0xf\n"
      "v_add_f32_dpp v34, v42, v34 row_ror:8 row_mask:0xf bank_mask:0xf\n"
      "v_add_f32_dpp v35, v43, v35 row_ror:8 row_mask:0xf bank_mask:0xf\n"
      "v_add_f32_dpp v36, v44, v36 row_ror:8 row_mask:0xf bank_mask:0xf\n"
      "v_add_f32_dpp v37, v45, v37 row_ror:8 row_mask:0xf bank_mask:0xf\n"
      "v_add_f32_dpp v38, v46, v38 row_ror:8 row_mask:0xf bank_mask:0xf\n"
      "v_add_f32_dpp v39, v47, v39 row_ror:8 row_mask:0xf bank_mask:0xf\n"
      "v_and_b32_e32 v1, 32, v0\n"
      "v_lshlrev_b32_e32 v1, 4, v1\n"
      "v_and_b32_e32 v2, 16, v0\n"
      "v_lshl_add_u32 v1, v2, 3, v1\n"
      "v_and_b32_e32 v2, 7, v0\n"
      "v_lshl_add_u32 v1, v2, 4, v1\n"
      "s_lshl_b32 s80, s48, 10\n"
      "s_add_u32 s80, s80, s72\n"
      "v_add_u32_e32 v1, s80, v1\n"
      "s_mov_b64 exec, s[88:89]\n"
      "ds_write_b128 v1, v[32:35]\n"
      "ds_write_b128 v1, v[36:39] offset:256\n"
      "s_mov_b32 exec_lo, 0x00010001\n"
      "s_mov_b32 exec_hi, 0x00010001\n"
      "v_lshrrev_b32_e32 v1, 2, v0\n"
      "s_lshl_b32 s80, s48, 4\n"
      "s_add_u32 s80, s80, s72\n"
      "v_add_u32_e32 v1, s80, v1\n"
      "ds_write_b32 v1, v62 offset:4096\n"
      "s_mov_b64 exec, 1\n"
      "s_lshl_b32 s80, s48, 2\n"
      "s_add_u32 s80, s80, s72\n"
      "v_mov_b32_e32 v1, s80\n"
      "v_mov_b32_e32 v2, s60\n"
      "ds_write_b32 v1, v2 offset:4160\n"
      "v_mov_b32_e32 v1, s73\n"
      "v_mov_b32_e32 v2, 1\n"
      "ds_add_rtn_u32 v3, v1, v2\n"
      "s_waitcnt lgkmcnt(0)\n"
      "v_readfirstlane_b32 s80, v3\n"
      "s_mov_b64 exec, -1\n"
      "s_cmp_lg_u32 s80, 3\n"
      "s_cbranch_scc1 FIND_116_%=\n"
      "v_mov_b32_e32 v1, s72\n"
      "ds_read_b128 v[4:7], v1 offset:4160\n"
      "ds_read_b128 v[8:11], v1 offset:4096\n"
      "ds_read_b128 v[12:15], v1 offset:4112\n"
      "ds_read_b128 v[16:19], v1 offset:4128\n"
      "ds_read_b128 v[20:23], v1 offset:4144\n"
      "v_lshlrev_b32_e32 v24, 4, v0\n"
      "v_add_u32_e32 v1, s72, v24\n"
      "ds_read_b128 v[32:35], v1 offset:0\n"
      "ds_read_b128 v[36:39], v1 offset:1024\n"
      "ds_read_b128 v[40:43], v1 offset:2048\n"
      "ds_read_b128 v[44:47], v1 offset:3072\n"
      "s_waitcnt lgkmcnt(4)\n"
      "v_max3_f32 v25, v4, v5, v6\n"
      "v_max_f32_e32 v25, v25, v7\n"
      "v_sub_f32_e32 v4, v4, v25\n"
      "v_sub_f32_e32 v5, v5, v25\n"
      "v_sub_f32_e32 v6, v6, v25\n"
      "v_sub_f32_e32 v7, v7, v25\n"
      "v_mul_f32_e32 v4, 0x3db8aa3b, v4\n"
      "v_mul_f32_e32 v5, 0x3db8aa3b, v5\n"
      "v_mul_f32_e32 v6, 0x3db8aa3b, v6\n"
      "v_mul_f32_e32 v7, 0x3db8aa3b, v7\n"
      "v_exp_f32_e32 v4, v4\n"
      "v_exp_f32_e32 v5, v5\n"
      "v_exp_f32_e32 v6, v6\n"
      "v_exp_f32_e32 v7, v7\n"
      "v_add_f32_e32 v8, v8, v9\n"
      "v_add_f32_e32 v10, v10, v11\n"
      "v_add_f32_e32 v8, v8, v10\n"
      "v_add_f32_e32 v12, v12, v13\n"
      "v_add_f32_e32 v14, v14, v15\n"
      "v_add_f32_e32 v12, v12, v14\n"
      "v_add_f32_e32 v16, v16, v17\n"
      "v_add_f32_e32 v18, v18, v19\n"
      "v_add_f32_e32 v16, v16, v18\n"
      "v_add_f32_e32 v20, v20, v21\n"
      "v_add_f32_e32 v22, v22, v23\n"
      "v_add_f32_e32 v20, v20, v22\n"
      "v_mul_f32_e32 v26, v4, v8\n"
      "v_fmac_f32_e32 v26, v5, v12\n"
      "v_fmac_f32_e32 v26, v6, v16\n"
      "v_fmac_f32_e32 v26, v7, v20\n"
      "s_waitcnt lgkmcnt(0)\n"
      "v_mul_f32_e32 v28, v4, v32\n"
      "v_mul_f32_e32 v29, v4, v33\n"
      "v_mul_f32_e32 v30, v4, v34\n"
      "v_mul_f32_e32 v31, v4, v35\n"
      "v_fmac_f32_e32 v28, v5, v36\n"
      "v_fmac_f32_e32 v29, v5, v37\n"
      "v_fmac_f32_e32 v30, v5, v38\n"
      "v_fmac_f32_e32 v31, v5, v39\n"
      "v_fmac_f32_e32 v28, v6, v40\n"
      "v_fmac_f32_e32 v29, v6, v41\n"
      "v_fmac_f32_e32 v30, v6, v42\n"
      "v_fmac_f32_e32 v31, v6, v43\n"
      "v_fmac_f32_e32 v28, v7, v44\n"
      "v_fmac_f32_e32 v29, v7, v45\n"
      "v_fmac_f32_e32 v30, v7, v46\n"
      "v_fmac_f32_e32 v31, v7, v47\n"
      "v_min_u32_e32 v28, -2, v28\n"
      "v_min_u32_e32 v29, -2, v29\n"
      "v_min_u32_e32 v30, -2, v30\n"
      "v_min_u32_e32 v31, -2, v31\n"
      "v_min_u32_e32 v25, -2, v25\n"
      "v_min_u32_e32 v26, -2, v26\n"
      "global_store_dwordx4 v24, v[28:31], s[74:75] sc1\n"
      "v_mov_b32_e32 v1, 0\n"
      "v_mov_b32_e32 v6, v25\n"
      "v_mov_b32_e32 v7, v26\n"
      "s_mov_b64 exec, 1\n"
      "global_store_dwordx2 v1, v[6:7], s[76:77] sc1\n"
      "s_mov_b64 exec, -1\n"
      "s_nop 1\n"
      "FIND_116_%=:\n"
      "s_setpc_b64 s[66:67]\n"
      :
      : [kb] "s"(kb), [vb] "s"(vb), [ws] "s"(wsp), [hd] "s"(h), [sp] "s"(s), [wid] "s"(w), [sl] "s"(sl), [x] "s"(x), [wq] "s"(wq), [bo] "s"(bo), [bi] "s"((int)blockIdx.x)
      : "v255", "a255", "s34", "s35", "s36", "s37", "s38", "s39", "s40", "s41", "s42", "s43", "s44", "s45", "s46", "s47", "s48", "s49", "s50", "s51", "s52", "s53", "s54", "s55", "s56", "s57", "s58", "s59", "s60", "s61", "s62", "s63", "s64", "s65", "s66", "s67", "s68", "s69", "s70", "s71", "s72", "s73", "s74", "s75", "s76", "s77", "s78", "s79", "s80", "s81", "s82", "s83", "s84", "s85", "s86", "s87", "s88", "s89", "s90", "s91", "s92", "s93", "s94", "s95", "s96", "s97", "s98", "s99", "s100", "s101", "vcc", "scc", "memory");
}

__global__ void __launch_bounds__(256, 1) fused_kernel(const u16* __restrict__ x, const u16* __restrict__ kc, const u16* __restrict__ vc, const u16* __restrict__ wq,
                                                       const u16* __restrict__ wo, const u16* __restrict__ wg, const u16* __restrict__ wu, const u16* __restrict__ wd,
                                                       char* __restrict__ ws, u16* __restrict__ out) {
  __shared__ __attribute__((aligned(16))) char smem[160 * 1024];
  asm volatile("" :: "v"(smem) : "memory");
  const int xcd = blockIdx.x & 7, i = blockIdx.x >> 3;
  if (i >= 28) chain_role(4 * xcd + (i - 28), x, kc, vc, wq, wo, wg, wu, wd, ws, out);
  else attn_role(2 * ((xcd + 1) & 7) + i / 14, i % 14, kc, vc, ws, x, wq, xcd, i);
}

extern "C" void kernel_launch(void* const* d_in, const int* in_sizes, int n_in, void* d_out, int out_size, void* d_ws, size_t ws_size, hipStream_t stream) {
  fused_kernel<<<256, 256, 0, stream>>>((const u16*)d_in[0], (const u16*)d_in[1], (const u16*)d_in[2], (const u16*)d_in[3], (const u16*)d_in[4],
                                        (const u16*)d_in[6], (const u16*)d_in[7], (const u16*)d_in[8], (char*)d_ws, (u16*)d_out);
}
```
